```python
import torch

def _softmax(x, axis=-1):
    return torch.softmax(x, dim=axis)
def _gelu(x):
    return torch.nn.functional.gelu(x, approximate="tanh")
def _silu(x):
    return torch.nn.functional.silu(x)
def _sigmoid(x):
    return torch.sigmoid(x)
def _layernorm(x, axis=-1, eps=1e-5):
    m = x.mean(dim=axis, keepdim=True)
    v = x.var(dim=axis, keepdim=True, unbiased=False)
    return (x - m) / torch.sqrt(v + eps)
def _scatter_add(tgt, idx, src, axis=0):
    return tgt.clone().index_add_(axis, idx, src)
def _segment_sum(src, seg, n):
    out = torch.zeros((n,) + src.shape[1:], dtype=src.dtype)
    return out.index_add_(0, seg, src)
def _pad(x, pw):
    flat = tuple(v for pair in reversed(pw) for v in pair)
    return torch.nn.functional.pad(x, flat)

kernel_name = 'concat_heads__mha_block_cached__mha_block_cached__near_ridge'

def setup_inputs():

    _w = (lambda a: (lambda t: t.to(torch.bfloat16) if t.is_floating_point() else t)(torch.as_tensor(a)))
    def _A_setup():
        B, Sq, D = 8, 112, 512
        H, Sk, Dh = 32, 512, 128
        return {
            "x": _w((lambda *s: torch.randn(*s, dtype=torch.bfloat16) * 0.8)(B, Sq, D)),
            "k_cache": _w((lambda *s: torch.randn(*s, dtype=torch.bfloat16) * 0.8)(B, H, Sk, Dh)),
            "v_cache": _w((lambda *s: torch.randn(*s, dtype=torch.bfloat16) * 0.05)(B, H, Sk, Dh)),
            "wq": _w((lambda *s: torch.randn(*s, dtype=torch.bfloat16) * 0.8)(D, H * Dh) * (D ** -0.5)),
            "wo": _w((lambda *s: torch.randn(*s, dtype=torch.bfloat16) * 0.05)(H * Dh, D) * ((H * Dh) ** -0.5)),
        }
    _A_in = _A_setup()

    def _B_setup():
        B, Sq, D = 8, 112, 512
        H, Sk, Dh = 32, 512, 128
        return {
            "x": _w((lambda *s: torch.randn(*s, dtype=torch.bfloat16) * 0.8)(B, Sq, D)),
            "k_cache": _w((lambda *s: torch.randn(*s, dtype=torch.bfloat16) * 0.8)(B, H, Sk, Dh)),
            "v_cache": _w((lambda *s: torch.randn(*s, dtype=torch.bfloat16) * 0.05)(B, H, Sk, Dh)),
            "wq": _w((lambda *s: torch.randn(*s, dtype=torch.bfloat16) * 0.8)(D, H * Dh) * (D ** -0.5)),
            "wo": _w((lambda *s: torch.randn(*s, dtype=torch.bfloat16) * 0.05)(H * Dh, D) * ((H * Dh) ** -0.5)),
        }
    _B_in = _B_setup()

    return {
        "A_x": _A_in["x"],
        "A_k_cache": _A_in["k_cache"],
        "A_v_cache": _A_in["v_cache"],
        "A_wq": _A_in["wq"],
        "A_wo": _A_in["wo"],
        "B_x": _B_in["x"],
        "B_k_cache": _B_in["k_cache"],
        "B_v_cache": _B_in["v_cache"],
        "B_wq": _B_in["wq"],
        "B_wo": _B_in["wo"],
    }

def reference(A_x, A_k_cache, A_v_cache, A_wq, A_wo, B_x, B_k_cache, B_v_cache, B_wq, B_wo):
    def _A_ref(x, k_cache, v_cache, wq, wo):
        B, Sq, D = x.shape
        H, Dh = 32, 128
        q = (lambda x, *d: x.permute(*d))((x @ wq).reshape(B, Sq, H, Dh), 0, 2, 1, 3)
        scores = torch.einsum("bhqd,bhkd->bhqk", q, k_cache) * (Dh ** -0.5)
        p = _softmax(scores, axis=-1)
        o = torch.einsum("bhqk,bhkd->bhqd", p, v_cache)
        return (lambda x, *d: x.permute(*d))(o, 0, 2, 1, 3).reshape(B, Sq, H * Dh) @ wo

    def _B_ref(x, k_cache, v_cache, wq, wo):
        B, Sq, D = x.shape
        H, Dh = 32, 128
        q = (lambda x, *d: x.permute(*d))((x @ wq).reshape(B, Sq, H, Dh), 0, 2, 1, 3)
        scores = torch.einsum("bhqd,bhkd->bhqk", q, k_cache) * (Dh ** -0.5)
        p = _softmax(scores, axis=-1)
        o = torch.einsum("bhqk,bhkd->bhqd", p, v_cache)
        return (lambda x, *d: x.permute(*d))(o, 0, 2, 1, 3).reshape(B, Sq, H * Dh) @ wo

    a = _A_ref(A_x, A_k_cache, A_v_cache, A_wq, A_wo)
    b = _B_ref(B_x, B_k_cache, B_v_cache, B_wq, B_wo)
    return torch.concatenate([a, b], axis=-1)
```

```cpp
#include <hip/hip_runtime.h>

struct KArgs { const void* p[16]; };

__global__ __launch_bounds__(256, 1) void mha2_fused_kernel(KArgs a) {
  __shared__ __attribute__((aligned(16))) char lds[163840];
  const unsigned wid = __builtin_amdgcn_readfirstlane(threadIdx.x >> 6);
  asm volatile(
R"(
s_mov_b32 s30, %2
s_mov_b32 s31, %1
s_bfe_u32 s27, s30, 0x10002
s_lshl_b32 s28, s27, 6
s_load_dwordx16 s[8:23], %0, s28
v_mbcnt_lo_u32_b32 v140, -1, 0
v_mbcnt_hi_u32_b32 v140, -1, v140
s_getpc_b64 s[64:65]
.Lk_pc:
s_and_b32 s65, s65, 0xffff
s_mov_b32 s66, .Lk_end-.Lk_pc
s_mov_b32 s67, 0x20000
s_lshr_b32 s28, s30, 3
s_lshl_b32 s28, s28, 2
s_add_u32 s28, s28, s31
s_lshl_b32 s28, s28, 6
v_lshl_add_u32 v254, v140, 13, s28
buffer_load_dword v251, v254, s[64:67], 0 offen
s_and_b32 s26, s30, 3
s_bfe_u32 s25, s30, 0x20003
s_lshr_b32 s24, s30, 5
s_lshl_b32 s68, s31, 12
v_bfe_u32 v128, v140, 2, 3
v_lshrrev_b32_e32 v129, 5, v140
v_and_b32_e32 v130, 3, v140
v_lshlrev_b32_e32 v134, 6, v129
v_lshl_add_u32 v132, v130, 4, v134
s_lshl_b32 s28, s31, 15
v_lshl_add_u32 v134, v128, 10, s28
v_add_u32_e32 v176, v134, v132
v_xor_b32_e32 v177, 16, v176
v_add_u32_e32 v177, 0x1c00, v177
v_add_u32_e32 v178, 0x3800, v176
v_add_u32_e32 v179, 0x3800, v177
s_and_b32 s29, s31, 1
s_lshl_b32 s29, s29, 5
v_xor_b32_e32 v135, s29, v132
s_lshl_b32 s28, s31, 17
s_add_u32 s28, s28, 0x400
v_lshl_add_u32 v134, v128, 13, s28
v_add_u32_e32 v221, v134, v135
v_subrev_u32_e32 v222, 0x380, v221
v_add_u32_e32 v223, 0xf800, v221
v_add_u32_e32 v224, 0xf800, v222
s_lshl_b32 s28, s25, 0
s_add_u32 s28, s28, s24
s_and_b32 s28, s28, 3
s_lshl_b32 s76, s28, 8
s_lshl_b32 s77, s28, 20
s_add_u32 s85, s77, 0x100
s_lshl_b32 s29, s26, 2
s_add_u32 s29, s29, s25
s_lshl_b32 s29, s29, 1
s_mul_i32 s28, s24, 0x1c000
s_lshl_b32 s78, s29, 8
s_waitcnt lgkmcnt(0)
s_add_u32 s36, s8, s28
s_addc_u32 s37, s9, 0
s_and_b32 s37, s37, 0xffff
s_mov_b32 s38, 0x1c000
s_mov_b32 s39, 0x20000
s_add_u32 s40, s14, s78
s_addc_u32 s41, s15, 0
s_sub_u32 s40, s40, 0x400
s_subb_u32 s41, s41, 0
s_and_b32 s41, s41, 0xffff
s_sub_u32 s42, 0x400400, s78
s_mov_b32 s43, 0x20000
s_mov_b32 m0, s68
s_nop 0
buffer_load_dwordx4 v176, s[36:39], s76 offen lds
buffer_load_dwordx4 v177, s[36:39], s76 offen offset:1024 lds
buffer_load_dwordx4 v178, s[36:39], s76 offen offset:2048 lds
buffer_load_dwordx4 v179, s[36:39], s76 offen offset:3072 lds
s_add_u32 s76, s76, 0x80
s_and_b32 s76, s76, 0x3ff
s_add_u32 m0, s68, 0x4000
s_nop 0
buffer_load_dwordx4 v221, s[40:43], s77 offen lds
buffer_load_dwordx4 v222, s[40:43], s77 offen offset:1024 lds
buffer_load_dwordx4 v223, s[40:43], s77 offen offset:2048 lds
buffer_load_dwordx4 v224, s[40:43], s77 offen offset:3072 lds
s_add_u32 s77, s77, 0x80000
s_and_b32 s77, s77, 0x3fffff
s_add_u32 m0, s68, 0x8000
s_nop 0
buffer_load_dwordx4 v221, s[40:43], s85 offen lds
buffer_load_dwordx4 v222, s[40:43], s85 offen offset:1024 lds
buffer_load_dwordx4 v223, s[40:43], s85 offen offset:2048 lds
buffer_load_dwordx4 v224, s[40:43], s85 offen offset:3072 lds
s_add_u32 s85, s85, 0x80000
s_and_b32 s85, s85, 0x3fffff
s_lshl_b32 s28, s24, 5
s_add_u32 s28, s28, s29
s_lshl_b32 s86, s28, 17
s_add_u32 s44, s10, s86
s_addc_u32 s45, s11, 0
s_sub_u32 s44, s44, 0x400
s_subb_u32 s45, s45, 0
s_and_b32 s45, s45, 0xffff
s_mov_b32 s46, 0x40400
s_mov_b32 s47, 0x20000
s_add_u32 s48, s12, s86
s_addc_u32 s49, s13, 0
s_sub_u32 s48, s48, 0x400
s_subb_u32 s49, s49, 0
s_and_b32 s49, s49, 0xffff
s_mov_b32 s50, 0x40400
s_mov_b32 s51, 0x20000
v_bfe_u32 v131, v140, 4, 1
v_lshlrev_b32_e32 v135, 5, v131
v_xor_b32_e32 v133, v135, v132
s_add_u32 s28, s68, 0x400
v_lshl_add_u32 v134, v128, 8, s28
v_add_u32_e32 v219, v134, v133
v_subrev_u32_e32 v220, 0x380, v219
v_add_u32_e32 v215, v134, v132
v_subrev_u32_e32 v216, 0x380, v215
v_xor_b32_e32 v217, 16, v215
v_xor_b32_e32 v218, 16, v216
s_lshl_b32 s28, s26, 20
s_lshl_b32 s29, s25, 8
s_add_u32 s87, s28, s29
s_add_u32 s52, s16, s87
s_addc_u32 s53, s17, 0
s_sub_u32 s52, s52, 0x400
s_subb_u32 s53, s53, 0
s_and_b32 s53, s53, 0xffff
s_sub_u32 s54, 0x400400, s87
s_mov_b32 s55, 0x20000
s_add_u32 s56, s20, 0
s_addc_u32 s57, s21, 0
s_and_b32 s57, s57, 0xffff
s_mov_b32 s58, 0x1a00000
s_mov_b32 s59, 0x20000
s_lshl_b32 s28, s27, 10
s_add_u32 s28, s28, s29
s_lshl_b32 s29, s26, 6
s_add_u32 s28, s28, s29
s_mul_i32 s84, s24, 0x38000
s_add_u32 s84, s84, s28
s_add_u32 s60, s18, s84
s_addc_u32 s61, s19, 0
s_and_b32 s61, s61, 0xffff
s_sub_u32 s62, 0x38000, s28
s_mov_b32 s63, 0x20000
s_lshl_b32 s80, s25, 18
s_lshl_b32 s81, s30, 15
s_lshl_b32 s28, s31, 13
s_add_u32 s81, s81, s28
s_lshl_b32 s82, s30, 13
s_lshl_b32 s28, s31, 11
s_add_u32 s82, s82, s28
s_add_u32 s82, s82, 0x1000000
s_mov_b32 s69, 0x3e0293ee
s_mov_b32 s70, 0x41000000
s_lshl_b32 s28, s31, 14
s_add_u32 s28, s28, 0x400
v_lshl_add_u32 v134, v128, 10, s28
v_xor_b32_e32 v135, s29, v133
v_add_u32_e32 v225, v134, v135
v_xor_b32_e32 v135, 0x80, v135
v_add_u32_e32 v226, v134, v135
v_subrev_u32_e32 v226, 0x400, v226
v_add_u32_e32 v227, 0x1800, v225
v_add_u32_e32 v228, 0x1800, v226
s_cmp_le_u32 s25, 0
s_cselect_b32 s28, 1, 0
s_lshl_b32 s34, s28, 18
s_xor_b32 s28, s28, s25
s_lshl_b32 s28, s28, 18
s_xor_b32 s96, s81, s28
s_cmp_le_u32 s25, 1
s_cselect_b32 s28, 2, 1
s_lshl_b32 s35, s28, 18
s_xor_b32 s28, s28, s25
s_lshl_b32 s28, s28, 18
s_xor_b32 s97, s81, s28
s_cmp_le_u32 s25, 2
s_cselect_b32 s28, 3, 2
s_lshl_b32 s73, s28, 18
s_xor_b32 s28, s28, s25
s_lshl_b32 s28, s28, 18
s_xor_b32 s98, s81, s28
v_lshrrev_b32_e32 v134, 4, v140
v_subrev_u32_e32 v135, 1, v134
v_cmp_lt_u32_e32 vcc, s25, v134
s_nop 1
v_cndmask_b32_e32 v135, v135, v134, vcc
v_mov_b32_e32 v136, s25
v_cmp_eq_u32_e32 vcc, 0, v134
s_nop 1
v_cndmask_b32_e32 v135, v135, v136, vcc
v_bfe_u32 v134, v140, 3, 1
v_lshl_add_u32 v134, v135, 1, v134
v_lshlrev_b32_e32 v134, 17, v134
s_lshl_b32 s28, s24, 2
s_add_u32 s28, s28, s31
s_lshl_b32 s28, s28, 12
s_add_u32 s28, s28, 0x400
)"
R"(
v_bfe_u32 v135, v140, 1, 2
v_lshl_add_u32 v134, v135, 10, v134
v_and_b32_e32 v135, 1, v140
v_lshl_add_u32 v134, v135, 7, v134
v_add_u32_e32 v252, s28, v134
v_and_b32_e32 v138, 15, v140
v_and_b32_e32 v137, 3, v128
v_lshl_add_u32 v139, v131, 1, v129
v_lshrrev_b32_e32 v135, 3, v138
v_xor_b32_e32 v134, v139, v135
v_lshlrev_b32_e32 v134, 4, v134
v_and_b32_e32 v136, 7, v138
v_lshl_add_u32 v134, v136, 6, v134
v_lshl_add_u32 v200, v135, 11, v134
v_lshl_add_u32 v134, v135, 10, v134
v_add_u32_e32 v180, s68, v134
v_lshlrev_b32_e32 v134, 6, v137
v_lshl_add_u32 v134, v131, 5, v134
v_lshl_add_u32 v134, v130, 3, v134
v_lshl_add_u32 v209, v139, 11, v134
v_xor_b32_e32 v210, 32, v209
v_lshl_add_u32 v134, v131, 8, v134
v_lshl_add_u32 v203, v129, 11, v134
v_xor_b32_e32 v204, 32, v203
v_add_u32_e32 v201, 0x10000, v200
v_add_u32_e32 v181, 0x10000, v180
v_add_u32_e32 v205, 0x10000, v203
v_add_u32_e32 v206, 0x10000, v204
v_add_u32_e32 v211, 0x10000, v209
v_add_u32_e32 v212, 0x10000, v210
v_add_u32_e32 v202, 0x20000, v200
v_add_u32_e32 v182, 0x20000, v180
v_add_u32_e32 v207, 0x20000, v203
v_add_u32_e32 v208, 0x20000, v204
v_add_u32_e32 v213, 0x20000, v209
v_add_u32_e32 v214, 0x20000, v210
s_lshl_b32 s28, s31, 5
v_add_u32_e32 v134, s28, v138
v_lshlrev_b32_e32 v135, 4, v139
v_lshl_add_u32 v230, v134, 11, v135
v_lshlrev_b32_e32 v229, 4, v140
v_mov_b32_e32 v136, 0x80000000
v_lshlrev_b32_e32 v134, 3, v130
v_cmp_gt_u32_e32 vcc, 8, v140
s_nop 1
v_cndmask_b32_e32 v134, v130, v134, vcc
v_xor_b32_e32 v134, s30, v134
s_lshl_b32 s28, s31, 8
s_add_u32 s28, s28, 0x1800000
v_lshl_add_u32 v135, v140, 2, s28
v_lshl_add_u32 v134, v134, 10, v135
s_lshl_b32 s28, s30, 10
v_add_u32_e32 v135, s28, v135
v_cmp_gt_u32_e32 vcc, 12, v140
s_nop 1
v_cndmask_b32_e32 v232, v136, v134, vcc
v_cndmask_b32_e32 v231, v136, v135, vcc
buffer_load_dword v233, v231, s[56:59], 0 offen sc1
s_add_u32 m0, s68, 0xc000
s_nop 0
buffer_load_dwordx4 v176, s[36:39], s76 offen lds
buffer_load_dwordx4 v177, s[36:39], s76 offen offset:1024 lds
buffer_load_dwordx4 v178, s[36:39], s76 offen offset:2048 lds
buffer_load_dwordx4 v179, s[36:39], s76 offen offset:3072 lds
s_add_u32 s76, s76, 0x80
s_and_b32 s76, s76, 0x3ff
s_cmp_eq_u32 s31, 3
s_cbranch_scc1 .Lk_w3
s_waitcnt vmcnt(9)
s_barrier
ds_read_b128 a[96:99], v180
ds_read_b128 a[100:103], v180 offset:2048
ds_read_b64_tr_b16 a[0:1], v209 offset:16384
ds_read_b64_tr_b16 a[2:3], v209 offset:16640
ds_read_b64_tr_b16 a[4:5], v210 offset:16384
ds_read_b64_tr_b16 a[6:7], v210 offset:16640
ds_read_b64_tr_b16 a[8:9], v209 offset:16896
ds_read_b64_tr_b16 a[10:11], v209 offset:17152
ds_read_b64_tr_b16 a[12:13], v210 offset:16896
ds_read_b64_tr_b16 a[14:15], v210 offset:17152
ds_read_b64_tr_b16 a[16:17], v209 offset:17408
ds_read_b64_tr_b16 a[18:19], v209 offset:17664
ds_read_b64_tr_b16 a[20:21], v210 offset:17408
ds_read_b64_tr_b16 a[22:23], v210 offset:17664
s_waitcnt lgkmcnt(10)
v_mfma_f32_16x16x32_bf16 v[0:3], a[0:3], a[96:99], 0
ds_read_b64_tr_b16 a[24:25], v209 offset:17920
v_mov_b32_e32 v242, 0
v_mfma_f32_16x16x32_bf16 v[8:11], a[0:3], a[100:103], 0
s_add_u32 m0, s68, 0x10000
s_nop 0
buffer_load_dwordx4 v221, s[40:43], s77 offen lds
ds_read_b64_tr_b16 a[26:27], v209 offset:18176
s_waitcnt lgkmcnt(8)
v_mfma_f32_16x16x32_bf16 v[4:7], a[4:7], a[96:99], 0
ds_read_b64_tr_b16 a[28:29], v210 offset:17920
v_mfma_f32_16x16x32_bf16 v[12:15], a[4:7], a[100:103], 0
ds_read_b64_tr_b16 a[30:31], v210 offset:18176
buffer_load_dwordx4 v222, s[40:43], s77 offen offset:1024 lds
v_mfma_f32_16x16x32_bf16 v[16:19], a[8:11], a[96:99], 0
ds_read_b128 a[104:107], v180 offset:512
ds_read_b128 a[108:111], v180 offset:2560
v_mfma_f32_16x16x32_bf16 v[24:27], a[8:11], a[100:103], 0
s_waitcnt lgkmcnt(8)
ds_read_b64_tr_b16 a[0:1], v209 offset:24576
ds_read_b64_tr_b16 a[2:3], v209 offset:24832
v_mfma_f32_16x16x32_bf16 v[20:23], a[12:15], a[96:99], 0
ds_read_b64_tr_b16 a[4:5], v210 offset:24576
v_mov_b32_e32 v243, 0
v_mfma_f32_16x16x32_bf16 v[28:31], a[12:15], a[100:103], 0
ds_read_b64_tr_b16 a[6:7], v210 offset:24832
buffer_load_dwordx4 v223, s[40:43], s77 offen offset:2048 lds
v_mfma_f32_16x16x32_bf16 v[32:35], a[16:19], a[96:99], 0
ds_read_b64_tr_b16 a[8:9], v209 offset:25088
v_mfma_f32_16x16x32_bf16 v[40:43], a[16:19], a[100:103], 0
s_waitcnt lgkmcnt(9)
ds_read_b64_tr_b16 a[10:11], v209 offset:25344
v_mov_b32_e32 v244, 0
v_mov_b32_e32 v245, 0
v_mfma_f32_16x16x32_bf16 v[36:39], a[20:23], a[96:99], 0
ds_read_b64_tr_b16 a[12:13], v210 offset:25088
v_mfma_f32_16x16x32_bf16 v[44:47], a[20:23], a[100:103], 0
buffer_load_dwordx4 v224, s[40:43], s77 offen offset:3072 lds
s_add_u32 s77, s77, 0x80000
s_and_b32 s77, s77, 0x3fffff
ds_read_b64_tr_b16 a[14:15], v210 offset:25344
v_mfma_f32_16x16x32_bf16 v[48:51], a[24:27], a[96:99], 0
s_add_u32 m0, s68, 0x14000
s_nop 0
buffer_load_dwordx4 v221, s[40:43], s85 offen lds
ds_read_b64_tr_b16 a[16:17], v209 offset:25600
v_mfma_f32_16x16x32_bf16 v[56:59], a[24:27], a[100:103], 0
s_waitcnt lgkmcnt(9)
ds_read_b64_tr_b16 a[18:19], v209 offset:25856
v_mfma_f32_16x16x32_bf16 v[52:55], a[28:31], a[96:99], 0
ds_read_b64_tr_b16 a[20:21], v210 offset:25600
buffer_load_dwordx4 v222, s[40:43], s85 offen offset:1024 lds
v_mfma_f32_16x16x32_bf16 v[60:63], a[28:31], a[100:103], 0
s_waitcnt lgkmcnt(7)
ds_read_b64_tr_b16 a[22:23], v210 offset:25856
v_mfma_f32_16x16x32_bf16 v[0:3], a[0:3], a[104:107], v[0:3]
ds_read_b64_tr_b16 a[24:25], v209 offset:26112
v_mfma_f32_16x16x32_bf16 v[8:11], a[0:3], a[108:111], v[8:11]
ds_read_b64_tr_b16 a[26:27], v209 offset:26368
v_mfma_f32_16x16x32_bf16 v[4:7], a[4:7], a[104:107], v[4:7]
ds_read_b64_tr_b16 a[28:29], v210 offset:26112
buffer_load_dwordx4 v223, s[40:43], s85 offen offset:2048 lds
v_mfma_f32_16x16x32_bf16 v[12:15], a[4:7], a[108:111], v[12:15]
s_waitcnt vmcnt(12)
s_barrier
ds_read_b64_tr_b16 a[30:31], v210 offset:26368
s_waitcnt lgkmcnt(8)
v_mfma_f32_16x16x32_bf16 v[16:19], a[8:11], a[104:107], v[16:19]
ds_read_b64_tr_b16 a[0:1], v209 offset:32768
buffer_load_dwordx4 v224, s[40:43], s85 offen offset:3072 lds
s_add_u32 s85, s85, 0x80000
s_and_b32 s85, s85, 0x3fffff
v_mfma_f32_16x16x32_bf16 v[24:27], a[8:11], a[108:111], v[24:27]
ds_read_b64_tr_b16 a[2:3], v209 offset:33024
v_mfma_f32_16x16x32_bf16 v[20:23], a[12:15], a[104:107], v[20:23]
ds_read_b64_tr_b16 a[4:5], v210 offset:32768
v_mfma_f32_16x16x32_bf16 v[28:31], a[12:15], a[108:111], v[28:31]
s_waitcnt lgkmcnt(7)
ds_read_b64_tr_b16 a[6:7], v210 offset:33024
v_mfma_f32_16x16x32_bf16 v[32:35], a[16:19], a[104:107], v[32:35]
s_add_u32 m0, s68, 0x18000
s_nop 0
buffer_load_dwordx4 v176, s[36:39], s76 offen lds
ds_read_b64_tr_b16 a[8:9], v209 offset:33280
v_mfma_f32_16x16x32_bf16 v[40:43], a[16:19], a[108:111], v[40:43]
ds_read_b64_tr_b16 a[10:11], v209 offset:33536
v_mfma_f32_16x16x32_bf16 v[36:39], a[20:23], a[104:107], v[36:39]
ds_read_b64_tr_b16 a[12:13], v210 offset:33280
buffer_load_dwordx4 v177, s[36:39], s76 offen offset:1024 lds
v_mfma_f32_16x16x32_bf16 v[44:47], a[20:23], a[108:111], v[44:47]
s_waitcnt lgkmcnt(7)
ds_read_b64_tr_b16 a[14:15], v210 offset:33536
v_mfma_f32_16x16x32_bf16 v[48:51], a[24:27], a[104:107], v[48:51]
ds_read_b64_tr_b16 a[16:17], v209 offset:33792
v_mfma_f32_16x16x32_bf16 v[56:59], a[24:27], a[108:111], v[56:59]
ds_read_b64_tr_b16 a[18:19], v209 offset:34048
v_mfma_f32_16x16x32_bf16 v[52:55], a[28:31], a[104:107], v[52:55]
ds_read_b64_tr_b16 a[20:21], v210 offset:33792
buffer_load_dwordx4 v178, s[36:39], s76 offen offset:2048 lds
v_mfma_f32_16x16x32_bf16 v[60:63], a[28:31], a[108:111], v[60:63]
s_barrier
ds_read_b64_tr_b16 a[22:23], v210 offset:34048
s_waitcnt lgkmcnt(8)
v_mfma_f32_16x16x32_bf16 v[64:67], a[0:3], a[96:99], 0
ds_read_b64_tr_b16 a[24:25], v209 offset:34304
buffer_load_dwordx4 v179, s[36:39], s76 offen offset:3072 lds
s_add_u32 s76, s76, 0x80
s_and_b32 s76, s76, 0x3ff
v_mfma_f32_16x16x32_bf16 v[72:75], a[0:3], a[100:103], 0
ds_read_b64_tr_b16 a[26:27], v209 offset:34560
v_mfma_f32_16x16x32_bf16 v[68:71], a[4:7], a[96:99], 0
ds_read_b64_tr_b16 a[28:29], v210 offset:34304
s_waitcnt vmcnt(16)
v_add_u32_e32 v233, 1, v233
buffer_store_dword v233, v231, s[56:59], 0 offen offset:128 sc1
)"
R"(
v_mfma_f32_16x16x32_bf16 v[76:79], a[4:7], a[100:103], 0
s_waitcnt lgkmcnt(7)
ds_read_b64_tr_b16 a[30:31], v210 offset:34560
v_mfma_f32_16x16x32_bf16 v[80:83], a[8:11], a[96:99], 0
ds_read_b64_tr_b16 a[0:1], v209 offset:40960
v_mfma_f32_16x16x32_bf16 v[88:91], a[8:11], a[100:103], 0
s_add_u32 m0, s68, 0x1c000
s_nop 0
buffer_load_dwordx4 v221, s[40:43], s77 offen lds
ds_read_b64_tr_b16 a[2:3], v209 offset:41216
v_mfma_f32_16x16x32_bf16 v[84:87], a[12:15], a[96:99], 0
ds_read_b64_tr_b16 a[4:5], v210 offset:40960
v_mfma_f32_16x16x32_bf16 v[92:95], a[12:15], a[100:103], 0
s_waitcnt lgkmcnt(7)
ds_read_b64_tr_b16 a[6:7], v210 offset:41216
buffer_load_dwordx4 v222, s[40:43], s77 offen offset:1024 lds
v_mfma_f32_16x16x32_bf16 v[96:99], a[16:19], a[96:99], 0
ds_read_b64_tr_b16 a[8:9], v209 offset:41472
v_mfma_f32_16x16x32_bf16 v[104:107], a[16:19], a[100:103], 0
ds_read_b64_tr_b16 a[10:11], v209 offset:41728
v_mfma_f32_16x16x32_bf16 v[100:103], a[20:23], a[96:99], 0
ds_read_b64_tr_b16 a[12:13], v210 offset:41472
buffer_load_dwordx4 v223, s[40:43], s77 offen offset:2048 lds
v_mfma_f32_16x16x32_bf16 v[108:111], a[20:23], a[100:103], 0
s_waitcnt lgkmcnt(7)
ds_read_b64_tr_b16 a[14:15], v210 offset:41728
v_mfma_f32_16x16x32_bf16 v[112:115], a[24:27], a[96:99], 0
ds_read_b64_tr_b16 a[16:17], v209 offset:41984
buffer_load_dwordx4 v224, s[40:43], s77 offen offset:3072 lds
s_add_u32 s77, s77, 0x80000
s_and_b32 s77, s77, 0x3fffff
v_mfma_f32_16x16x32_bf16 v[120:123], a[24:27], a[100:103], 0
ds_read_b64_tr_b16 a[18:19], v209 offset:42240
v_mfma_f32_16x16x32_bf16 v[116:119], a[28:31], a[96:99], 0
ds_read_b64_tr_b16 a[20:21], v210 offset:41984
v_mfma_f32_16x16x32_bf16 v[124:127], a[28:31], a[100:103], 0
s_waitcnt lgkmcnt(7)
ds_read_b64_tr_b16 a[22:23], v210 offset:42240
v_mfma_f32_16x16x32_bf16 v[64:67], a[0:3], a[104:107], v[64:67]
s_add_u32 m0, s68, 0x20000
s_nop 0
buffer_load_dwordx4 v176, s[36:39], s76 offen lds
ds_read_b64_tr_b16 a[24:25], v209 offset:42496
v_mfma_f32_16x16x32_bf16 v[72:75], a[0:3], a[108:111], v[72:75]
ds_read_b64_tr_b16 a[26:27], v209 offset:42752
v_mfma_f32_16x16x32_bf16 v[68:71], a[4:7], a[104:107], v[68:71]
ds_read_b64_tr_b16 a[28:29], v210 offset:42496
buffer_load_dwordx4 v177, s[36:39], s76 offen offset:1024 lds
v_mfma_f32_16x16x32_bf16 v[76:79], a[4:7], a[108:111], v[76:79]
s_waitcnt vmcnt(15)
s_barrier
ds_read_b64_tr_b16 a[30:31], v210 offset:42752
s_waitcnt lgkmcnt(8)
v_mfma_f32_16x16x32_bf16 v[80:83], a[8:11], a[104:107], v[80:83]
ds_read_b128 a[112:115], v180 offset:49152
ds_read_b128 a[116:119], v180 offset:51200
v_mfma_f32_16x16x32_bf16 v[88:91], a[8:11], a[108:111], v[88:91]
ds_read_b64_tr_b16 a[0:1], v211
ds_read_b64_tr_b16 a[2:3], v211 offset:256
v_mfma_f32_16x16x32_bf16 v[84:87], a[12:15], a[104:107], v[84:87]
ds_read_b64_tr_b16 a[4:5], v212
buffer_load_dwordx4 v178, s[36:39], s76 offen offset:2048 lds
v_mfma_f32_16x16x32_bf16 v[92:95], a[12:15], a[108:111], v[92:95]
s_waitcnt lgkmcnt(9)
ds_read_b64_tr_b16 a[6:7], v212 offset:256
v_mfma_f32_16x16x32_bf16 v[96:99], a[16:19], a[104:107], v[96:99]
ds_read_b64_tr_b16 a[8:9], v211 offset:512
buffer_load_dwordx4 v179, s[36:39], s76 offen offset:3072 lds
s_add_u32 s76, s76, 0x80
s_and_b32 s76, s76, 0x3ff
v_mfma_f32_16x16x32_bf16 v[104:107], a[16:19], a[108:111], v[104:107]
ds_read_b64_tr_b16 a[10:11], v211 offset:768
v_mfma_f32_16x16x32_bf16 v[100:103], a[20:23], a[104:107], v[100:103]
ds_read_b64_tr_b16 a[12:13], v212 offset:512
v_mfma_f32_16x16x32_bf16 v[108:111], a[20:23], a[108:111], v[108:111]
s_waitcnt lgkmcnt(9)
ds_read_b64_tr_b16 a[14:15], v212 offset:768
v_mfma_f32_16x16x32_bf16 v[112:115], a[24:27], a[104:107], v[112:115]
s_add_u32 m0, s68, 0x24000
s_nop 0
buffer_load_dwordx4 v221, s[40:43], s77 offen lds
ds_read_b64_tr_b16 a[16:17], v211 offset:1024
v_mfma_f32_16x16x32_bf16 v[120:123], a[24:27], a[108:111], v[120:123]
ds_read_b64_tr_b16 a[18:19], v211 offset:1280
v_mfma_f32_16x16x32_bf16 v[116:119], a[28:31], a[104:107], v[116:119]
ds_read_b64_tr_b16 a[20:21], v212 offset:1024
buffer_load_dwordx4 v222, s[40:43], s77 offen offset:1024 lds
v_mfma_f32_16x16x32_bf16 v[124:127], a[28:31], a[108:111], v[124:127]
s_barrier
ds_read_b64_tr_b16 a[22:23], v212 offset:1280
s_waitcnt lgkmcnt(8)
v_mfma_f32_16x16x32_bf16 v[0:3], a[0:3], a[112:115], v[0:3]
ds_read_b64_tr_b16 a[24:25], v211 offset:1536
v_mfma_f32_16x16x32_bf16 v[8:11], a[0:3], a[116:119], v[8:11]
ds_read_b64_tr_b16 a[26:27], v211 offset:1792
v_mfma_f32_16x16x32_bf16 v[4:7], a[4:7], a[112:115], v[4:7]
ds_read_b64_tr_b16 a[28:29], v212 offset:1536
buffer_load_dwordx4 v223, s[40:43], s77 offen offset:2048 lds
v_mfma_f32_16x16x32_bf16 v[12:15], a[4:7], a[116:119], v[12:15]
s_waitcnt lgkmcnt(7)
ds_read_b64_tr_b16 a[30:31], v212 offset:1792
v_mfma_f32_16x16x32_bf16 v[16:19], a[8:11], a[112:115], v[16:19]
ds_read_b128 a[120:123], v180 offset:49664
ds_read_b128 a[124:127], v180 offset:51712
buffer_load_dwordx4 v224, s[40:43], s77 offen offset:3072 lds
s_add_u32 s77, s77, 0x80000
s_and_b32 s77, s77, 0x3fffff
v_mfma_f32_16x16x32_bf16 v[24:27], a[8:11], a[116:119], v[24:27]
ds_read_b64_tr_b16 a[0:1], v211 offset:8192
ds_read_b64_tr_b16 a[2:3], v211 offset:8448
v_mfma_f32_16x16x32_bf16 v[20:23], a[12:15], a[112:115], v[20:23]
ds_read_b64_tr_b16 a[4:5], v212 offset:8192
v_mfma_f32_16x16x32_bf16 v[28:31], a[12:15], a[116:119], v[28:31]
s_waitcnt lgkmcnt(9)
ds_read_b64_tr_b16 a[6:7], v212 offset:8448
v_mfma_f32_16x16x32_bf16 v[32:35], a[16:19], a[112:115], v[32:35]
s_mov_b32 m0, s68
s_nop 0
buffer_load_dwordx4 v176, s[36:39], s76 offen lds
ds_read_b64_tr_b16 a[8:9], v211 offset:8704
v_mfma_f32_16x16x32_bf16 v[40:43], a[16:19], a[116:119], v[40:43]
ds_read_b64_tr_b16 a[10:11], v211 offset:8960
v_mfma_f32_16x16x32_bf16 v[36:39], a[20:23], a[112:115], v[36:39]
ds_read_b64_tr_b16 a[12:13], v212 offset:8704
buffer_load_dwordx4 v177, s[36:39], s76 offen offset:1024 lds
v_mfma_f32_16x16x32_bf16 v[44:47], a[20:23], a[116:119], v[44:47]
s_waitcnt lgkmcnt(9)
ds_read_b64_tr_b16 a[14:15], v212 offset:8960
v_mfma_f32_16x16x32_bf16 v[48:51], a[24:27], a[112:115], v[48:51]
ds_read_b64_tr_b16 a[16:17], v211 offset:9216
v_mfma_f32_16x16x32_bf16 v[56:59], a[24:27], a[116:119], v[56:59]
ds_read_b64_tr_b16 a[18:19], v211 offset:9472
v_mfma_f32_16x16x32_bf16 v[52:55], a[28:31], a[112:115], v[52:55]
ds_read_b64_tr_b16 a[20:21], v212 offset:9216
buffer_load_dwordx4 v178, s[36:39], s76 offen offset:2048 lds
v_mfma_f32_16x16x32_bf16 v[60:63], a[28:31], a[116:119], v[60:63]
s_waitcnt lgkmcnt(7)
ds_read_b64_tr_b16 a[22:23], v212 offset:9472
v_mfma_f32_16x16x32_bf16 v[0:3], a[0:3], a[120:123], v[0:3]
ds_read_b64_tr_b16 a[24:25], v211 offset:9728
buffer_load_dwordx4 v179, s[36:39], s76 offen offset:3072 lds
s_add_u32 s76, s76, 0x80
s_and_b32 s76, s76, 0x3ff
v_mfma_f32_16x16x32_bf16 v[8:11], a[0:3], a[124:127], v[8:11]
ds_read_b64_tr_b16 a[26:27], v211 offset:9984
v_mfma_f32_16x16x32_bf16 v[4:7], a[4:7], a[120:123], v[4:7]
ds_read_b64_tr_b16 a[28:29], v212 offset:9728
v_mfma_f32_16x16x32_bf16 v[12:15], a[4:7], a[124:127], v[12:15]
s_waitcnt vmcnt(21)
s_barrier
ds_read_b64_tr_b16 a[30:31], v212 offset:9984
s_waitcnt lgkmcnt(8)
v_mfma_f32_16x16x32_bf16 v[16:19], a[8:11], a[120:123], v[16:19]
s_add_u32 m0, s68, 0x4000
s_nop 0
buffer_load_dwordx4 v221, s[40:43], s77 offen lds
ds_read_b64_tr_b16 a[0:1], v211 offset:16384
v_mfma_f32_16x16x32_bf16 v[24:27], a[8:11], a[124:127], v[24:27]
ds_read_b64_tr_b16 a[2:3], v211 offset:16640
v_mfma_f32_16x16x32_bf16 v[20:23], a[12:15], a[120:123], v[20:23]
ds_read_b64_tr_b16 a[4:5], v212 offset:16384
buffer_load_dwordx4 v222, s[40:43], s77 offen offset:1024 lds
v_mfma_f32_16x16x32_bf16 v[28:31], a[12:15], a[124:127], v[28:31]
s_waitcnt lgkmcnt(7)
ds_read_b64_tr_b16 a[6:7], v212 offset:16640
v_mfma_f32_16x16x32_bf16 v[32:35], a[16:19], a[120:123], v[32:35]
ds_read_b64_tr_b16 a[8:9], v211 offset:16896
v_mfma_f32_16x16x32_bf16 v[40:43], a[16:19], a[124:127], v[40:43]
ds_read_b64_tr_b16 a[10:11], v211 offset:17152
v_mfma_f32_16x16x32_bf16 v[36:39], a[20:23], a[120:123], v[36:39]
ds_read_b64_tr_b16 a[12:13], v212 offset:16896
buffer_load_dwordx4 v223, s[40:43], s77 offen offset:2048 lds
v_mfma_f32_16x16x32_bf16 v[44:47], a[20:23], a[124:127], v[44:47]
s_waitcnt lgkmcnt(7)
ds_read_b64_tr_b16 a[14:15], v212 offset:17152
v_mfma_f32_16x16x32_bf16 v[48:51], a[24:27], a[120:123], v[48:51]
ds_read_b64_tr_b16 a[16:17], v211 offset:17408
buffer_load_dwordx4 v224, s[40:43], s77 offen offset:3072 lds
s_add_u32 s77, s77, 0x80000
s_and_b32 s77, s77, 0x3fffff
v_mfma_f32_16x16x32_bf16 v[56:59], a[24:27], a[124:127], v[56:59]
ds_read_b64_tr_b16 a[18:19], v211 offset:17664
v_mfma_f32_16x16x32_bf16 v[52:55], a[28:31], a[120:123], v[52:55]
ds_read_b64_tr_b16 a[20:21], v212 offset:17408
v_mfma_f32_16x16x32_bf16 v[60:63], a[28:31], a[124:127], v[60:63]
s_barrier
ds_read_b64_tr_b16 a[22:23], v212 offset:17664
s_waitcnt lgkmcnt(8)
v_mfma_f32_16x16x32_bf16 v[64:67], a[0:3], a[112:115], v[64:67]
s_add_u32 m0, s68, 0x8000
s_nop 0
buffer_load_dwordx4 v176, s[36:39], s76 offen lds
ds_read_b64_tr_b16 a[24:25], v211 offset:17920
v_mfma_f32_16x16x32_bf16 v[72:75], a[0:3], a[116:119], v[72:75]
ds_read_b64_tr_b16 a[26:27], v211 offset:18176
v_mfma_f32_16x16x32_bf16 v[68:71], a[4:7], a[112:115], v[68:71]
ds_read_b64_tr_b16 a[28:29], v212 offset:17920
buffer_load_dwordx4 v177, s[36:39], s76 offen offset:1024 lds
v_mfma_f32_16x16x32_bf16 v[76:79], a[4:7], a[116:119], v[76:79]
s_waitcnt lgkmcnt(7)
)"
R"(
ds_read_b64_tr_b16 a[30:31], v212 offset:18176
v_mfma_f32_16x16x32_bf16 v[80:83], a[8:11], a[112:115], v[80:83]
s_mov_b32 exec_lo, 0xffff
s_mov_b32 exec_hi, 0
buffer_load_dword v251, v252, s[52:55], 0 offen
s_mov_b64 exec, -1
ds_read_b64_tr_b16 a[0:1], v211 offset:24576
v_mfma_f32_16x16x32_bf16 v[88:91], a[8:11], a[116:119], v[88:91]
ds_read_b64_tr_b16 a[2:3], v211 offset:24832
v_mfma_f32_16x16x32_bf16 v[84:87], a[12:15], a[112:115], v[84:87]
ds_read_b64_tr_b16 a[4:5], v212 offset:24576
v_mfma_f32_16x16x32_bf16 v[92:95], a[12:15], a[116:119], v[92:95]
s_waitcnt lgkmcnt(7)
ds_read_b64_tr_b16 a[6:7], v212 offset:24832
buffer_load_dwordx4 v178, s[36:39], s76 offen offset:2048 lds
v_mfma_f32_16x16x32_bf16 v[96:99], a[16:19], a[112:115], v[96:99]
ds_read_b64_tr_b16 a[8:9], v211 offset:25088
v_mfma_f32_16x16x32_bf16 v[104:107], a[16:19], a[116:119], v[104:107]
ds_read_b64_tr_b16 a[10:11], v211 offset:25344
buffer_load_dwordx4 v179, s[36:39], s76 offen offset:3072 lds
s_add_u32 s76, s76, 0x80
s_and_b32 s76, s76, 0x3ff
v_mfma_f32_16x16x32_bf16 v[100:103], a[20:23], a[112:115], v[100:103]
ds_read_b64_tr_b16 a[12:13], v212 offset:25088
v_mfma_f32_16x16x32_bf16 v[108:111], a[20:23], a[116:119], v[108:111]
s_waitcnt lgkmcnt(7)
ds_read_b64_tr_b16 a[14:15], v212 offset:25344
v_mfma_f32_16x16x32_bf16 v[112:115], a[24:27], a[112:115], v[112:115]
s_add_u32 m0, s68, 0xc000
s_nop 0
buffer_load_dwordx4 v221, s[40:43], s77 offen lds
ds_read_b64_tr_b16 a[16:17], v211 offset:25600
v_mfma_f32_16x16x32_bf16 v[120:123], a[24:27], a[116:119], v[120:123]
ds_read_b64_tr_b16 a[18:19], v211 offset:25856
v_mfma_f32_16x16x32_bf16 v[116:119], a[28:31], a[112:115], v[116:119]
ds_read_b64_tr_b16 a[20:21], v212 offset:25600
buffer_load_dwordx4 v222, s[40:43], s77 offen offset:1024 lds
v_mfma_f32_16x16x32_bf16 v[124:127], a[28:31], a[116:119], v[124:127]
s_waitcnt lgkmcnt(7)
ds_read_b64_tr_b16 a[22:23], v212 offset:25856
v_mfma_f32_16x16x32_bf16 v[64:67], a[0:3], a[120:123], v[64:67]
ds_read_b64_tr_b16 a[24:25], v211 offset:26112
v_mfma_f32_16x16x32_bf16 v[72:75], a[0:3], a[124:127], v[72:75]
ds_read_b64_tr_b16 a[26:27], v211 offset:26368
v_mfma_f32_16x16x32_bf16 v[68:71], a[4:7], a[120:123], v[68:71]
ds_read_b64_tr_b16 a[28:29], v212 offset:26112
buffer_load_dwordx4 v223, s[40:43], s77 offen offset:2048 lds
v_mfma_f32_16x16x32_bf16 v[76:79], a[4:7], a[124:127], v[76:79]
s_waitcnt vmcnt(24)
s_barrier
ds_read_b64_tr_b16 a[30:31], v212 offset:26368
s_waitcnt lgkmcnt(8)
v_mfma_f32_16x16x32_bf16 v[80:83], a[8:11], a[120:123], v[80:83]
ds_read_b128 a[128:131], v181 offset:32768
ds_read_b128 a[132:135], v181 offset:34816
buffer_load_dwordx4 v224, s[40:43], s77 offen offset:3072 lds
s_add_u32 s77, s77, 0x80000
s_and_b32 s77, s77, 0x3fffff
v_mfma_f32_16x16x32_bf16 v[88:91], a[8:11], a[124:127], v[88:91]
ds_read_b64_tr_b16 a[0:1], v211 offset:49152
ds_read_b64_tr_b16 a[2:3], v211 offset:49408
v_mfma_f32_16x16x32_bf16 v[84:87], a[12:15], a[120:123], v[84:87]
ds_read_b64_tr_b16 a[4:5], v212 offset:49152
v_mfma_f32_16x16x32_bf16 v[92:95], a[12:15], a[124:127], v[92:95]
s_waitcnt lgkmcnt(9)
ds_read_b64_tr_b16 a[6:7], v212 offset:49408
v_mfma_f32_16x16x32_bf16 v[96:99], a[16:19], a[120:123], v[96:99]
s_add_u32 m0, s68, 0x10000
s_nop 0
buffer_load_dwordx4 v176, s[36:39], s76 offen lds
ds_read_b64_tr_b16 a[8:9], v211 offset:49664
v_mfma_f32_16x16x32_bf16 v[104:107], a[16:19], a[124:127], v[104:107]
ds_read_b64_tr_b16 a[10:11], v211 offset:49920
v_mfma_f32_16x16x32_bf16 v[100:103], a[20:23], a[120:123], v[100:103]
ds_read_b64_tr_b16 a[12:13], v212 offset:49664
buffer_load_dwordx4 v177, s[36:39], s76 offen offset:1024 lds
v_mfma_f32_16x16x32_bf16 v[108:111], a[20:23], a[124:127], v[108:111]
s_waitcnt lgkmcnt(9)
ds_read_b64_tr_b16 a[14:15], v212 offset:49920
v_mfma_f32_16x16x32_bf16 v[112:115], a[24:27], a[120:123], v[112:115]
ds_read_b64_tr_b16 a[16:17], v211 offset:50176
v_mfma_f32_16x16x32_bf16 v[120:123], a[24:27], a[124:127], v[120:123]
ds_read_b64_tr_b16 a[18:19], v211 offset:50432
v_mfma_f32_16x16x32_bf16 v[116:119], a[28:31], a[120:123], v[116:119]
ds_read_b64_tr_b16 a[20:21], v212 offset:50176
buffer_load_dwordx4 v178, s[36:39], s76 offen offset:2048 lds
v_mfma_f32_16x16x32_bf16 v[124:127], a[28:31], a[124:127], v[124:127]
s_barrier
ds_read_b64_tr_b16 a[22:23], v212 offset:50432
s_waitcnt lgkmcnt(8)
v_mfma_f32_16x16x32_bf16 v[0:3], a[0:3], a[128:131], v[0:3]
ds_read_b64_tr_b16 a[24:25], v211 offset:50688
buffer_load_dwordx4 v179, s[36:39], s76 offen offset:3072 lds
s_add_u32 s76, s76, 0x80
s_and_b32 s76, s76, 0x3ff
v_mfma_f32_16x16x32_bf16 v[8:11], a[0:3], a[132:135], v[8:11]
ds_read_b64_tr_b16 a[26:27], v211 offset:50944
v_mfma_f32_16x16x32_bf16 v[4:7], a[4:7], a[128:131], v[4:7]
ds_read_b64_tr_b16 a[28:29], v212 offset:50688
v_mfma_f32_16x16x32_bf16 v[12:15], a[4:7], a[132:135], v[12:15]
s_waitcnt lgkmcnt(7)
ds_read_b64_tr_b16 a[30:31], v212 offset:50944
v_mfma_f32_16x16x32_bf16 v[16:19], a[8:11], a[128:131], v[16:19]
s_add_u32 m0, s68, 0x14000
s_nop 0
buffer_load_dwordx4 v221, s[40:43], s77 offen lds
ds_read_b128 a[136:139], v181 offset:33280
ds_read_b128 a[140:143], v181 offset:35328
v_mfma_f32_16x16x32_bf16 v[24:27], a[8:11], a[132:135], v[24:27]
ds_read_b64_tr_b16 a[0:1], v211 offset:57344
ds_read_b64_tr_b16 a[2:3], v211 offset:57600
v_mfma_f32_16x16x32_bf16 v[20:23], a[12:15], a[128:131], v[20:23]
ds_read_b64_tr_b16 a[4:5], v212 offset:57344
v_mfma_f32_16x16x32_bf16 v[28:31], a[12:15], a[132:135], v[28:31]
s_waitcnt lgkmcnt(9)
ds_read_b64_tr_b16 a[6:7], v212 offset:57600
v_mfma_f32_16x16x32_bf16 v[32:35], a[16:19], a[128:131], v[32:35]
ds_read_b64_tr_b16 a[8:9], v211 offset:57856
buffer_load_dwordx4 v222, s[40:43], s77 offen offset:1024 lds
v_mfma_f32_16x16x32_bf16 v[40:43], a[16:19], a[132:135], v[40:43]
ds_read_b64_tr_b16 a[10:11], v211 offset:58112
v_mfma_f32_16x16x32_bf16 v[36:39], a[20:23], a[128:131], v[36:39]
ds_read_b64_tr_b16 a[12:13], v212 offset:57856
v_mfma_f32_16x16x32_bf16 v[44:47], a[20:23], a[132:135], v[44:47]
s_waitcnt lgkmcnt(9)
ds_read_b64_tr_b16 a[14:15], v212 offset:58112
v_mfma_f32_16x16x32_bf16 v[48:51], a[24:27], a[128:131], v[48:51]
ds_read_b64_tr_b16 a[16:17], v211 offset:58368
buffer_load_dwordx4 v223, s[40:43], s77 offen offset:2048 lds
v_mfma_f32_16x16x32_bf16 v[56:59], a[24:27], a[132:135], v[56:59]
ds_read_b64_tr_b16 a[18:19], v211 offset:58624
v_mfma_f32_16x16x32_bf16 v[52:55], a[28:31], a[128:131], v[52:55]
ds_read_b64_tr_b16 a[20:21], v212 offset:58368
v_mfma_f32_16x16x32_bf16 v[60:63], a[28:31], a[132:135], v[60:63]
s_waitcnt lgkmcnt(7)
ds_read_b64_tr_b16 a[22:23], v212 offset:58624
v_mfma_f32_16x16x32_bf16 v[0:3], a[0:3], a[136:139], v[0:3]
ds_read_b64_tr_b16 a[24:25], v211 offset:58880
buffer_load_dwordx4 v224, s[40:43], s77 offen offset:3072 lds
s_add_u32 s77, s77, 0x80000
s_and_b32 s77, s77, 0x3fffff
v_mfma_f32_16x16x32_bf16 v[8:11], a[0:3], a[140:143], v[8:11]
ds_read_b64_tr_b16 a[26:27], v211 offset:59136
v_mfma_f32_16x16x32_bf16 v[4:7], a[4:7], a[136:139], v[4:7]
ds_read_b64_tr_b16 a[28:29], v212 offset:58880
v_mfma_f32_16x16x32_bf16 v[12:15], a[4:7], a[140:143], v[12:15]
s_waitcnt vmcnt(25)
s_barrier
ds_read_b64_tr_b16 a[30:31], v212 offset:59136
s_waitcnt lgkmcnt(8)
v_mfma_f32_16x16x32_bf16 v[16:19], a[8:11], a[136:139], v[16:19]
s_add_u32 m0, s68, 0x18000
s_nop 0
buffer_load_dwordx4 v176, s[36:39], s76 offen lds
ds_read_b128 a[144:147], v182
ds_read_b128 a[148:151], v182 offset:2048
v_mfma_f32_16x16x32_bf16 v[24:27], a[8:11], a[140:143], v[24:27]
ds_read_b64_tr_b16 a[0:1], v213 offset:16384
ds_read_b64_tr_b16 a[2:3], v213 offset:16640
v_mfma_f32_16x16x32_bf16 v[20:23], a[12:15], a[136:139], v[20:23]
ds_read_b64_tr_b16 a[4:5], v214 offset:16384
v_mfma_f32_16x16x32_bf16 v[28:31], a[12:15], a[140:143], v[28:31]
s_waitcnt lgkmcnt(9)
ds_read_b64_tr_b16 a[6:7], v214 offset:16640
buffer_load_dwordx4 v177, s[36:39], s76 offen offset:1024 lds
v_mfma_f32_16x16x32_bf16 v[32:35], a[16:19], a[136:139], v[32:35]
ds_read_b64_tr_b16 a[8:9], v213 offset:16896
v_mfma_f32_16x16x32_bf16 v[40:43], a[16:19], a[140:143], v[40:43]
ds_read_b64_tr_b16 a[10:11], v213 offset:17152
v_mfma_f32_16x16x32_bf16 v[36:39], a[20:23], a[136:139], v[36:39]
ds_read_b64_tr_b16 a[12:13], v214 offset:16896
buffer_load_dwordx4 v178, s[36:39], s76 offen offset:2048 lds
v_mfma_f32_16x16x32_bf16 v[44:47], a[20:23], a[140:143], v[44:47]
s_waitcnt lgkmcnt(9)
ds_read_b64_tr_b16 a[14:15], v214 offset:17152
v_mfma_f32_16x16x32_bf16 v[48:51], a[24:27], a[136:139], v[48:51]
ds_read_b64_tr_b16 a[16:17], v213 offset:17408
buffer_load_dwordx4 v179, s[36:39], s76 offen offset:3072 lds
s_add_u32 s76, s76, 0x80
s_and_b32 s76, s76, 0x3ff
v_mfma_f32_16x16x32_bf16 v[56:59], a[24:27], a[140:143], v[56:59]
ds_read_b64_tr_b16 a[18:19], v213 offset:17664
v_mfma_f32_16x16x32_bf16 v[52:55], a[28:31], a[136:139], v[52:55]
ds_read_b64_tr_b16 a[20:21], v214 offset:17408
v_mfma_f32_16x16x32_bf16 v[60:63], a[28:31], a[140:143], v[60:63]
s_barrier
ds_read_b64_tr_b16 a[22:23], v214 offset:17664
s_waitcnt lgkmcnt(8)
v_mfma_f32_16x16x32_bf16 v[0:3], a[0:3], a[144:147], v[0:3]
s_add_u32 m0, s68, 0x1c000
s_nop 0
buffer_load_dwordx4 v221, s[40:43], s77 offen lds
ds_read_b64_tr_b16 a[24:25], v213 offset:17920
v_mfma_f32_16x16x32_bf16 v[8:11], a[0:3], a[148:151], v[8:11]
ds_read_b64_tr_b16 a[26:27], v213 offset:18176
v_mfma_f32_16x16x32_bf16 v[4:7], a[4:7], a[144:147], v[4:7]
ds_read_b64_tr_b16 a[28:29], v214 offset:17920
v_mfma_f32_16x16x32_bf16 v[12:15], a[4:7], a[148:151], v[12:15]
s_waitcnt lgkmcnt(7)
ds_read_b64_tr_b16 a[30:31], v214 offset:18176
)"
R"(
v_mfma_f32_16x16x32_bf16 v[16:19], a[8:11], a[144:147], v[16:19]
ds_read_b128 a[152:155], v182 offset:512
ds_read_b128 a[156:159], v182 offset:2560
buffer_load_dwordx4 v222, s[40:43], s77 offen offset:1024 lds
v_mfma_f32_16x16x32_bf16 v[24:27], a[8:11], a[148:151], v[24:27]
ds_read_b64_tr_b16 a[0:1], v213 offset:24576
ds_read_b64_tr_b16 a[2:3], v213 offset:24832
v_mfma_f32_16x16x32_bf16 v[20:23], a[12:15], a[144:147], v[20:23]
s_mov_b32 exec_lo, 0xff0000
s_mov_b32 exec_hi, 0xff
buffer_load_dword v251, v252, s[52:55], 0 offen
s_mov_b64 exec, -1
ds_read_b64_tr_b16 a[4:5], v214 offset:24576
v_mfma_f32_16x16x32_bf16 v[28:31], a[12:15], a[148:151], v[28:31]
s_waitcnt lgkmcnt(9)
ds_read_b64_tr_b16 a[6:7], v214 offset:24832
v_mfma_f32_16x16x32_bf16 v[32:35], a[16:19], a[144:147], v[32:35]
ds_read_b64_tr_b16 a[8:9], v213 offset:25088
v_mfma_f32_16x16x32_bf16 v[40:43], a[16:19], a[148:151], v[40:43]
ds_read_b64_tr_b16 a[10:11], v213 offset:25344
v_mfma_f32_16x16x32_bf16 v[36:39], a[20:23], a[144:147], v[36:39]
ds_read_b64_tr_b16 a[12:13], v214 offset:25088
v_mfma_f32_16x16x32_bf16 v[44:47], a[20:23], a[148:151], v[44:47]
s_waitcnt lgkmcnt(9)
ds_read_b64_tr_b16 a[14:15], v214 offset:25344
v_mfma_f32_16x16x32_bf16 v[48:51], a[24:27], a[144:147], v[48:51]
ds_read_b64_tr_b16 a[16:17], v213 offset:25600
buffer_load_dwordx4 v223, s[40:43], s77 offen offset:2048 lds
v_mfma_f32_16x16x32_bf16 v[56:59], a[24:27], a[148:151], v[56:59]
ds_read_b64_tr_b16 a[18:19], v213 offset:25856
v_mfma_f32_16x16x32_bf16 v[52:55], a[28:31], a[144:147], v[52:55]
ds_read_b64_tr_b16 a[20:21], v214 offset:25600
v_mfma_f32_16x16x32_bf16 v[60:63], a[28:31], a[148:151], v[60:63]
s_waitcnt lgkmcnt(7)
ds_read_b64_tr_b16 a[22:23], v214 offset:25856
buffer_load_dwordx4 v224, s[40:43], s77 offen offset:3072 lds
s_add_u32 s77, s77, 0x80000
s_and_b32 s77, s77, 0x3fffff
v_mfma_f32_16x16x32_bf16 v[0:3], a[0:3], a[152:155], v[0:3]
ds_read_b64_tr_b16 a[24:25], v213 offset:26112
v_mfma_f32_16x16x32_bf16 v[8:11], a[0:3], a[156:159], v[8:11]
ds_read_b64_tr_b16 a[26:27], v213 offset:26368
v_mfma_f32_16x16x32_bf16 v[4:7], a[4:7], a[152:155], v[4:7]
ds_read_b64_tr_b16 a[28:29], v214 offset:26112
v_mfma_f32_16x16x32_bf16 v[12:15], a[4:7], a[156:159], v[12:15]
s_waitcnt vmcnt(26)
s_barrier
ds_read_b64_tr_b16 a[30:31], v214 offset:26368
s_waitcnt lgkmcnt(8)
v_mfma_f32_16x16x32_bf16 v[16:19], a[8:11], a[152:155], v[16:19]
ds_read_b128 a[160:163], v180
ds_read_b128 a[164:167], v180 offset:2048
v_mfma_f32_16x16x32_bf16 v[24:27], a[8:11], a[156:159], v[24:27]
ds_read_b64_tr_b16 a[0:1], v209 offset:16384
ds_read_b64_tr_b16 a[2:3], v209 offset:16640
v_mfma_f32_16x16x32_bf16 v[20:23], a[12:15], a[152:155], v[20:23]
ds_read_b64_tr_b16 a[4:5], v210 offset:16384
v_mfma_f32_16x16x32_bf16 v[28:31], a[12:15], a[156:159], v[28:31]
s_waitcnt lgkmcnt(9)
ds_read_b64_tr_b16 a[6:7], v210 offset:16640
v_mfma_f32_16x16x32_bf16 v[32:35], a[16:19], a[152:155], v[32:35]
ds_read_b64_tr_b16 a[8:9], v209 offset:16896
v_mfma_f32_16x16x32_bf16 v[40:43], a[16:19], a[156:159], v[40:43]
ds_read_b64_tr_b16 a[10:11], v209 offset:17152
v_mfma_f32_16x16x32_bf16 v[36:39], a[20:23], a[152:155], v[36:39]
ds_read_b64_tr_b16 a[12:13], v210 offset:16896
v_mfma_f32_16x16x32_bf16 v[44:47], a[20:23], a[156:159], v[44:47]
s_waitcnt lgkmcnt(9)
ds_read_b64_tr_b16 a[14:15], v210 offset:17152
v_mfma_f32_16x16x32_bf16 v[48:51], a[24:27], a[152:155], v[48:51]
ds_read_b64_tr_b16 a[16:17], v209 offset:17408
v_mfma_f32_16x16x32_bf16 v[56:59], a[24:27], a[156:159], v[56:59]
ds_read_b64_tr_b16 a[18:19], v209 offset:17664
v_mfma_f32_16x16x32_bf16 v[52:55], a[28:31], a[152:155], v[52:55]
ds_read_b64_tr_b16 a[20:21], v210 offset:17408
v_mfma_f32_16x16x32_bf16 v[60:63], a[28:31], a[156:159], v[60:63]
s_barrier
ds_read_b64_tr_b16 a[22:23], v210 offset:17664
s_waitcnt lgkmcnt(8)
v_mfma_f32_16x16x32_bf16 v[0:3], a[0:3], a[160:163], v[0:3]
ds_read_b64_tr_b16 a[24:25], v209 offset:17920
v_mfma_f32_16x16x32_bf16 v[8:11], a[0:3], a[164:167], v[8:11]
ds_read_b64_tr_b16 a[26:27], v209 offset:18176
v_mfma_f32_16x16x32_bf16 v[4:7], a[4:7], a[160:163], v[4:7]
ds_read_b64_tr_b16 a[28:29], v210 offset:17920
v_mfma_f32_16x16x32_bf16 v[12:15], a[4:7], a[164:167], v[12:15]
s_waitcnt lgkmcnt(7)
ds_read_b64_tr_b16 a[30:31], v210 offset:18176
v_mfma_f32_16x16x32_bf16 v[16:19], a[8:11], a[160:163], v[16:19]
ds_read_b128 a[168:171], v180 offset:512
ds_read_b128 a[172:175], v180 offset:2560
v_mfma_f32_16x16x32_bf16 v[24:27], a[8:11], a[164:167], v[24:27]
ds_read_b64_tr_b16 a[0:1], v209 offset:24576
ds_read_b64_tr_b16 a[2:3], v209 offset:24832
v_mfma_f32_16x16x32_bf16 v[20:23], a[12:15], a[160:163], v[20:23]
ds_read_b64_tr_b16 a[4:5], v210 offset:24576
v_mfma_f32_16x16x32_bf16 v[28:31], a[12:15], a[164:167], v[28:31]
s_waitcnt lgkmcnt(9)
ds_read_b64_tr_b16 a[6:7], v210 offset:24832
v_mfma_f32_16x16x32_bf16 v[32:35], a[16:19], a[160:163], v[32:35]
ds_read_b64_tr_b16 a[8:9], v209 offset:25088
v_mfma_f32_16x16x32_bf16 v[40:43], a[16:19], a[164:167], v[40:43]
ds_read_b64_tr_b16 a[10:11], v209 offset:25344
v_mfma_f32_16x16x32_bf16 v[36:39], a[20:23], a[160:163], v[36:39]
ds_read_b64_tr_b16 a[12:13], v210 offset:25088
v_mfma_f32_16x16x32_bf16 v[44:47], a[20:23], a[164:167], v[44:47]
s_waitcnt lgkmcnt(9)
ds_read_b64_tr_b16 a[14:15], v210 offset:25344
v_mfma_f32_16x16x32_bf16 v[48:51], a[24:27], a[160:163], v[48:51]
ds_read_b64_tr_b16 a[16:17], v209 offset:25600
v_mfma_f32_16x16x32_bf16 v[56:59], a[24:27], a[164:167], v[56:59]
ds_read_b64_tr_b16 a[18:19], v209 offset:25856
v_mfma_f32_16x16x32_bf16 v[52:55], a[28:31], a[160:163], v[52:55]
ds_read_b64_tr_b16 a[20:21], v210 offset:25600
v_mfma_f32_16x16x32_bf16 v[60:63], a[28:31], a[164:167], v[60:63]
s_waitcnt lgkmcnt(7)
ds_read_b64_tr_b16 a[22:23], v210 offset:25856
v_mfma_f32_16x16x32_bf16 v[0:3], a[0:3], a[168:171], v[0:3]
ds_read_b64_tr_b16 a[24:25], v209 offset:26112
v_mfma_f32_16x16x32_bf16 v[8:11], a[0:3], a[172:175], v[8:11]
ds_read_b64_tr_b16 a[26:27], v209 offset:26368
v_mfma_f32_16x16x32_bf16 v[4:7], a[4:7], a[168:171], v[4:7]
ds_read_b64_tr_b16 a[28:29], v210 offset:26112
v_mfma_f32_16x16x32_bf16 v[12:15], a[4:7], a[172:175], v[12:15]
s_waitcnt vmcnt(17)
s_barrier
ds_read_b64_tr_b16 a[30:31], v210 offset:26368
s_waitcnt lgkmcnt(8)
v_mfma_f32_16x16x32_bf16 v[16:19], a[8:11], a[168:171], v[16:19]
ds_read_b128 a[176:179], v180 offset:32768
ds_read_b128 a[180:183], v180 offset:34816
v_mfma_f32_16x16x32_bf16 v[24:27], a[8:11], a[172:175], v[24:27]
ds_read_b64_tr_b16 a[0:1], v209 offset:49152
ds_read_b64_tr_b16 a[2:3], v209 offset:49408
v_mfma_f32_16x16x32_bf16 v[20:23], a[12:15], a[168:171], v[20:23]
ds_read_b64_tr_b16 a[4:5], v210 offset:49152
v_mfma_f32_16x16x32_bf16 v[28:31], a[12:15], a[172:175], v[28:31]
s_waitcnt lgkmcnt(9)
ds_read_b64_tr_b16 a[6:7], v210 offset:49408
v_mfma_f32_16x16x32_bf16 v[32:35], a[16:19], a[168:171], v[32:35]
ds_read_b64_tr_b16 a[8:9], v209 offset:49664
v_mfma_f32_16x16x32_bf16 v[40:43], a[16:19], a[172:175], v[40:43]
ds_read_b64_tr_b16 a[10:11], v209 offset:49920
v_mfma_f32_16x16x32_bf16 v[36:39], a[20:23], a[168:171], v[36:39]
ds_read_b64_tr_b16 a[12:13], v210 offset:49664
v_mfma_f32_16x16x32_bf16 v[44:47], a[20:23], a[172:175], v[44:47]
s_waitcnt lgkmcnt(9)
ds_read_b64_tr_b16 a[14:15], v210 offset:49920
v_mfma_f32_16x16x32_bf16 v[48:51], a[24:27], a[168:171], v[48:51]
ds_read_b64_tr_b16 a[16:17], v209 offset:50176
v_mfma_f32_16x16x32_bf16 v[56:59], a[24:27], a[172:175], v[56:59]
ds_read_b64_tr_b16 a[18:19], v209 offset:50432
v_mfma_f32_16x16x32_bf16 v[52:55], a[28:31], a[168:171], v[52:55]
ds_read_b64_tr_b16 a[20:21], v210 offset:50176
v_mfma_f32_16x16x32_bf16 v[60:63], a[28:31], a[172:175], v[60:63]
s_barrier
ds_read_b64_tr_b16 a[22:23], v210 offset:50432
s_waitcnt lgkmcnt(8)
v_mfma_f32_16x16x32_bf16 v[0:3], a[0:3], a[176:179], v[0:3]
ds_read_b64_tr_b16 a[24:25], v209 offset:50688
v_mfma_f32_16x16x32_bf16 v[8:11], a[0:3], a[180:183], v[8:11]
ds_read_b64_tr_b16 a[26:27], v209 offset:50944
v_mfma_f32_16x16x32_bf16 v[4:7], a[4:7], a[176:179], v[4:7]
ds_read_b64_tr_b16 a[28:29], v210 offset:50688
v_mfma_f32_16x16x32_bf16 v[12:15], a[4:7], a[180:183], v[12:15]
s_waitcnt lgkmcnt(7)
ds_read_b64_tr_b16 a[30:31], v210 offset:50944
v_mfma_f32_16x16x32_bf16 v[16:19], a[8:11], a[176:179], v[16:19]
ds_read_b128 a[184:187], v180 offset:33280
ds_read_b128 a[188:191], v180 offset:35328
v_mfma_f32_16x16x32_bf16 v[24:27], a[8:11], a[180:183], v[24:27]
ds_read_b64_tr_b16 a[0:1], v209 offset:57344
ds_read_b64_tr_b16 a[2:3], v209 offset:57600
v_mfma_f32_16x16x32_bf16 v[20:23], a[12:15], a[176:179], v[20:23]
ds_read_b64_tr_b16 a[4:5], v210 offset:57344
v_mfma_f32_16x16x32_bf16 v[28:31], a[12:15], a[180:183], v[28:31]
s_waitcnt lgkmcnt(9)
ds_read_b64_tr_b16 a[6:7], v210 offset:57600
v_mfma_f32_16x16x32_bf16 v[32:35], a[16:19], a[176:179], v[32:35]
ds_read_b64_tr_b16 a[8:9], v209 offset:57856
v_mfma_f32_16x16x32_bf16 v[40:43], a[16:19], a[180:183], v[40:43]
ds_read_b64_tr_b16 a[10:11], v209 offset:58112
v_mfma_f32_16x16x32_bf16 v[36:39], a[20:23], a[176:179], v[36:39]
ds_read_b64_tr_b16 a[12:13], v210 offset:57856
v_mfma_f32_16x16x32_bf16 v[44:47], a[20:23], a[180:183], v[44:47]
s_waitcnt lgkmcnt(9)
ds_read_b64_tr_b16 a[14:15], v210 offset:58112
v_mfma_f32_16x16x32_bf16 v[48:51], a[24:27], a[176:179], v[48:51]
ds_read_b64_tr_b16 a[16:17], v209 offset:58368
v_mfma_f32_16x16x32_bf16 v[56:59], a[24:27], a[180:183], v[56:59]
ds_read_b64_tr_b16 a[18:19], v209 offset:58624
v_mfma_f32_16x16x32_bf16 v[52:55], a[28:31], a[176:179], v[52:55]
ds_read_b64_tr_b16 a[20:21], v210 offset:58368
v_mfma_f32_16x16x32_bf16 v[60:63], a[28:31], a[180:183], v[60:63]
s_waitcnt lgkmcnt(7)
ds_read_b64_tr_b16 a[22:23], v210 offset:58624
v_mfma_f32_16x16x32_bf16 v[0:3], a[0:3], a[184:187], v[0:3]
ds_read_b64_tr_b16 a[24:25], v209 offset:58880
v_mfma_f32_16x16x32_bf16 v[8:11], a[0:3], a[188:191], v[8:11]
ds_read_b64_tr_b16 a[26:27], v209 offset:59136
)"
R"(
v_mfma_f32_16x16x32_bf16 v[4:7], a[4:7], a[184:187], v[4:7]
ds_read_b64_tr_b16 a[28:29], v210 offset:58880
v_mfma_f32_16x16x32_bf16 v[12:15], a[4:7], a[188:191], v[12:15]
s_waitcnt vmcnt(9)
s_barrier
ds_read_b64_tr_b16 a[30:31], v210 offset:59136
s_waitcnt lgkmcnt(8)
v_mfma_f32_16x16x32_bf16 v[16:19], a[8:11], a[184:187], v[16:19]
ds_read_b128 a[192:195], v181
ds_read_b128 a[196:199], v181 offset:2048
v_mfma_f32_16x16x32_bf16 v[24:27], a[8:11], a[188:191], v[24:27]
ds_read_b64_tr_b16 a[0:1], v211 offset:16384
ds_read_b64_tr_b16 a[2:3], v211 offset:16640
v_mfma_f32_16x16x32_bf16 v[20:23], a[12:15], a[184:187], v[20:23]
ds_read_b64_tr_b16 a[4:5], v212 offset:16384
v_mfma_f32_16x16x32_bf16 v[28:31], a[12:15], a[188:191], v[28:31]
s_waitcnt lgkmcnt(9)
ds_read_b64_tr_b16 a[6:7], v212 offset:16640
v_mfma_f32_16x16x32_bf16 v[32:35], a[16:19], a[184:187], v[32:35]
ds_read_b64_tr_b16 a[8:9], v211 offset:16896
v_mfma_f32_16x16x32_bf16 v[40:43], a[16:19], a[188:191], v[40:43]
ds_read_b64_tr_b16 a[10:11], v211 offset:17152
v_mfma_f32_16x16x32_bf16 v[36:39], a[20:23], a[184:187], v[36:39]
ds_read_b64_tr_b16 a[12:13], v212 offset:16896
v_mfma_f32_16x16x32_bf16 v[44:47], a[20:23], a[188:191], v[44:47]
s_waitcnt lgkmcnt(9)
ds_read_b64_tr_b16 a[14:15], v212 offset:17152
v_mfma_f32_16x16x32_bf16 v[48:51], a[24:27], a[184:187], v[48:51]
ds_read_b64_tr_b16 a[16:17], v211 offset:17408
v_mfma_f32_16x16x32_bf16 v[56:59], a[24:27], a[188:191], v[56:59]
ds_read_b64_tr_b16 a[18:19], v211 offset:17664
v_mfma_f32_16x16x32_bf16 v[52:55], a[28:31], a[184:187], v[52:55]
ds_read_b64_tr_b16 a[20:21], v212 offset:17408
v_mfma_f32_16x16x32_bf16 v[60:63], a[28:31], a[188:191], v[60:63]
s_barrier
ds_read_b64_tr_b16 a[22:23], v212 offset:17664
s_waitcnt lgkmcnt(8)
v_mfma_f32_16x16x32_bf16 v[0:3], a[0:3], a[192:195], v[0:3]
ds_read_b64_tr_b16 a[24:25], v211 offset:17920
v_mfma_f32_16x16x32_bf16 v[8:11], a[0:3], a[196:199], v[8:11]
ds_read_b64_tr_b16 a[26:27], v211 offset:18176
v_mfma_f32_16x16x32_bf16 v[4:7], a[4:7], a[192:195], v[4:7]
ds_read_b64_tr_b16 a[28:29], v212 offset:17920
v_mfma_f32_16x16x32_bf16 v[12:15], a[4:7], a[196:199], v[12:15]
s_waitcnt lgkmcnt(7)
ds_read_b64_tr_b16 a[30:31], v212 offset:18176
v_mfma_f32_16x16x32_bf16 v[16:19], a[8:11], a[192:195], v[16:19]
ds_read_b128 a[200:203], v181 offset:512
ds_read_b128 a[204:207], v181 offset:2560
v_mfma_f32_16x16x32_bf16 v[24:27], a[8:11], a[196:199], v[24:27]
ds_read_b64_tr_b16 a[0:1], v211 offset:24576
ds_read_b64_tr_b16 a[2:3], v211 offset:24832
v_mfma_f32_16x16x32_bf16 v[20:23], a[12:15], a[192:195], v[20:23]
ds_read_b64_tr_b16 a[4:5], v212 offset:24576
v_mfma_f32_16x16x32_bf16 v[28:31], a[12:15], a[196:199], v[28:31]
s_waitcnt lgkmcnt(9)
ds_read_b64_tr_b16 a[6:7], v212 offset:24832
v_mfma_f32_16x16x32_bf16 v[32:35], a[16:19], a[192:195], v[32:35]
ds_read_b64_tr_b16 a[8:9], v211 offset:25088
v_mfma_f32_16x16x32_bf16 v[40:43], a[16:19], a[196:199], v[40:43]
ds_read_b64_tr_b16 a[10:11], v211 offset:25344
v_mfma_f32_16x16x32_bf16 v[36:39], a[20:23], a[192:195], v[36:39]
ds_read_b64_tr_b16 a[12:13], v212 offset:25088
v_mfma_f32_16x16x32_bf16 v[44:47], a[20:23], a[196:199], v[44:47]
s_waitcnt lgkmcnt(9)
ds_read_b64_tr_b16 a[14:15], v212 offset:25344
v_mfma_f32_16x16x32_bf16 v[48:51], a[24:27], a[192:195], v[48:51]
ds_read_b64_tr_b16 a[16:17], v211 offset:25600
v_mfma_f32_16x16x32_bf16 v[56:59], a[24:27], a[196:199], v[56:59]
ds_read_b64_tr_b16 a[18:19], v211 offset:25856
v_mfma_f32_16x16x32_bf16 v[52:55], a[28:31], a[192:195], v[52:55]
ds_read_b64_tr_b16 a[20:21], v212 offset:25600
v_mfma_f32_16x16x32_bf16 v[60:63], a[28:31], a[196:199], v[60:63]
s_waitcnt lgkmcnt(7)
ds_read_b64_tr_b16 a[22:23], v212 offset:25856
v_mfma_f32_16x16x32_bf16 v[0:3], a[0:3], a[200:203], v[0:3]
ds_read_b64_tr_b16 a[24:25], v211 offset:26112
v_mfma_f32_16x16x32_bf16 v[8:11], a[0:3], a[204:207], v[8:11]
ds_read_b64_tr_b16 a[26:27], v211 offset:26368
v_mfma_f32_16x16x32_bf16 v[4:7], a[4:7], a[200:203], v[4:7]
ds_read_b64_tr_b16 a[28:29], v212 offset:26112
v_mfma_f32_16x16x32_bf16 v[12:15], a[4:7], a[204:207], v[12:15]
s_waitcnt vmcnt(0)
s_barrier
ds_read_b64_tr_b16 a[30:31], v212 offset:26368
s_waitcnt lgkmcnt(8)
v_mfma_f32_16x16x32_bf16 v[16:19], a[8:11], a[200:203], v[16:19]
ds_read_b128 a[208:211], v181 offset:32768
ds_read_b128 a[212:215], v181 offset:34816
v_mfma_f32_16x16x32_bf16 v[24:27], a[8:11], a[204:207], v[24:27]
ds_read_b64_tr_b16 a[0:1], v211 offset:49152
ds_read_b64_tr_b16 a[2:3], v211 offset:49408
v_mfma_f32_16x16x32_bf16 v[20:23], a[12:15], a[200:203], v[20:23]
ds_read_b64_tr_b16 a[4:5], v212 offset:49152
v_mfma_f32_16x16x32_bf16 v[28:31], a[12:15], a[204:207], v[28:31]
s_waitcnt lgkmcnt(9)
ds_read_b64_tr_b16 a[6:7], v212 offset:49408
v_mfma_f32_16x16x32_bf16 v[32:35], a[16:19], a[200:203], v[32:35]
ds_read_b64_tr_b16 a[8:9], v211 offset:49664
v_mfma_f32_16x16x32_bf16 v[40:43], a[16:19], a[204:207], v[40:43]
ds_read_b64_tr_b16 a[10:11], v211 offset:49920
v_mfma_f32_16x16x32_bf16 v[36:39], a[20:23], a[200:203], v[36:39]
ds_read_b64_tr_b16 a[12:13], v212 offset:49664
v_mfma_f32_16x16x32_bf16 v[44:47], a[20:23], a[204:207], v[44:47]
s_waitcnt lgkmcnt(9)
ds_read_b64_tr_b16 a[14:15], v212 offset:49920
v_mfma_f32_16x16x32_bf16 v[48:51], a[24:27], a[200:203], v[48:51]
ds_read_b64_tr_b16 a[16:17], v211 offset:50176
v_mfma_f32_16x16x32_bf16 v[56:59], a[24:27], a[204:207], v[56:59]
ds_read_b64_tr_b16 a[18:19], v211 offset:50432
v_mfma_f32_16x16x32_bf16 v[52:55], a[28:31], a[200:203], v[52:55]
ds_read_b64_tr_b16 a[20:21], v212 offset:50176
v_mfma_f32_16x16x32_bf16 v[60:63], a[28:31], a[204:207], v[60:63]
s_barrier
ds_read_b64_tr_b16 a[22:23], v212 offset:50432
s_waitcnt lgkmcnt(8)
v_mfma_f32_16x16x32_bf16 v[0:3], a[0:3], a[208:211], v[0:3]
ds_read_b64_tr_b16 a[24:25], v211 offset:50688
v_mfma_f32_16x16x32_bf16 v[8:11], a[0:3], a[212:215], v[8:11]
ds_read_b64_tr_b16 a[26:27], v211 offset:50944
v_mfma_f32_16x16x32_bf16 v[4:7], a[4:7], a[208:211], v[4:7]
ds_read_b64_tr_b16 a[28:29], v212 offset:50688
v_mfma_f32_16x16x32_bf16 v[12:15], a[4:7], a[212:215], v[12:15]
s_waitcnt lgkmcnt(7)
ds_read_b64_tr_b16 a[30:31], v212 offset:50944
v_mfma_f32_16x16x32_bf16 v[16:19], a[8:11], a[208:211], v[16:19]
ds_read_b128 a[216:219], v181 offset:33280
ds_read_b128 a[220:223], v181 offset:35328
v_mfma_f32_16x16x32_bf16 v[24:27], a[8:11], a[212:215], v[24:27]
ds_read_b64_tr_b16 a[0:1], v211 offset:57344
ds_read_b64_tr_b16 a[2:3], v211 offset:57600
v_mfma_f32_16x16x32_bf16 v[20:23], a[12:15], a[208:211], v[20:23]
ds_read_b64_tr_b16 a[4:5], v212 offset:57344
v_mfma_f32_16x16x32_bf16 v[28:31], a[12:15], a[212:215], v[28:31]
s_waitcnt lgkmcnt(9)
ds_read_b64_tr_b16 a[6:7], v212 offset:57600
v_mfma_f32_16x16x32_bf16 v[32:35], a[16:19], a[208:211], v[32:35]
ds_read_b64_tr_b16 a[8:9], v211 offset:57856
v_mfma_f32_16x16x32_bf16 v[40:43], a[16:19], a[212:215], v[40:43]
ds_read_b64_tr_b16 a[10:11], v211 offset:58112
v_mfma_f32_16x16x32_bf16 v[36:39], a[20:23], a[208:211], v[36:39]
ds_read_b64_tr_b16 a[12:13], v212 offset:57856
v_mfma_f32_16x16x32_bf16 v[44:47], a[20:23], a[212:215], v[44:47]
s_waitcnt lgkmcnt(9)
ds_read_b64_tr_b16 a[14:15], v212 offset:58112
v_mfma_f32_16x16x32_bf16 v[48:51], a[24:27], a[208:211], v[48:51]
ds_read_b64_tr_b16 a[16:17], v211 offset:58368
v_mfma_f32_16x16x32_bf16 v[56:59], a[24:27], a[212:215], v[56:59]
ds_read_b64_tr_b16 a[18:19], v211 offset:58624
v_mfma_f32_16x16x32_bf16 v[52:55], a[28:31], a[208:211], v[52:55]
ds_read_b64_tr_b16 a[20:21], v212 offset:58368
v_mfma_f32_16x16x32_bf16 v[60:63], a[28:31], a[212:215], v[60:63]
s_waitcnt lgkmcnt(7)
ds_read_b64_tr_b16 a[22:23], v212 offset:58624
v_mfma_f32_16x16x32_bf16 v[0:3], a[0:3], a[216:219], v[0:3]
ds_read_b64_tr_b16 a[24:25], v211 offset:58880
v_mfma_f32_16x16x32_bf16 v[8:11], a[0:3], a[220:223], v[8:11]
ds_read_b64_tr_b16 a[26:27], v211 offset:59136
v_mfma_f32_16x16x32_bf16 v[4:7], a[4:7], a[216:219], v[4:7]
ds_read_b64_tr_b16 a[28:29], v212 offset:58880
v_mfma_f32_16x16x32_bf16 v[12:15], a[4:7], a[220:223], v[12:15]
s_barrier
ds_read_b64_tr_b16 a[30:31], v212 offset:59136
s_waitcnt lgkmcnt(8)
v_mfma_f32_16x16x32_bf16 v[16:19], a[8:11], a[216:219], v[16:19]
ds_read_b64_tr_b16 a[0:1], v213 offset:16384
v_mul_f32_e32 v0, s69, v0
v_mul_f32_e32 v1, s69, v1
v_mul_f32_e32 v2, s69, v2
v_mul_f32_e32 v3, s69, v3
v_cvt_pk_bf16_f32 v0, v0, v1
v_mfma_f32_16x16x32_bf16 v[24:27], a[8:11], a[220:223], v[24:27]
ds_read_b64_tr_b16 a[2:3], v213 offset:16640
v_cvt_pk_bf16_f32 v1, v2, v3
v_mul_f32_e32 v8, s69, v8
v_mfma_f32_16x16x32_bf16 v[20:23], a[12:15], a[216:219], v[20:23]
ds_read_b64_tr_b16 a[4:5], v214 offset:16384
v_mul_f32_e32 v4, s69, v4
v_mul_f32_e32 v5, s69, v5
v_mul_f32_e32 v6, s69, v6
v_mfma_f32_16x16x32_bf16 v[28:31], a[12:15], a[220:223], v[28:31]
s_waitcnt lgkmcnt(7)
ds_read_b64_tr_b16 a[6:7], v214 offset:16640
v_mul_f32_e32 v7, s69, v7
v_cvt_pk_bf16_f32 v2, v4, v5
v_cvt_pk_bf16_f32 v3, v6, v7
v_mfma_f32_16x16x32_bf16 v[32:35], a[16:19], a[216:219], v[32:35]
ds_read_b64_tr_b16 a[8:9], v213 offset:16896
v_permlane16_swap_b32_e32 v0, v2
v_permlane16_swap_b32_e32 v1, v3
v_accvgpr_write_b32 a32, v0
v_mfma_f32_16x16x32_bf16 v[40:43], a[16:19], a[220:223], v[40:43]
ds_read_b64_tr_b16 a[10:11], v213 offset:17152
v_accvgpr_write_b32 a33, v1
v_accvgpr_write_b32 a34, v2
v_mfma_f32_16x16x32_bf16 v[36:39], a[20:23], a[216:219], v[36:39]
ds_read_b64_tr_b16 a[12:13], v214 offset:16896
v_accvgpr_write_b32 a35, v3
v_mul_f32_e32 v9, s69, v9
v_mul_f32_e32 v10, s69, v10
)"
R"(
v_mfma_f32_16x16x32_bf16 v[44:47], a[20:23], a[220:223], v[44:47]
s_waitcnt lgkmcnt(7)
ds_read_b64_tr_b16 a[14:15], v214 offset:17152
v_mul_f32_e32 v11, s69, v11
v_mul_f32_e32 v12, s69, v12
v_mul_f32_e32 v13, s69, v13
v_mfma_f32_16x16x32_bf16 v[48:51], a[24:27], a[216:219], v[48:51]
ds_read_b64_tr_b16 a[16:17], v213 offset:17408
v_mul_f32_e32 v14, s69, v14
v_mul_f32_e32 v15, s69, v15
v_cvt_pk_bf16_f32 v8, v8, v9
v_mfma_f32_16x16x32_bf16 v[56:59], a[24:27], a[220:223], v[56:59]
ds_read_b64_tr_b16 a[18:19], v213 offset:17664
v_cvt_pk_bf16_f32 v9, v10, v11
v_cvt_pk_bf16_f32 v10, v12, v13
v_mfma_f32_16x16x32_bf16 v[52:55], a[28:31], a[216:219], v[52:55]
ds_read_b64_tr_b16 a[20:21], v214 offset:17408
v_cvt_pk_bf16_f32 v11, v14, v15
v_permlane16_swap_b32_e32 v8, v10
s_nop 0
v_permlane16_swap_b32_e32 v9, v11
v_mfma_f32_16x16x32_bf16 v[60:63], a[28:31], a[220:223], v[60:63]
s_barrier
ds_read_b64_tr_b16 a[22:23], v214 offset:17664
v_accvgpr_write_b32 a36, v8
s_waitcnt lgkmcnt(8)
v_mfma_f32_16x16x32_bf16 v[64:67], a[0:3], a[128:131], v[64:67]
ds_read_b64_tr_b16 a[24:25], v213 offset:17920
v_accvgpr_write_b32 a37, v9
v_accvgpr_write_b32 a38, v10
v_mfma_f32_16x16x32_bf16 v[72:75], a[0:3], a[132:135], v[72:75]
ds_read_b64_tr_b16 a[26:27], v213 offset:18176
v_accvgpr_write_b32 a39, v11
v_mul_f32_e32 v16, s69, v16
v_mul_f32_e32 v17, s69, v17
v_mfma_f32_16x16x32_bf16 v[68:71], a[4:7], a[128:131], v[68:71]
ds_read_b64_tr_b16 a[28:29], v214 offset:17920
v_mul_f32_e32 v18, s69, v18
v_mul_f32_e32 v19, s69, v19
v_mul_f32_e32 v20, s69, v20
v_mfma_f32_16x16x32_bf16 v[76:79], a[4:7], a[132:135], v[76:79]
s_waitcnt lgkmcnt(7)
ds_read_b64_tr_b16 a[30:31], v214 offset:18176
v_mul_f32_e32 v21, s69, v21
v_mul_f32_e32 v22, s69, v22
v_mul_f32_e32 v23, s69, v23
v_mfma_f32_16x16x32_bf16 v[80:83], a[8:11], a[128:131], v[80:83]
ds_read_b64_tr_b16 a[0:1], v213 offset:24576
v_cvt_pk_bf16_f32 v16, v16, v17
v_cvt_pk_bf16_f32 v17, v18, v19
v_mfma_f32_16x16x32_bf16 v[88:91], a[8:11], a[132:135], v[88:91]
ds_read_b64_tr_b16 a[2:3], v213 offset:24832
v_cvt_pk_bf16_f32 v18, v20, v21
v_cvt_pk_bf16_f32 v19, v22, v23
s_nop 0
v_permlane16_swap_b32_e32 v16, v18
v_mfma_f32_16x16x32_bf16 v[84:87], a[12:15], a[128:131], v[84:87]
ds_read_b64_tr_b16 a[4:5], v214 offset:24576
v_permlane16_swap_b32_e32 v17, v19
v_accvgpr_write_b32 a40, v16
v_accvgpr_write_b32 a41, v17
v_mfma_f32_16x16x32_bf16 v[92:95], a[12:15], a[132:135], v[92:95]
s_waitcnt lgkmcnt(7)
ds_read_b64_tr_b16 a[6:7], v214 offset:24832
v_accvgpr_write_b32 a42, v18
v_accvgpr_write_b32 a43, v19
v_mul_f32_e32 v24, s69, v24
v_mfma_f32_16x16x32_bf16 v[96:99], a[16:19], a[128:131], v[96:99]
ds_read_b64_tr_b16 a[8:9], v213 offset:25088
v_mul_f32_e32 v25, s69, v25
v_mul_f32_e32 v26, s69, v26
v_mfma_f32_16x16x32_bf16 v[104:107], a[16:19], a[132:135], v[104:107]
ds_read_b64_tr_b16 a[10:11], v213 offset:25344
v_mul_f32_e32 v27, s69, v27
v_mul_f32_e32 v28, s69, v28
v_mul_f32_e32 v29, s69, v29
v_mfma_f32_16x16x32_bf16 v[100:103], a[20:23], a[128:131], v[100:103]
ds_read_b64_tr_b16 a[12:13], v214 offset:25088
v_mul_f32_e32 v30, s69, v30
v_mul_f32_e32 v31, s69, v31
v_cvt_pk_bf16_f32 v24, v24, v25
v_mfma_f32_16x16x32_bf16 v[108:111], a[20:23], a[132:135], v[108:111]
s_waitcnt lgkmcnt(7)
ds_read_b64_tr_b16 a[14:15], v214 offset:25344
v_cvt_pk_bf16_f32 v25, v26, v27
v_cvt_pk_bf16_f32 v26, v28, v29
v_mfma_f32_16x16x32_bf16 v[112:115], a[24:27], a[128:131], v[112:115]
ds_read_b64_tr_b16 a[16:17], v213 offset:25600
v_cvt_pk_bf16_f32 v27, v30, v31
v_permlane16_swap_b32_e32 v24, v26
s_nop 0
v_permlane16_swap_b32_e32 v25, v27
v_mfma_f32_16x16x32_bf16 v[120:123], a[24:27], a[132:135], v[120:123]
ds_read_b64_tr_b16 a[18:19], v213 offset:25856
v_accvgpr_write_b32 a44, v24
v_accvgpr_write_b32 a45, v25
v_accvgpr_write_b32 a46, v26
v_mfma_f32_16x16x32_bf16 v[116:119], a[28:31], a[128:131], v[116:119]
ds_read_b64_tr_b16 a[20:21], v214 offset:25600
v_accvgpr_write_b32 a47, v27
v_mul_f32_e32 v32, s69, v32
v_mul_f32_e32 v33, s69, v33
v_mfma_f32_16x16x32_bf16 v[124:127], a[28:31], a[132:135], v[124:127]
s_waitcnt lgkmcnt(7)
ds_read_b64_tr_b16 a[22:23], v214 offset:25856
v_mul_f32_e32 v34, s69, v34
v_mul_f32_e32 v35, s69, v35
v_mfma_f32_16x16x32_bf16 v[64:67], a[0:3], a[136:139], v[64:67]
ds_read_b64_tr_b16 a[24:25], v213 offset:26112
v_mul_f32_e32 v36, s69, v36
v_mul_f32_e32 v37, s69, v37
v_mul_f32_e32 v38, s69, v38
v_mfma_f32_16x16x32_bf16 v[72:75], a[0:3], a[140:143], v[72:75]
ds_read_b64_tr_b16 a[26:27], v213 offset:26368
v_mul_f32_e32 v39, s69, v39
v_cvt_pk_bf16_f32 v32, v32, v33
v_cvt_pk_bf16_f32 v33, v34, v35
v_mfma_f32_16x16x32_bf16 v[68:71], a[4:7], a[136:139], v[68:71]
ds_read_b64_tr_b16 a[28:29], v214 offset:26112
v_cvt_pk_bf16_f32 v34, v36, v37
v_cvt_pk_bf16_f32 v35, v38, v39
s_nop 0
v_permlane16_swap_b32_e32 v32, v34
v_mfma_f32_16x16x32_bf16 v[76:79], a[4:7], a[140:143], v[76:79]
s_barrier
ds_read_b64_tr_b16 a[30:31], v214 offset:26368
s_waitcnt lgkmcnt(8)
v_mfma_f32_16x16x32_bf16 v[80:83], a[8:11], a[136:139], v[80:83]
ds_read_b128 a[0:3], v202
v_permlane16_swap_b32_e32 v33, v35
v_accvgpr_write_b32 a48, v32
v_accvgpr_write_b32 a49, v33
v_mfma_f32_16x16x32_bf16 v[88:91], a[8:11], a[140:143], v[88:91]
v_accvgpr_write_b32 a50, v34
v_accvgpr_write_b32 a51, v35
v_mul_f32_e32 v40, s69, v40
v_mul_f32_e32 v41, s69, v41
v_mfma_f32_16x16x32_bf16 v[84:87], a[12:15], a[136:139], v[84:87]
ds_read_b128 a[4:7], v202 offset:512
v_mul_f32_e32 v42, s69, v42
v_mul_f32_e32 v43, s69, v43
v_mfma_f32_16x16x32_bf16 v[92:95], a[12:15], a[140:143], v[92:95]
s_waitcnt lgkmcnt(6)
v_mul_f32_e32 v44, s69, v44
v_mul_f32_e32 v45, s69, v45
v_mul_f32_e32 v46, s69, v46
v_mul_f32_e32 v47, s69, v47
v_mfma_f32_16x16x32_bf16 v[96:99], a[16:19], a[136:139], v[96:99]
ds_read_b128 a[8:11], v202 offset:1024
v_cvt_pk_bf16_f32 v40, v40, v41
v_cvt_pk_bf16_f32 v41, v42, v43
v_cvt_pk_bf16_f32 v42, v44, v45
v_mfma_f32_16x16x32_bf16 v[104:107], a[16:19], a[140:143], v[104:107]
v_cvt_pk_bf16_f32 v43, v46, v47
v_permlane16_swap_b32_e32 v40, v42
s_nop 0
v_permlane16_swap_b32_e32 v41, v43
v_accvgpr_write_b32 a52, v40
v_mfma_f32_16x16x32_bf16 v[100:103], a[20:23], a[136:139], v[100:103]
ds_read_b128 a[12:15], v202 offset:1536
v_accvgpr_write_b32 a53, v41
v_accvgpr_write_b32 a54, v42
v_mfma_f32_16x16x32_bf16 v[108:111], a[20:23], a[140:143], v[108:111]
s_waitcnt lgkmcnt(4)
v_accvgpr_write_b32 a55, v43
v_mul_f32_e32 v48, s69, v48
v_mul_f32_e32 v49, s69, v49
v_mul_f32_e32 v50, s69, v50
v_mfma_f32_16x16x32_bf16 v[112:115], a[24:27], a[136:139], v[112:115]
ds_read_b128 a[16:19], v202 offset:4096
v_mul_f32_e32 v51, s69, v51
v_mul_f32_e32 v52, s69, v52
v_mul_f32_e32 v53, s69, v53
v_mfma_f32_16x16x32_bf16 v[120:123], a[24:27], a[140:143], v[120:123]
v_mul_f32_e32 v54, s69, v54
v_mul_f32_e32 v55, s69, v55
v_cvt_pk_bf16_f32 v48, v48, v49
v_mfma_f32_16x16x32_bf16 v[116:119], a[28:31], a[136:139], v[116:119]
ds_read_b128 a[20:23], v202 offset:4608
v_cvt_pk_bf16_f32 v49, v50, v51
v_cvt_pk_bf16_f32 v50, v52, v53
v_cvt_pk_bf16_f32 v51, v54, v55
v_mfma_f32_16x16x32_bf16 v[124:127], a[28:31], a[140:143], v[124:127]
s_barrier
v_permlane16_swap_b32_e32 v48, v50
v_permlane16_swap_b32_e32 v49, v51
s_waitcnt lgkmcnt(4)
v_mfma_f32_16x16x32_bf16 v[128:131], a[0:3], a[32:35], 0
ds_read_b128 a[24:27], v202 offset:5120
v_accvgpr_write_b32 a56, v48
v_accvgpr_write_b32 a57, v49
v_accvgpr_write_b32 a58, v50
v_mfma_f32_16x16x32_bf16 v[136:139], a[0:3], a[36:39], 0
v_accvgpr_write_b32 a59, v51
v_mul_f32_e32 v56, s69, v56
v_mul_f32_e32 v57, s69, v57
v_mfma_f32_16x16x32_bf16 v[128:131], a[4:7], a[40:43], v[128:131]
ds_read_b128 a[28:31], v202 offset:5632
v_mul_f32_e32 v58, s69, v58
v_mul_f32_e32 v59, s69, v59
)"
R"(
v_mul_f32_e32 v60, s69, v60
v_mfma_f32_16x16x32_bf16 v[136:139], a[4:7], a[44:47], v[136:139]
s_waitcnt lgkmcnt(4)
v_mul_f32_e32 v61, s69, v61
v_mul_f32_e32 v62, s69, v62
v_mul_f32_e32 v63, s69, v63
v_cvt_pk_bf16_f32 v56, v56, v57
v_mfma_f32_16x16x32_bf16 v[128:131], a[8:11], a[48:51], v[128:131]
ds_read_b128 a[0:3], v202 offset:8192
v_cvt_pk_bf16_f32 v57, v58, v59
v_cvt_pk_bf16_f32 v58, v60, v61
v_cvt_pk_bf16_f32 v59, v62, v63
v_mfma_f32_16x16x32_bf16 v[136:139], a[8:11], a[52:55], v[136:139]
v_permlane16_swap_b32_e32 v56, v58
v_permlane16_swap_b32_e32 v57, v59
v_accvgpr_write_b32 a60, v56
v_mfma_f32_16x16x32_bf16 v[128:131], a[12:15], a[56:59], v[128:131]
ds_read_b128 a[4:7], v202 offset:8704
v_accvgpr_write_b32 a61, v57
v_accvgpr_write_b32 a62, v58
v_accvgpr_write_b32 a63, v59
s_nop 1
v_mfma_f32_16x16x32_bf16 v[136:139], a[12:15], a[60:63], v[136:139]
s_waitcnt lgkmcnt(4)
v_mfma_f32_16x16x32_bf16 v[132:135], a[16:19], a[32:35], 0
ds_read_b128 a[8:11], v202 offset:9216
v_mfma_f32_16x16x32_bf16 v[140:143], a[16:19], a[36:39], 0
v_mfma_f32_16x16x32_bf16 v[132:135], a[20:23], a[40:43], v[132:135]
ds_read_b128 a[12:15], v202 offset:9728
v_max3_f32 v236, v128, v129, v130
v_mfma_f32_16x16x32_bf16 v[140:143], a[20:23], a[44:47], v[140:143]
s_waitcnt lgkmcnt(4)
v_max3_f32 v237, v136, v137, v138
v_mfma_f32_16x16x32_bf16 v[132:135], a[24:27], a[48:51], v[132:135]
ds_read_b128 a[16:19], v202 offset:12288
v_mfma_f32_16x16x32_bf16 v[140:143], a[24:27], a[52:55], v[140:143]
v_mfma_f32_16x16x32_bf16 v[132:135], a[28:31], a[56:59], v[132:135]
ds_read_b128 a[20:23], v202 offset:12800
v_mfma_f32_16x16x32_bf16 v[140:143], a[28:31], a[60:63], v[140:143]
s_waitcnt lgkmcnt(4)
v_mfma_f32_16x16x32_bf16 v[144:147], a[0:3], a[32:35], 0
ds_read_b128 a[24:27], v202 offset:13312
v_mfma_f32_16x16x32_bf16 v[152:155], a[0:3], a[36:39], 0
v_mfma_f32_16x16x32_bf16 v[144:147], a[4:7], a[40:43], v[144:147]
ds_read_b128 a[28:31], v202 offset:13824
v_max3_f32 v236, v236, v131, v132
v_max3_f32 v236, v236, v133, v134
v_mfma_f32_16x16x32_bf16 v[152:155], a[4:7], a[44:47], v[152:155]
s_barrier
v_max3_f32 v237, v237, v139, v140
v_max3_f32 v237, v237, v141, v142
s_waitcnt lgkmcnt(4)
v_mfma_f32_16x16x32_bf16 v[144:147], a[8:11], a[48:51], v[144:147]
ds_read_b64_tr_b16 a[0:1], v209 offset:16384
v_mfma_f32_16x16x32_bf16 v[152:155], a[8:11], a[52:55], v[152:155]
ds_read_b64_tr_b16 a[2:3], v209 offset:16640
v_mfma_f32_16x16x32_bf16 v[144:147], a[12:15], a[56:59], v[144:147]
ds_read_b64_tr_b16 a[4:5], v210 offset:16384
v_mfma_f32_16x16x32_bf16 v[152:155], a[12:15], a[60:63], v[152:155]
s_waitcnt lgkmcnt(5)
ds_read_b64_tr_b16 a[6:7], v210 offset:16640
v_mfma_f32_16x16x32_bf16 v[148:151], a[16:19], a[32:35], 0
ds_read_b64_tr_b16 a[8:9], v209 offset:16896
v_mfma_f32_16x16x32_bf16 v[156:159], a[16:19], a[36:39], 0
ds_read_b64_tr_b16 a[10:11], v209 offset:17152
v_mfma_f32_16x16x32_bf16 v[148:151], a[20:23], a[40:43], v[148:151]
ds_read_b64_tr_b16 a[12:13], v210 offset:16896
v_max3_f32 v236, v236, v135, v144
v_max3_f32 v236, v236, v145, v146
v_mfma_f32_16x16x32_bf16 v[156:159], a[20:23], a[44:47], v[156:159]
s_waitcnt lgkmcnt(7)
ds_read_b64_tr_b16 a[14:15], v210 offset:17152
v_max3_f32 v237, v237, v143, v152
v_max3_f32 v237, v237, v153, v154
v_mfma_f32_16x16x32_bf16 v[148:151], a[24:27], a[48:51], v[148:151]
ds_read_b64_tr_b16 a[16:17], v209 offset:17408
v_mfma_f32_16x16x32_bf16 v[156:159], a[24:27], a[52:55], v[156:159]
ds_read_b64_tr_b16 a[18:19], v209 offset:17664
v_mfma_f32_16x16x32_bf16 v[148:151], a[28:31], a[56:59], v[148:151]
ds_read_b64_tr_b16 a[20:21], v210 offset:17408
v_mfma_f32_16x16x32_bf16 v[156:159], a[28:31], a[60:63], v[156:159]
s_waitcnt lgkmcnt(7)
ds_read_b64_tr_b16 a[22:23], v210 offset:17664
v_mfma_f32_16x16x32_bf16 v[64:67], a[0:3], a[144:147], v[64:67]
ds_read_b64_tr_b16 a[24:25], v209 offset:17920
v_mfma_f32_16x16x32_bf16 v[72:75], a[0:3], a[148:151], v[72:75]
ds_read_b64_tr_b16 a[26:27], v209 offset:18176
v_mfma_f32_16x16x32_bf16 v[68:71], a[4:7], a[144:147], v[68:71]
ds_read_b64_tr_b16 a[28:29], v210 offset:17920
v_max3_f32 v236, v236, v147, v148
v_max3_f32 v236, v236, v149, v150
v_max_f32_e32 v236, v236, v151
v_mov_b32_e32 v246, v236
v_mfma_f32_16x16x32_bf16 v[76:79], a[4:7], a[148:151], v[76:79]
s_waitcnt lgkmcnt(7)
ds_read_b64_tr_b16 a[30:31], v210 offset:18176
v_permlane16_swap_b32_e32 v236, v246
v_max_f32_e32 v236, v236, v246
v_mfma_f32_16x16x32_bf16 v[80:83], a[8:11], a[144:147], v[80:83]
ds_read_b64_tr_b16 a[0:1], v209 offset:24576
v_mov_b32_e32 v246, v236
v_max3_f32 v237, v237, v155, v156
v_mfma_f32_16x16x32_bf16 v[88:91], a[8:11], a[148:151], v[88:91]
ds_read_b64_tr_b16 a[2:3], v209 offset:24832
v_permlane32_swap_b32_e32 v236, v246
v_mfma_f32_16x16x32_bf16 v[84:87], a[12:15], a[144:147], v[84:87]
ds_read_b64_tr_b16 a[4:5], v210 offset:24576
v_max_f32_e32 v236, v236, v246
v_sub_f32_e32 v192, 0, v236
v_mfma_f32_16x16x32_bf16 v[92:95], a[12:15], a[148:151], v[92:95]
s_waitcnt lgkmcnt(7)
ds_read_b64_tr_b16 a[6:7], v210 offset:24832
v_sub_f32_e32 v193, 0, v236
v_sub_f32_e32 v194, 0, v236
v_mfma_f32_16x16x32_bf16 v[96:99], a[16:19], a[144:147], v[96:99]
ds_read_b64_tr_b16 a[8:9], v209 offset:25088
v_sub_f32_e32 v195, 0, v236
v_max3_f32 v237, v237, v157, v158
v_mfma_f32_16x16x32_bf16 v[104:107], a[16:19], a[148:151], v[104:107]
ds_read_b64_tr_b16 a[10:11], v209 offset:25344
v_max_f32_e32 v237, v237, v159
v_mov_b32_e32 v247, v237
v_mfma_f32_16x16x32_bf16 v[100:103], a[20:23], a[144:147], v[100:103]
ds_read_b64_tr_b16 a[12:13], v210 offset:25088
v_permlane16_swap_b32_e32 v237, v247
v_max_f32_e32 v237, v237, v247
v_mfma_f32_16x16x32_bf16 v[108:111], a[20:23], a[148:151], v[108:111]
s_waitcnt lgkmcnt(7)
ds_read_b64_tr_b16 a[14:15], v210 offset:25344
v_mov_b32_e32 v247, v237
v_mfma_f32_16x16x32_bf16 v[112:115], a[24:27], a[144:147], v[112:115]
ds_read_b64_tr_b16 a[16:17], v209 offset:25600
v_permlane32_swap_b32_e32 v237, v247
v_max_f32_e32 v237, v237, v247
v_mfma_f32_16x16x32_bf16 v[120:123], a[24:27], a[148:151], v[120:123]
ds_read_b64_tr_b16 a[18:19], v209 offset:25856
v_sub_f32_e32 v196, 0, v237
v_sub_f32_e32 v197, 0, v237
v_mfma_f32_16x16x32_bf16 v[116:119], a[28:31], a[144:147], v[116:119]
ds_read_b64_tr_b16 a[20:21], v210 offset:25600
v_sub_f32_e32 v198, 0, v237
v_sub_f32_e32 v199, 0, v237
v_mfma_f32_16x16x32_bf16 v[124:127], a[28:31], a[148:151], v[124:127]
s_waitcnt lgkmcnt(7)
ds_read_b64_tr_b16 a[22:23], v210 offset:25856
v_sub_f32_e32 v128, v128, v236
v_sub_f32_e32 v129, v129, v236
v_mfma_f32_16x16x32_bf16 v[64:67], a[0:3], a[152:155], v[64:67]
ds_read_b64_tr_b16 a[24:25], v209 offset:26112
v_sub_f32_e32 v130, v130, v236
v_sub_f32_e32 v131, v131, v236
v_mfma_f32_16x16x32_bf16 v[72:75], a[0:3], a[156:159], v[72:75]
ds_read_b64_tr_b16 a[26:27], v209 offset:26368
v_exp_f32_e32 v128, v128
v_mfma_f32_16x16x32_bf16 v[68:71], a[4:7], a[152:155], v[68:71]
ds_read_b64_tr_b16 a[28:29], v210 offset:26112
v_add_f32_e32 v242, v242, v128
v_mfma_f32_16x16x32_bf16 v[76:79], a[4:7], a[156:159], v[76:79]
s_barrier
ds_read_b64_tr_b16 a[30:31], v210 offset:26368
s_waitcnt lgkmcnt(8)
v_mfma_f32_16x16x32_bf16 v[80:83], a[8:11], a[152:155], v[80:83]
ds_read_b128 a[0:3], v200
v_exp_f32_e32 v129, v129
v_mfma_f32_16x16x32_bf16 v[88:91], a[8:11], a[156:159], v[88:91]
v_add_f32_e32 v242, v242, v129
v_cvt_pk_bf16_f32 v176, v128, v129
v_exp_f32_e32 v130, v130
v_mfma_f32_16x16x32_bf16 v[84:87], a[12:15], a[152:155], v[84:87]
ds_read_b128 a[4:7], v200 offset:512
v_add_f32_e32 v242, v242, v130
v_mfma_f32_16x16x32_bf16 v[92:95], a[12:15], a[156:159], v[92:95]
s_waitcnt lgkmcnt(6)
v_exp_f32_e32 v131, v131
v_sub_f32_e32 v136, v136, v237
v_mfma_f32_16x16x32_bf16 v[96:99], a[16:19], a[152:155], v[96:99]
ds_read_b128 a[8:11], v200 offset:1024
v_add_f32_e32 v242, v242, v131
v_cvt_pk_bf16_f32 v177, v130, v131
v_mfma_f32_16x16x32_bf16 v[104:107], a[16:19], a[156:159], v[104:107]
v_sub_f32_e32 v137, v137, v237
v_sub_f32_e32 v138, v138, v237
v_sub_f32_e32 v139, v139, v237
v_mfma_f32_16x16x32_bf16 v[100:103], a[20:23], a[152:155], v[100:103]
ds_read_b128 a[12:15], v200 offset:1536
v_exp_f32_e32 v136, v136
v_mfma_f32_16x16x32_bf16 v[108:111], a[20:23], a[156:159], v[108:111]
s_waitcnt lgkmcnt(4)
v_add_f32_e32 v243, v243, v136
v_exp_f32_e32 v137, v137
v_mfma_f32_16x16x32_bf16 v[112:115], a[24:27], a[152:155], v[112:115]
ds_read_b128 a[16:19], v200 offset:4096
v_add_f32_e32 v243, v243, v137
v_mfma_f32_16x16x32_bf16 v[120:123], a[24:27], a[156:159], v[120:123]
v_cvt_pk_bf16_f32 v180, v136, v137
v_exp_f32_e32 v138, v138
v_mfma_f32_16x16x32_bf16 v[116:119], a[28:31], a[152:155], v[116:119]
ds_read_b128 a[20:23], v200 offset:4608
v_add_f32_e32 v243, v243, v138
v_sub_f32_e32 v132, v132, v236
)"
R"(
v_mfma_f32_16x16x32_bf16 v[124:127], a[28:31], a[156:159], v[124:127]
s_waitcnt lgkmcnt(4)
v_exp_f32_e32 v139, v139
v_sub_f32_e32 v133, v133, v236
v_mfma_f32_16x16x32_bf16 v[160:163], a[0:3], a[32:35], v[192:195]
ds_read_b128 a[24:27], v200 offset:5120
v_add_f32_e32 v243, v243, v139
v_mfma_f32_16x16x32_bf16 v[168:171], a[0:3], a[36:39], v[196:199]
v_cvt_pk_bf16_f32 v181, v138, v139
v_sub_f32_e32 v134, v134, v236
v_sub_f32_e32 v135, v135, v236
v_mfma_f32_16x16x32_bf16 v[160:163], a[4:7], a[40:43], v[160:163]
ds_read_b128 a[28:31], v200 offset:5632
v_exp_f32_e32 v132, v132
v_mfma_f32_16x16x32_bf16 v[168:171], a[4:7], a[44:47], v[168:171]
s_waitcnt lgkmcnt(4)
v_add_f32_e32 v242, v242, v132
v_exp_f32_e32 v133, v133
v_mfma_f32_16x16x32_bf16 v[160:163], a[8:11], a[48:51], v[160:163]
ds_read_b128 a[0:3], v200 offset:8192
v_add_f32_e32 v242, v242, v133
v_cvt_pk_bf16_f32 v178, v132, v133
v_mfma_f32_16x16x32_bf16 v[168:171], a[8:11], a[52:55], v[168:171]
v_exp_f32_e32 v134, v134
v_sub_f32_e32 v140, v140, v237
v_mfma_f32_16x16x32_bf16 v[160:163], a[12:15], a[56:59], v[160:163]
ds_read_b128 a[4:7], v200 offset:8704
v_add_f32_e32 v242, v242, v134
v_sub_f32_e32 v141, v141, v237
v_mfma_f32_16x16x32_bf16 v[168:171], a[12:15], a[60:63], v[168:171]
s_waitcnt lgkmcnt(4)
v_exp_f32_e32 v135, v135
v_mfma_f32_16x16x32_bf16 v[164:167], a[16:19], a[32:35], v[192:195]
ds_read_b128 a[8:11], v200 offset:9216
v_add_f32_e32 v242, v242, v135
v_cvt_pk_bf16_f32 v179, v134, v135
v_mfma_f32_16x16x32_bf16 v[172:175], a[16:19], a[36:39], v[196:199]
v_sub_f32_e32 v142, v142, v237
v_sub_f32_e32 v143, v143, v237
v_exp_f32_e32 v140, v140
v_mfma_f32_16x16x32_bf16 v[164:167], a[20:23], a[40:43], v[164:167]
ds_read_b128 a[12:15], v200 offset:9728
v_add_f32_e32 v243, v243, v140
v_mfma_f32_16x16x32_bf16 v[172:175], a[20:23], a[44:47], v[172:175]
s_waitcnt lgkmcnt(4)
v_exp_f32_e32 v141, v141
v_sub_f32_e32 v144, v144, v236
v_mfma_f32_16x16x32_bf16 v[164:167], a[24:27], a[48:51], v[164:167]
ds_read_b128 a[16:19], v200 offset:12288
v_add_f32_e32 v243, v243, v141
v_cvt_pk_bf16_f32 v182, v140, v141
v_mfma_f32_16x16x32_bf16 v[172:175], a[24:27], a[52:55], v[172:175]
v_exp_f32_e32 v142, v142
v_mfma_f32_16x16x32_bf16 v[164:167], a[28:31], a[56:59], v[164:167]
ds_read_b128 a[20:23], v200 offset:12800
v_add_f32_e32 v243, v243, v142
v_exp_f32_e32 v143, v143
v_mfma_f32_16x16x32_bf16 v[172:175], a[28:31], a[60:63], v[172:175]
s_waitcnt lgkmcnt(4)
v_add_f32_e32 v243, v243, v143
v_cvt_pk_bf16_f32 v183, v142, v143
v_mfma_f32_16x16x32_bf16 v[128:131], a[0:3], a[32:35], v[192:195]
ds_read_b128 a[24:27], v200 offset:13312
v_sub_f32_e32 v145, v145, v236
v_sub_f32_e32 v146, v146, v236
v_mfma_f32_16x16x32_bf16 v[136:139], a[0:3], a[36:39], v[196:199]
v_sub_f32_e32 v147, v147, v236
v_sub_f32_e32 v148, v148, v236
v_sub_f32_e32 v149, v149, v236
v_mfma_f32_16x16x32_bf16 v[128:131], a[4:7], a[40:43], v[128:131]
ds_read_b128 a[28:31], v200 offset:13824
v_sub_f32_e32 v150, v150, v236
v_sub_f32_e32 v151, v151, v236
v_mfma_f32_16x16x32_bf16 v[136:139], a[4:7], a[44:47], v[136:139]
s_barrier
s_waitcnt lgkmcnt(4)
v_mfma_f32_16x16x32_bf16 v[128:131], a[8:11], a[48:51], v[128:131]
ds_read_b64_tr_b16 a[0:1], v203 offset:32768
v_exp_f32_e32 v144, v144
v_mfma_f32_16x16x32_bf16 v[136:139], a[8:11], a[52:55], v[136:139]
ds_read_b64_tr_b16 a[2:3], v203 offset:36864
v_exp_f32_e32 v145, v145
v_mfma_f32_16x16x32_bf16 v[128:131], a[12:15], a[56:59], v[128:131]
ds_read_b64_tr_b16 a[4:5], v204 offset:32768
v_cvt_pk_bf16_f32 v184, v144, v145
v_exp_f32_e32 v146, v146
v_mfma_f32_16x16x32_bf16 v[136:139], a[12:15], a[60:63], v[136:139]
s_waitcnt lgkmcnt(5)
ds_read_b64_tr_b16 a[6:7], v204 offset:36864
v_sub_f32_e32 v152, v152, v237
v_mfma_f32_16x16x32_bf16 v[132:135], a[16:19], a[32:35], v[192:195]
ds_read_b64_tr_b16 a[8:9], v203 offset:33280
v_exp_f32_e32 v147, v147
v_mfma_f32_16x16x32_bf16 v[140:143], a[16:19], a[36:39], v[196:199]
ds_read_b64_tr_b16 a[10:11], v203 offset:37376
v_cvt_pk_bf16_f32 v185, v146, v147
v_sub_f32_e32 v153, v153, v237
v_mfma_f32_16x16x32_bf16 v[132:135], a[20:23], a[40:43], v[132:135]
ds_read_b64_tr_b16 a[12:13], v204 offset:33280
v_exp_f32_e32 v148, v148
v_mfma_f32_16x16x32_bf16 v[140:143], a[20:23], a[44:47], v[140:143]
s_waitcnt lgkmcnt(7)
ds_read_b64_tr_b16 a[14:15], v204 offset:37376
v_exp_f32_e32 v149, v149
v_mfma_f32_16x16x32_bf16 v[132:135], a[24:27], a[48:51], v[132:135]
ds_read_b64_tr_b16 a[16:17], v203 offset:33792
v_cvt_pk_bf16_f32 v186, v148, v149
v_mfma_f32_16x16x32_bf16 v[140:143], a[24:27], a[52:55], v[140:143]
ds_read_b64_tr_b16 a[18:19], v203 offset:37888
v_exp_f32_e32 v150, v150
v_mfma_f32_16x16x32_bf16 v[132:135], a[28:31], a[56:59], v[132:135]
ds_read_b64_tr_b16 a[20:21], v204 offset:33792
v_exp_f32_e32 v151, v151
v_mfma_f32_16x16x32_bf16 v[140:143], a[28:31], a[60:63], v[140:143]
s_waitcnt lgkmcnt(7)
ds_read_b64_tr_b16 a[22:23], v204 offset:37888
v_cvt_pk_bf16_f32 v187, v150, v151
v_sub_f32_e32 v154, v154, v237
v_mfma_f32_16x16x32_bf16 v[0:3], a[0:3], v[176:179], 0
ds_read_b64_tr_b16 a[24:25], v203 offset:34304
v_sub_f32_e32 v155, v155, v237
v_mfma_f32_16x16x32_bf16 v[8:11], a[0:3], v[180:183], 0
ds_read_b64_tr_b16 a[26:27], v203 offset:38400
v_sub_f32_e32 v156, v156, v237
v_sub_f32_e32 v157, v157, v237
v_mfma_f32_16x16x32_bf16 v[4:7], a[4:7], v[176:179], 0
ds_read_b64_tr_b16 a[28:29], v204 offset:34304
v_sub_f32_e32 v158, v158, v237
v_sub_f32_e32 v159, v159, v237
v_mfma_f32_16x16x32_bf16 v[12:15], a[4:7], v[180:183], 0
s_waitcnt lgkmcnt(7)
ds_read_b64_tr_b16 a[30:31], v204 offset:38400
v_exp_f32_e32 v152, v152
v_mfma_f32_16x16x32_bf16 v[16:19], a[8:11], v[176:179], 0
ds_read_b64_tr_b16 a[0:1], v203 offset:40960
v_exp_f32_e32 v153, v153
v_mfma_f32_16x16x32_bf16 v[24:27], a[8:11], v[180:183], 0
ds_read_b64_tr_b16 a[2:3], v203 offset:45056
v_cvt_pk_bf16_f32 v188, v152, v153
v_add_f32_e32 v242, v242, v144
v_mfma_f32_16x16x32_bf16 v[20:23], a[12:15], v[176:179], 0
ds_read_b64_tr_b16 a[4:5], v204 offset:40960
v_exp_f32_e32 v154, v154
v_mfma_f32_16x16x32_bf16 v[28:31], a[12:15], v[180:183], 0
s_waitcnt lgkmcnt(7)
ds_read_b64_tr_b16 a[6:7], v204 offset:45056
v_exp_f32_e32 v155, v155
v_mfma_f32_16x16x32_bf16 v[32:35], a[16:19], v[176:179], 0
ds_read_b64_tr_b16 a[8:9], v203 offset:41472
v_cvt_pk_bf16_f32 v189, v154, v155
v_mfma_f32_16x16x32_bf16 v[40:43], a[16:19], v[180:183], 0
ds_read_b64_tr_b16 a[10:11], v203 offset:45568
v_exp_f32_e32 v156, v156
v_mfma_f32_16x16x32_bf16 v[36:39], a[20:23], v[176:179], 0
ds_read_b64_tr_b16 a[12:13], v204 offset:41472
v_exp_f32_e32 v157, v157
v_mfma_f32_16x16x32_bf16 v[44:47], a[20:23], v[180:183], 0
s_waitcnt lgkmcnt(7)
ds_read_b64_tr_b16 a[14:15], v204 offset:45568
v_cvt_pk_bf16_f32 v190, v156, v157
v_add_f32_e32 v243, v243, v152
v_mfma_f32_16x16x32_bf16 v[48:51], a[24:27], v[176:179], 0
ds_read_b64_tr_b16 a[16:17], v203 offset:41984
v_exp_f32_e32 v158, v158
v_mfma_f32_16x16x32_bf16 v[56:59], a[24:27], v[180:183], 0
ds_read_b64_tr_b16 a[18:19], v203 offset:46080
v_exp_f32_e32 v159, v159
v_mfma_f32_16x16x32_bf16 v[52:55], a[28:31], v[176:179], 0
ds_read_b64_tr_b16 a[20:21], v204 offset:41984
v_cvt_pk_bf16_f32 v191, v158, v159
v_mfma_f32_16x16x32_bf16 v[60:63], a[28:31], v[180:183], 0
s_waitcnt lgkmcnt(7)
ds_read_b64_tr_b16 a[22:23], v204 offset:46080
v_add_f32_e32 v242, v242, v145
v_add_f32_e32 v243, v243, v153
v_mfma_f32_16x16x32_bf16 v[0:3], a[0:3], v[184:187], v[0:3]
ds_read_b64_tr_b16 a[24:25], v203 offset:42496
v_add_f32_e32 v242, v242, v146
v_add_f32_e32 v243, v243, v154
v_mfma_f32_16x16x32_bf16 v[8:11], a[0:3], v[188:191], v[8:11]
ds_read_b64_tr_b16 a[26:27], v203 offset:46592
v_add_f32_e32 v242, v242, v147
v_add_f32_e32 v243, v243, v155
v_mfma_f32_16x16x32_bf16 v[4:7], a[4:7], v[184:187], v[4:7]
ds_read_b64_tr_b16 a[28:29], v204 offset:42496
v_add_f32_e32 v242, v242, v148
v_add_f32_e32 v243, v243, v156
v_mfma_f32_16x16x32_bf16 v[12:15], a[4:7], v[188:191], v[12:15]
s_barrier
ds_read_b64_tr_b16 a[30:31], v204 offset:46592
s_waitcnt lgkmcnt(8)
v_mfma_f32_16x16x32_bf16 v[16:19], a[8:11], v[184:187], v[16:19]
ds_read_b128 a[0:3], v200 offset:49152
v_add_f32_e32 v242, v242, v149
v_add_f32_e32 v243, v243, v157
v_mfma_f32_16x16x32_bf16 v[24:27], a[8:11], v[188:191], v[24:27]
v_add_f32_e32 v242, v242, v150
v_add_f32_e32 v243, v243, v158
v_mfma_f32_16x16x32_bf16 v[20:23], a[12:15], v[184:187], v[20:23]
ds_read_b128 a[4:7], v200 offset:49664
)"
R"(
v_add_f32_e32 v242, v242, v151
v_add_f32_e32 v243, v243, v159
v_mfma_f32_16x16x32_bf16 v[28:31], a[12:15], v[188:191], v[28:31]
s_waitcnt lgkmcnt(6)
v_max3_f32 v236, v160, v161, v162
v_max3_f32 v237, v168, v169, v170
v_max3_f32 v236, v236, v163, v164
v_mfma_f32_16x16x32_bf16 v[32:35], a[16:19], v[184:187], v[32:35]
ds_read_b128 a[8:11], v200 offset:50176
v_max3_f32 v237, v237, v171, v172
v_max3_f32 v236, v236, v165, v166
v_mfma_f32_16x16x32_bf16 v[40:43], a[16:19], v[188:191], v[40:43]
v_max3_f32 v237, v237, v173, v174
v_max3_f32 v236, v236, v167, v128
v_max3_f32 v237, v237, v175, v136
v_mfma_f32_16x16x32_bf16 v[36:39], a[20:23], v[184:187], v[36:39]
ds_read_b128 a[12:15], v200 offset:50688
v_max3_f32 v236, v236, v129, v130
v_max3_f32 v237, v237, v137, v138
v_mfma_f32_16x16x32_bf16 v[44:47], a[20:23], v[188:191], v[44:47]
s_waitcnt lgkmcnt(4)
v_max3_f32 v236, v236, v131, v132
v_max3_f32 v237, v237, v139, v140
v_mfma_f32_16x16x32_bf16 v[48:51], a[24:27], v[184:187], v[48:51]
ds_read_b128 a[16:19], v200 offset:53248
v_max3_f32 v236, v236, v133, v134
v_max3_f32 v237, v237, v141, v142
v_mfma_f32_16x16x32_bf16 v[56:59], a[24:27], v[188:191], v[56:59]
v_max3_f32 v248, v236, v135, v143
v_max_f32_e32 v248, v248, v237
v_cmp_lt_f32_e32 vcc, s70, v248
v_mfma_f32_16x16x32_bf16 v[52:55], a[28:31], v[184:187], v[52:55]
s_nop 0
s_cbranch_vccnz .Lk_ara1
.Lk_aba1:
ds_read_b128 a[20:23], v200 offset:53760
v_mfma_f32_16x16x32_bf16 v[60:63], a[28:31], v[188:191], v[60:63]
s_waitcnt lgkmcnt(4)
v_exp_f32_e32 v160, v160
v_mfma_f32_16x16x32_bf16 v[144:147], a[0:3], a[32:35], v[192:195]
ds_read_b128 a[24:27], v200 offset:54272
v_add_f32_e32 v242, v242, v160
v_mfma_f32_16x16x32_bf16 v[152:155], a[0:3], a[36:39], v[196:199]
v_exp_f32_e32 v161, v161
v_exp_f32_e32 v162, v162
v_mfma_f32_16x16x32_bf16 v[144:147], a[4:7], a[40:43], v[144:147]
ds_read_b128 a[28:31], v200 offset:54784
v_add_f32_e32 v242, v242, v161
v_mfma_f32_16x16x32_bf16 v[152:155], a[4:7], a[44:47], v[152:155]
s_waitcnt lgkmcnt(4)
v_cvt_pk_bf16_f32 v176, v160, v161
v_add_f32_e32 v242, v242, v162
v_exp_f32_e32 v163, v163
v_mfma_f32_16x16x32_bf16 v[144:147], a[8:11], a[48:51], v[144:147]
ds_read_b128 a[0:3], v200 offset:57344
v_add_f32_e32 v242, v242, v163
v_mfma_f32_16x16x32_bf16 v[152:155], a[8:11], a[52:55], v[152:155]
v_cvt_pk_bf16_f32 v177, v162, v163
v_exp_f32_e32 v168, v168
v_mfma_f32_16x16x32_bf16 v[144:147], a[12:15], a[56:59], v[144:147]
ds_read_b128 a[4:7], v200 offset:57856
v_add_f32_e32 v243, v243, v168
v_mfma_f32_16x16x32_bf16 v[152:155], a[12:15], a[60:63], v[152:155]
s_waitcnt lgkmcnt(4)
v_exp_f32_e32 v169, v169
v_exp_f32_e32 v170, v170
v_mfma_f32_16x16x32_bf16 v[148:151], a[16:19], a[32:35], v[192:195]
ds_read_b128 a[8:11], v200 offset:58368
v_add_f32_e32 v243, v243, v169
v_mfma_f32_16x16x32_bf16 v[156:159], a[16:19], a[36:39], v[196:199]
v_cvt_pk_bf16_f32 v180, v168, v169
v_add_f32_e32 v243, v243, v170
v_exp_f32_e32 v171, v171
v_mfma_f32_16x16x32_bf16 v[148:151], a[20:23], a[40:43], v[148:151]
ds_read_b128 a[12:15], v200 offset:58880
v_add_f32_e32 v243, v243, v171
v_mfma_f32_16x16x32_bf16 v[156:159], a[20:23], a[44:47], v[156:159]
s_waitcnt lgkmcnt(4)
v_cvt_pk_bf16_f32 v181, v170, v171
v_exp_f32_e32 v164, v164
v_mfma_f32_16x16x32_bf16 v[148:151], a[24:27], a[48:51], v[148:151]
ds_read_b128 a[16:19], v200 offset:61440
v_add_f32_e32 v242, v242, v164
v_max3_f32 v236, v144, v145, v146
v_mfma_f32_16x16x32_bf16 v[156:159], a[24:27], a[52:55], v[156:159]
v_exp_f32_e32 v165, v165
v_max3_f32 v237, v152, v153, v154
v_mfma_f32_16x16x32_bf16 v[148:151], a[28:31], a[56:59], v[148:151]
ds_read_b128 a[20:23], v200 offset:61952
v_add_f32_e32 v242, v242, v165
v_cvt_pk_bf16_f32 v178, v164, v165
v_mfma_f32_16x16x32_bf16 v[156:159], a[28:31], a[60:63], v[156:159]
s_waitcnt lgkmcnt(4)
v_exp_f32_e32 v166, v166
s_nop 0
v_add_f32_e32 v242, v242, v166
v_mfma_f32_16x16x32_bf16 v[160:163], a[0:3], a[32:35], v[192:195]
ds_read_b128 a[24:27], v200 offset:62464
v_exp_f32_e32 v167, v167
v_mfma_f32_16x16x32_bf16 v[168:171], a[0:3], a[36:39], v[196:199]
v_add_f32_e32 v242, v242, v167
v_cvt_pk_bf16_f32 v179, v166, v167
v_mfma_f32_16x16x32_bf16 v[160:163], a[4:7], a[40:43], v[160:163]
ds_read_b128 a[28:31], v200 offset:62976
v_exp_f32_e32 v172, v172
v_max3_f32 v236, v236, v147, v148
v_mfma_f32_16x16x32_bf16 v[168:171], a[4:7], a[44:47], v[168:171]
s_barrier
v_add_f32_e32 v243, v243, v172
s_waitcnt lgkmcnt(4)
v_mfma_f32_16x16x32_bf16 v[160:163], a[8:11], a[48:51], v[160:163]
ds_read_b64_tr_b16 a[0:1], v211 offset:16384
v_exp_f32_e32 v173, v173
v_mfma_f32_16x16x32_bf16 v[168:171], a[8:11], a[52:55], v[168:171]
ds_read_b64_tr_b16 a[2:3], v211 offset:16640
v_add_f32_e32 v243, v243, v173
v_cvt_pk_bf16_f32 v182, v172, v173
v_max3_f32 v237, v237, v155, v156
v_mfma_f32_16x16x32_bf16 v[160:163], a[12:15], a[56:59], v[160:163]
ds_read_b64_tr_b16 a[4:5], v212 offset:16384
v_exp_f32_e32 v174, v174
v_mfma_f32_16x16x32_bf16 v[168:171], a[12:15], a[60:63], v[168:171]
s_waitcnt lgkmcnt(5)
ds_read_b64_tr_b16 a[6:7], v212 offset:16640
v_add_f32_e32 v243, v243, v174
v_exp_f32_e32 v175, v175
v_mfma_f32_16x16x32_bf16 v[164:167], a[16:19], a[32:35], v[192:195]
ds_read_b64_tr_b16 a[8:9], v211 offset:16896
v_add_f32_e32 v243, v243, v175
v_cvt_pk_bf16_f32 v183, v174, v175
v_mfma_f32_16x16x32_bf16 v[172:175], a[16:19], a[36:39], v[196:199]
ds_read_b64_tr_b16 a[10:11], v211 offset:17152
v_max3_f32 v236, v236, v149, v150
v_mfma_f32_16x16x32_bf16 v[164:167], a[20:23], a[40:43], v[164:167]
ds_read_b64_tr_b16 a[12:13], v212 offset:16896
v_exp_f32_e32 v128, v128
v_mfma_f32_16x16x32_bf16 v[172:175], a[20:23], a[44:47], v[172:175]
s_waitcnt lgkmcnt(7)
ds_read_b64_tr_b16 a[14:15], v212 offset:17152
v_mfma_f32_16x16x32_bf16 v[164:167], a[24:27], a[48:51], v[164:167]
s_cbranch_vccnz .Lk_arb1
.Lk_abb1:
ds_read_b64_tr_b16 a[16:17], v211 offset:17408
v_mfma_f32_16x16x32_bf16 v[172:175], a[24:27], a[52:55], v[172:175]
ds_read_b64_tr_b16 a[18:19], v211 offset:17664
v_mfma_f32_16x16x32_bf16 v[164:167], a[28:31], a[56:59], v[164:167]
ds_read_b64_tr_b16 a[20:21], v212 offset:17408
v_exp_f32_e32 v129, v129
v_mfma_f32_16x16x32_bf16 v[172:175], a[28:31], a[60:63], v[172:175]
s_waitcnt lgkmcnt(7)
ds_read_b64_tr_b16 a[22:23], v212 offset:17664
v_cvt_pk_bf16_f32 v184, v128, v129
v_mfma_f32_16x16x32_bf16 v[64:67], a[0:3], a[160:163], v[64:67]
ds_read_b64_tr_b16 a[24:25], v211 offset:17920
v_exp_f32_e32 v130, v130
v_mfma_f32_16x16x32_bf16 v[72:75], a[0:3], a[164:167], v[72:75]
ds_read_b64_tr_b16 a[26:27], v211 offset:18176
v_add_f32_e32 v242, v242, v128
v_mfma_f32_16x16x32_bf16 v[68:71], a[4:7], a[160:163], v[68:71]
ds_read_b64_tr_b16 a[28:29], v212 offset:17920
v_exp_f32_e32 v131, v131
v_mfma_f32_16x16x32_bf16 v[76:79], a[4:7], a[164:167], v[76:79]
s_waitcnt lgkmcnt(7)
ds_read_b64_tr_b16 a[30:31], v212 offset:18176
v_mfma_f32_16x16x32_bf16 v[80:83], a[8:11], a[160:163], v[80:83]
ds_read_b64_tr_b16 a[0:1], v211 offset:24576
v_cvt_pk_bf16_f32 v185, v130, v131
v_add_f32_e32 v242, v242, v129
v_mfma_f32_16x16x32_bf16 v[88:91], a[8:11], a[164:167], v[88:91]
ds_read_b64_tr_b16 a[2:3], v211 offset:24832
v_exp_f32_e32 v132, v132
v_mfma_f32_16x16x32_bf16 v[84:87], a[12:15], a[160:163], v[84:87]
ds_read_b64_tr_b16 a[4:5], v212 offset:24576
v_add_f32_e32 v242, v242, v130
v_mfma_f32_16x16x32_bf16 v[92:95], a[12:15], a[164:167], v[92:95]
s_waitcnt lgkmcnt(7)
ds_read_b64_tr_b16 a[6:7], v212 offset:24832
v_add_f32_e32 v242, v242, v131
v_mfma_f32_16x16x32_bf16 v[96:99], a[16:19], a[160:163], v[96:99]
ds_read_b64_tr_b16 a[8:9], v211 offset:25088
v_exp_f32_e32 v133, v133
v_mfma_f32_16x16x32_bf16 v[104:107], a[16:19], a[164:167], v[104:107]
ds_read_b64_tr_b16 a[10:11], v211 offset:25344
v_cvt_pk_bf16_f32 v186, v132, v133
v_mfma_f32_16x16x32_bf16 v[100:103], a[20:23], a[160:163], v[100:103]
ds_read_b64_tr_b16 a[12:13], v212 offset:25088
v_exp_f32_e32 v134, v134
v_mfma_f32_16x16x32_bf16 v[108:111], a[20:23], a[164:167], v[108:111]
s_waitcnt lgkmcnt(7)
ds_read_b64_tr_b16 a[14:15], v212 offset:25344
v_mfma_f32_16x16x32_bf16 v[112:115], a[24:27], a[160:163], v[112:115]
ds_read_b64_tr_b16 a[16:17], v211 offset:25600
v_exp_f32_e32 v135, v135
v_mfma_f32_16x16x32_bf16 v[120:123], a[24:27], a[164:167], v[120:123]
ds_read_b64_tr_b16 a[18:19], v211 offset:25856
v_cvt_pk_bf16_f32 v187, v134, v135
v_mfma_f32_16x16x32_bf16 v[116:119], a[28:31], a[160:163], v[116:119]
ds_read_b64_tr_b16 a[20:21], v212 offset:25600
v_exp_f32_e32 v136, v136
v_mfma_f32_16x16x32_bf16 v[124:127], a[28:31], a[164:167], v[124:127]
)"
R"(
s_waitcnt lgkmcnt(7)
ds_read_b64_tr_b16 a[22:23], v212 offset:25856
v_add_f32_e32 v243, v243, v136
v_mfma_f32_16x16x32_bf16 v[64:67], a[0:3], a[168:171], v[64:67]
ds_read_b64_tr_b16 a[24:25], v211 offset:26112
v_exp_f32_e32 v137, v137
v_mfma_f32_16x16x32_bf16 v[72:75], a[0:3], a[172:175], v[72:75]
ds_read_b64_tr_b16 a[26:27], v211 offset:26368
v_cvt_pk_bf16_f32 v188, v136, v137
v_mfma_f32_16x16x32_bf16 v[68:71], a[4:7], a[168:171], v[68:71]
ds_read_b64_tr_b16 a[28:29], v212 offset:26112
v_add_f32_e32 v243, v243, v137
v_mfma_f32_16x16x32_bf16 v[76:79], a[4:7], a[172:175], v[76:79]
s_barrier
ds_read_b64_tr_b16 a[30:31], v212 offset:26368
s_waitcnt lgkmcnt(8)
v_mfma_f32_16x16x32_bf16 v[80:83], a[8:11], a[168:171], v[80:83]
ds_read_b64_tr_b16 a[0:1], v205
v_exp_f32_e32 v138, v138
v_mfma_f32_16x16x32_bf16 v[88:91], a[8:11], a[172:175], v[88:91]
ds_read_b64_tr_b16 a[2:3], v205 offset:4096
v_add_f32_e32 v243, v243, v138
v_mfma_f32_16x16x32_bf16 v[84:87], a[12:15], a[168:171], v[84:87]
ds_read_b64_tr_b16 a[4:5], v206
v_exp_f32_e32 v139, v139
v_mfma_f32_16x16x32_bf16 v[92:95], a[12:15], a[172:175], v[92:95]
s_waitcnt lgkmcnt(7)
ds_read_b64_tr_b16 a[6:7], v206 offset:4096
v_mfma_f32_16x16x32_bf16 v[96:99], a[16:19], a[168:171], v[96:99]
ds_read_b64_tr_b16 a[8:9], v205 offset:512
v_cvt_pk_bf16_f32 v189, v138, v139
v_add_f32_e32 v243, v243, v139
v_mfma_f32_16x16x32_bf16 v[104:107], a[16:19], a[172:175], v[104:107]
ds_read_b64_tr_b16 a[10:11], v205 offset:4608
v_exp_f32_e32 v140, v140
v_mfma_f32_16x16x32_bf16 v[100:103], a[20:23], a[168:171], v[100:103]
ds_read_b64_tr_b16 a[12:13], v206 offset:512
v_mfma_f32_16x16x32_bf16 v[108:111], a[20:23], a[172:175], v[108:111]
s_waitcnt lgkmcnt(7)
ds_read_b64_tr_b16 a[14:15], v206 offset:4608
v_exp_f32_e32 v141, v141
v_mfma_f32_16x16x32_bf16 v[112:115], a[24:27], a[168:171], v[112:115]
ds_read_b64_tr_b16 a[16:17], v205 offset:1024
v_cvt_pk_bf16_f32 v190, v140, v141
v_mfma_f32_16x16x32_bf16 v[120:123], a[24:27], a[172:175], v[120:123]
ds_read_b64_tr_b16 a[18:19], v205 offset:5120
v_exp_f32_e32 v142, v142
v_mfma_f32_16x16x32_bf16 v[116:119], a[28:31], a[168:171], v[116:119]
ds_read_b64_tr_b16 a[20:21], v206 offset:1024
v_add_f32_e32 v242, v242, v132
v_mfma_f32_16x16x32_bf16 v[124:127], a[28:31], a[172:175], v[124:127]
s_waitcnt lgkmcnt(7)
ds_read_b64_tr_b16 a[22:23], v206 offset:5120
v_exp_f32_e32 v143, v143
v_mfma_f32_16x16x32_bf16 v[0:3], a[0:3], v[176:179], v[0:3]
ds_read_b64_tr_b16 a[24:25], v205 offset:1536
v_cvt_pk_bf16_f32 v191, v142, v143
v_mfma_f32_16x16x32_bf16 v[8:11], a[0:3], v[180:183], v[8:11]
ds_read_b64_tr_b16 a[26:27], v205 offset:5632
v_add_f32_e32 v243, v243, v140
v_mfma_f32_16x16x32_bf16 v[4:7], a[4:7], v[176:179], v[4:7]
ds_read_b64_tr_b16 a[28:29], v206 offset:1536
v_add_f32_e32 v242, v242, v133
v_mfma_f32_16x16x32_bf16 v[12:15], a[4:7], v[180:183], v[12:15]
s_waitcnt lgkmcnt(7)
ds_read_b64_tr_b16 a[30:31], v206 offset:5632
v_add_f32_e32 v243, v243, v141
v_add_f32_e32 v242, v242, v134
v_mfma_f32_16x16x32_bf16 v[16:19], a[8:11], v[176:179], v[16:19]
ds_read_b64_tr_b16 a[0:1], v205 offset:8192
v_add_f32_e32 v243, v243, v142
v_mfma_f32_16x16x32_bf16 v[24:27], a[8:11], v[180:183], v[24:27]
ds_read_b64_tr_b16 a[2:3], v205 offset:12288
v_add_f32_e32 v242, v242, v135
v_add_f32_e32 v243, v243, v143
v_mfma_f32_16x16x32_bf16 v[20:23], a[12:15], v[176:179], v[20:23]
ds_read_b64_tr_b16 a[4:5], v206 offset:8192
v_max3_f32 v237, v237, v157, v158
v_mfma_f32_16x16x32_bf16 v[28:31], a[12:15], v[180:183], v[28:31]
s_waitcnt lgkmcnt(7)
ds_read_b64_tr_b16 a[6:7], v206 offset:12288
v_max3_f32 v236, v236, v151, v160
v_mfma_f32_16x16x32_bf16 v[32:35], a[16:19], v[176:179], v[32:35]
ds_read_b64_tr_b16 a[8:9], v205 offset:8704
v_max3_f32 v237, v237, v159, v168
v_max3_f32 v236, v236, v161, v162
v_mfma_f32_16x16x32_bf16 v[40:43], a[16:19], v[180:183], v[40:43]
ds_read_b64_tr_b16 a[10:11], v205 offset:12800
v_max3_f32 v237, v237, v169, v170
v_mfma_f32_16x16x32_bf16 v[36:39], a[20:23], v[176:179], v[36:39]
ds_read_b64_tr_b16 a[12:13], v206 offset:8704
v_max3_f32 v236, v236, v163, v164
v_mfma_f32_16x16x32_bf16 v[44:47], a[20:23], v[180:183], v[44:47]
s_waitcnt lgkmcnt(7)
ds_read_b64_tr_b16 a[14:15], v206 offset:12800
v_max3_f32 v237, v237, v171, v172
v_max3_f32 v236, v236, v165, v166
v_mfma_f32_16x16x32_bf16 v[48:51], a[24:27], v[176:179], v[48:51]
ds_read_b64_tr_b16 a[16:17], v205 offset:9216
v_max3_f32 v237, v237, v173, v174
v_mfma_f32_16x16x32_bf16 v[56:59], a[24:27], v[180:183], v[56:59]
ds_read_b64_tr_b16 a[18:19], v205 offset:13312
v_max3_f32 v248, v236, v167, v175
v_mfma_f32_16x16x32_bf16 v[52:55], a[28:31], v[176:179], v[52:55]
ds_read_b64_tr_b16 a[20:21], v206 offset:9216
v_max_f32_e32 v248, v248, v237
v_cmp_lt_f32_e32 vcc, s70, v248
v_mfma_f32_16x16x32_bf16 v[60:63], a[28:31], v[180:183], v[60:63]
s_waitcnt lgkmcnt(7)
ds_read_b64_tr_b16 a[22:23], v206 offset:13312
v_mfma_f32_16x16x32_bf16 v[0:3], a[0:3], v[184:187], v[0:3]
s_cbranch_vccnz .Lk_ara2
.Lk_aba2:
ds_read_b64_tr_b16 a[24:25], v205 offset:9728
v_mfma_f32_16x16x32_bf16 v[8:11], a[0:3], v[188:191], v[8:11]
ds_read_b64_tr_b16 a[26:27], v205 offset:13824
v_mfma_f32_16x16x32_bf16 v[4:7], a[4:7], v[184:187], v[4:7]
ds_read_b64_tr_b16 a[28:29], v206 offset:9728
v_exp_f32_e32 v144, v144
v_mfma_f32_16x16x32_bf16 v[12:15], a[4:7], v[188:191], v[12:15]
s_barrier
ds_read_b64_tr_b16 a[30:31], v206 offset:13824
s_waitcnt lgkmcnt(8)
v_mfma_f32_16x16x32_bf16 v[16:19], a[8:11], v[184:187], v[16:19]
ds_read_b128 a[0:3], v201 offset:32768
v_add_f32_e32 v242, v242, v144
v_mfma_f32_16x16x32_bf16 v[24:27], a[8:11], v[188:191], v[24:27]
v_exp_f32_e32 v145, v145
v_exp_f32_e32 v146, v146
v_mfma_f32_16x16x32_bf16 v[20:23], a[12:15], v[184:187], v[20:23]
ds_read_b128 a[4:7], v201 offset:33280
v_mfma_f32_16x16x32_bf16 v[28:31], a[12:15], v[188:191], v[28:31]
s_waitcnt lgkmcnt(6)
v_add_f32_e32 v242, v242, v145
v_cvt_pk_bf16_f32 v176, v144, v145
v_add_f32_e32 v242, v242, v146
v_mfma_f32_16x16x32_bf16 v[32:35], a[16:19], v[184:187], v[32:35]
ds_read_b128 a[8:11], v201 offset:33792
v_exp_f32_e32 v147, v147
v_mfma_f32_16x16x32_bf16 v[40:43], a[16:19], v[188:191], v[40:43]
v_add_f32_e32 v242, v242, v147
v_mfma_f32_16x16x32_bf16 v[36:39], a[20:23], v[184:187], v[36:39]
ds_read_b128 a[12:15], v201 offset:34304
v_cvt_pk_bf16_f32 v177, v146, v147
v_mfma_f32_16x16x32_bf16 v[44:47], a[20:23], v[188:191], v[44:47]
s_waitcnt lgkmcnt(4)
v_exp_f32_e32 v152, v152
v_exp_f32_e32 v153, v153
v_mfma_f32_16x16x32_bf16 v[48:51], a[24:27], v[184:187], v[48:51]
ds_read_b128 a[16:19], v201 offset:36864
v_mfma_f32_16x16x32_bf16 v[56:59], a[24:27], v[188:191], v[56:59]
v_add_f32_e32 v243, v243, v152
v_add_f32_e32 v243, v243, v153
v_cvt_pk_bf16_f32 v180, v152, v153
v_mfma_f32_16x16x32_bf16 v[52:55], a[28:31], v[184:187], v[52:55]
ds_read_b128 a[20:23], v201 offset:37376
v_exp_f32_e32 v154, v154
v_mfma_f32_16x16x32_bf16 v[60:63], a[28:31], v[188:191], v[60:63]
s_waitcnt lgkmcnt(4)
v_add_f32_e32 v243, v243, v154
v_mfma_f32_16x16x32_bf16 v[128:131], a[0:3], a[32:35], v[192:195]
ds_read_b128 a[24:27], v201 offset:37888
v_exp_f32_e32 v155, v155
v_mfma_f32_16x16x32_bf16 v[136:139], a[0:3], a[36:39], v[196:199]
v_add_f32_e32 v243, v243, v155
v_cvt_pk_bf16_f32 v181, v154, v155
v_mfma_f32_16x16x32_bf16 v[128:131], a[4:7], a[40:43], v[128:131]
ds_read_b128 a[28:31], v201 offset:38400
v_exp_f32_e32 v148, v148
v_mfma_f32_16x16x32_bf16 v[136:139], a[4:7], a[44:47], v[136:139]
s_waitcnt lgkmcnt(4)
v_add_f32_e32 v242, v242, v148
v_mfma_f32_16x16x32_bf16 v[128:131], a[8:11], a[48:51], v[128:131]
ds_read_b128 a[0:3], v201 offset:40960
v_exp_f32_e32 v149, v149
v_mfma_f32_16x16x32_bf16 v[136:139], a[8:11], a[52:55], v[136:139]
v_add_f32_e32 v242, v242, v149
v_cvt_pk_bf16_f32 v178, v148, v149
v_mfma_f32_16x16x32_bf16 v[128:131], a[12:15], a[56:59], v[128:131]
ds_read_b128 a[4:7], v201 offset:41472
v_exp_f32_e32 v150, v150
v_mfma_f32_16x16x32_bf16 v[136:139], a[12:15], a[60:63], v[136:139]
s_waitcnt lgkmcnt(4)
v_add_f32_e32 v242, v242, v150
v_exp_f32_e32 v151, v151
v_mfma_f32_16x16x32_bf16 v[132:135], a[16:19], a[32:35], v[192:195]
ds_read_b128 a[8:11], v201 offset:41984
v_add_f32_e32 v242, v242, v151
v_mfma_f32_16x16x32_bf16 v[140:143], a[16:19], a[36:39], v[196:199]
v_cvt_pk_bf16_f32 v179, v150, v151
v_exp_f32_e32 v156, v156
v_mfma_f32_16x16x32_bf16 v[132:135], a[20:23], a[40:43], v[132:135]
ds_read_b128 a[12:15], v201 offset:42496
v_mfma_f32_16x16x32_bf16 v[140:143], a[20:23], a[44:47], v[140:143]
s_waitcnt lgkmcnt(4)
v_add_f32_e32 v243, v243, v156
v_exp_f32_e32 v157, v157
v_mfma_f32_16x16x32_bf16 v[132:135], a[24:27], a[48:51], v[132:135]
ds_read_b128 a[16:19], v201 offset:45056
v_add_f32_e32 v243, v243, v157
)"
R"(
v_mfma_f32_16x16x32_bf16 v[140:143], a[24:27], a[52:55], v[140:143]
v_cvt_pk_bf16_f32 v182, v156, v157
v_exp_f32_e32 v158, v158
v_mfma_f32_16x16x32_bf16 v[132:135], a[28:31], a[56:59], v[132:135]
ds_read_b128 a[20:23], v201 offset:45568
v_add_f32_e32 v243, v243, v158
v_mfma_f32_16x16x32_bf16 v[140:143], a[28:31], a[60:63], v[140:143]
s_waitcnt lgkmcnt(4)
v_exp_f32_e32 v159, v159
v_mfma_f32_16x16x32_bf16 v[144:147], a[0:3], a[32:35], v[192:195]
ds_read_b128 a[24:27], v201 offset:46080
v_add_f32_e32 v243, v243, v159
v_cvt_pk_bf16_f32 v183, v158, v159
v_mfma_f32_16x16x32_bf16 v[152:155], a[0:3], a[36:39], v[196:199]
s_cbranch_vccnz .Lk_arb2
.Lk_abb2:
v_mfma_f32_16x16x32_bf16 v[144:147], a[4:7], a[40:43], v[144:147]
ds_read_b128 a[28:31], v201 offset:46592
v_mfma_f32_16x16x32_bf16 v[152:155], a[4:7], a[44:47], v[152:155]
s_barrier
v_max3_f32 v236, v128, v129, v130
s_waitcnt lgkmcnt(4)
v_mfma_f32_16x16x32_bf16 v[144:147], a[8:11], a[48:51], v[144:147]
ds_read_b64_tr_b16 a[0:1], v205 offset:49152
v_exp_f32_e32 v160, v160
v_mfma_f32_16x16x32_bf16 v[152:155], a[8:11], a[52:55], v[152:155]
ds_read_b64_tr_b16 a[2:3], v205 offset:53248
v_mfma_f32_16x16x32_bf16 v[144:147], a[12:15], a[56:59], v[144:147]
ds_read_b64_tr_b16 a[4:5], v206 offset:49152
v_exp_f32_e32 v161, v161
v_mfma_f32_16x16x32_bf16 v[152:155], a[12:15], a[60:63], v[152:155]
s_waitcnt lgkmcnt(5)
ds_read_b64_tr_b16 a[6:7], v206 offset:53248
v_cvt_pk_bf16_f32 v184, v160, v161
v_mfma_f32_16x16x32_bf16 v[148:151], a[16:19], a[32:35], v[192:195]
ds_read_b64_tr_b16 a[8:9], v205 offset:49664
v_exp_f32_e32 v162, v162
v_mfma_f32_16x16x32_bf16 v[156:159], a[16:19], a[36:39], v[196:199]
ds_read_b64_tr_b16 a[10:11], v205 offset:53760
v_add_f32_e32 v242, v242, v160
v_mfma_f32_16x16x32_bf16 v[148:151], a[20:23], a[40:43], v[148:151]
ds_read_b64_tr_b16 a[12:13], v206 offset:49664
v_exp_f32_e32 v163, v163
v_mfma_f32_16x16x32_bf16 v[156:159], a[20:23], a[44:47], v[156:159]
s_waitcnt lgkmcnt(7)
ds_read_b64_tr_b16 a[14:15], v206 offset:53760
v_cvt_pk_bf16_f32 v185, v162, v163
v_mfma_f32_16x16x32_bf16 v[148:151], a[24:27], a[48:51], v[148:151]
ds_read_b64_tr_b16 a[16:17], v205 offset:50176
v_exp_f32_e32 v164, v164
v_mfma_f32_16x16x32_bf16 v[156:159], a[24:27], a[52:55], v[156:159]
ds_read_b64_tr_b16 a[18:19], v205 offset:54272
v_add_f32_e32 v242, v242, v161
v_mfma_f32_16x16x32_bf16 v[148:151], a[28:31], a[56:59], v[148:151]
ds_read_b64_tr_b16 a[20:21], v206 offset:50176
v_exp_f32_e32 v165, v165
v_mfma_f32_16x16x32_bf16 v[156:159], a[28:31], a[60:63], v[156:159]
s_waitcnt lgkmcnt(7)
ds_read_b64_tr_b16 a[22:23], v206 offset:54272
v_cvt_pk_bf16_f32 v186, v164, v165
v_mfma_f32_16x16x32_bf16 v[0:3], a[0:3], v[176:179], v[0:3]
ds_read_b64_tr_b16 a[24:25], v205 offset:50688
v_exp_f32_e32 v166, v166
v_mfma_f32_16x16x32_bf16 v[8:11], a[0:3], v[180:183], v[8:11]
ds_read_b64_tr_b16 a[26:27], v205 offset:54784
v_mfma_f32_16x16x32_bf16 v[4:7], a[4:7], v[176:179], v[4:7]
ds_read_b64_tr_b16 a[28:29], v206 offset:50688
v_exp_f32_e32 v167, v167
v_mfma_f32_16x16x32_bf16 v[12:15], a[4:7], v[180:183], v[12:15]
s_waitcnt lgkmcnt(7)
ds_read_b64_tr_b16 a[30:31], v206 offset:54784
v_cvt_pk_bf16_f32 v187, v166, v167
v_mfma_f32_16x16x32_bf16 v[16:19], a[8:11], v[176:179], v[16:19]
ds_read_b64_tr_b16 a[0:1], v205 offset:57344
v_exp_f32_e32 v168, v168
v_mfma_f32_16x16x32_bf16 v[24:27], a[8:11], v[180:183], v[24:27]
ds_read_b64_tr_b16 a[2:3], v205 offset:61440
v_exp_f32_e32 v169, v169
v_mfma_f32_16x16x32_bf16 v[20:23], a[12:15], v[176:179], v[20:23]
ds_read_b64_tr_b16 a[4:5], v206 offset:57344
v_cvt_pk_bf16_f32 v188, v168, v169
v_mfma_f32_16x16x32_bf16 v[28:31], a[12:15], v[180:183], v[28:31]
s_waitcnt lgkmcnt(7)
ds_read_b64_tr_b16 a[6:7], v206 offset:61440
v_exp_f32_e32 v170, v170
v_mfma_f32_16x16x32_bf16 v[32:35], a[16:19], v[176:179], v[32:35]
ds_read_b64_tr_b16 a[8:9], v205 offset:57856
v_add_f32_e32 v243, v243, v168
v_mfma_f32_16x16x32_bf16 v[40:43], a[16:19], v[180:183], v[40:43]
ds_read_b64_tr_b16 a[10:11], v205 offset:61952
v_exp_f32_e32 v171, v171
v_mfma_f32_16x16x32_bf16 v[36:39], a[20:23], v[176:179], v[36:39]
ds_read_b64_tr_b16 a[12:13], v206 offset:57856
v_cvt_pk_bf16_f32 v189, v170, v171
v_mfma_f32_16x16x32_bf16 v[44:47], a[20:23], v[180:183], v[44:47]
s_waitcnt lgkmcnt(7)
ds_read_b64_tr_b16 a[14:15], v206 offset:61952
v_exp_f32_e32 v172, v172
v_mfma_f32_16x16x32_bf16 v[48:51], a[24:27], v[176:179], v[48:51]
ds_read_b64_tr_b16 a[16:17], v205 offset:58368
v_exp_f32_e32 v173, v173
v_mfma_f32_16x16x32_bf16 v[56:59], a[24:27], v[180:183], v[56:59]
ds_read_b64_tr_b16 a[18:19], v205 offset:62464
v_cvt_pk_bf16_f32 v190, v172, v173
v_mfma_f32_16x16x32_bf16 v[52:55], a[28:31], v[176:179], v[52:55]
ds_read_b64_tr_b16 a[20:21], v206 offset:58368
v_exp_f32_e32 v174, v174
v_mfma_f32_16x16x32_bf16 v[60:63], a[28:31], v[180:183], v[60:63]
s_waitcnt lgkmcnt(7)
ds_read_b64_tr_b16 a[22:23], v206 offset:62464
v_exp_f32_e32 v175, v175
v_mfma_f32_16x16x32_bf16 v[0:3], a[0:3], v[184:187], v[0:3]
ds_read_b64_tr_b16 a[24:25], v205 offset:58880
v_cvt_pk_bf16_f32 v191, v174, v175
s_nop 1
v_mfma_f32_16x16x32_bf16 v[8:11], a[0:3], v[188:191], v[8:11]
ds_read_b64_tr_b16 a[26:27], v205 offset:62976
v_add_f32_e32 v243, v243, v169
v_mfma_f32_16x16x32_bf16 v[4:7], a[4:7], v[184:187], v[4:7]
ds_read_b64_tr_b16 a[28:29], v206 offset:58880
v_add_f32_e32 v242, v242, v162
v_mfma_f32_16x16x32_bf16 v[12:15], a[4:7], v[188:191], v[12:15]
s_barrier
ds_read_b64_tr_b16 a[30:31], v206 offset:62976
s_waitcnt lgkmcnt(8)
v_mfma_f32_16x16x32_bf16 v[16:19], a[8:11], v[184:187], v[16:19]
ds_read_b64_tr_b16 a[0:1], v213
v_add_f32_e32 v243, v243, v170
v_mfma_f32_16x16x32_bf16 v[24:27], a[8:11], v[188:191], v[24:27]
ds_read_b64_tr_b16 a[2:3], v213 offset:256
v_add_f32_e32 v242, v242, v163
v_mfma_f32_16x16x32_bf16 v[20:23], a[12:15], v[184:187], v[20:23]
ds_read_b64_tr_b16 a[4:5], v214
v_add_f32_e32 v243, v243, v171
v_add_f32_e32 v242, v242, v164
v_mfma_f32_16x16x32_bf16 v[28:31], a[12:15], v[188:191], v[28:31]
s_waitcnt lgkmcnt(7)
ds_read_b64_tr_b16 a[6:7], v214 offset:256
v_add_f32_e32 v243, v243, v172
v_mfma_f32_16x16x32_bf16 v[32:35], a[16:19], v[184:187], v[32:35]
ds_read_b64_tr_b16 a[8:9], v213 offset:512
v_add_f32_e32 v242, v242, v165
v_mfma_f32_16x16x32_bf16 v[40:43], a[16:19], v[188:191], v[40:43]
ds_read_b64_tr_b16 a[10:11], v213 offset:768
v_add_f32_e32 v243, v243, v173
v_mfma_f32_16x16x32_bf16 v[36:39], a[20:23], v[184:187], v[36:39]
ds_read_b64_tr_b16 a[12:13], v214 offset:512
v_add_f32_e32 v242, v242, v166
v_add_f32_e32 v243, v243, v174
v_mfma_f32_16x16x32_bf16 v[44:47], a[20:23], v[188:191], v[44:47]
s_waitcnt lgkmcnt(7)
ds_read_b64_tr_b16 a[14:15], v214 offset:768
v_add_f32_e32 v242, v242, v167
v_mfma_f32_16x16x32_bf16 v[48:51], a[24:27], v[184:187], v[48:51]
ds_read_b64_tr_b16 a[16:17], v213 offset:1024
v_add_f32_e32 v243, v243, v175
v_mfma_f32_16x16x32_bf16 v[56:59], a[24:27], v[188:191], v[56:59]
ds_read_b64_tr_b16 a[18:19], v213 offset:1280
v_max3_f32 v237, v136, v137, v138
v_mfma_f32_16x16x32_bf16 v[52:55], a[28:31], v[184:187], v[52:55]
ds_read_b64_tr_b16 a[20:21], v214 offset:1024
v_max3_f32 v236, v236, v131, v132
v_max3_f32 v237, v237, v139, v140
v_mfma_f32_16x16x32_bf16 v[60:63], a[28:31], v[188:191], v[60:63]
s_waitcnt lgkmcnt(7)
ds_read_b64_tr_b16 a[22:23], v214 offset:1280
v_max3_f32 v236, v236, v133, v134
v_mfma_f32_16x16x32_bf16 v[64:67], a[0:3], a[176:179], v[64:67]
ds_read_b64_tr_b16 a[24:25], v213 offset:1536
v_max3_f32 v237, v237, v141, v142
v_mfma_f32_16x16x32_bf16 v[72:75], a[0:3], a[180:183], v[72:75]
ds_read_b64_tr_b16 a[26:27], v213 offset:1792
v_max3_f32 v236, v236, v135, v144
v_mfma_f32_16x16x32_bf16 v[68:71], a[4:7], a[176:179], v[68:71]
ds_read_b64_tr_b16 a[28:29], v214 offset:1536
v_max3_f32 v237, v237, v143, v152
v_max3_f32 v236, v236, v145, v146
v_mfma_f32_16x16x32_bf16 v[76:79], a[4:7], a[180:183], v[76:79]
s_waitcnt lgkmcnt(7)
ds_read_b64_tr_b16 a[30:31], v214 offset:1792
v_max3_f32 v237, v237, v153, v154
v_mfma_f32_16x16x32_bf16 v[80:83], a[8:11], a[176:179], v[80:83]
ds_read_b64_tr_b16 a[0:1], v213 offset:8192
v_max3_f32 v236, v236, v147, v148
v_mfma_f32_16x16x32_bf16 v[88:91], a[8:11], a[180:183], v[88:91]
ds_read_b64_tr_b16 a[2:3], v213 offset:8448
v_max3_f32 v237, v237, v155, v156
v_max3_f32 v236, v236, v149, v150
v_mfma_f32_16x16x32_bf16 v[84:87], a[12:15], a[176:179], v[84:87]
ds_read_b64_tr_b16 a[4:5], v214 offset:8192
v_max3_f32 v237, v237, v157, v158
v_mfma_f32_16x16x32_bf16 v[92:95], a[12:15], a[180:183], v[92:95]
s_waitcnt lgkmcnt(7)
ds_read_b64_tr_b16 a[6:7], v214 offset:8448
v_max3_f32 v248, v236, v151, v159
v_mfma_f32_16x16x32_bf16 v[96:99], a[16:19], a[176:179], v[96:99]
ds_read_b64_tr_b16 a[8:9], v213 offset:8704
v_max_f32_e32 v248, v248, v237
v_mfma_f32_16x16x32_bf16 v[104:107], a[16:19], a[180:183], v[104:107]
ds_read_b64_tr_b16 a[10:11], v213 offset:8960
)"
R"(
v_cmp_lt_f32_e32 vcc, s70, v248
v_mfma_f32_16x16x32_bf16 v[100:103], a[20:23], a[176:179], v[100:103]
s_nop 0
s_cbranch_vccnz .Lk_ara3
.Lk_aba3:
ds_read_b64_tr_b16 a[12:13], v214 offset:8704
v_mfma_f32_16x16x32_bf16 v[108:111], a[20:23], a[180:183], v[108:111]
s_waitcnt lgkmcnt(7)
ds_read_b64_tr_b16 a[14:15], v214 offset:8960
v_mfma_f32_16x16x32_bf16 v[112:115], a[24:27], a[176:179], v[112:115]
ds_read_b64_tr_b16 a[16:17], v213 offset:9216
v_exp_f32_e32 v128, v128
v_mfma_f32_16x16x32_bf16 v[120:123], a[24:27], a[180:183], v[120:123]
ds_read_b64_tr_b16 a[18:19], v213 offset:9472
v_add_f32_e32 v242, v242, v128
v_mfma_f32_16x16x32_bf16 v[116:119], a[28:31], a[176:179], v[116:119]
ds_read_b64_tr_b16 a[20:21], v214 offset:9216
v_mfma_f32_16x16x32_bf16 v[124:127], a[28:31], a[180:183], v[124:127]
s_waitcnt lgkmcnt(7)
ds_read_b64_tr_b16 a[22:23], v214 offset:9472
v_exp_f32_e32 v129, v129
v_mfma_f32_16x16x32_bf16 v[64:67], a[0:3], a[184:187], v[64:67]
ds_read_b64_tr_b16 a[24:25], v213 offset:9728
v_add_f32_e32 v242, v242, v129
v_mfma_f32_16x16x32_bf16 v[72:75], a[0:3], a[188:191], v[72:75]
ds_read_b64_tr_b16 a[26:27], v213 offset:9984
v_cvt_pk_bf16_f32 v176, v128, v129
v_mfma_f32_16x16x32_bf16 v[68:71], a[4:7], a[184:187], v[68:71]
ds_read_b64_tr_b16 a[28:29], v214 offset:9728
v_exp_f32_e32 v130, v130
v_mfma_f32_16x16x32_bf16 v[76:79], a[4:7], a[188:191], v[76:79]
s_barrier
ds_read_b64_tr_b16 a[30:31], v214 offset:9984
s_waitcnt lgkmcnt(8)
v_mfma_f32_16x16x32_bf16 v[80:83], a[8:11], a[184:187], v[80:83]
ds_read_b128 a[0:3], v202 offset:16384
v_add_f32_e32 v242, v242, v130
v_mfma_f32_16x16x32_bf16 v[88:91], a[8:11], a[188:191], v[88:91]
v_exp_f32_e32 v131, v131
s_nop 0
v_add_f32_e32 v242, v242, v131
v_mfma_f32_16x16x32_bf16 v[84:87], a[12:15], a[184:187], v[84:87]
ds_read_b128 a[4:7], v202 offset:16896
v_cvt_pk_bf16_f32 v177, v130, v131
v_mfma_f32_16x16x32_bf16 v[92:95], a[12:15], a[188:191], v[92:95]
s_waitcnt lgkmcnt(6)
v_exp_f32_e32 v136, v136
v_mfma_f32_16x16x32_bf16 v[96:99], a[16:19], a[184:187], v[96:99]
ds_read_b128 a[8:11], v202 offset:17408
v_add_f32_e32 v243, v243, v136
v_mfma_f32_16x16x32_bf16 v[104:107], a[16:19], a[188:191], v[104:107]
v_exp_f32_e32 v137, v137
s_nop 0
v_add_f32_e32 v243, v243, v137
v_mfma_f32_16x16x32_bf16 v[100:103], a[20:23], a[184:187], v[100:103]
ds_read_b128 a[12:15], v202 offset:17920
v_cvt_pk_bf16_f32 v180, v136, v137
v_mfma_f32_16x16x32_bf16 v[108:111], a[20:23], a[188:191], v[108:111]
s_waitcnt lgkmcnt(4)
v_exp_f32_e32 v138, v138
v_mfma_f32_16x16x32_bf16 v[112:115], a[24:27], a[184:187], v[112:115]
ds_read_b128 a[16:19], v202 offset:20480
v_add_f32_e32 v243, v243, v138
v_mfma_f32_16x16x32_bf16 v[120:123], a[24:27], a[188:191], v[120:123]
v_exp_f32_e32 v139, v139
s_nop 0
v_add_f32_e32 v243, v243, v139
v_mfma_f32_16x16x32_bf16 v[116:119], a[28:31], a[184:187], v[116:119]
ds_read_b128 a[20:23], v202 offset:20992
v_cvt_pk_bf16_f32 v181, v138, v139
v_mfma_f32_16x16x32_bf16 v[124:127], a[28:31], a[188:191], v[124:127]
s_waitcnt lgkmcnt(4)
v_exp_f32_e32 v132, v132
v_mfma_f32_16x16x32_bf16 v[160:163], a[0:3], a[32:35], v[192:195]
ds_read_b128 a[24:27], v202 offset:21504
v_add_f32_e32 v242, v242, v132
v_mfma_f32_16x16x32_bf16 v[168:171], a[0:3], a[36:39], v[196:199]
v_exp_f32_e32 v133, v133
v_exp_f32_e32 v134, v134
v_mfma_f32_16x16x32_bf16 v[160:163], a[4:7], a[40:43], v[160:163]
ds_read_b128 a[28:31], v202 offset:22016
v_mfma_f32_16x16x32_bf16 v[168:171], a[4:7], a[44:47], v[168:171]
s_waitcnt lgkmcnt(4)
v_add_f32_e32 v242, v242, v133
v_cvt_pk_bf16_f32 v178, v132, v133
v_add_f32_e32 v242, v242, v134
v_mfma_f32_16x16x32_bf16 v[160:163], a[8:11], a[48:51], v[160:163]
ds_read_b128 a[0:3], v202 offset:24576
v_exp_f32_e32 v135, v135
v_mfma_f32_16x16x32_bf16 v[168:171], a[8:11], a[52:55], v[168:171]
v_add_f32_e32 v242, v242, v135
v_mfma_f32_16x16x32_bf16 v[160:163], a[12:15], a[56:59], v[160:163]
ds_read_b128 a[4:7], v202 offset:25088
v_cvt_pk_bf16_f32 v179, v134, v135
v_mfma_f32_16x16x32_bf16 v[168:171], a[12:15], a[60:63], v[168:171]
s_waitcnt lgkmcnt(4)
v_exp_f32_e32 v140, v140
v_exp_f32_e32 v141, v141
v_mfma_f32_16x16x32_bf16 v[164:167], a[16:19], a[32:35], v[192:195]
ds_read_b128 a[8:11], v202 offset:25600
v_add_f32_e32 v243, v243, v140
v_mfma_f32_16x16x32_bf16 v[172:175], a[16:19], a[36:39], v[196:199]
v_add_f32_e32 v243, v243, v141
v_cvt_pk_bf16_f32 v182, v140, v141
v_mfma_f32_16x16x32_bf16 v[164:167], a[20:23], a[40:43], v[164:167]
ds_read_b128 a[12:15], v202 offset:26112
v_exp_f32_e32 v142, v142
v_mfma_f32_16x16x32_bf16 v[172:175], a[20:23], a[44:47], v[172:175]
s_waitcnt lgkmcnt(4)
v_add_f32_e32 v243, v243, v142
v_max3_f32 v236, v160, v161, v162
v_mfma_f32_16x16x32_bf16 v[164:167], a[24:27], a[48:51], v[164:167]
ds_read_b128 a[16:19], v202 offset:28672
v_max3_f32 v237, v168, v169, v170
v_mfma_f32_16x16x32_bf16 v[172:175], a[24:27], a[52:55], v[172:175]
v_exp_f32_e32 v143, v143
v_mfma_f32_16x16x32_bf16 v[164:167], a[28:31], a[56:59], v[164:167]
ds_read_b128 a[20:23], v202 offset:29184
v_add_f32_e32 v243, v243, v143
v_cvt_pk_bf16_f32 v183, v142, v143
v_mfma_f32_16x16x32_bf16 v[172:175], a[28:31], a[60:63], v[172:175]
s_cbranch_vccnz .Lk_arb3
.Lk_abb3:
s_waitcnt lgkmcnt(4)
v_mfma_f32_16x16x32_bf16 v[128:131], a[0:3], a[32:35], v[192:195]
ds_read_b128 a[24:27], v202 offset:29696
v_mfma_f32_16x16x32_bf16 v[136:139], a[0:3], a[36:39], v[196:199]
v_exp_f32_e32 v144, v144
s_nop 0
v_add_f32_e32 v242, v242, v144
v_mfma_f32_16x16x32_bf16 v[128:131], a[4:7], a[40:43], v[128:131]
ds_read_b128 a[28:31], v202 offset:30208
v_exp_f32_e32 v145, v145
v_mfma_f32_16x16x32_bf16 v[136:139], a[4:7], a[44:47], v[136:139]
s_barrier
s_waitcnt lgkmcnt(4)
v_mfma_f32_16x16x32_bf16 v[128:131], a[8:11], a[48:51], v[128:131]
ds_read_b64_tr_b16 a[0:1], v203 offset:16384
v_cvt_pk_bf16_f32 v184, v144, v145
v_mfma_f32_16x16x32_bf16 v[136:139], a[8:11], a[52:55], v[136:139]
ds_read_b64_tr_b16 a[2:3], v203 offset:20480
v_exp_f32_e32 v146, v146
v_mfma_f32_16x16x32_bf16 v[128:131], a[12:15], a[56:59], v[128:131]
ds_read_b64_tr_b16 a[4:5], v204 offset:16384
v_add_f32_e32 v242, v242, v145
v_mfma_f32_16x16x32_bf16 v[136:139], a[12:15], a[60:63], v[136:139]
s_waitcnt lgkmcnt(5)
ds_read_b64_tr_b16 a[6:7], v204 offset:20480
v_add_f32_e32 v242, v242, v146
v_mfma_f32_16x16x32_bf16 v[132:135], a[16:19], a[32:35], v[192:195]
ds_read_b64_tr_b16 a[8:9], v203 offset:16896
v_exp_f32_e32 v147, v147
v_mfma_f32_16x16x32_bf16 v[140:143], a[16:19], a[36:39], v[196:199]
ds_read_b64_tr_b16 a[10:11], v203 offset:20992
v_cvt_pk_bf16_f32 v185, v146, v147
v_mfma_f32_16x16x32_bf16 v[132:135], a[20:23], a[40:43], v[132:135]
ds_read_b64_tr_b16 a[12:13], v204 offset:16896
v_exp_f32_e32 v148, v148
v_mfma_f32_16x16x32_bf16 v[140:143], a[20:23], a[44:47], v[140:143]
s_waitcnt lgkmcnt(7)
ds_read_b64_tr_b16 a[14:15], v204 offset:20992
v_add_f32_e32 v242, v242, v147
v_mfma_f32_16x16x32_bf16 v[132:135], a[24:27], a[48:51], v[132:135]
ds_read_b64_tr_b16 a[16:17], v203 offset:17408
v_exp_f32_e32 v149, v149
v_mfma_f32_16x16x32_bf16 v[140:143], a[24:27], a[52:55], v[140:143]
ds_read_b64_tr_b16 a[18:19], v203 offset:21504
v_cvt_pk_bf16_f32 v186, v148, v149
v_mfma_f32_16x16x32_bf16 v[132:135], a[28:31], a[56:59], v[132:135]
ds_read_b64_tr_b16 a[20:21], v204 offset:17408
v_add_f32_e32 v242, v242, v148
v_mfma_f32_16x16x32_bf16 v[140:143], a[28:31], a[60:63], v[140:143]
s_waitcnt lgkmcnt(7)
ds_read_b64_tr_b16 a[22:23], v204 offset:21504
v_exp_f32_e32 v150, v150
v_mfma_f32_16x16x32_bf16 v[0:3], a[0:3], v[176:179], v[0:3]
ds_read_b64_tr_b16 a[24:25], v203 offset:17920
v_add_f32_e32 v242, v242, v149
v_mfma_f32_16x16x32_bf16 v[8:11], a[0:3], v[180:183], v[8:11]
ds_read_b64_tr_b16 a[26:27], v203 offset:22016
v_exp_f32_e32 v151, v151
v_mfma_f32_16x16x32_bf16 v[4:7], a[4:7], v[176:179], v[4:7]
ds_read_b64_tr_b16 a[28:29], v204 offset:17920
v_cvt_pk_bf16_f32 v187, v150, v151
v_mfma_f32_16x16x32_bf16 v[12:15], a[4:7], v[180:183], v[12:15]
s_waitcnt lgkmcnt(7)
ds_read_b64_tr_b16 a[30:31], v204 offset:22016
v_exp_f32_e32 v152, v152
v_mfma_f32_16x16x32_bf16 v[16:19], a[8:11], v[176:179], v[16:19]
ds_read_b64_tr_b16 a[0:1], v203 offset:24576
v_add_f32_e32 v243, v243, v152
v_mfma_f32_16x16x32_bf16 v[24:27], a[8:11], v[180:183], v[24:27]
ds_read_b64_tr_b16 a[2:3], v203 offset:28672
v_exp_f32_e32 v153, v153
v_mfma_f32_16x16x32_bf16 v[20:23], a[12:15], v[176:179], v[20:23]
ds_read_b64_tr_b16 a[4:5], v204 offset:24576
v_cvt_pk_bf16_f32 v188, v152, v153
v_mfma_f32_16x16x32_bf16 v[28:31], a[12:15], v[180:183], v[28:31]
s_waitcnt lgkmcnt(7)
ds_read_b64_tr_b16 a[6:7], v204 offset:28672
)"
R"(
v_exp_f32_e32 v154, v154
v_mfma_f32_16x16x32_bf16 v[32:35], a[16:19], v[176:179], v[32:35]
ds_read_b64_tr_b16 a[8:9], v203 offset:25088
v_add_f32_e32 v243, v243, v153
v_mfma_f32_16x16x32_bf16 v[40:43], a[16:19], v[180:183], v[40:43]
ds_read_b64_tr_b16 a[10:11], v203 offset:29184
v_exp_f32_e32 v155, v155
v_mfma_f32_16x16x32_bf16 v[36:39], a[20:23], v[176:179], v[36:39]
ds_read_b64_tr_b16 a[12:13], v204 offset:25088
v_cvt_pk_bf16_f32 v189, v154, v155
v_mfma_f32_16x16x32_bf16 v[44:47], a[20:23], v[180:183], v[44:47]
s_waitcnt lgkmcnt(7)
ds_read_b64_tr_b16 a[14:15], v204 offset:29184
v_exp_f32_e32 v156, v156
v_mfma_f32_16x16x32_bf16 v[48:51], a[24:27], v[176:179], v[48:51]
ds_read_b64_tr_b16 a[16:17], v203 offset:25600
v_add_f32_e32 v243, v243, v154
v_mfma_f32_16x16x32_bf16 v[56:59], a[24:27], v[180:183], v[56:59]
ds_read_b64_tr_b16 a[18:19], v203 offset:29696
v_exp_f32_e32 v157, v157
v_mfma_f32_16x16x32_bf16 v[52:55], a[28:31], v[176:179], v[52:55]
ds_read_b64_tr_b16 a[20:21], v204 offset:25600
v_cvt_pk_bf16_f32 v190, v156, v157
v_add_f32_e32 v243, v243, v155
v_mfma_f32_16x16x32_bf16 v[60:63], a[28:31], v[180:183], v[60:63]
s_waitcnt lgkmcnt(7)
ds_read_b64_tr_b16 a[22:23], v204 offset:29696
v_exp_f32_e32 v158, v158
v_mfma_f32_16x16x32_bf16 v[0:3], a[0:3], v[184:187], v[0:3]
ds_read_b64_tr_b16 a[24:25], v203 offset:26112
v_exp_f32_e32 v159, v159
s_nop 0
v_cvt_pk_bf16_f32 v191, v158, v159
s_nop 1
v_mfma_f32_16x16x32_bf16 v[8:11], a[0:3], v[188:191], v[8:11]
ds_read_b64_tr_b16 a[26:27], v203 offset:30208
v_add_f32_e32 v243, v243, v156
v_add_f32_e32 v243, v243, v157
v_mfma_f32_16x16x32_bf16 v[4:7], a[4:7], v[184:187], v[4:7]
ds_read_b64_tr_b16 a[28:29], v204 offset:26112
v_add_f32_e32 v242, v242, v150
v_mfma_f32_16x16x32_bf16 v[12:15], a[4:7], v[188:191], v[12:15]
s_barrier
ds_read_b64_tr_b16 a[30:31], v204 offset:30208
s_waitcnt lgkmcnt(8)
v_mfma_f32_16x16x32_bf16 v[16:19], a[8:11], v[184:187], v[16:19]
ds_read_b64_tr_b16 a[0:1], v209 offset:49152
v_add_f32_e32 v243, v243, v158
v_add_f32_e32 v242, v242, v151
v_mfma_f32_16x16x32_bf16 v[24:27], a[8:11], v[188:191], v[24:27]
ds_read_b64_tr_b16 a[2:3], v209 offset:49408
v_add_f32_e32 v243, v243, v159
v_mfma_f32_16x16x32_bf16 v[20:23], a[12:15], v[184:187], v[20:23]
ds_read_b64_tr_b16 a[4:5], v210 offset:49152
v_max3_f32 v236, v236, v163, v164
v_max3_f32 v237, v237, v171, v172
v_mfma_f32_16x16x32_bf16 v[28:31], a[12:15], v[188:191], v[28:31]
s_waitcnt lgkmcnt(7)
ds_read_b64_tr_b16 a[6:7], v210 offset:49408
v_max3_f32 v236, v236, v165, v166
v_mfma_f32_16x16x32_bf16 v[32:35], a[16:19], v[184:187], v[32:35]
ds_read_b64_tr_b16 a[8:9], v209 offset:49664
v_max3_f32 v237, v237, v173, v174
v_mfma_f32_16x16x32_bf16 v[40:43], a[16:19], v[188:191], v[40:43]
ds_read_b64_tr_b16 a[10:11], v209 offset:49920
v_max3_f32 v236, v236, v167, v128
v_max3_f32 v237, v237, v175, v136
v_mfma_f32_16x16x32_bf16 v[36:39], a[20:23], v[184:187], v[36:39]
ds_read_b64_tr_b16 a[12:13], v210 offset:49664
v_max3_f32 v236, v236, v129, v130
v_mfma_f32_16x16x32_bf16 v[44:47], a[20:23], v[188:191], v[44:47]
s_waitcnt lgkmcnt(7)
ds_read_b64_tr_b16 a[14:15], v210 offset:49920
v_max3_f32 v237, v237, v137, v138
v_max3_f32 v236, v236, v131, v132
v_mfma_f32_16x16x32_bf16 v[48:51], a[24:27], v[184:187], v[48:51]
ds_read_b64_tr_b16 a[16:17], v209 offset:50176
v_max3_f32 v237, v237, v139, v140
v_mfma_f32_16x16x32_bf16 v[56:59], a[24:27], v[188:191], v[56:59]
ds_read_b64_tr_b16 a[18:19], v209 offset:50432
v_max3_f32 v236, v236, v133, v134
v_mfma_f32_16x16x32_bf16 v[52:55], a[28:31], v[184:187], v[52:55]
ds_read_b64_tr_b16 a[20:21], v210 offset:50176
v_max3_f32 v237, v237, v141, v142
v_max3_f32 v248, v236, v135, v143
v_mfma_f32_16x16x32_bf16 v[60:63], a[28:31], v[188:191], v[60:63]
s_waitcnt lgkmcnt(7)
ds_read_b64_tr_b16 a[22:23], v210 offset:50432
v_max_f32_e32 v248, v248, v237
v_mfma_f32_16x16x32_bf16 v[64:67], a[0:3], a[192:195], v[64:67]
ds_read_b64_tr_b16 a[24:25], v209 offset:50688
v_cmp_lt_f32_e32 vcc, s70, v248
v_mfma_f32_16x16x32_bf16 v[72:75], a[0:3], a[196:199], v[72:75]
s_nop 0
s_cbranch_vccnz .Lk_ara4
.Lk_aba4:
ds_read_b64_tr_b16 a[26:27], v209 offset:50944
v_mfma_f32_16x16x32_bf16 v[68:71], a[4:7], a[192:195], v[68:71]
ds_read_b64_tr_b16 a[28:29], v210 offset:50688
v_mfma_f32_16x16x32_bf16 v[76:79], a[4:7], a[196:199], v[76:79]
s_waitcnt lgkmcnt(7)
ds_read_b64_tr_b16 a[30:31], v210 offset:50944
v_exp_f32_e32 v160, v160
v_mfma_f32_16x16x32_bf16 v[80:83], a[8:11], a[192:195], v[80:83]
ds_read_b64_tr_b16 a[0:1], v209 offset:57344
v_add_f32_e32 v242, v242, v160
v_mfma_f32_16x16x32_bf16 v[88:91], a[8:11], a[196:199], v[88:91]
ds_read_b64_tr_b16 a[2:3], v209 offset:57600
v_exp_f32_e32 v161, v161
v_mfma_f32_16x16x32_bf16 v[84:87], a[12:15], a[192:195], v[84:87]
ds_read_b64_tr_b16 a[4:5], v210 offset:57344
v_add_f32_e32 v242, v242, v161
v_mfma_f32_16x16x32_bf16 v[92:95], a[12:15], a[196:199], v[92:95]
s_waitcnt lgkmcnt(7)
ds_read_b64_tr_b16 a[6:7], v210 offset:57600
v_cvt_pk_bf16_f32 v176, v160, v161
v_mfma_f32_16x16x32_bf16 v[96:99], a[16:19], a[192:195], v[96:99]
ds_read_b64_tr_b16 a[8:9], v209 offset:57856
v_exp_f32_e32 v162, v162
v_mfma_f32_16x16x32_bf16 v[104:107], a[16:19], a[196:199], v[104:107]
ds_read_b64_tr_b16 a[10:11], v209 offset:58112
v_add_f32_e32 v242, v242, v162
v_mfma_f32_16x16x32_bf16 v[100:103], a[20:23], a[192:195], v[100:103]
ds_read_b64_tr_b16 a[12:13], v210 offset:57856
v_exp_f32_e32 v163, v163
v_mfma_f32_16x16x32_bf16 v[108:111], a[20:23], a[196:199], v[108:111]
s_waitcnt lgkmcnt(7)
ds_read_b64_tr_b16 a[14:15], v210 offset:58112
v_add_f32_e32 v242, v242, v163
v_mfma_f32_16x16x32_bf16 v[112:115], a[24:27], a[192:195], v[112:115]
ds_read_b64_tr_b16 a[16:17], v209 offset:58368
v_cvt_pk_bf16_f32 v177, v162, v163
v_mfma_f32_16x16x32_bf16 v[120:123], a[24:27], a[196:199], v[120:123]
ds_read_b64_tr_b16 a[18:19], v209 offset:58624
v_exp_f32_e32 v168, v168
v_mfma_f32_16x16x32_bf16 v[116:119], a[28:31], a[192:195], v[116:119]
ds_read_b64_tr_b16 a[20:21], v210 offset:58368
v_add_f32_e32 v243, v243, v168
v_mfma_f32_16x16x32_bf16 v[124:127], a[28:31], a[196:199], v[124:127]
s_waitcnt lgkmcnt(7)
ds_read_b64_tr_b16 a[22:23], v210 offset:58624
v_exp_f32_e32 v169, v169
v_mfma_f32_16x16x32_bf16 v[64:67], a[0:3], a[200:203], v[64:67]
ds_read_b64_tr_b16 a[24:25], v209 offset:58880
v_add_f32_e32 v243, v243, v169
v_mfma_f32_16x16x32_bf16 v[72:75], a[0:3], a[204:207], v[72:75]
ds_read_b64_tr_b16 a[26:27], v209 offset:59136
v_cvt_pk_bf16_f32 v180, v168, v169
v_mfma_f32_16x16x32_bf16 v[68:71], a[4:7], a[200:203], v[68:71]
ds_read_b64_tr_b16 a[28:29], v210 offset:58880
v_exp_f32_e32 v170, v170
v_mfma_f32_16x16x32_bf16 v[76:79], a[4:7], a[204:207], v[76:79]
s_barrier
ds_read_b64_tr_b16 a[30:31], v210 offset:59136
s_waitcnt lgkmcnt(8)
v_mfma_f32_16x16x32_bf16 v[80:83], a[8:11], a[200:203], v[80:83]
ds_read_b128 a[0:3], v200
v_add_f32_e32 v243, v243, v170
v_mfma_f32_16x16x32_bf16 v[88:91], a[8:11], a[204:207], v[88:91]
v_exp_f32_e32 v171, v171
s_nop 0
v_add_f32_e32 v243, v243, v171
v_mfma_f32_16x16x32_bf16 v[84:87], a[12:15], a[200:203], v[84:87]
ds_read_b128 a[4:7], v200 offset:512
v_cvt_pk_bf16_f32 v181, v170, v171
v_mfma_f32_16x16x32_bf16 v[92:95], a[12:15], a[204:207], v[92:95]
s_waitcnt lgkmcnt(6)
v_exp_f32_e32 v164, v164
s_nop 0
v_add_f32_e32 v242, v242, v164
v_mfma_f32_16x16x32_bf16 v[96:99], a[16:19], a[200:203], v[96:99]
ds_read_b128 a[8:11], v200 offset:1024
v_exp_f32_e32 v165, v165
v_mfma_f32_16x16x32_bf16 v[104:107], a[16:19], a[204:207], v[104:107]
v_add_f32_e32 v242, v242, v165
v_cvt_pk_bf16_f32 v178, v164, v165
v_mfma_f32_16x16x32_bf16 v[100:103], a[20:23], a[200:203], v[100:103]
ds_read_b128 a[12:15], v200 offset:1536
v_mfma_f32_16x16x32_bf16 v[108:111], a[20:23], a[204:207], v[108:111]
s_waitcnt lgkmcnt(4)
v_exp_f32_e32 v166, v166
v_exp_f32_e32 v167, v167
v_mfma_f32_16x16x32_bf16 v[112:115], a[24:27], a[200:203], v[112:115]
ds_read_b128 a[16:19], v200 offset:4096
v_add_f32_e32 v242, v242, v166
v_mfma_f32_16x16x32_bf16 v[120:123], a[24:27], a[204:207], v[120:123]
v_add_f32_e32 v242, v242, v167
v_cvt_pk_bf16_f32 v179, v166, v167
v_mfma_f32_16x16x32_bf16 v[116:119], a[28:31], a[200:203], v[116:119]
ds_read_b128 a[20:23], v200 offset:4608
v_exp_f32_e32 v172, v172
v_mfma_f32_16x16x32_bf16 v[124:127], a[28:31], a[204:207], v[124:127]
s_waitcnt lgkmcnt(4)
v_add_f32_e32 v243, v243, v172
v_mfma_f32_16x16x32_bf16 v[144:147], a[0:3], a[32:35], v[192:195]
ds_read_b128 a[24:27], v200 offset:5120
v_exp_f32_e32 v173, v173
v_mfma_f32_16x16x32_bf16 v[152:155], a[0:3], a[36:39], v[196:199]
v_add_f32_e32 v243, v243, v173
v_cvt_pk_bf16_f32 v182, v172, v173
)"
R"(
v_mfma_f32_16x16x32_bf16 v[144:147], a[4:7], a[40:43], v[144:147]
ds_read_b128 a[28:31], v200 offset:5632
v_exp_f32_e32 v174, v174
v_mfma_f32_16x16x32_bf16 v[152:155], a[4:7], a[44:47], v[152:155]
s_waitcnt lgkmcnt(4)
v_add_f32_e32 v243, v243, v174
v_exp_f32_e32 v175, v175
v_mfma_f32_16x16x32_bf16 v[144:147], a[8:11], a[48:51], v[144:147]
ds_read_b128 a[0:3], v200 offset:8192
v_add_f32_e32 v243, v243, v175
v_mfma_f32_16x16x32_bf16 v[152:155], a[8:11], a[52:55], v[152:155]
v_cvt_pk_bf16_f32 v183, v174, v175
v_exp_f32_e32 v128, v128
v_mfma_f32_16x16x32_bf16 v[144:147], a[12:15], a[56:59], v[144:147]
ds_read_b128 a[4:7], v200 offset:8704
v_add_f32_e32 v242, v242, v128
v_mfma_f32_16x16x32_bf16 v[152:155], a[12:15], a[60:63], v[152:155]
s_cbranch_vccnz .Lk_arb4
.Lk_abb4:
s_waitcnt lgkmcnt(4)
v_mfma_f32_16x16x32_bf16 v[148:151], a[16:19], a[32:35], v[192:195]
ds_read_b128 a[8:11], v200 offset:9216
v_mfma_f32_16x16x32_bf16 v[156:159], a[16:19], a[36:39], v[196:199]
v_exp_f32_e32 v129, v129
v_exp_f32_e32 v130, v130
v_mfma_f32_16x16x32_bf16 v[148:151], a[20:23], a[40:43], v[148:151]
ds_read_b128 a[12:15], v200 offset:9728
v_mfma_f32_16x16x32_bf16 v[156:159], a[20:23], a[44:47], v[156:159]
s_waitcnt lgkmcnt(4)
v_cvt_pk_bf16_f32 v184, v128, v129
v_exp_f32_e32 v131, v131
v_mfma_f32_16x16x32_bf16 v[148:151], a[24:27], a[48:51], v[148:151]
ds_read_b128 a[16:19], v200 offset:12288
v_cvt_pk_bf16_f32 v185, v130, v131
v_mfma_f32_16x16x32_bf16 v[156:159], a[24:27], a[52:55], v[156:159]
v_exp_f32_e32 v132, v132
v_add_f32_e32 v242, v242, v129
v_mfma_f32_16x16x32_bf16 v[148:151], a[28:31], a[56:59], v[148:151]
ds_read_b128 a[20:23], v200 offset:12800
v_exp_f32_e32 v133, v133
v_mfma_f32_16x16x32_bf16 v[156:159], a[28:31], a[60:63], v[156:159]
s_waitcnt lgkmcnt(4)
v_cvt_pk_bf16_f32 v186, v132, v133
v_mfma_f32_16x16x32_bf16 v[160:163], a[0:3], a[32:35], v[192:195]
ds_read_b128 a[24:27], v200 offset:13312
v_exp_f32_e32 v134, v134
v_mfma_f32_16x16x32_bf16 v[168:171], a[0:3], a[36:39], v[196:199]
v_exp_f32_e32 v135, v135
v_mfma_f32_16x16x32_bf16 v[160:163], a[4:7], a[40:43], v[160:163]
ds_read_b128 a[28:31], v200 offset:13824
v_cvt_pk_bf16_f32 v187, v134, v135
v_add_f32_e32 v242, v242, v130
v_mfma_f32_16x16x32_bf16 v[168:171], a[4:7], a[44:47], v[168:171]
s_barrier
s_waitcnt lgkmcnt(4)
v_mfma_f32_16x16x32_bf16 v[160:163], a[8:11], a[48:51], v[160:163]
ds_read_b64_tr_b16 a[0:1], v203 offset:32768
v_exp_f32_e32 v136, v136
v_mfma_f32_16x16x32_bf16 v[168:171], a[8:11], a[52:55], v[168:171]
ds_read_b64_tr_b16 a[2:3], v203 offset:36864
v_add_f32_e32 v243, v243, v136
v_mfma_f32_16x16x32_bf16 v[160:163], a[12:15], a[56:59], v[160:163]
ds_read_b64_tr_b16 a[4:5], v204 offset:32768
v_exp_f32_e32 v137, v137
v_mfma_f32_16x16x32_bf16 v[168:171], a[12:15], a[60:63], v[168:171]
s_waitcnt lgkmcnt(5)
ds_read_b64_tr_b16 a[6:7], v204 offset:36864
v_cvt_pk_bf16_f32 v188, v136, v137
v_mfma_f32_16x16x32_bf16 v[164:167], a[16:19], a[32:35], v[192:195]
ds_read_b64_tr_b16 a[8:9], v203 offset:33280
v_exp_f32_e32 v138, v138
v_mfma_f32_16x16x32_bf16 v[172:175], a[16:19], a[36:39], v[196:199]
ds_read_b64_tr_b16 a[10:11], v203 offset:37376
v_mfma_f32_16x16x32_bf16 v[164:167], a[20:23], a[40:43], v[164:167]
ds_read_b64_tr_b16 a[12:13], v204 offset:33280
v_exp_f32_e32 v139, v139
v_mfma_f32_16x16x32_bf16 v[172:175], a[20:23], a[44:47], v[172:175]
s_waitcnt lgkmcnt(7)
ds_read_b64_tr_b16 a[14:15], v204 offset:37376
v_cvt_pk_bf16_f32 v189, v138, v139
v_mfma_f32_16x16x32_bf16 v[164:167], a[24:27], a[48:51], v[164:167]
ds_read_b64_tr_b16 a[16:17], v203 offset:33792
v_exp_f32_e32 v140, v140
v_mfma_f32_16x16x32_bf16 v[172:175], a[24:27], a[52:55], v[172:175]
ds_read_b64_tr_b16 a[18:19], v203 offset:37888
v_exp_f32_e32 v141, v141
v_mfma_f32_16x16x32_bf16 v[164:167], a[28:31], a[56:59], v[164:167]
ds_read_b64_tr_b16 a[20:21], v204 offset:33792
v_mfma_f32_16x16x32_bf16 v[172:175], a[28:31], a[60:63], v[172:175]
s_waitcnt lgkmcnt(7)
ds_read_b64_tr_b16 a[22:23], v204 offset:37888
v_cvt_pk_bf16_f32 v190, v140, v141
v_add_f32_e32 v243, v243, v137
v_mfma_f32_16x16x32_bf16 v[0:3], a[0:3], v[176:179], v[0:3]
ds_read_b64_tr_b16 a[24:25], v203 offset:34304
v_exp_f32_e32 v142, v142
v_mfma_f32_16x16x32_bf16 v[8:11], a[0:3], v[180:183], v[8:11]
ds_read_b64_tr_b16 a[26:27], v203 offset:38400
v_add_f32_e32 v243, v243, v138
v_mfma_f32_16x16x32_bf16 v[4:7], a[4:7], v[176:179], v[4:7]
ds_read_b64_tr_b16 a[28:29], v204 offset:34304
v_exp_f32_e32 v143, v143
v_mfma_f32_16x16x32_bf16 v[12:15], a[4:7], v[180:183], v[12:15]
s_waitcnt lgkmcnt(7)
ds_read_b64_tr_b16 a[30:31], v204 offset:38400
v_mfma_f32_16x16x32_bf16 v[16:19], a[8:11], v[176:179], v[16:19]
ds_read_b64_tr_b16 a[0:1], v203 offset:40960
v_cvt_pk_bf16_f32 v191, v142, v143
v_add_f32_e32 v242, v242, v131
v_mfma_f32_16x16x32_bf16 v[24:27], a[8:11], v[180:183], v[24:27]
ds_read_b64_tr_b16 a[2:3], v203 offset:45056
v_add_f32_e32 v243, v243, v139
v_mfma_f32_16x16x32_bf16 v[20:23], a[12:15], v[176:179], v[20:23]
ds_read_b64_tr_b16 a[4:5], v204 offset:40960
v_add_f32_e32 v242, v242, v132
v_add_f32_e32 v243, v243, v140
v_mfma_f32_16x16x32_bf16 v[28:31], a[12:15], v[180:183], v[28:31]
s_waitcnt lgkmcnt(7)
ds_read_b64_tr_b16 a[6:7], v204 offset:45056
v_add_f32_e32 v242, v242, v133
v_mfma_f32_16x16x32_bf16 v[32:35], a[16:19], v[176:179], v[32:35]
ds_read_b64_tr_b16 a[8:9], v203 offset:41472
v_add_f32_e32 v243, v243, v141
v_mfma_f32_16x16x32_bf16 v[40:43], a[16:19], v[180:183], v[40:43]
ds_read_b64_tr_b16 a[10:11], v203 offset:45568
v_add_f32_e32 v242, v242, v134
v_add_f32_e32 v243, v243, v142
v_mfma_f32_16x16x32_bf16 v[36:39], a[20:23], v[176:179], v[36:39]
ds_read_b64_tr_b16 a[12:13], v204 offset:41472
v_add_f32_e32 v242, v242, v135
v_mfma_f32_16x16x32_bf16 v[44:47], a[20:23], v[180:183], v[44:47]
s_waitcnt lgkmcnt(7)
ds_read_b64_tr_b16 a[14:15], v204 offset:45568
v_add_f32_e32 v243, v243, v143
v_max3_f32 v236, v144, v145, v146
v_mfma_f32_16x16x32_bf16 v[48:51], a[24:27], v[176:179], v[48:51]
ds_read_b64_tr_b16 a[16:17], v203 offset:41984
v_max3_f32 v237, v152, v153, v154
v_mfma_f32_16x16x32_bf16 v[56:59], a[24:27], v[180:183], v[56:59]
ds_read_b64_tr_b16 a[18:19], v203 offset:46080
v_max3_f32 v236, v236, v147, v148
v_max3_f32 v237, v237, v155, v156
v_mfma_f32_16x16x32_bf16 v[52:55], a[28:31], v[176:179], v[52:55]
ds_read_b64_tr_b16 a[20:21], v204 offset:41984
v_max3_f32 v236, v236, v149, v150
v_mfma_f32_16x16x32_bf16 v[60:63], a[28:31], v[180:183], v[60:63]
s_waitcnt lgkmcnt(7)
ds_read_b64_tr_b16 a[22:23], v204 offset:46080
v_max3_f32 v237, v237, v157, v158
v_mfma_f32_16x16x32_bf16 v[0:3], a[0:3], v[184:187], v[0:3]
ds_read_b64_tr_b16 a[24:25], v203 offset:42496
v_max3_f32 v236, v236, v151, v160
v_max3_f32 v237, v237, v159, v168
v_mfma_f32_16x16x32_bf16 v[8:11], a[0:3], v[188:191], v[8:11]
ds_read_b64_tr_b16 a[26:27], v203 offset:46592
v_max3_f32 v236, v236, v161, v162
v_mfma_f32_16x16x32_bf16 v[4:7], a[4:7], v[184:187], v[4:7]
ds_read_b64_tr_b16 a[28:29], v204 offset:42496
v_max3_f32 v237, v237, v169, v170
v_max3_f32 v236, v236, v163, v164
v_mfma_f32_16x16x32_bf16 v[12:15], a[4:7], v[188:191], v[12:15]
s_barrier
ds_read_b64_tr_b16 a[30:31], v204 offset:46592
s_waitcnt lgkmcnt(8)
v_mfma_f32_16x16x32_bf16 v[16:19], a[8:11], v[184:187], v[16:19]
ds_read_b128 a[0:3], v201 offset:16384
v_max3_f32 v237, v237, v171, v172
v_mfma_f32_16x16x32_bf16 v[24:27], a[8:11], v[188:191], v[24:27]
v_max3_f32 v236, v236, v165, v166
v_max3_f32 v237, v237, v173, v174
v_mfma_f32_16x16x32_bf16 v[20:23], a[12:15], v[184:187], v[20:23]
ds_read_b128 a[4:7], v201 offset:16896
v_max3_f32 v248, v236, v167, v175
v_max_f32_e32 v248, v248, v237
v_mfma_f32_16x16x32_bf16 v[28:31], a[12:15], v[188:191], v[28:31]
s_waitcnt lgkmcnt(6)
v_cmp_lt_f32_e32 vcc, s70, v248
v_mfma_f32_16x16x32_bf16 v[32:35], a[16:19], v[184:187], v[32:35]
s_nop 0
s_cbranch_vccnz .Lk_ara5
.Lk_aba5:
ds_read_b128 a[8:11], v201 offset:17408
v_mfma_f32_16x16x32_bf16 v[40:43], a[16:19], v[188:191], v[40:43]
v_exp_f32_e32 v144, v144
v_mfma_f32_16x16x32_bf16 v[36:39], a[20:23], v[184:187], v[36:39]
ds_read_b128 a[12:15], v201 offset:17920
v_add_f32_e32 v242, v242, v144
v_mfma_f32_16x16x32_bf16 v[44:47], a[20:23], v[188:191], v[44:47]
s_waitcnt lgkmcnt(4)
v_exp_f32_e32 v145, v145
s_nop 0
v_add_f32_e32 v242, v242, v145
v_mfma_f32_16x16x32_bf16 v[48:51], a[24:27], v[184:187], v[48:51]
ds_read_b128 a[16:19], v201 offset:20480
v_cvt_pk_bf16_f32 v176, v144, v145
v_mfma_f32_16x16x32_bf16 v[56:59], a[24:27], v[188:191], v[56:59]
v_exp_f32_e32 v146, v146
s_nop 0
v_add_f32_e32 v242, v242, v146
v_mfma_f32_16x16x32_bf16 v[52:55], a[28:31], v[184:187], v[52:55]
)"
R"(
ds_read_b128 a[20:23], v201 offset:20992
v_exp_f32_e32 v147, v147
v_mfma_f32_16x16x32_bf16 v[60:63], a[28:31], v[188:191], v[60:63]
s_waitcnt lgkmcnt(4)
v_add_f32_e32 v242, v242, v147
v_cvt_pk_bf16_f32 v177, v146, v147
v_mfma_f32_16x16x32_bf16 v[128:131], a[0:3], a[32:35], v[192:195]
ds_read_b128 a[24:27], v201 offset:21504
v_mfma_f32_16x16x32_bf16 v[136:139], a[0:3], a[36:39], v[196:199]
v_exp_f32_e32 v152, v152
v_exp_f32_e32 v153, v153
v_mfma_f32_16x16x32_bf16 v[128:131], a[4:7], a[40:43], v[128:131]
ds_read_b128 a[28:31], v201 offset:22016
v_add_f32_e32 v243, v243, v152
v_mfma_f32_16x16x32_bf16 v[136:139], a[4:7], a[44:47], v[136:139]
s_waitcnt lgkmcnt(4)
v_add_f32_e32 v243, v243, v153
v_cvt_pk_bf16_f32 v180, v152, v153
v_mfma_f32_16x16x32_bf16 v[128:131], a[8:11], a[48:51], v[128:131]
ds_read_b128 a[0:3], v201 offset:24576
v_exp_f32_e32 v154, v154
s_nop 0
v_add_f32_e32 v243, v243, v154
v_mfma_f32_16x16x32_bf16 v[136:139], a[8:11], a[52:55], v[136:139]
v_exp_f32_e32 v155, v155
s_nop 0
v_add_f32_e32 v243, v243, v155
v_mfma_f32_16x16x32_bf16 v[128:131], a[12:15], a[56:59], v[128:131]
ds_read_b128 a[4:7], v201 offset:25088
v_cvt_pk_bf16_f32 v181, v154, v155
v_mfma_f32_16x16x32_bf16 v[136:139], a[12:15], a[60:63], v[136:139]
s_waitcnt lgkmcnt(4)
v_exp_f32_e32 v148, v148
v_exp_f32_e32 v149, v149
v_mfma_f32_16x16x32_bf16 v[132:135], a[16:19], a[32:35], v[192:195]
ds_read_b128 a[8:11], v201 offset:25600
v_add_f32_e32 v242, v242, v148
v_add_f32_e32 v242, v242, v149
v_mfma_f32_16x16x32_bf16 v[140:143], a[16:19], a[36:39], v[196:199]
v_cvt_pk_bf16_f32 v178, v148, v149
v_exp_f32_e32 v150, v150
v_mfma_f32_16x16x32_bf16 v[132:135], a[20:23], a[40:43], v[132:135]
ds_read_b128 a[12:15], v201 offset:26112
v_add_f32_e32 v242, v242, v150
v_exp_f32_e32 v151, v151
v_mfma_f32_16x16x32_bf16 v[140:143], a[20:23], a[44:47], v[140:143]
s_waitcnt lgkmcnt(4)
v_add_f32_e32 v242, v242, v151
v_cvt_pk_bf16_f32 v179, v150, v151
v_mfma_f32_16x16x32_bf16 v[132:135], a[24:27], a[48:51], v[132:135]
ds_read_b128 a[16:19], v201 offset:28672
v_exp_f32_e32 v156, v156
v_mfma_f32_16x16x32_bf16 v[140:143], a[24:27], a[52:55], v[140:143]
v_add_f32_e32 v243, v243, v156
v_exp_f32_e32 v157, v157
v_mfma_f32_16x16x32_bf16 v[132:135], a[28:31], a[56:59], v[132:135]
ds_read_b128 a[20:23], v201 offset:29184
v_add_f32_e32 v243, v243, v157
v_cvt_pk_bf16_f32 v182, v156, v157
v_max3_f32 v236, v128, v129, v130
v_mfma_f32_16x16x32_bf16 v[140:143], a[28:31], a[60:63], v[140:143]
s_waitcnt lgkmcnt(4)
v_exp_f32_e32 v158, v158
v_max3_f32 v237, v136, v137, v138
v_mfma_f32_16x16x32_bf16 v[144:147], a[0:3], a[32:35], v[192:195]
ds_read_b128 a[24:27], v201 offset:29696
v_add_f32_e32 v243, v243, v158
v_mfma_f32_16x16x32_bf16 v[152:155], a[0:3], a[36:39], v[196:199]
s_cbranch_vccnz .Lk_arb5
.Lk_abb5:
v_exp_f32_e32 v159, v159
v_mfma_f32_16x16x32_bf16 v[144:147], a[4:7], a[40:43], v[144:147]
ds_read_b128 a[28:31], v201 offset:30208
v_add_f32_e32 v243, v243, v159
v_cvt_pk_bf16_f32 v183, v158, v159
v_mfma_f32_16x16x32_bf16 v[152:155], a[4:7], a[44:47], v[152:155]
s_barrier
v_max3_f32 v236, v236, v131, v132
s_waitcnt lgkmcnt(4)
v_mfma_f32_16x16x32_bf16 v[144:147], a[8:11], a[48:51], v[144:147]
ds_read_b64_tr_b16 a[0:1], v211 offset:49152
v_exp_f32_e32 v160, v160
v_mfma_f32_16x16x32_bf16 v[152:155], a[8:11], a[52:55], v[152:155]
ds_read_b64_tr_b16 a[2:3], v211 offset:49408
v_exp_f32_e32 v161, v161
v_mfma_f32_16x16x32_bf16 v[144:147], a[12:15], a[56:59], v[144:147]
ds_read_b64_tr_b16 a[4:5], v212 offset:49152
v_cvt_pk_bf16_f32 v184, v160, v161
v_exp_f32_e32 v162, v162
v_mfma_f32_16x16x32_bf16 v[152:155], a[12:15], a[60:63], v[152:155]
s_waitcnt lgkmcnt(5)
ds_read_b64_tr_b16 a[6:7], v212 offset:49408
v_exp_f32_e32 v163, v163
v_mfma_f32_16x16x32_bf16 v[148:151], a[16:19], a[32:35], v[192:195]
ds_read_b64_tr_b16 a[8:9], v211 offset:49664
v_cvt_pk_bf16_f32 v185, v162, v163
v_exp_f32_e32 v164, v164
v_mfma_f32_16x16x32_bf16 v[156:159], a[16:19], a[36:39], v[196:199]
ds_read_b64_tr_b16 a[10:11], v211 offset:49920
v_exp_f32_e32 v165, v165
v_mfma_f32_16x16x32_bf16 v[148:151], a[20:23], a[40:43], v[148:151]
ds_read_b64_tr_b16 a[12:13], v212 offset:49664
v_cvt_pk_bf16_f32 v186, v164, v165
v_exp_f32_e32 v166, v166
v_mfma_f32_16x16x32_bf16 v[156:159], a[20:23], a[44:47], v[156:159]
s_waitcnt lgkmcnt(7)
ds_read_b64_tr_b16 a[14:15], v212 offset:49920
v_exp_f32_e32 v167, v167
v_mfma_f32_16x16x32_bf16 v[148:151], a[24:27], a[48:51], v[148:151]
ds_read_b64_tr_b16 a[16:17], v211 offset:50176
v_cvt_pk_bf16_f32 v187, v166, v167
v_add_f32_e32 v242, v242, v160
v_mfma_f32_16x16x32_bf16 v[156:159], a[24:27], a[52:55], v[156:159]
ds_read_b64_tr_b16 a[18:19], v211 offset:50432
v_exp_f32_e32 v168, v168
v_mfma_f32_16x16x32_bf16 v[148:151], a[28:31], a[56:59], v[148:151]
ds_read_b64_tr_b16 a[20:21], v212 offset:50176
v_exp_f32_e32 v169, v169
v_mfma_f32_16x16x32_bf16 v[156:159], a[28:31], a[60:63], v[156:159]
s_waitcnt lgkmcnt(7)
ds_read_b64_tr_b16 a[22:23], v212 offset:50432
v_cvt_pk_bf16_f32 v188, v168, v169
v_exp_f32_e32 v170, v170
v_mfma_f32_16x16x32_bf16 v[64:67], a[0:3], a[208:211], v[64:67]
ds_read_b64_tr_b16 a[24:25], v211 offset:50688
v_exp_f32_e32 v171, v171
v_mfma_f32_16x16x32_bf16 v[72:75], a[0:3], a[212:215], v[72:75]
ds_read_b64_tr_b16 a[26:27], v211 offset:50944
v_cvt_pk_bf16_f32 v189, v170, v171
v_exp_f32_e32 v172, v172
v_mfma_f32_16x16x32_bf16 v[68:71], a[4:7], a[208:211], v[68:71]
ds_read_b64_tr_b16 a[28:29], v212 offset:50688
v_exp_f32_e32 v173, v173
v_mfma_f32_16x16x32_bf16 v[76:79], a[4:7], a[212:215], v[76:79]
s_waitcnt lgkmcnt(7)
ds_read_b64_tr_b16 a[30:31], v212 offset:50944
v_cvt_pk_bf16_f32 v190, v172, v173
v_exp_f32_e32 v174, v174
v_mfma_f32_16x16x32_bf16 v[80:83], a[8:11], a[208:211], v[80:83]
ds_read_b64_tr_b16 a[0:1], v211 offset:57344
v_exp_f32_e32 v175, v175
v_mfma_f32_16x16x32_bf16 v[88:91], a[8:11], a[212:215], v[88:91]
ds_read_b64_tr_b16 a[2:3], v211 offset:57600
v_cvt_pk_bf16_f32 v191, v174, v175
v_add_f32_e32 v243, v243, v168
v_mfma_f32_16x16x32_bf16 v[84:87], a[12:15], a[208:211], v[84:87]
ds_read_b64_tr_b16 a[4:5], v212 offset:57344
v_add_f32_e32 v242, v242, v161
v_add_f32_e32 v243, v243, v169
v_add_f32_e32 v242, v242, v162
v_mfma_f32_16x16x32_bf16 v[92:95], a[12:15], a[212:215], v[92:95]
s_waitcnt lgkmcnt(7)
ds_read_b64_tr_b16 a[6:7], v212 offset:57600
v_add_f32_e32 v243, v243, v170
v_add_f32_e32 v242, v242, v163
v_mfma_f32_16x16x32_bf16 v[96:99], a[16:19], a[208:211], v[96:99]
ds_read_b64_tr_b16 a[8:9], v211 offset:57856
v_add_f32_e32 v243, v243, v171
v_add_f32_e32 v242, v242, v164
v_add_f32_e32 v243, v243, v172
v_mfma_f32_16x16x32_bf16 v[104:107], a[16:19], a[212:215], v[104:107]
ds_read_b64_tr_b16 a[10:11], v211 offset:58112
v_add_f32_e32 v242, v242, v165
v_add_f32_e32 v243, v243, v173
v_mfma_f32_16x16x32_bf16 v[100:103], a[20:23], a[208:211], v[100:103]
ds_read_b64_tr_b16 a[12:13], v212 offset:57856
v_add_f32_e32 v242, v242, v166
v_add_f32_e32 v243, v243, v174
v_add_f32_e32 v242, v242, v167
v_mfma_f32_16x16x32_bf16 v[108:111], a[20:23], a[212:215], v[108:111]
s_waitcnt lgkmcnt(7)
ds_read_b64_tr_b16 a[14:15], v212 offset:58112
v_add_f32_e32 v243, v243, v175
v_max3_f32 v237, v237, v139, v140
v_mfma_f32_16x16x32_bf16 v[112:115], a[24:27], a[208:211], v[112:115]
ds_read_b64_tr_b16 a[16:17], v211 offset:58368
v_max3_f32 v236, v236, v133, v134
v_max3_f32 v237, v237, v141, v142
v_max3_f32 v236, v236, v135, v144
v_mfma_f32_16x16x32_bf16 v[120:123], a[24:27], a[212:215], v[120:123]
ds_read_b64_tr_b16 a[18:19], v211 offset:58624
v_max3_f32 v237, v237, v143, v152
v_max3_f32 v236, v236, v145, v146
v_mfma_f32_16x16x32_bf16 v[116:119], a[28:31], a[208:211], v[116:119]
ds_read_b64_tr_b16 a[20:21], v212 offset:58368
v_max3_f32 v237, v237, v153, v154
v_max3_f32 v236, v236, v147, v148
v_max3_f32 v237, v237, v155, v156
v_mfma_f32_16x16x32_bf16 v[124:127], a[28:31], a[212:215], v[124:127]
s_waitcnt lgkmcnt(7)
ds_read_b64_tr_b16 a[22:23], v212 offset:58624
v_max3_f32 v236, v236, v149, v150
v_max3_f32 v237, v237, v157, v158
v_mfma_f32_16x16x32_bf16 v[64:67], a[0:3], a[216:219], v[64:67]
ds_read_b64_tr_b16 a[24:25], v211 offset:58880
v_max3_f32 v248, v236, v151, v159
v_max_f32_e32 v248, v248, v237
v_cmp_lt_f32_e32 vcc, s70, v248
v_mfma_f32_16x16x32_bf16 v[72:75], a[0:3], a[220:223], v[72:75]
s_nop 0
)"
R"(
s_cbranch_vccnz .Lk_ara6
.Lk_aba6:
ds_read_b64_tr_b16 a[26:27], v211 offset:59136
v_mfma_f32_16x16x32_bf16 v[68:71], a[4:7], a[216:219], v[68:71]
ds_read_b64_tr_b16 a[28:29], v212 offset:58880
v_exp_f32_e32 v128, v128
v_mfma_f32_16x16x32_bf16 v[76:79], a[4:7], a[220:223], v[76:79]
s_barrier
ds_read_b64_tr_b16 a[30:31], v212 offset:59136
s_waitcnt lgkmcnt(8)
v_mfma_f32_16x16x32_bf16 v[80:83], a[8:11], a[216:219], v[80:83]
ds_read_b64_tr_b16 a[0:1], v205
v_add_f32_e32 v242, v242, v128
v_exp_f32_e32 v129, v129
v_mfma_f32_16x16x32_bf16 v[88:91], a[8:11], a[220:223], v[88:91]
ds_read_b64_tr_b16 a[2:3], v205 offset:4096
v_add_f32_e32 v242, v242, v129
v_mul_f32_e32 v64, s69, v64
v_mfma_f32_16x16x32_bf16 v[84:87], a[12:15], a[216:219], v[84:87]
ds_read_b64_tr_b16 a[4:5], v206
v_exp_f32_e32 v130, v130
v_mfma_f32_16x16x32_bf16 v[92:95], a[12:15], a[220:223], v[92:95]
s_waitcnt lgkmcnt(7)
ds_read_b64_tr_b16 a[6:7], v206 offset:4096
v_add_f32_e32 v242, v242, v130
v_exp_f32_e32 v131, v131
v_mfma_f32_16x16x32_bf16 v[96:99], a[16:19], a[216:219], v[96:99]
ds_read_b64_tr_b16 a[8:9], v205 offset:512
v_add_f32_e32 v242, v242, v131
v_exp_f32_e32 v136, v136
v_mfma_f32_16x16x32_bf16 v[104:107], a[16:19], a[220:223], v[104:107]
ds_read_b64_tr_b16 a[10:11], v205 offset:4608
v_add_f32_e32 v243, v243, v136
v_mul_f32_e32 v65, s69, v65
v_mfma_f32_16x16x32_bf16 v[100:103], a[20:23], a[216:219], v[100:103]
ds_read_b64_tr_b16 a[12:13], v206 offset:512
v_exp_f32_e32 v137, v137
v_mfma_f32_16x16x32_bf16 v[108:111], a[20:23], a[220:223], v[108:111]
s_waitcnt lgkmcnt(7)
ds_read_b64_tr_b16 a[14:15], v206 offset:4608
v_add_f32_e32 v243, v243, v137
v_exp_f32_e32 v138, v138
v_mfma_f32_16x16x32_bf16 v[112:115], a[24:27], a[216:219], v[112:115]
ds_read_b64_tr_b16 a[16:17], v205 offset:1024
v_add_f32_e32 v243, v243, v138
v_exp_f32_e32 v139, v139
v_mfma_f32_16x16x32_bf16 v[120:123], a[24:27], a[220:223], v[120:123]
ds_read_b64_tr_b16 a[18:19], v205 offset:5120
v_add_f32_e32 v243, v243, v139
v_mul_f32_e32 v66, s69, v66
v_mfma_f32_16x16x32_bf16 v[116:119], a[28:31], a[216:219], v[116:119]
ds_read_b64_tr_b16 a[20:21], v206 offset:1024
v_exp_f32_e32 v132, v132
v_mfma_f32_16x16x32_bf16 v[124:127], a[28:31], a[220:223], v[124:127]
s_waitcnt lgkmcnt(7)
ds_read_b64_tr_b16 a[22:23], v206 offset:5120
v_add_f32_e32 v242, v242, v132
v_exp_f32_e32 v133, v133
v_mfma_f32_16x16x32_bf16 v[0:3], a[0:3], v[176:179], v[0:3]
ds_read_b64_tr_b16 a[24:25], v205 offset:1536
buffer_load_dword v234, v232, s[56:59], 0 offen offset:128 sc1
v_mfma_f32_16x16x32_bf16 v[8:11], a[0:3], v[180:183], v[8:11]
ds_read_b64_tr_b16 a[26:27], v205 offset:5632
v_add_f32_e32 v242, v242, v133
v_exp_f32_e32 v134, v134
v_mfma_f32_16x16x32_bf16 v[4:7], a[4:7], v[176:179], v[4:7]
ds_read_b64_tr_b16 a[28:29], v206 offset:1536
v_add_f32_e32 v242, v242, v134
v_mul_f32_e32 v67, s69, v67
v_mfma_f32_16x16x32_bf16 v[12:15], a[4:7], v[180:183], v[12:15]
s_waitcnt lgkmcnt(7)
ds_read_b64_tr_b16 a[30:31], v206 offset:5632
v_exp_f32_e32 v135, v135
v_mfma_f32_16x16x32_bf16 v[16:19], a[8:11], v[176:179], v[16:19]
ds_read_b64_tr_b16 a[0:1], v205 offset:8192
v_add_f32_e32 v242, v242, v135
v_exp_f32_e32 v140, v140
v_mfma_f32_16x16x32_bf16 v[24:27], a[8:11], v[180:183], v[24:27]
ds_read_b64_tr_b16 a[2:3], v205 offset:12288
v_add_f32_e32 v243, v243, v140
v_exp_f32_e32 v141, v141
v_mfma_f32_16x16x32_bf16 v[20:23], a[12:15], v[176:179], v[20:23]
ds_read_b64_tr_b16 a[4:5], v206 offset:8192
v_add_f32_e32 v243, v243, v141
v_mul_f32_e32 v68, s69, v68
v_mfma_f32_16x16x32_bf16 v[28:31], a[12:15], v[180:183], v[28:31]
s_waitcnt lgkmcnt(7)
ds_read_b64_tr_b16 a[6:7], v206 offset:12288
v_exp_f32_e32 v142, v142
v_mfma_f32_16x16x32_bf16 v[32:35], a[16:19], v[176:179], v[32:35]
ds_read_b64_tr_b16 a[8:9], v205 offset:8704
v_add_f32_e32 v243, v243, v142
v_exp_f32_e32 v143, v143
v_mfma_f32_16x16x32_bf16 v[40:43], a[16:19], v[180:183], v[40:43]
ds_read_b64_tr_b16 a[10:11], v205 offset:12800
v_add_f32_e32 v243, v243, v143
v_mul_f32_e32 v69, s69, v69
v_mfma_f32_16x16x32_bf16 v[36:39], a[20:23], v[176:179], v[36:39]
ds_read_b64_tr_b16 a[12:13], v206 offset:8704
v_exp_f32_e32 v144, v144
v_mfma_f32_16x16x32_bf16 v[44:47], a[20:23], v[180:183], v[44:47]
s_waitcnt lgkmcnt(7)
ds_read_b64_tr_b16 a[14:15], v206 offset:12800
v_exp_f32_e32 v145, v145
v_mul_f32_e32 v70, s69, v70
v_mfma_f32_16x16x32_bf16 v[48:51], a[24:27], v[176:179], v[48:51]
ds_read_b64_tr_b16 a[16:17], v205 offset:9216
v_exp_f32_e32 v146, v146
v_mfma_f32_16x16x32_bf16 v[56:59], a[24:27], v[180:183], v[56:59]
ds_read_b64_tr_b16 a[18:19], v205 offset:13312
v_exp_f32_e32 v147, v147
v_mul_f32_e32 v71, s69, v71
v_mfma_f32_16x16x32_bf16 v[52:55], a[28:31], v[176:179], v[52:55]
ds_read_b64_tr_b16 a[20:21], v206 offset:9216
v_cvt_pk_bf16_f32 v176, v128, v129
v_cvt_pk_bf16_f32 v177, v130, v131
v_mfma_f32_16x16x32_bf16 v[60:63], a[28:31], v[180:183], v[60:63]
s_waitcnt lgkmcnt(7)
ds_read_b64_tr_b16 a[22:23], v206 offset:13312
v_cvt_pk_bf16_f32 v180, v136, v137
v_cvt_pk_bf16_f32 v181, v138, v139
v_cvt_pk_bf16_f32 v178, v132, v133
v_mfma_f32_16x16x32_bf16 v[0:3], a[0:3], v[184:187], v[0:3]
ds_read_b64_tr_b16 a[24:25], v205 offset:9728
v_cvt_pk_bf16_f32 v179, v134, v135
v_cvt_pk_bf16_f32 v182, v140, v141
v_mfma_f32_16x16x32_bf16 v[8:11], a[0:3], v[188:191], v[8:11]
ds_read_b64_tr_b16 a[26:27], v205 offset:13824
v_cvt_pk_bf16_f32 v183, v142, v143
v_exp_f32_e32 v148, v148
v_mfma_f32_16x16x32_bf16 v[4:7], a[4:7], v[184:187], v[4:7]
ds_read_b64_tr_b16 a[28:29], v206 offset:9728
v_exp_f32_e32 v149, v149
v_mfma_f32_16x16x32_bf16 v[12:15], a[4:7], v[188:191], v[12:15]
s_barrier
ds_read_b64_tr_b16 a[30:31], v206 offset:13824
v_cvt_pk_bf16_f32 v64, v64, v65
s_waitcnt lgkmcnt(8)
v_mfma_f32_16x16x32_bf16 v[16:19], a[8:11], v[184:187], v[16:19]
ds_read_b128 a[0:3], v201 offset:32768
v_exp_f32_e32 v150, v150
v_mfma_f32_16x16x32_bf16 v[24:27], a[8:11], v[188:191], v[24:27]
v_exp_f32_e32 v151, v151
v_exp_f32_e32 v152, v152
v_mfma_f32_16x16x32_bf16 v[20:23], a[12:15], v[184:187], v[20:23]
ds_read_b128 a[4:7], v201 offset:33280
v_exp_f32_e32 v153, v153
v_mfma_f32_16x16x32_bf16 v[28:31], a[12:15], v[188:191], v[28:31]
s_waitcnt lgkmcnt(6)
v_exp_f32_e32 v154, v154
v_exp_f32_e32 v155, v155
v_mfma_f32_16x16x32_bf16 v[32:35], a[16:19], v[184:187], v[32:35]
ds_read_b128 a[8:11], v201 offset:33792
v_exp_f32_e32 v156, v156
v_mfma_f32_16x16x32_bf16 v[40:43], a[16:19], v[188:191], v[40:43]
v_exp_f32_e32 v157, v157
v_exp_f32_e32 v158, v158
v_mfma_f32_16x16x32_bf16 v[36:39], a[20:23], v[184:187], v[36:39]
ds_read_b128 a[12:15], v201 offset:34304
v_exp_f32_e32 v159, v159
v_mfma_f32_16x16x32_bf16 v[44:47], a[20:23], v[188:191], v[44:47]
s_waitcnt lgkmcnt(4)
v_cvt_pk_bf16_f32 v65, v66, v67
v_cvt_pk_bf16_f32 v66, v68, v69
v_cvt_pk_bf16_f32 v67, v70, v71
s_nop 0
v_permlane16_swap_b32_e32 v64, v66
v_mfma_f32_16x16x32_bf16 v[48:51], a[24:27], v[184:187], v[48:51]
ds_read_b128 a[16:19], v201 offset:36864
v_permlane16_swap_b32_e32 v65, v67
v_accvgpr_write_b32 a64, v64
v_mfma_f32_16x16x32_bf16 v[56:59], a[24:27], v[188:191], v[56:59]
v_accvgpr_write_b32 a65, v65
v_accvgpr_write_b32 a66, v66
v_accvgpr_write_b32 a67, v67
v_add_f32_e32 v242, v242, v144
v_mfma_f32_16x16x32_bf16 v[52:55], a[28:31], v[184:187], v[52:55]
ds_read_b128 a[20:23], v201 offset:37376
v_cvt_pk_bf16_f32 v184, v144, v145
v_cvt_pk_bf16_f32 v185, v146, v147
v_mfma_f32_16x16x32_bf16 v[60:63], a[28:31], v[188:191], v[60:63]
s_cbranch_vccnz .Lk_arb6
.Lk_abb6:
v_cvt_pk_bf16_f32 v186, v148, v149
s_waitcnt lgkmcnt(4)
v_mfma_f32_16x16x32_bf16 v[160:163], a[0:3], a[32:35], v[192:195]
ds_read_b128 a[24:27], v201 offset:37888
v_cvt_pk_bf16_f32 v187, v150, v151
v_cvt_pk_bf16_f32 v188, v152, v153
v_mfma_f32_16x16x32_bf16 v[168:171], a[0:3], a[36:39], v[196:199]
v_cvt_pk_bf16_f32 v189, v154, v155
v_cvt_pk_bf16_f32 v190, v156, v157
v_cvt_pk_bf16_f32 v191, v158, v159
v_mfma_f32_16x16x32_bf16 v[160:163], a[4:7], a[40:43], v[160:163]
ds_read_b128 a[28:31], v201 offset:38400
v_add_f32_e32 v242, v242, v145
v_add_f32_e32 v242, v242, v146
v_add_f32_e32 v242, v242, v147
v_mfma_f32_16x16x32_bf16 v[168:171], a[4:7], a[44:47], v[168:171]
s_waitcnt lgkmcnt(4)
)"
R"(
v_mul_f32_e32 v72, s69, v72
v_mul_f32_e32 v73, s69, v73
v_mul_f32_e32 v74, s69, v74
v_mfma_f32_16x16x32_bf16 v[160:163], a[8:11], a[48:51], v[160:163]
ds_read_b128 a[0:3], v201 offset:40960
v_mul_f32_e32 v75, s69, v75
v_mul_f32_e32 v76, s69, v76
v_mul_f32_e32 v77, s69, v77
v_mfma_f32_16x16x32_bf16 v[168:171], a[8:11], a[52:55], v[168:171]
v_mul_f32_e32 v78, s69, v78
v_mul_f32_e32 v79, s69, v79
v_cvt_pk_bf16_f32 v72, v72, v73
v_mfma_f32_16x16x32_bf16 v[160:163], a[12:15], a[56:59], v[160:163]
ds_read_b128 a[4:7], v201 offset:41472
v_cvt_pk_bf16_f32 v73, v74, v75
v_cvt_pk_bf16_f32 v74, v76, v77
v_cvt_pk_bf16_f32 v75, v78, v79
v_mfma_f32_16x16x32_bf16 v[168:171], a[12:15], a[60:63], v[168:171]
s_waitcnt lgkmcnt(4)
v_permlane16_swap_b32_e32 v72, v74
v_permlane16_swap_b32_e32 v73, v75
v_accvgpr_write_b32 a68, v72
v_mfma_f32_16x16x32_bf16 v[164:167], a[16:19], a[32:35], v[192:195]
ds_read_b128 a[8:11], v201 offset:41984
v_accvgpr_write_b32 a69, v73
v_accvgpr_write_b32 a70, v74
v_accvgpr_write_b32 a71, v75
v_mfma_f32_16x16x32_bf16 v[172:175], a[16:19], a[36:39], v[196:199]
v_add_f32_e32 v243, v243, v152
v_add_f32_e32 v243, v243, v153
v_add_f32_e32 v243, v243, v154
v_mfma_f32_16x16x32_bf16 v[164:167], a[20:23], a[40:43], v[164:167]
ds_read_b128 a[12:15], v201 offset:42496
v_add_f32_e32 v243, v243, v155
v_mul_f32_e32 v80, s69, v80
v_mul_f32_e32 v81, s69, v81
v_mfma_f32_16x16x32_bf16 v[172:175], a[20:23], a[44:47], v[172:175]
s_waitcnt lgkmcnt(4)
v_mul_f32_e32 v82, s69, v82
v_mul_f32_e32 v83, s69, v83
v_mul_f32_e32 v84, s69, v84
v_mfma_f32_16x16x32_bf16 v[164:167], a[24:27], a[48:51], v[164:167]
ds_read_b128 a[16:19], v201 offset:45056
v_mul_f32_e32 v85, s69, v85
v_mul_f32_e32 v86, s69, v86
v_mul_f32_e32 v87, s69, v87
v_mfma_f32_16x16x32_bf16 v[172:175], a[24:27], a[52:55], v[172:175]
v_cvt_pk_bf16_f32 v80, v80, v81
v_cvt_pk_bf16_f32 v81, v82, v83
v_cvt_pk_bf16_f32 v82, v84, v85
v_mfma_f32_16x16x32_bf16 v[164:167], a[28:31], a[56:59], v[164:167]
ds_read_b128 a[20:23], v201 offset:45568
v_cvt_pk_bf16_f32 v83, v86, v87
v_permlane16_swap_b32_e32 v80, v82
s_nop 0
v_permlane16_swap_b32_e32 v81, v83
v_mfma_f32_16x16x32_bf16 v[172:175], a[28:31], a[60:63], v[172:175]
s_waitcnt lgkmcnt(4)
v_accvgpr_write_b32 a72, v80
v_accvgpr_write_b32 a73, v81
v_accvgpr_write_b32 a74, v82
v_mfma_f32_16x16x32_bf16 v[128:131], a[0:3], a[32:35], v[192:195]
ds_read_b128 a[24:27], v201 offset:46080
v_accvgpr_write_b32 a75, v83
v_mul_f32_e32 v88, s69, v88
v_mul_f32_e32 v89, s69, v89
v_mfma_f32_16x16x32_bf16 v[136:139], a[0:3], a[36:39], v[196:199]
v_mul_f32_e32 v90, s69, v90
v_mul_f32_e32 v91, s69, v91
v_mul_f32_e32 v92, s69, v92
v_mfma_f32_16x16x32_bf16 v[128:131], a[4:7], a[40:43], v[128:131]
ds_read_b128 a[28:31], v201 offset:46592
v_mul_f32_e32 v93, s69, v93
v_mul_f32_e32 v94, s69, v94
v_mul_f32_e32 v95, s69, v95
v_mfma_f32_16x16x32_bf16 v[136:139], a[4:7], a[44:47], v[136:139]
s_barrier
v_cvt_pk_bf16_f32 v88, v88, v89
s_waitcnt lgkmcnt(4)
v_mfma_f32_16x16x32_bf16 v[128:131], a[8:11], a[48:51], v[128:131]
ds_read_b64_tr_b16 a[0:1], v207
v_cvt_pk_bf16_f32 v89, v90, v91
v_cvt_pk_bf16_f32 v90, v92, v93
v_cvt_pk_bf16_f32 v91, v94, v95
v_mfma_f32_16x16x32_bf16 v[136:139], a[8:11], a[52:55], v[136:139]
ds_read_b64_tr_b16 a[2:3], v207 offset:4096
v_permlane16_swap_b32_e32 v88, v90
v_permlane16_swap_b32_e32 v89, v91
v_mfma_f32_16x16x32_bf16 v[128:131], a[12:15], a[56:59], v[128:131]
ds_read_b64_tr_b16 a[4:5], v208
v_accvgpr_write_b32 a76, v88
v_accvgpr_write_b32 a77, v89
v_accvgpr_write_b32 a78, v90
v_mfma_f32_16x16x32_bf16 v[136:139], a[12:15], a[60:63], v[136:139]
s_waitcnt lgkmcnt(5)
ds_read_b64_tr_b16 a[6:7], v208 offset:4096
v_accvgpr_write_b32 a79, v91
v_mul_f32_e32 v96, s69, v96
v_mfma_f32_16x16x32_bf16 v[132:135], a[16:19], a[32:35], v[192:195]
ds_read_b64_tr_b16 a[8:9], v207 offset:512
v_mul_f32_e32 v97, s69, v97
v_mul_f32_e32 v98, s69, v98
v_mul_f32_e32 v99, s69, v99
v_mfma_f32_16x16x32_bf16 v[140:143], a[16:19], a[36:39], v[196:199]
ds_read_b64_tr_b16 a[10:11], v207 offset:4608
v_mul_f32_e32 v100, s69, v100
v_mul_f32_e32 v101, s69, v101
v_mfma_f32_16x16x32_bf16 v[132:135], a[20:23], a[40:43], v[132:135]
ds_read_b64_tr_b16 a[12:13], v208 offset:512
v_mul_f32_e32 v102, s69, v102
v_mul_f32_e32 v103, s69, v103
v_mfma_f32_16x16x32_bf16 v[140:143], a[20:23], a[44:47], v[140:143]
s_waitcnt lgkmcnt(7)
ds_read_b64_tr_b16 a[14:15], v208 offset:4608
v_cvt_pk_bf16_f32 v96, v96, v97
v_cvt_pk_bf16_f32 v97, v98, v99
v_cvt_pk_bf16_f32 v98, v100, v101
v_mfma_f32_16x16x32_bf16 v[132:135], a[24:27], a[48:51], v[132:135]
ds_read_b64_tr_b16 a[16:17], v207 offset:1024
v_cvt_pk_bf16_f32 v99, v102, v103
v_permlane16_swap_b32_e32 v96, v98
v_mfma_f32_16x16x32_bf16 v[140:143], a[24:27], a[52:55], v[140:143]
ds_read_b64_tr_b16 a[18:19], v207 offset:5120
v_permlane16_swap_b32_e32 v97, v99
v_accvgpr_write_b32 a80, v96
v_accvgpr_write_b32 a81, v97
v_mfma_f32_16x16x32_bf16 v[132:135], a[28:31], a[56:59], v[132:135]
ds_read_b64_tr_b16 a[20:21], v208 offset:1024
v_accvgpr_write_b32 a82, v98
v_accvgpr_write_b32 a83, v99
v_mfma_f32_16x16x32_bf16 v[140:143], a[28:31], a[60:63], v[140:143]
s_waitcnt lgkmcnt(7)
ds_read_b64_tr_b16 a[22:23], v208 offset:5120
v_mul_f32_e32 v104, s69, v104
v_mul_f32_e32 v105, s69, v105
v_mul_f32_e32 v106, s69, v106
v_mfma_f32_16x16x32_bf16 v[0:3], a[0:3], v[176:179], v[0:3]
ds_read_b64_tr_b16 a[24:25], v207 offset:1536
v_mul_f32_e32 v107, s69, v107
v_mul_f32_e32 v108, s69, v108
v_mfma_f32_16x16x32_bf16 v[8:11], a[0:3], v[180:183], v[8:11]
ds_read_b64_tr_b16 a[26:27], v207 offset:5632
v_mul_f32_e32 v109, s69, v109
v_mul_f32_e32 v110, s69, v110
v_mul_f32_e32 v111, s69, v111
v_mfma_f32_16x16x32_bf16 v[4:7], a[4:7], v[176:179], v[4:7]
ds_read_b64_tr_b16 a[28:29], v208 offset:1536
v_cvt_pk_bf16_f32 v104, v104, v105
v_cvt_pk_bf16_f32 v105, v106, v107
v_mfma_f32_16x16x32_bf16 v[12:15], a[4:7], v[180:183], v[12:15]
s_waitcnt lgkmcnt(7)
ds_read_b64_tr_b16 a[30:31], v208 offset:5632
v_cvt_pk_bf16_f32 v106, v108, v109
v_cvt_pk_bf16_f32 v107, v110, v111
s_nop 0
v_permlane16_swap_b32_e32 v104, v106
v_mfma_f32_16x16x32_bf16 v[16:19], a[8:11], v[176:179], v[16:19]
ds_read_b64_tr_b16 a[0:1], v207 offset:8192
v_permlane16_swap_b32_e32 v105, v107
v_accvgpr_write_b32 a84, v104
v_mfma_f32_16x16x32_bf16 v[24:27], a[8:11], v[180:183], v[24:27]
ds_read_b64_tr_b16 a[2:3], v207 offset:12288
v_accvgpr_write_b32 a85, v105
v_accvgpr_write_b32 a86, v106
v_accvgpr_write_b32 a87, v107
v_mfma_f32_16x16x32_bf16 v[20:23], a[12:15], v[176:179], v[20:23]
ds_read_b64_tr_b16 a[4:5], v208 offset:8192
v_mul_f32_e32 v112, s69, v112
v_mul_f32_e32 v113, s69, v113
v_mfma_f32_16x16x32_bf16 v[28:31], a[12:15], v[180:183], v[28:31]
s_waitcnt lgkmcnt(7)
ds_read_b64_tr_b16 a[6:7], v208 offset:12288
v_mul_f32_e32 v114, s69, v114
v_mul_f32_e32 v115, s69, v115
v_mul_f32_e32 v116, s69, v116
v_mfma_f32_16x16x32_bf16 v[32:35], a[16:19], v[176:179], v[32:35]
ds_read_b64_tr_b16 a[8:9], v207 offset:8704
v_mul_f32_e32 v117, s69, v117
v_mul_f32_e32 v118, s69, v118
v_mfma_f32_16x16x32_bf16 v[40:43], a[16:19], v[180:183], v[40:43]
ds_read_b64_tr_b16 a[10:11], v207 offset:12800
v_mul_f32_e32 v119, s69, v119
v_cvt_pk_bf16_f32 v112, v112, v113
v_cvt_pk_bf16_f32 v113, v114, v115
v_mfma_f32_16x16x32_bf16 v[36:39], a[20:23], v[176:179], v[36:39]
ds_read_b64_tr_b16 a[12:13], v208 offset:8704
v_cvt_pk_bf16_f32 v114, v116, v117
v_cvt_pk_bf16_f32 v115, v118, v119
v_mfma_f32_16x16x32_bf16 v[44:47], a[20:23], v[180:183], v[44:47]
s_waitcnt lgkmcnt(7)
ds_read_b64_tr_b16 a[14:15], v208 offset:12800
v_permlane16_swap_b32_e32 v112, v114
v_permlane16_swap_b32_e32 v113, v115
v_accvgpr_write_b32 a88, v112
v_mfma_f32_16x16x32_bf16 v[48:51], a[24:27], v[176:179], v[48:51]
ds_read_b64_tr_b16 a[16:17], v207 offset:9216
v_accvgpr_write_b32 a89, v113
v_accvgpr_write_b32 a90, v114
v_mfma_f32_16x16x32_bf16 v[56:59], a[24:27], v[180:183], v[56:59]
ds_read_b64_tr_b16 a[18:19], v207 offset:13312
)"
R"(
v_accvgpr_write_b32 a91, v115
v_mul_f32_e32 v120, s69, v120
v_mul_f32_e32 v121, s69, v121
v_mfma_f32_16x16x32_bf16 v[52:55], a[28:31], v[176:179], v[52:55]
ds_read_b64_tr_b16 a[20:21], v208 offset:9216
v_mul_f32_e32 v122, s69, v122
v_mul_f32_e32 v123, s69, v123
v_mfma_f32_16x16x32_bf16 v[60:63], a[28:31], v[180:183], v[60:63]
s_waitcnt lgkmcnt(7)
ds_read_b64_tr_b16 a[22:23], v208 offset:13312
v_mul_f32_e32 v124, s69, v124
v_mul_f32_e32 v125, s69, v125
v_mul_f32_e32 v126, s69, v126
v_mfma_f32_16x16x32_bf16 v[0:3], a[0:3], v[184:187], v[0:3]
ds_read_b64_tr_b16 a[24:25], v207 offset:9728
v_mul_f32_e32 v127, s69, v127
v_cvt_pk_bf16_f32 v120, v120, v121
v_mfma_f32_16x16x32_bf16 v[8:11], a[0:3], v[188:191], v[8:11]
ds_read_b64_tr_b16 a[26:27], v207 offset:13824
v_cvt_pk_bf16_f32 v121, v122, v123
v_cvt_pk_bf16_f32 v122, v124, v125
v_cvt_pk_bf16_f32 v123, v126, v127
v_mfma_f32_16x16x32_bf16 v[4:7], a[4:7], v[184:187], v[4:7]
ds_read_b64_tr_b16 a[28:29], v208 offset:9728
v_permlane16_swap_b32_e32 v120, v122
v_permlane16_swap_b32_e32 v121, v123
v_accvgpr_write_b32 a92, v120
v_mfma_f32_16x16x32_bf16 v[12:15], a[4:7], v[188:191], v[12:15]
s_barrier
ds_read_b64_tr_b16 a[30:31], v208 offset:13824
s_waitcnt lgkmcnt(8)
v_mfma_f32_16x16x32_bf16 v[16:19], a[8:11], v[184:187], v[16:19]
ds_read_b128 a[0:3], v202 offset:16384
v_accvgpr_write_b32 a93, v121
v_accvgpr_write_b32 a94, v122
v_accvgpr_write_b32 a95, v123
v_mfma_f32_16x16x32_bf16 v[24:27], a[8:11], v[188:191], v[24:27]
v_add_f32_e32 v242, v242, v148
v_add_f32_e32 v242, v242, v149
v_add_f32_e32 v242, v242, v150
v_add_f32_e32 v242, v242, v151
v_mfma_f32_16x16x32_bf16 v[20:23], a[12:15], v[184:187], v[20:23]
ds_read_b128 a[4:7], v202 offset:16896
v_add_f32_e32 v243, v243, v156
v_add_f32_e32 v243, v243, v157
v_add_f32_e32 v243, v243, v158
v_mfma_f32_16x16x32_bf16 v[28:31], a[12:15], v[188:191], v[28:31]
s_waitcnt lgkmcnt(6)
v_add_f32_e32 v243, v243, v159
v_max3_f32 v236, v160, v161, v162
v_max3_f32 v237, v168, v169, v170
v_mfma_f32_16x16x32_bf16 v[32:35], a[16:19], v[184:187], v[32:35]
ds_read_b128 a[8:11], v202 offset:17408
v_max3_f32 v236, v236, v163, v164
v_max3_f32 v237, v237, v171, v172
v_max3_f32 v236, v236, v165, v166
v_mfma_f32_16x16x32_bf16 v[40:43], a[16:19], v[188:191], v[40:43]
v_max3_f32 v237, v237, v173, v174
v_max3_f32 v236, v236, v167, v128
v_max3_f32 v237, v237, v175, v136
v_max3_f32 v236, v236, v129, v130
v_mfma_f32_16x16x32_bf16 v[36:39], a[20:23], v[184:187], v[36:39]
ds_read_b128 a[12:15], v202 offset:17920
v_max3_f32 v237, v237, v137, v138
v_max3_f32 v236, v236, v131, v132
v_mfma_f32_16x16x32_bf16 v[44:47], a[20:23], v[188:191], v[44:47]
s_waitcnt lgkmcnt(4)
v_max3_f32 v237, v237, v139, v140
v_max3_f32 v236, v236, v133, v134
v_max3_f32 v237, v237, v141, v142
v_max3_f32 v248, v236, v135, v143
v_mfma_f32_16x16x32_bf16 v[48:51], a[24:27], v[184:187], v[48:51]
ds_read_b128 a[16:19], v202 offset:20480
v_max_f32_e32 v248, v248, v237
v_cmp_lt_f32_e32 vcc, s70, v248
v_mfma_f32_16x16x32_bf16 v[56:59], a[24:27], v[188:191], v[56:59]
s_nop 0
s_cbranch_vccnz .Lk_ara7
.Lk_aba7:
v_exp_f32_e32 v160, v160
v_mfma_f32_16x16x32_bf16 v[52:55], a[28:31], v[184:187], v[52:55]
ds_read_b128 a[20:23], v202 offset:20992
v_add_f32_e32 v242, v242, v160
v_exp_f32_e32 v161, v161
v_mfma_f32_16x16x32_bf16 v[60:63], a[28:31], v[188:191], v[60:63]
s_waitcnt lgkmcnt(4)
v_add_f32_e32 v242, v242, v161
v_cvt_pk_bf16_f32 v176, v160, v161
v_exp_f32_e32 v162, v162
v_mfma_f32_16x16x32_bf16 v[144:147], a[0:3], a[64:67], 0
ds_read_b128 a[24:27], v202 offset:21504
v_add_f32_e32 v242, v242, v162
v_mfma_f32_16x16x32_bf16 v[152:155], a[0:3], a[68:71], 0
v_exp_f32_e32 v163, v163
v_exp_f32_e32 v168, v168
v_add_f32_e32 v242, v242, v163
v_mfma_f32_16x16x32_bf16 v[144:147], a[4:7], a[72:75], v[144:147]
ds_read_b128 a[28:31], v202 offset:22016
v_cvt_pk_bf16_f32 v177, v162, v163
v_add_f32_e32 v243, v243, v168
v_mfma_f32_16x16x32_bf16 v[152:155], a[4:7], a[76:79], v[152:155]
s_waitcnt lgkmcnt(4)
v_exp_f32_e32 v169, v169
v_exp_f32_e32 v170, v170
v_mfma_f32_16x16x32_bf16 v[144:147], a[8:11], a[80:83], v[144:147]
ds_read_b128 a[0:3], v202 offset:24576
v_add_f32_e32 v243, v243, v169
v_cvt_pk_bf16_f32 v180, v168, v169
v_add_f32_e32 v243, v243, v170
v_mfma_f32_16x16x32_bf16 v[152:155], a[8:11], a[84:87], v[152:155]
v_exp_f32_e32 v171, v171
v_exp_f32_e32 v164, v164
v_mfma_f32_16x16x32_bf16 v[144:147], a[12:15], a[88:91], v[144:147]
ds_read_b128 a[4:7], v202 offset:25088
v_add_f32_e32 v243, v243, v171
v_cvt_pk_bf16_f32 v181, v170, v171
v_mfma_f32_16x16x32_bf16 v[152:155], a[12:15], a[92:95], v[152:155]
s_waitcnt lgkmcnt(4)
v_add_f32_e32 v242, v242, v164
v_exp_f32_e32 v165, v165
v_exp_f32_e32 v166, v166
v_mfma_f32_16x16x32_bf16 v[148:151], a[16:19], a[64:67], 0
ds_read_b128 a[8:11], v202 offset:25600
v_add_f32_e32 v242, v242, v165
v_cvt_pk_bf16_f32 v178, v164, v165
v_mfma_f32_16x16x32_bf16 v[156:159], a[16:19], a[68:71], 0
v_add_f32_e32 v242, v242, v166
v_exp_f32_e32 v167, v167
s_nop 0
v_add_f32_e32 v242, v242, v167
v_mfma_f32_16x16x32_bf16 v[148:151], a[20:23], a[72:75], v[148:151]
ds_read_b128 a[12:15], v202 offset:26112
v_cvt_pk_bf16_f32 v179, v166, v167
v_exp_f32_e32 v172, v172
v_mfma_f32_16x16x32_bf16 v[156:159], a[20:23], a[76:79], v[156:159]
s_waitcnt lgkmcnt(4)
v_add_f32_e32 v243, v243, v172
v_exp_f32_e32 v173, v173
v_mfma_f32_16x16x32_bf16 v[148:151], a[24:27], a[80:83], v[148:151]
ds_read_b128 a[16:19], v202 offset:28672
v_add_f32_e32 v243, v243, v173
v_cvt_pk_bf16_f32 v182, v172, v173
v_max3_f32 v236, v144, v145, v146
v_mfma_f32_16x16x32_bf16 v[156:159], a[24:27], a[84:87], v[156:159]
v_exp_f32_e32 v174, v174
v_exp_f32_e32 v175, v175
v_mfma_f32_16x16x32_bf16 v[148:151], a[28:31], a[88:91], v[148:151]
ds_read_b128 a[20:23], v202 offset:29184
v_add_f32_e32 v243, v243, v174
v_add_f32_e32 v243, v243, v175
v_mfma_f32_16x16x32_bf16 v[156:159], a[28:31], a[92:95], v[156:159]
s_cbranch_vccnz .Lk_arb7
.Lk_abb7:
v_cvt_pk_bf16_f32 v183, v174, v175
s_waitcnt lgkmcnt(4)
v_mfma_f32_16x16x32_bf16 v[160:163], a[0:3], a[64:67], 0
ds_read_b128 a[24:27], v202 offset:29696
v_exp_f32_e32 v128, v128
v_max3_f32 v237, v152, v153, v154
v_mfma_f32_16x16x32_bf16 v[168:171], a[0:3], a[68:71], 0
v_exp_f32_e32 v129, v129
v_exp_f32_e32 v130, v130
v_mfma_f32_16x16x32_bf16 v[160:163], a[4:7], a[72:75], v[160:163]
ds_read_b128 a[28:31], v202 offset:30208
v_cvt_pk_bf16_f32 v184, v128, v129
v_exp_f32_e32 v131, v131
v_mfma_f32_16x16x32_bf16 v[168:171], a[4:7], a[76:79], v[168:171]
s_barrier
v_cvt_pk_bf16_f32 v185, v130, v131
s_waitcnt lgkmcnt(4)
v_mfma_f32_16x16x32_bf16 v[160:163], a[8:11], a[80:83], v[160:163]
ds_read_b64_tr_b16 a[0:1], v203 offset:16384
v_exp_f32_e32 v132, v132
v_add_f32_e32 v242, v242, v128
v_mfma_f32_16x16x32_bf16 v[168:171], a[8:11], a[84:87], v[168:171]
ds_read_b64_tr_b16 a[2:3], v203 offset:20480
v_exp_f32_e32 v133, v133
v_mfma_f32_16x16x32_bf16 v[160:163], a[12:15], a[88:91], v[160:163]
ds_read_b64_tr_b16 a[4:5], v203 offset:24576
v_cvt_pk_bf16_f32 v186, v132, v133
v_exp_f32_e32 v134, v134
v_mfma_f32_16x16x32_bf16 v[168:171], a[12:15], a[92:95], v[168:171]
s_waitcnt lgkmcnt(5)
ds_read_b64_tr_b16 a[6:7], v203 offset:28672
v_exp_f32_e32 v135, v135
v_add_f32_e32 v242, v242, v129
v_mfma_f32_16x16x32_bf16 v[164:167], a[16:19], a[64:67], 0
ds_read_b64_tr_b16 a[8:9], v204 offset:16384
v_cvt_pk_bf16_f32 v187, v134, v135
v_exp_f32_e32 v136, v136
v_mfma_f32_16x16x32_bf16 v[172:175], a[16:19], a[68:71], 0
ds_read_b64_tr_b16 a[10:11], v204 offset:20480
v_exp_f32_e32 v137, v137
v_mfma_f32_16x16x32_bf16 v[164:167], a[20:23], a[72:75], v[164:167]
ds_read_b64_tr_b16 a[12:13], v204 offset:24576
v_cvt_pk_bf16_f32 v188, v136, v137
v_exp_f32_e32 v138, v138
v_mfma_f32_16x16x32_bf16 v[172:175], a[20:23], a[76:79], v[172:175]
s_waitcnt lgkmcnt(7)
ds_read_b64_tr_b16 a[14:15], v204 offset:28672
)"
R"(
v_exp_f32_e32 v139, v139
v_add_f32_e32 v242, v242, v130
v_mfma_f32_16x16x32_bf16 v[164:167], a[24:27], a[80:83], v[164:167]
ds_read_b64_tr_b16 a[16:17], v203 offset:16896
v_cvt_pk_bf16_f32 v189, v138, v139
v_exp_f32_e32 v140, v140
v_mfma_f32_16x16x32_bf16 v[172:175], a[24:27], a[84:87], v[172:175]
ds_read_b64_tr_b16 a[18:19], v203 offset:20992
v_exp_f32_e32 v141, v141
v_mfma_f32_16x16x32_bf16 v[164:167], a[28:31], a[88:91], v[164:167]
ds_read_b64_tr_b16 a[20:21], v203 offset:25088
v_cvt_pk_bf16_f32 v190, v140, v141
v_exp_f32_e32 v142, v142
v_mfma_f32_16x16x32_bf16 v[172:175], a[28:31], a[92:95], v[172:175]
s_waitcnt lgkmcnt(7)
ds_read_b64_tr_b16 a[22:23], v203 offset:29184
v_exp_f32_e32 v143, v143
v_add_f32_e32 v242, v242, v131
v_mfma_f32_16x16x32_bf16 v[0:3], a[0:3], v[176:179], v[0:3]
ds_read_b64_tr_b16 a[24:25], v204 offset:16896
v_cvt_pk_bf16_f32 v191, v142, v143
v_add_f32_e32 v242, v242, v132
v_mfma_f32_16x16x32_bf16 v[8:11], a[0:3], v[180:183], v[8:11]
ds_read_b64_tr_b16 a[26:27], v204 offset:20992
v_add_f32_e32 v242, v242, v133
v_add_f32_e32 v242, v242, v134
v_add_f32_e32 v242, v242, v135
v_mfma_f32_16x16x32_bf16 v[0:3], a[4:7], v[184:187], v[0:3]
ds_read_b64_tr_b16 a[28:29], v204 offset:25088
v_mov_b32_e32 v248, v242
v_add_f32_e32 v243, v243, v136
s_nop 0
v_permlane16_swap_b32_e32 v242, v248
v_mfma_f32_16x16x32_bf16 v[8:11], a[4:7], v[188:191], v[8:11]
s_waitcnt lgkmcnt(7)
ds_read_b64_tr_b16 a[30:31], v204 offset:29184
v_add_f32_e32 v242, v242, v248
v_mov_b32_e32 v248, v242
v_add_f32_e32 v243, v243, v137
v_mfma_f32_16x16x32_bf16 v[4:7], a[8:11], v[176:179], v[4:7]
ds_read_b64_tr_b16 a[0:1], v203 offset:17408
v_permlane32_swap_b32_e32 v242, v248
v_add_f32_e32 v242, v242, v248
v_mfma_f32_16x16x32_bf16 v[12:15], a[8:11], v[180:183], v[12:15]
ds_read_b64_tr_b16 a[2:3], v203 offset:21504
v_rcp_f32_e32 v240, v242
v_add_f32_e32 v243, v243, v138
v_mfma_f32_16x16x32_bf16 v[4:7], a[12:15], v[184:187], v[4:7]
ds_read_b64_tr_b16 a[4:5], v203 offset:25600
v_mul_f32_e32 v0, v240, v0
v_mul_f32_e32 v1, v240, v1
v_mul_f32_e32 v2, v240, v2
v_mfma_f32_16x16x32_bf16 v[12:15], a[12:15], v[188:191], v[12:15]
s_waitcnt lgkmcnt(7)
ds_read_b64_tr_b16 a[6:7], v203 offset:29696
v_mul_f32_e32 v3, v240, v3
v_cvt_pk_bf16_f32 v0, v0, v1
v_cvt_pk_bf16_f32 v1, v2, v3
v_mfma_f32_16x16x32_bf16 v[16:19], a[16:19], v[176:179], v[16:19]
ds_read_b64_tr_b16 a[8:9], v204 offset:17408
v_add_f32_e32 v243, v243, v139
v_add_f32_e32 v243, v243, v140
v_mfma_f32_16x16x32_bf16 v[24:27], a[16:19], v[180:183], v[24:27]
ds_read_b64_tr_b16 a[10:11], v204 offset:21504
v_add_f32_e32 v243, v243, v141
v_add_f32_e32 v243, v243, v142
v_add_f32_e32 v243, v243, v143
v_mfma_f32_16x16x32_bf16 v[16:19], a[20:23], v[184:187], v[16:19]
ds_read_b64_tr_b16 a[12:13], v204 offset:25600
v_mul_f32_e32 v4, v240, v4
v_mul_f32_e32 v5, v240, v5
v_mul_f32_e32 v6, v240, v6
v_mfma_f32_16x16x32_bf16 v[24:27], a[20:23], v[188:191], v[24:27]
s_waitcnt lgkmcnt(7)
ds_read_b64_tr_b16 a[14:15], v204 offset:29696
v_mul_f32_e32 v7, v240, v7
v_cvt_pk_bf16_f32 v2, v4, v5
v_cvt_pk_bf16_f32 v3, v6, v7
v_mfma_f32_16x16x32_bf16 v[20:23], a[24:27], v[176:179], v[20:23]
ds_read_b64_tr_b16 a[16:17], v203 offset:17920
v_mov_b32_e32 v249, v243
v_max3_f32 v236, v236, v147, v148
v_mfma_f32_16x16x32_bf16 v[28:31], a[24:27], v[180:183], v[28:31]
ds_read_b64_tr_b16 a[18:19], v203 offset:22016
v_permlane16_swap_b32_e32 v243, v249
v_add_f32_e32 v243, v243, v249
v_mov_b32_e32 v249, v243
v_mfma_f32_16x16x32_bf16 v[20:23], a[28:31], v[184:187], v[20:23]
ds_read_b64_tr_b16 a[20:21], v203 offset:26112
v_permlane32_swap_b32_e32 v243, v249
v_add_f32_e32 v243, v243, v249
v_mul_f32_e32 v16, v240, v16
v_mfma_f32_16x16x32_bf16 v[28:31], a[28:31], v[188:191], v[28:31]
s_waitcnt lgkmcnt(7)
ds_read_b64_tr_b16 a[22:23], v203 offset:30208
v_rcp_f32_e32 v241, v243
s_nop 0
v_mul_f32_e32 v8, v241, v8
v_mfma_f32_16x16x32_bf16 v[32:35], a[0:3], v[176:179], v[32:35]
ds_read_b64_tr_b16 a[24:25], v204 offset:17920
v_mul_f32_e32 v9, v241, v9
v_mul_f32_e32 v10, v241, v10
v_mfma_f32_16x16x32_bf16 v[40:43], a[0:3], v[180:183], v[40:43]
ds_read_b64_tr_b16 a[26:27], v204 offset:22016
v_mul_f32_e32 v11, v241, v11
v_mul_f32_e32 v12, v241, v12
v_mul_f32_e32 v13, v241, v13
v_mfma_f32_16x16x32_bf16 v[32:35], a[4:7], v[184:187], v[32:35]
ds_read_b64_tr_b16 a[28:29], v204 offset:26112
v_mul_f32_e32 v14, v241, v14
v_mul_f32_e32 v15, v241, v15
v_cvt_pk_bf16_f32 v8, v8, v9
v_mfma_f32_16x16x32_bf16 v[40:43], a[4:7], v[188:191], v[40:43]
s_barrier
ds_read_b64_tr_b16 a[30:31], v204 offset:30208
s_waitcnt lgkmcnt(8)
v_mfma_f32_16x16x32_bf16 v[36:39], a[8:11], v[176:179], v[36:39]
ds_read_b64_tr_b16 a[0:1], v203 offset:512
v_cvt_pk_bf16_f32 v9, v10, v11
v_cvt_pk_bf16_f32 v10, v12, v13
v_cvt_pk_bf16_f32 v11, v14, v15
v_mfma_f32_16x16x32_bf16 v[44:47], a[8:11], v[180:183], v[44:47]
ds_read_b64_tr_b16 a[2:3], v203 offset:4608
v_mul_f32_e32 v17, v240, v17
v_mul_f32_e32 v18, v240, v18
v_mul_f32_e32 v19, v240, v19
v_mfma_f32_16x16x32_bf16 v[36:39], a[12:15], v[184:187], v[36:39]
ds_read_b64_tr_b16 a[4:5], v204 offset:512
v_mul_f32_e32 v20, v240, v20
v_mul_f32_e32 v21, v240, v21
v_mul_f32_e32 v22, v240, v22
v_mfma_f32_16x16x32_bf16 v[44:47], a[12:15], v[188:191], v[44:47]
s_waitcnt lgkmcnt(7)
ds_read_b64_tr_b16 a[6:7], v204 offset:4608
v_mul_f32_e32 v23, v240, v23
v_cvt_pk_bf16_f32 v16, v16, v17
v_mfma_f32_16x16x32_bf16 v[48:51], a[16:19], v[176:179], v[48:51]
ds_read_b64_tr_b16 a[8:9], v203 offset:1024
v_cvt_pk_bf16_f32 v17, v18, v19
v_cvt_pk_bf16_f32 v18, v20, v21
v_cvt_pk_bf16_f32 v19, v22, v23
v_mfma_f32_16x16x32_bf16 v[56:59], a[16:19], v[180:183], v[56:59]
ds_read_b64_tr_b16 a[10:11], v203 offset:5120
v_mul_f32_e32 v24, v241, v24
v_mul_f32_e32 v25, v241, v25
v_mul_f32_e32 v26, v241, v26
v_mfma_f32_16x16x32_bf16 v[48:51], a[20:23], v[184:187], v[48:51]
ds_read_b64_tr_b16 a[12:13], v204 offset:1024
v_mul_f32_e32 v27, v241, v27
v_mul_f32_e32 v28, v241, v28
v_mul_f32_e32 v29, v241, v29
v_mfma_f32_16x16x32_bf16 v[56:59], a[20:23], v[188:191], v[56:59]
s_waitcnt lgkmcnt(7)
ds_read_b64_tr_b16 a[14:15], v204 offset:5120
v_mul_f32_e32 v30, v241, v30
v_mul_f32_e32 v31, v241, v31
v_mfma_f32_16x16x32_bf16 v[52:55], a[24:27], v[176:179], v[52:55]
ds_read_b64_tr_b16 a[16:17], v203 offset:1536
v_cvt_pk_bf16_f32 v24, v24, v25
v_cvt_pk_bf16_f32 v25, v26, v27
v_cvt_pk_bf16_f32 v26, v28, v29
v_mfma_f32_16x16x32_bf16 v[60:63], a[24:27], v[180:183], v[60:63]
ds_read_b64_tr_b16 a[18:19], v203 offset:5632
v_cvt_pk_bf16_f32 v27, v30, v31
v_max3_f32 v236, v236, v149, v150
v_max3_f32 v236, v236, v151, v160
v_mfma_f32_16x16x32_bf16 v[52:55], a[28:31], v[184:187], v[52:55]
ds_read_b64_tr_b16 a[20:21], v204 offset:1536
v_max3_f32 v236, v236, v161, v162
v_max3_f32 v236, v236, v163, v164
v_max3_f32 v236, v236, v165, v166
v_mfma_f32_16x16x32_bf16 v[60:63], a[28:31], v[188:191], v[60:63]
s_waitcnt lgkmcnt(7)
ds_read_b64_tr_b16 a[22:23], v204 offset:5632
v_max_f32_e32 v236, v236, v167
v_mov_b32_e32 v246, v236
v_mfma_f32_16x16x32_bf16 v[80:83], a[0:3], v[0:3], 0
ds_read_b64_tr_b16 a[24:25], v203
v_permlane16_swap_b32_e32 v236, v246
v_max_f32_e32 v236, v236, v246
v_mov_b32_e32 v246, v236
v_mfma_f32_16x16x32_bf16 v[88:91], a[0:3], v[8:11], 0
ds_read_b64_tr_b16 a[26:27], v203 offset:4096
v_permlane32_swap_b32_e32 v236, v246
v_max_f32_e32 v236, v236, v246
v_sub_f32_e32 v192, 0, v236
v_mfma_f32_16x16x32_bf16 v[84:87], a[4:7], v[0:3], 0
ds_read_b64_tr_b16 a[28:29], v204
v_sub_f32_e32 v193, 0, v236
v_sub_f32_e32 v194, 0, v236
v_sub_f32_e32 v195, 0, v236
v_mfma_f32_16x16x32_bf16 v[92:95], a[4:7], v[8:11], 0
s_waitcnt lgkmcnt(7)
ds_read_b64_tr_b16 a[30:31], v204 offset:4096
v_max3_f32 v237, v237, v155, v156
v_max3_f32 v237, v237, v157, v158
v_mfma_f32_16x16x32_bf16 v[96:99], a[8:11], v[0:3], 0
ds_read_b64_tr_b16 a[0:1], v203 offset:8704
v_max3_f32 v237, v237, v159, v168
v_max3_f32 v237, v237, v169, v170
)"
R"(
v_max3_f32 v237, v237, v171, v172
v_mfma_f32_16x16x32_bf16 v[104:107], a[8:11], v[8:11], 0
ds_read_b64_tr_b16 a[2:3], v203 offset:12800
v_max3_f32 v237, v237, v173, v174
v_max_f32_e32 v237, v237, v175
v_mov_b32_e32 v247, v237
v_mfma_f32_16x16x32_bf16 v[100:103], a[12:15], v[0:3], 0
ds_read_b64_tr_b16 a[4:5], v204 offset:8704
v_permlane16_swap_b32_e32 v237, v247
v_max_f32_e32 v237, v237, v247
v_mfma_f32_16x16x32_bf16 v[108:111], a[12:15], v[8:11], 0
s_waitcnt lgkmcnt(7)
ds_read_b64_tr_b16 a[6:7], v204 offset:12800
v_mov_b32_e32 v247, v237
v_sub_f32_e32 v144, v144, v236
s_nop 0
v_permlane32_swap_b32_e32 v237, v247
v_mfma_f32_16x16x32_bf16 v[112:115], a[16:19], v[0:3], 0
ds_read_b64_tr_b16 a[8:9], v203 offset:9216
v_max_f32_e32 v237, v237, v247
v_sub_f32_e32 v196, 0, v237
v_sub_f32_e32 v197, 0, v237
v_mfma_f32_16x16x32_bf16 v[120:123], a[16:19], v[8:11], 0
ds_read_b64_tr_b16 a[10:11], v203 offset:13312
v_sub_f32_e32 v198, 0, v237
v_sub_f32_e32 v199, 0, v237
v_sub_f32_e32 v145, v145, v236
v_mfma_f32_16x16x32_bf16 v[116:119], a[20:23], v[0:3], 0
ds_read_b64_tr_b16 a[12:13], v204 offset:9216
v_sub_f32_e32 v146, v146, v236
v_sub_f32_e32 v147, v147, v236
v_mfma_f32_16x16x32_bf16 v[124:127], a[20:23], v[8:11], 0
s_waitcnt lgkmcnt(7)
ds_read_b64_tr_b16 a[14:15], v204 offset:13312
v_exp_f32_e32 v144, v144
v_exp_f32_e32 v145, v145
v_mfma_f32_16x16x32_bf16 v[64:67], a[24:27], v[0:3], 0
ds_read_b64_tr_b16 a[16:17], v203 offset:9728
v_add_f32_e32 v244, v244, v144
v_add_f32_e32 v244, v244, v145
v_mfma_f32_16x16x32_bf16 v[72:75], a[24:27], v[8:11], 0
ds_read_b64_tr_b16 a[18:19], v203 offset:13824
v_cvt_pk_bf16_f32 v176, v144, v145
v_exp_f32_e32 v146, v146
v_mfma_f32_16x16x32_bf16 v[68:71], a[28:31], v[0:3], 0
ds_read_b64_tr_b16 a[20:21], v204 offset:9728
v_add_f32_e32 v244, v244, v146
v_exp_f32_e32 v147, v147
v_mfma_f32_16x16x32_bf16 v[76:79], a[28:31], v[8:11], 0
s_waitcnt lgkmcnt(7)
ds_read_b64_tr_b16 a[22:23], v204 offset:13824
v_add_f32_e32 v244, v244, v147
v_cvt_pk_bf16_f32 v177, v146, v147
v_mfma_f32_16x16x32_bf16 v[80:83], a[0:3], v[16:19], v[80:83]
ds_read_b64_tr_b16 a[24:25], v203 offset:8192
v_sub_f32_e32 v152, v152, v237
v_sub_f32_e32 v153, v153, v237
v_sub_f32_e32 v154, v154, v237
v_mfma_f32_16x16x32_bf16 v[88:91], a[0:3], v[24:27], v[88:91]
ds_read_b64_tr_b16 a[26:27], v203 offset:12288
v_sub_f32_e32 v155, v155, v237
v_exp_f32_e32 v152, v152
v_mfma_f32_16x16x32_bf16 v[84:87], a[4:7], v[16:19], v[84:87]
ds_read_b64_tr_b16 a[28:29], v204 offset:8192
v_add_f32_e32 v245, v245, v152
v_exp_f32_e32 v153, v153
v_mfma_f32_16x16x32_bf16 v[92:95], a[4:7], v[24:27], v[92:95]
s_barrier
ds_read_b64_tr_b16 a[30:31], v204 offset:12288
s_waitcnt lgkmcnt(8)
v_mfma_f32_16x16x32_bf16 v[96:99], a[8:11], v[16:19], v[96:99]
ds_read_b128 a[0:3], v200 offset:49152
v_add_f32_e32 v245, v245, v153
v_cvt_pk_bf16_f32 v180, v152, v153
v_sub_f32_e32 v148, v148, v236
v_mfma_f32_16x16x32_bf16 v[104:107], a[8:11], v[24:27], v[104:107]
v_exp_f32_e32 v154, v154
v_exp_f32_e32 v155, v155
v_mfma_f32_16x16x32_bf16 v[100:103], a[12:15], v[16:19], v[100:103]
ds_read_b128 a[4:7], v200 offset:49664
v_add_f32_e32 v245, v245, v154
v_add_f32_e32 v245, v245, v155
v_mfma_f32_16x16x32_bf16 v[108:111], a[12:15], v[24:27], v[108:111]
s_waitcnt lgkmcnt(6)
v_cvt_pk_bf16_f32 v181, v154, v155
v_sub_f32_e32 v149, v149, v236
v_sub_f32_e32 v150, v150, v236
v_sub_f32_e32 v151, v151, v236
v_mfma_f32_16x16x32_bf16 v[112:115], a[16:19], v[16:19], v[112:115]
ds_read_b128 a[8:11], v200 offset:50176
v_exp_f32_e32 v148, v148
v_sub_f32_e32 v156, v156, v237
v_mfma_f32_16x16x32_bf16 v[120:123], a[16:19], v[24:27], v[120:123]
v_add_f32_e32 v244, v244, v148
v_exp_f32_e32 v149, v149
v_mfma_f32_16x16x32_bf16 v[116:119], a[20:23], v[16:19], v[116:119]
ds_read_b128 a[12:15], v200 offset:50688
v_add_f32_e32 v244, v244, v149
v_cvt_pk_bf16_f32 v178, v148, v149
v_exp_f32_e32 v150, v150
v_mfma_f32_16x16x32_bf16 v[124:127], a[20:23], v[24:27], v[124:127]
s_waitcnt lgkmcnt(4)
v_add_f32_e32 v244, v244, v150
v_exp_f32_e32 v151, v151
v_mfma_f32_16x16x32_bf16 v[64:67], a[24:27], v[16:19], v[64:67]
ds_read_b128 a[16:19], v200 offset:53248
v_add_f32_e32 v244, v244, v151
v_cvt_pk_bf16_f32 v179, v150, v151
v_sub_f32_e32 v157, v157, v237
v_mfma_f32_16x16x32_bf16 v[72:75], a[24:27], v[24:27], v[72:75]
v_sub_f32_e32 v158, v158, v237
v_sub_f32_e32 v159, v159, v237
v_exp_f32_e32 v156, v156
v_mfma_f32_16x16x32_bf16 v[68:71], a[28:31], v[16:19], v[68:71]
ds_read_b128 a[20:23], v200 offset:53760
v_add_f32_e32 v245, v245, v156
v_exp_f32_e32 v157, v157
v_mfma_f32_16x16x32_bf16 v[76:79], a[28:31], v[24:27], v[76:79]
s_waitcnt lgkmcnt(4)
v_add_f32_e32 v245, v245, v157
v_cvt_pk_bf16_f32 v182, v156, v157
v_accvgpr_write_b32 a32, v0
v_mfma_f32_16x16x32_bf16 v[128:131], a[0:3], a[64:67], v[192:195]
ds_read_b128 a[24:27], v200 offset:54272
v_exp_f32_e32 v158, v158
v_accvgpr_write_b32 a33, v1
v_mfma_f32_16x16x32_bf16 v[136:139], a[0:3], a[68:71], v[196:199]
v_add_f32_e32 v245, v245, v158
v_exp_f32_e32 v159, v159
v_mfma_f32_16x16x32_bf16 v[128:131], a[4:7], a[72:75], v[128:131]
ds_read_b128 a[28:31], v200 offset:54784
v_add_f32_e32 v245, v245, v159
v_cvt_pk_bf16_f32 v183, v158, v159
v_accvgpr_write_b32 a34, v2
v_mfma_f32_16x16x32_bf16 v[136:139], a[4:7], a[76:79], v[136:139]
s_waitcnt lgkmcnt(4)
v_accvgpr_write_b32 a35, v3
v_accvgpr_write_b32 a36, v8
v_accvgpr_write_b32 a37, v9
v_accvgpr_write_b32 a38, v10
v_mfma_f32_16x16x32_bf16 v[128:131], a[8:11], a[80:83], v[128:131]
ds_read_b128 a[0:3], v200 offset:57344
v_accvgpr_write_b32 a39, v11
v_accvgpr_write_b32 a40, v16
v_mfma_f32_16x16x32_bf16 v[136:139], a[8:11], a[84:87], v[136:139]
v_accvgpr_write_b32 a41, v17
v_accvgpr_write_b32 a42, v18
v_accvgpr_write_b32 a43, v19
v_accvgpr_write_b32 a44, v24
v_mfma_f32_16x16x32_bf16 v[128:131], a[12:15], a[88:91], v[128:131]
ds_read_b128 a[4:7], v200 offset:57856
v_accvgpr_write_b32 a45, v25
v_accvgpr_write_b32 a46, v26
v_accvgpr_write_b32 a47, v27
v_mfma_f32_16x16x32_bf16 v[136:139], a[12:15], a[92:95], v[136:139]
s_waitcnt lgkmcnt(4)
v_mul_f32_e32 v32, v240, v32
v_mul_f32_e32 v33, v240, v33
v_mul_f32_e32 v34, v240, v34
v_mul_f32_e32 v35, v240, v35
v_mfma_f32_16x16x32_bf16 v[132:135], a[16:19], a[64:67], v[192:195]
ds_read_b128 a[8:11], v200 offset:58368
v_mul_f32_e32 v36, v240, v36
v_mul_f32_e32 v37, v240, v37
v_mfma_f32_16x16x32_bf16 v[140:143], a[16:19], a[68:71], v[196:199]
v_mul_f32_e32 v38, v240, v38
v_mul_f32_e32 v39, v240, v39
v_cvt_pk_bf16_f32 v32, v32, v33
v_cvt_pk_bf16_f32 v33, v34, v35
v_mfma_f32_16x16x32_bf16 v[132:135], a[20:23], a[72:75], v[132:135]
ds_read_b128 a[12:15], v200 offset:58880
v_cvt_pk_bf16_f32 v34, v36, v37
v_cvt_pk_bf16_f32 v35, v38, v39
v_accvgpr_write_b32 a48, v32
v_mfma_f32_16x16x32_bf16 v[140:143], a[20:23], a[76:79], v[140:143]
s_waitcnt lgkmcnt(4)
v_accvgpr_write_b32 a49, v33
v_accvgpr_write_b32 a50, v34
v_accvgpr_write_b32 a51, v35
v_mul_f32_e32 v40, v241, v40
v_mfma_f32_16x16x32_bf16 v[132:135], a[24:27], a[80:83], v[132:135]
ds_read_b128 a[16:19], v200 offset:61440
v_mul_f32_e32 v41, v241, v41
v_mul_f32_e32 v42, v241, v42
v_mfma_f32_16x16x32_bf16 v[140:143], a[24:27], a[84:87], v[140:143]
v_mul_f32_e32 v43, v241, v43
v_mul_f32_e32 v44, v241, v44
v_mul_f32_e32 v45, v241, v45
v_mul_f32_e32 v46, v241, v46
v_mfma_f32_16x16x32_bf16 v[132:135], a[28:31], a[88:91], v[132:135]
ds_read_b128 a[20:23], v200 offset:61952
v_mul_f32_e32 v47, v241, v47
v_cvt_pk_bf16_f32 v40, v40, v41
v_cvt_pk_bf16_f32 v41, v42, v43
v_mfma_f32_16x16x32_bf16 v[140:143], a[28:31], a[92:95], v[140:143]
s_waitcnt lgkmcnt(4)
v_cvt_pk_bf16_f32 v42, v44, v45
v_cvt_pk_bf16_f32 v43, v46, v47
v_accvgpr_write_b32 a52, v40
v_accvgpr_write_b32 a53, v41
)"
R"(
v_mfma_f32_16x16x32_bf16 v[144:147], a[0:3], a[64:67], v[192:195]
ds_read_b128 a[24:27], v200 offset:62464
v_accvgpr_write_b32 a54, v42
v_accvgpr_write_b32 a55, v43
v_mfma_f32_16x16x32_bf16 v[152:155], a[0:3], a[68:71], v[196:199]
v_mul_f32_e32 v48, v240, v48
v_mul_f32_e32 v49, v240, v49
v_mul_f32_e32 v50, v240, v50
v_mul_f32_e32 v51, v240, v51
v_mfma_f32_16x16x32_bf16 v[144:147], a[4:7], a[72:75], v[144:147]
ds_read_b128 a[28:31], v200 offset:62976
v_mul_f32_e32 v52, v240, v52
v_mul_f32_e32 v53, v240, v53
v_mul_f32_e32 v54, v240, v54
v_mfma_f32_16x16x32_bf16 v[152:155], a[4:7], a[76:79], v[152:155]
s_barrier
v_mul_f32_e32 v55, v240, v55
v_cvt_pk_bf16_f32 v48, v48, v49
s_waitcnt lgkmcnt(4)
v_mfma_f32_16x16x32_bf16 v[144:147], a[8:11], a[80:83], v[144:147]
ds_read_b64_tr_b16 a[0:1], v203 offset:32768
v_cvt_pk_bf16_f32 v49, v50, v51
v_cvt_pk_bf16_f32 v50, v52, v53
v_mfma_f32_16x16x32_bf16 v[152:155], a[8:11], a[84:87], v[152:155]
ds_read_b64_tr_b16 a[2:3], v203 offset:36864
v_cvt_pk_bf16_f32 v51, v54, v55
v_accvgpr_write_b32 a56, v48
v_accvgpr_write_b32 a57, v49
v_mfma_f32_16x16x32_bf16 v[144:147], a[12:15], a[88:91], v[144:147]
ds_read_b64_tr_b16 a[4:5], v204 offset:32768
v_accvgpr_write_b32 a58, v50
v_accvgpr_write_b32 a59, v51
v_mul_f32_e32 v56, v241, v56
v_mfma_f32_16x16x32_bf16 v[152:155], a[12:15], a[92:95], v[152:155]
s_waitcnt lgkmcnt(5)
ds_read_b64_tr_b16 a[6:7], v204 offset:36864
v_mul_f32_e32 v57, v241, v57
v_mul_f32_e32 v58, v241, v58
v_mul_f32_e32 v59, v241, v59
v_mfma_f32_16x16x32_bf16 v[148:151], a[16:19], a[64:67], v[192:195]
ds_read_b64_tr_b16 a[8:9], v203 offset:33280
v_mul_f32_e32 v60, v241, v60
v_mul_f32_e32 v61, v241, v61
v_mfma_f32_16x16x32_bf16 v[156:159], a[16:19], a[68:71], v[196:199]
ds_read_b64_tr_b16 a[10:11], v203 offset:37376
v_mul_f32_e32 v62, v241, v62
v_mul_f32_e32 v63, v241, v63
v_cvt_pk_bf16_f32 v56, v56, v57
v_mfma_f32_16x16x32_bf16 v[148:151], a[20:23], a[72:75], v[148:151]
ds_read_b64_tr_b16 a[12:13], v204 offset:33280
v_cvt_pk_bf16_f32 v57, v58, v59
v_cvt_pk_bf16_f32 v58, v60, v61
v_cvt_pk_bf16_f32 v59, v62, v63
v_mfma_f32_16x16x32_bf16 v[156:159], a[20:23], a[76:79], v[156:159]
s_waitcnt lgkmcnt(7)
ds_read_b64_tr_b16 a[14:15], v204 offset:37376
v_accvgpr_write_b32 a60, v56
v_accvgpr_write_b32 a61, v57
v_mfma_f32_16x16x32_bf16 v[148:151], a[24:27], a[80:83], v[148:151]
ds_read_b64_tr_b16 a[16:17], v203 offset:33792
v_accvgpr_write_b32 a62, v58
v_accvgpr_write_b32 a63, v59
v_sub_f32_e32 v160, v160, v236
v_mfma_f32_16x16x32_bf16 v[156:159], a[24:27], a[84:87], v[156:159]
ds_read_b64_tr_b16 a[18:19], v203 offset:37888
v_sub_f32_e32 v161, v161, v236
v_sub_f32_e32 v162, v162, v236
v_sub_f32_e32 v163, v163, v236
v_mfma_f32_16x16x32_bf16 v[148:151], a[28:31], a[88:91], v[148:151]
ds_read_b64_tr_b16 a[20:21], v204 offset:33792
v_sub_f32_e32 v164, v164, v236
v_sub_f32_e32 v165, v165, v236
v_sub_f32_e32 v166, v166, v236
v_mfma_f32_16x16x32_bf16 v[156:159], a[28:31], a[92:95], v[156:159]
s_waitcnt lgkmcnt(7)
ds_read_b64_tr_b16 a[22:23], v204 offset:37888
v_sub_f32_e32 v167, v167, v236
v_exp_f32_e32 v160, v160
v_mfma_f32_16x16x32_bf16 v[0:3], a[0:3], v[176:179], 0
ds_read_b64_tr_b16 a[24:25], v203 offset:34304
v_exp_f32_e32 v161, v161
v_mfma_f32_16x16x32_bf16 v[8:11], a[0:3], v[180:183], 0
ds_read_b64_tr_b16 a[26:27], v203 offset:38400
v_cvt_pk_bf16_f32 v184, v160, v161
v_exp_f32_e32 v162, v162
v_mfma_f32_16x16x32_bf16 v[4:7], a[4:7], v[176:179], 0
ds_read_b64_tr_b16 a[28:29], v204 offset:34304
v_exp_f32_e32 v163, v163
v_sub_f32_e32 v168, v168, v237
v_mfma_f32_16x16x32_bf16 v[12:15], a[4:7], v[180:183], 0
s_waitcnt lgkmcnt(7)
ds_read_b64_tr_b16 a[30:31], v204 offset:38400
v_cvt_pk_bf16_f32 v185, v162, v163
v_exp_f32_e32 v164, v164
v_mfma_f32_16x16x32_bf16 v[16:19], a[8:11], v[176:179], 0
ds_read_b64_tr_b16 a[0:1], v203 offset:40960
v_exp_f32_e32 v165, v165
v_mfma_f32_16x16x32_bf16 v[24:27], a[8:11], v[180:183], 0
ds_read_b64_tr_b16 a[2:3], v203 offset:45056
v_cvt_pk_bf16_f32 v186, v164, v165
v_exp_f32_e32 v166, v166
v_mfma_f32_16x16x32_bf16 v[20:23], a[12:15], v[176:179], 0
ds_read_b64_tr_b16 a[4:5], v204 offset:40960
v_exp_f32_e32 v167, v167
v_mfma_f32_16x16x32_bf16 v[28:31], a[12:15], v[180:183], 0
s_waitcnt lgkmcnt(7)
ds_read_b64_tr_b16 a[6:7], v204 offset:45056
v_cvt_pk_bf16_f32 v187, v166, v167
v_sub_f32_e32 v169, v169, v237
v_sub_f32_e32 v170, v170, v237
v_mfma_f32_16x16x32_bf16 v[32:35], a[16:19], v[176:179], 0
ds_read_b64_tr_b16 a[8:9], v203 offset:41472
v_sub_f32_e32 v171, v171, v237
v_sub_f32_e32 v172, v172, v237
v_sub_f32_e32 v173, v173, v237
v_mfma_f32_16x16x32_bf16 v[40:43], a[16:19], v[180:183], 0
ds_read_b64_tr_b16 a[10:11], v203 offset:45568
v_sub_f32_e32 v174, v174, v237
v_sub_f32_e32 v175, v175, v237
v_add_f32_e32 v244, v244, v160
v_mfma_f32_16x16x32_bf16 v[36:39], a[20:23], v[176:179], 0
ds_read_b64_tr_b16 a[12:13], v204 offset:41472
v_exp_f32_e32 v168, v168
v_add_f32_e32 v244, v244, v161
v_mfma_f32_16x16x32_bf16 v[44:47], a[20:23], v[180:183], 0
s_waitcnt lgkmcnt(7)
ds_read_b64_tr_b16 a[14:15], v204 offset:45568
v_exp_f32_e32 v169, v169
v_add_f32_e32 v245, v245, v168
v_mfma_f32_16x16x32_bf16 v[48:51], a[24:27], v[176:179], 0
ds_read_b64_tr_b16 a[16:17], v203 offset:41984
v_cvt_pk_bf16_f32 v188, v168, v169
v_exp_f32_e32 v170, v170
v_mfma_f32_16x16x32_bf16 v[56:59], a[24:27], v[180:183], 0
ds_read_b64_tr_b16 a[18:19], v203 offset:46080
v_exp_f32_e32 v171, v171
v_exp_f32_e32 v172, v172
v_mfma_f32_16x16x32_bf16 v[52:55], a[28:31], v[176:179], 0
ds_read_b64_tr_b16 a[20:21], v204 offset:41984
v_cvt_pk_bf16_f32 v189, v170, v171
v_exp_f32_e32 v173, v173
v_mfma_f32_16x16x32_bf16 v[60:63], a[28:31], v[180:183], 0
s_waitcnt lgkmcnt(7)
ds_read_b64_tr_b16 a[22:23], v204 offset:46080
v_cvt_pk_bf16_f32 v190, v172, v173
v_exp_f32_e32 v174, v174
v_mfma_f32_16x16x32_bf16 v[0:3], a[0:3], v[184:187], v[0:3]
ds_read_b64_tr_b16 a[24:25], v203 offset:42496
v_exp_f32_e32 v175, v175
s_nop 0
v_cvt_pk_bf16_f32 v191, v174, v175
s_nop 1
v_mfma_f32_16x16x32_bf16 v[8:11], a[0:3], v[188:191], v[8:11]
ds_read_b64_tr_b16 a[26:27], v203 offset:46592
v_add_f32_e32 v245, v245, v169
v_mfma_f32_16x16x32_bf16 v[4:7], a[4:7], v[184:187], v[4:7]
ds_read_b64_tr_b16 a[28:29], v204 offset:42496
v_add_f32_e32 v244, v244, v162
v_add_f32_e32 v245, v245, v170
v_mfma_f32_16x16x32_bf16 v[12:15], a[4:7], v[188:191], v[12:15]
s_barrier
ds_read_b64_tr_b16 a[30:31], v204 offset:46592
s_waitcnt lgkmcnt(8)
v_mfma_f32_16x16x32_bf16 v[16:19], a[8:11], v[184:187], v[16:19]
ds_read_b128 a[0:3], v201 offset:16384
v_add_f32_e32 v244, v244, v163
v_add_f32_e32 v245, v245, v171
v_mfma_f32_16x16x32_bf16 v[24:27], a[8:11], v[188:191], v[24:27]
v_add_f32_e32 v244, v244, v164
v_add_f32_e32 v245, v245, v172
v_add_f32_e32 v244, v244, v165
v_mfma_f32_16x16x32_bf16 v[20:23], a[12:15], v[184:187], v[20:23]
ds_read_b128 a[4:7], v201 offset:16896
v_add_f32_e32 v245, v245, v173
v_add_f32_e32 v244, v244, v166
v_mfma_f32_16x16x32_bf16 v[28:31], a[12:15], v[188:191], v[28:31]
s_waitcnt lgkmcnt(6)
v_add_f32_e32 v245, v245, v174
v_add_f32_e32 v244, v244, v167
v_add_f32_e32 v245, v245, v175
v_mfma_f32_16x16x32_bf16 v[32:35], a[16:19], v[184:187], v[32:35]
ds_read_b128 a[8:11], v201 offset:17408
v_max3_f32 v236, v128, v129, v130
v_max3_f32 v237, v136, v137, v138
v_mfma_f32_16x16x32_bf16 v[40:43], a[16:19], v[188:191], v[40:43]
v_max3_f32 v236, v236, v131, v132
v_max3_f32 v237, v237, v139, v140
v_max3_f32 v236, v236, v133, v134
v_mfma_f32_16x16x32_bf16 v[36:39], a[20:23], v[184:187], v[36:39]
ds_read_b128 a[12:15], v201 offset:17920
v_max3_f32 v237, v237, v141, v142
v_max3_f32 v236, v236, v135, v144
v_mfma_f32_16x16x32_bf16 v[44:47], a[20:23], v[188:191], v[44:47]
s_waitcnt lgkmcnt(4)
v_max3_f32 v237, v237, v143, v152
v_max3_f32 v236, v236, v145, v146
v_max3_f32 v237, v237, v153, v154
v_mfma_f32_16x16x32_bf16 v[48:51], a[24:27], v[184:187], v[48:51]
ds_read_b128 a[16:19], v201 offset:20480
v_max3_f32 v236, v236, v147, v148
)"
R"(
v_max3_f32 v237, v237, v155, v156
v_mfma_f32_16x16x32_bf16 v[56:59], a[24:27], v[188:191], v[56:59]
v_max3_f32 v236, v236, v149, v150
v_max3_f32 v237, v237, v157, v158
v_max3_f32 v248, v236, v151, v159
v_mfma_f32_16x16x32_bf16 v[52:55], a[28:31], v[184:187], v[52:55]
ds_read_b128 a[20:23], v201 offset:20992
v_max_f32_e32 v248, v248, v237
v_cmp_lt_f32_e32 vcc, s70, v248
v_mfma_f32_16x16x32_bf16 v[60:63], a[28:31], v[188:191], v[60:63]
s_nop 0
s_cbranch_vccnz .Lk_ara9
.Lk_aba9:
s_waitcnt lgkmcnt(4)
v_mfma_f32_16x16x32_bf16 v[160:163], a[0:3], a[64:67], v[192:195]
ds_read_b128 a[24:27], v201 offset:21504
v_exp_f32_e32 v128, v128
v_mfma_f32_16x16x32_bf16 v[168:171], a[0:3], a[68:71], v[196:199]
v_add_f32_e32 v244, v244, v128
v_exp_f32_e32 v129, v129
v_mfma_f32_16x16x32_bf16 v[160:163], a[4:7], a[72:75], v[160:163]
ds_read_b128 a[28:31], v201 offset:22016
v_add_f32_e32 v244, v244, v129
v_cvt_pk_bf16_f32 v176, v128, v129
v_mfma_f32_16x16x32_bf16 v[168:171], a[4:7], a[76:79], v[168:171]
s_waitcnt lgkmcnt(4)
v_exp_f32_e32 v130, v130
s_nop 0
v_add_f32_e32 v244, v244, v130
v_mfma_f32_16x16x32_bf16 v[160:163], a[8:11], a[80:83], v[160:163]
ds_read_b128 a[0:3], v201 offset:24576
v_exp_f32_e32 v131, v131
v_mfma_f32_16x16x32_bf16 v[168:171], a[8:11], a[84:87], v[168:171]
v_add_f32_e32 v244, v244, v131
v_cvt_pk_bf16_f32 v177, v130, v131
v_mfma_f32_16x16x32_bf16 v[160:163], a[12:15], a[88:91], v[160:163]
ds_read_b128 a[4:7], v201 offset:25088
v_exp_f32_e32 v136, v136
v_mfma_f32_16x16x32_bf16 v[168:171], a[12:15], a[92:95], v[168:171]
s_waitcnt lgkmcnt(4)
v_add_f32_e32 v245, v245, v136
v_exp_f32_e32 v137, v137
v_mfma_f32_16x16x32_bf16 v[164:167], a[16:19], a[64:67], v[192:195]
ds_read_b128 a[8:11], v201 offset:25600
v_add_f32_e32 v245, v245, v137
v_cvt_pk_bf16_f32 v180, v136, v137
v_mfma_f32_16x16x32_bf16 v[172:175], a[16:19], a[68:71], v[196:199]
v_exp_f32_e32 v138, v138
v_exp_f32_e32 v139, v139
v_mfma_f32_16x16x32_bf16 v[164:167], a[20:23], a[72:75], v[164:167]
ds_read_b128 a[12:15], v201 offset:26112
v_add_f32_e32 v245, v245, v138
v_mfma_f32_16x16x32_bf16 v[172:175], a[20:23], a[76:79], v[172:175]
s_waitcnt lgkmcnt(4)
v_add_f32_e32 v245, v245, v139
v_cvt_pk_bf16_f32 v181, v138, v139
v_exp_f32_e32 v132, v132
v_mfma_f32_16x16x32_bf16 v[164:167], a[24:27], a[80:83], v[164:167]
ds_read_b128 a[16:19], v201 offset:28672
v_add_f32_e32 v244, v244, v132
v_mfma_f32_16x16x32_bf16 v[172:175], a[24:27], a[84:87], v[172:175]
v_exp_f32_e32 v133, v133
v_max3_f32 v236, v160, v161, v162
v_mfma_f32_16x16x32_bf16 v[164:167], a[28:31], a[88:91], v[164:167]
ds_read_b128 a[20:23], v201 offset:29184
v_add_f32_e32 v244, v244, v133
v_cvt_pk_bf16_f32 v178, v132, v133
v_mfma_f32_16x16x32_bf16 v[172:175], a[28:31], a[92:95], v[172:175]
s_waitcnt lgkmcnt(4)
v_exp_f32_e32 v134, v134
v_max3_f32 v237, v168, v169, v170
v_mfma_f32_16x16x32_bf16 v[128:131], a[0:3], a[64:67], v[192:195]
ds_read_b128 a[24:27], v201 offset:29696
v_add_f32_e32 v244, v244, v134
v_mfma_f32_16x16x32_bf16 v[136:139], a[0:3], a[68:71], v[196:199]
v_exp_f32_e32 v135, v135
v_exp_f32_e32 v140, v140
v_mfma_f32_16x16x32_bf16 v[128:131], a[4:7], a[72:75], v[128:131]
ds_read_b128 a[28:31], v201 offset:30208
v_add_f32_e32 v244, v244, v135
v_cvt_pk_bf16_f32 v179, v134, v135
v_mfma_f32_16x16x32_bf16 v[136:139], a[4:7], a[76:79], v[136:139]
s_barrier
v_add_f32_e32 v245, v245, v140
v_max3_f32 v236, v236, v163, v164
s_waitcnt lgkmcnt(4)
v_mfma_f32_16x16x32_bf16 v[128:131], a[8:11], a[80:83], v[128:131]
ds_read_b64_tr_b16 a[0:1], v205 offset:49152
v_exp_f32_e32 v141, v141
v_mfma_f32_16x16x32_bf16 v[136:139], a[8:11], a[84:87], v[136:139]
ds_read_b64_tr_b16 a[2:3], v205 offset:53248
v_add_f32_e32 v245, v245, v141
v_cvt_pk_bf16_f32 v182, v140, v141
v_mfma_f32_16x16x32_bf16 v[128:131], a[12:15], a[88:91], v[128:131]
ds_read_b64_tr_b16 a[4:5], v206 offset:49152
v_exp_f32_e32 v142, v142
v_mfma_f32_16x16x32_bf16 v[136:139], a[12:15], a[92:95], v[136:139]
s_waitcnt lgkmcnt(5)
ds_read_b64_tr_b16 a[6:7], v206 offset:53248
v_add_f32_e32 v245, v245, v142
v_exp_f32_e32 v143, v143
v_mfma_f32_16x16x32_bf16 v[132:135], a[16:19], a[64:67], v[192:195]
ds_read_b64_tr_b16 a[8:9], v205 offset:49664
v_add_f32_e32 v245, v245, v143
v_cvt_pk_bf16_f32 v183, v142, v143
v_mfma_f32_16x16x32_bf16 v[140:143], a[16:19], a[68:71], v[196:199]
s_cbranch_vccnz .Lk_arb9
.Lk_abb9:
ds_read_b64_tr_b16 a[10:11], v205 offset:53760
v_mfma_f32_16x16x32_bf16 v[132:135], a[20:23], a[72:75], v[132:135]
ds_read_b64_tr_b16 a[12:13], v206 offset:49664
v_mfma_f32_16x16x32_bf16 v[140:143], a[20:23], a[76:79], v[140:143]
s_waitcnt lgkmcnt(7)
ds_read_b64_tr_b16 a[14:15], v206 offset:53760
v_exp_f32_e32 v144, v144
v_mfma_f32_16x16x32_bf16 v[132:135], a[24:27], a[80:83], v[132:135]
ds_read_b64_tr_b16 a[16:17], v205 offset:50176
v_exp_f32_e32 v145, v145
v_mfma_f32_16x16x32_bf16 v[140:143], a[24:27], a[84:87], v[140:143]
ds_read_b64_tr_b16 a[18:19], v205 offset:54272
v_cvt_pk_bf16_f32 v184, v144, v145
v_add_f32_e32 v244, v244, v144
v_mfma_f32_16x16x32_bf16 v[132:135], a[28:31], a[88:91], v[132:135]
ds_read_b64_tr_b16 a[20:21], v206 offset:50176
v_exp_f32_e32 v146, v146
v_mfma_f32_16x16x32_bf16 v[140:143], a[28:31], a[92:95], v[140:143]
s_waitcnt lgkmcnt(7)
ds_read_b64_tr_b16 a[22:23], v206 offset:54272
v_exp_f32_e32 v147, v147
v_mfma_f32_16x16x32_bf16 v[0:3], a[0:3], v[176:179], v[0:3]
ds_read_b64_tr_b16 a[24:25], v205 offset:50688
v_cvt_pk_bf16_f32 v185, v146, v147
v_mfma_f32_16x16x32_bf16 v[8:11], a[0:3], v[180:183], v[8:11]
ds_read_b64_tr_b16 a[26:27], v205 offset:54784
v_exp_f32_e32 v148, v148
v_mfma_f32_16x16x32_bf16 v[4:7], a[4:7], v[176:179], v[4:7]
ds_read_b64_tr_b16 a[28:29], v206 offset:50688
v_exp_f32_e32 v149, v149
v_mfma_f32_16x16x32_bf16 v[12:15], a[4:7], v[180:183], v[12:15]
s_waitcnt lgkmcnt(7)
ds_read_b64_tr_b16 a[30:31], v206 offset:54784
v_cvt_pk_bf16_f32 v186, v148, v149
v_add_f32_e32 v244, v244, v145
v_mfma_f32_16x16x32_bf16 v[16:19], a[8:11], v[176:179], v[16:19]
ds_read_b64_tr_b16 a[0:1], v205 offset:57344
v_exp_f32_e32 v150, v150
v_mfma_f32_16x16x32_bf16 v[24:27], a[8:11], v[180:183], v[24:27]
ds_read_b64_tr_b16 a[2:3], v205 offset:61440
v_exp_f32_e32 v151, v151
v_mfma_f32_16x16x32_bf16 v[20:23], a[12:15], v[176:179], v[20:23]
ds_read_b64_tr_b16 a[4:5], v206 offset:57344
v_cvt_pk_bf16_f32 v187, v150, v151
v_add_f32_e32 v244, v244, v146
v_mfma_f32_16x16x32_bf16 v[28:31], a[12:15], v[180:183], v[28:31]
s_waitcnt lgkmcnt(7)
ds_read_b64_tr_b16 a[6:7], v206 offset:61440
v_exp_f32_e32 v152, v152
v_mfma_f32_16x16x32_bf16 v[32:35], a[16:19], v[176:179], v[32:35]
ds_read_b64_tr_b16 a[8:9], v205 offset:57856
v_exp_f32_e32 v153, v153
v_mfma_f32_16x16x32_bf16 v[40:43], a[16:19], v[180:183], v[40:43]
ds_read_b64_tr_b16 a[10:11], v205 offset:61952
v_cvt_pk_bf16_f32 v188, v152, v153
v_add_f32_e32 v245, v245, v152
v_mfma_f32_16x16x32_bf16 v[36:39], a[20:23], v[176:179], v[36:39]
ds_read_b64_tr_b16 a[12:13], v206 offset:57856
v_exp_f32_e32 v154, v154
v_mfma_f32_16x16x32_bf16 v[44:47], a[20:23], v[180:183], v[44:47]
s_waitcnt lgkmcnt(7)
ds_read_b64_tr_b16 a[14:15], v206 offset:61952
v_exp_f32_e32 v155, v155
v_mfma_f32_16x16x32_bf16 v[48:51], a[24:27], v[176:179], v[48:51]
ds_read_b64_tr_b16 a[16:17], v205 offset:58368
v_cvt_pk_bf16_f32 v189, v154, v155
v_exp_f32_e32 v156, v156
v_mfma_f32_16x16x32_bf16 v[56:59], a[24:27], v[180:183], v[56:59]
ds_read_b64_tr_b16 a[18:19], v205 offset:62464
v_exp_f32_e32 v157, v157
v_mfma_f32_16x16x32_bf16 v[52:55], a[28:31], v[176:179], v[52:55]
ds_read_b64_tr_b16 a[20:21], v206 offset:58368
v_cvt_pk_bf16_f32 v190, v156, v157
v_mfma_f32_16x16x32_bf16 v[60:63], a[28:31], v[180:183], v[60:63]
s_waitcnt lgkmcnt(7)
ds_read_b64_tr_b16 a[22:23], v206 offset:62464
v_exp_f32_e32 v158, v158
v_add_f32_e32 v245, v245, v153
v_mfma_f32_16x16x32_bf16 v[0:3], a[0:3], v[184:187], v[0:3]
ds_read_b64_tr_b16 a[24:25], v205 offset:58880
v_exp_f32_e32 v159, v159
s_nop 0
v_cvt_pk_bf16_f32 v191, v158, v159
s_nop 1
v_mfma_f32_16x16x32_bf16 v[8:11], a[0:3], v[188:191], v[8:11]
ds_read_b64_tr_b16 a[26:27], v205 offset:62976
v_add_f32_e32 v245, v245, v154
v_mfma_f32_16x16x32_bf16 v[4:7], a[4:7], v[184:187], v[4:7]
ds_read_b64_tr_b16 a[28:29], v206 offset:58880
v_add_f32_e32 v244, v244, v147
v_add_f32_e32 v245, v245, v155
v_mfma_f32_16x16x32_bf16 v[12:15], a[4:7], v[188:191], v[12:15]
)"
R"(
s_barrier
ds_read_b64_tr_b16 a[30:31], v206 offset:62976
s_waitcnt lgkmcnt(8)
v_mfma_f32_16x16x32_bf16 v[16:19], a[8:11], v[184:187], v[16:19]
ds_read_b64_tr_b16 a[0:1], v207 offset:512
v_add_f32_e32 v244, v244, v148
v_mfma_f32_16x16x32_bf16 v[24:27], a[8:11], v[188:191], v[24:27]
ds_read_b64_tr_b16 a[2:3], v207 offset:4608
v_add_f32_e32 v245, v245, v156
v_add_f32_e32 v244, v244, v149
v_mfma_f32_16x16x32_bf16 v[20:23], a[12:15], v[184:187], v[20:23]
ds_read_b64_tr_b16 a[4:5], v208 offset:512
v_add_f32_e32 v245, v245, v157
v_mfma_f32_16x16x32_bf16 v[28:31], a[12:15], v[188:191], v[28:31]
s_waitcnt lgkmcnt(7)
ds_read_b64_tr_b16 a[6:7], v208 offset:4608
v_add_f32_e32 v244, v244, v150
v_add_f32_e32 v245, v245, v158
v_mfma_f32_16x16x32_bf16 v[32:35], a[16:19], v[184:187], v[32:35]
ds_read_b64_tr_b16 a[8:9], v207 offset:1024
v_add_f32_e32 v244, v244, v151
v_mfma_f32_16x16x32_bf16 v[40:43], a[16:19], v[188:191], v[40:43]
ds_read_b64_tr_b16 a[10:11], v207 offset:5120
v_add_f32_e32 v245, v245, v159
v_max3_f32 v237, v237, v171, v172
v_mfma_f32_16x16x32_bf16 v[36:39], a[20:23], v[184:187], v[36:39]
ds_read_b64_tr_b16 a[12:13], v208 offset:1024
v_max3_f32 v236, v236, v165, v166
v_mfma_f32_16x16x32_bf16 v[44:47], a[20:23], v[188:191], v[44:47]
s_waitcnt lgkmcnt(7)
ds_read_b64_tr_b16 a[14:15], v208 offset:5120
v_max3_f32 v237, v237, v173, v174
v_max3_f32 v236, v236, v167, v128
v_mfma_f32_16x16x32_bf16 v[48:51], a[24:27], v[184:187], v[48:51]
ds_read_b64_tr_b16 a[16:17], v207 offset:1536
v_max3_f32 v237, v237, v175, v136
v_mfma_f32_16x16x32_bf16 v[56:59], a[24:27], v[188:191], v[56:59]
ds_read_b64_tr_b16 a[18:19], v207 offset:5632
v_max3_f32 v236, v236, v129, v130
v_max3_f32 v237, v237, v137, v138
v_mfma_f32_16x16x32_bf16 v[52:55], a[28:31], v[184:187], v[52:55]
ds_read_b64_tr_b16 a[20:21], v208 offset:1536
v_max3_f32 v236, v236, v131, v132
v_mfma_f32_16x16x32_bf16 v[60:63], a[28:31], v[188:191], v[60:63]
s_waitcnt lgkmcnt(7)
ds_read_b64_tr_b16 a[22:23], v208 offset:5632
v_max3_f32 v237, v237, v139, v140
v_max3_f32 v236, v236, v133, v134
v_mfma_f32_16x16x32_bf16 v[80:83], a[0:3], a[48:51], v[80:83]
ds_read_b64_tr_b16 a[24:25], v207
v_max3_f32 v237, v237, v141, v142
v_mfma_f32_16x16x32_bf16 v[88:91], a[0:3], a[52:55], v[88:91]
ds_read_b64_tr_b16 a[26:27], v207 offset:4096
v_max3_f32 v248, v236, v135, v143
v_max_f32_e32 v248, v248, v237
v_mfma_f32_16x16x32_bf16 v[84:87], a[4:7], a[48:51], v[84:87]
ds_read_b64_tr_b16 a[28:29], v208
v_cmp_lt_f32_e32 vcc, s70, v248
v_mfma_f32_16x16x32_bf16 v[92:95], a[4:7], a[52:55], v[92:95]
s_nop 0
s_cbranch_vccnz .Lk_ara10
.Lk_aba10:
ds_read_b64_tr_b16 a[30:31], v208 offset:4096
s_waitcnt lgkmcnt(8)
v_mfma_f32_16x16x32_bf16 v[96:99], a[8:11], a[48:51], v[96:99]
ds_read_b64_tr_b16 a[0:1], v207 offset:8704
v_mfma_f32_16x16x32_bf16 v[104:107], a[8:11], a[52:55], v[104:107]
ds_read_b64_tr_b16 a[2:3], v207 offset:12800
v_exp_f32_e32 v160, v160
v_mfma_f32_16x16x32_bf16 v[100:103], a[12:15], a[48:51], v[100:103]
ds_read_b64_tr_b16 a[4:5], v208 offset:8704
v_add_f32_e32 v244, v244, v160
v_mfma_f32_16x16x32_bf16 v[108:111], a[12:15], a[52:55], v[108:111]
s_waitcnt lgkmcnt(7)
ds_read_b64_tr_b16 a[6:7], v208 offset:12800
v_exp_f32_e32 v161, v161
v_mfma_f32_16x16x32_bf16 v[112:115], a[16:19], a[48:51], v[112:115]
ds_read_b64_tr_b16 a[8:9], v207 offset:9216
v_add_f32_e32 v244, v244, v161
v_cvt_pk_bf16_f32 v176, v160, v161
v_mfma_f32_16x16x32_bf16 v[120:123], a[16:19], a[52:55], v[120:123]
ds_read_b64_tr_b16 a[10:11], v207 offset:13312
v_exp_f32_e32 v162, v162
v_mfma_f32_16x16x32_bf16 v[116:119], a[20:23], a[48:51], v[116:119]
ds_read_b64_tr_b16 a[12:13], v208 offset:9216
v_add_f32_e32 v244, v244, v162
v_mfma_f32_16x16x32_bf16 v[124:127], a[20:23], a[52:55], v[124:127]
s_waitcnt lgkmcnt(7)
ds_read_b64_tr_b16 a[14:15], v208 offset:13312
v_exp_f32_e32 v163, v163
v_mfma_f32_16x16x32_bf16 v[64:67], a[24:27], a[48:51], v[64:67]
ds_read_b64_tr_b16 a[16:17], v207 offset:9728
v_add_f32_e32 v244, v244, v163
v_mfma_f32_16x16x32_bf16 v[72:75], a[24:27], a[52:55], v[72:75]
ds_read_b64_tr_b16 a[18:19], v207 offset:13824
v_cvt_pk_bf16_f32 v177, v162, v163
v_mfma_f32_16x16x32_bf16 v[68:71], a[28:31], a[48:51], v[68:71]
ds_read_b64_tr_b16 a[20:21], v208 offset:9728
v_exp_f32_e32 v168, v168
v_mfma_f32_16x16x32_bf16 v[76:79], a[28:31], a[52:55], v[76:79]
s_waitcnt lgkmcnt(7)
ds_read_b64_tr_b16 a[22:23], v208 offset:13824
v_add_f32_e32 v245, v245, v168
v_mfma_f32_16x16x32_bf16 v[80:83], a[0:3], a[56:59], v[80:83]
ds_read_b64_tr_b16 a[24:25], v207 offset:8192
v_exp_f32_e32 v169, v169
v_mfma_f32_16x16x32_bf16 v[88:91], a[0:3], a[60:63], v[88:91]
ds_read_b64_tr_b16 a[26:27], v207 offset:12288
v_add_f32_e32 v245, v245, v169
v_cvt_pk_bf16_f32 v180, v168, v169
v_mfma_f32_16x16x32_bf16 v[84:87], a[4:7], a[56:59], v[84:87]
ds_read_b64_tr_b16 a[28:29], v208 offset:8192
v_exp_f32_e32 v170, v170
v_mfma_f32_16x16x32_bf16 v[92:95], a[4:7], a[60:63], v[92:95]
s_barrier
ds_read_b64_tr_b16 a[30:31], v208 offset:12288
s_waitcnt lgkmcnt(8)
v_mfma_f32_16x16x32_bf16 v[96:99], a[8:11], a[56:59], v[96:99]
ds_read_b128 a[0:3], v201
v_add_f32_e32 v245, v245, v170
v_mfma_f32_16x16x32_bf16 v[104:107], a[8:11], a[60:63], v[104:107]
v_exp_f32_e32 v171, v171
v_mfma_f32_16x16x32_bf16 v[100:103], a[12:15], a[56:59], v[100:103]
ds_read_b128 a[4:7], v201 offset:512
v_add_f32_e32 v245, v245, v171
v_cvt_pk_bf16_f32 v181, v170, v171
v_mfma_f32_16x16x32_bf16 v[108:111], a[12:15], a[60:63], v[108:111]
s_waitcnt lgkmcnt(6)
v_exp_f32_e32 v164, v164
v_mfma_f32_16x16x32_bf16 v[112:115], a[16:19], a[56:59], v[112:115]
ds_read_b128 a[8:11], v201 offset:1024
v_add_f32_e32 v244, v244, v164
v_mfma_f32_16x16x32_bf16 v[120:123], a[16:19], a[60:63], v[120:123]
v_exp_f32_e32 v165, v165
v_exp_f32_e32 v166, v166
v_mfma_f32_16x16x32_bf16 v[116:119], a[20:23], a[56:59], v[116:119]
ds_read_b128 a[12:15], v201 offset:1536
v_add_f32_e32 v244, v244, v165
v_mfma_f32_16x16x32_bf16 v[124:127], a[20:23], a[60:63], v[124:127]
s_waitcnt lgkmcnt(4)
v_cvt_pk_bf16_f32 v178, v164, v165
v_add_f32_e32 v244, v244, v166
v_mfma_f32_16x16x32_bf16 v[64:67], a[24:27], a[56:59], v[64:67]
ds_read_b128 a[16:19], v201 offset:4096
v_exp_f32_e32 v167, v167
s_nop 0
v_add_f32_e32 v244, v244, v167
v_mfma_f32_16x16x32_bf16 v[72:75], a[24:27], a[60:63], v[72:75]
v_cvt_pk_bf16_f32 v179, v166, v167
v_exp_f32_e32 v172, v172
v_mfma_f32_16x16x32_bf16 v[68:71], a[28:31], a[56:59], v[68:71]
ds_read_b128 a[20:23], v201 offset:4608
v_add_f32_e32 v245, v245, v172
v_mfma_f32_16x16x32_bf16 v[76:79], a[28:31], a[60:63], v[76:79]
s_waitcnt lgkmcnt(4)
v_exp_f32_e32 v173, v173
s_nop 0
v_add_f32_e32 v245, v245, v173
v_mfma_f32_16x16x32_bf16 v[144:147], a[0:3], a[64:67], v[192:195]
ds_read_b128 a[24:27], v201 offset:5120
v_cvt_pk_bf16_f32 v182, v172, v173
v_mfma_f32_16x16x32_bf16 v[152:155], a[0:3], a[68:71], v[196:199]
v_exp_f32_e32 v174, v174
s_nop 0
v_add_f32_e32 v245, v245, v174
v_mfma_f32_16x16x32_bf16 v[144:147], a[4:7], a[72:75], v[144:147]
ds_read_b128 a[28:31], v201 offset:5632
v_exp_f32_e32 v175, v175
v_mfma_f32_16x16x32_bf16 v[152:155], a[4:7], a[76:79], v[152:155]
s_waitcnt lgkmcnt(4)
v_add_f32_e32 v245, v245, v175
v_cvt_pk_bf16_f32 v183, v174, v175
v_mfma_f32_16x16x32_bf16 v[144:147], a[8:11], a[80:83], v[144:147]
s_cbranch_vccnz .Lk_arb10
.Lk_abb10:
ds_read_b128 a[0:3], v201 offset:8192
v_mfma_f32_16x16x32_bf16 v[152:155], a[8:11], a[84:87], v[152:155]
v_exp_f32_e32 v128, v128
v_mfma_f32_16x16x32_bf16 v[144:147], a[12:15], a[88:91], v[144:147]
ds_read_b128 a[4:7], v201 offset:8704
v_exp_f32_e32 v129, v129
v_mfma_f32_16x16x32_bf16 v[152:155], a[12:15], a[92:95], v[152:155]
s_waitcnt lgkmcnt(4)
v_cvt_pk_bf16_f32 v184, v128, v129
v_add_f32_e32 v244, v244, v128
v_mfma_f32_16x16x32_bf16 v[148:151], a[16:19], a[64:67], v[192:195]
ds_read_b128 a[8:11], v201 offset:9216
v_exp_f32_e32 v130, v130
v_mfma_f32_16x16x32_bf16 v[156:159], a[16:19], a[68:71], v[196:199]
v_exp_f32_e32 v131, v131
v_mfma_f32_16x16x32_bf16 v[148:151], a[20:23], a[72:75], v[148:151]
ds_read_b128 a[12:15], v201 offset:9728
v_cvt_pk_bf16_f32 v185, v130, v131
v_add_f32_e32 v244, v244, v129
v_mfma_f32_16x16x32_bf16 v[156:159], a[20:23], a[76:79], v[156:159]
s_waitcnt lgkmcnt(4)
v_exp_f32_e32 v132, v132
v_mfma_f32_16x16x32_bf16 v[148:151], a[24:27], a[80:83], v[148:151]
ds_read_b128 a[16:19], v201 offset:12288
v_exp_f32_e32 v133, v133
)"
R"(
v_mfma_f32_16x16x32_bf16 v[156:159], a[24:27], a[84:87], v[156:159]
v_cvt_pk_bf16_f32 v186, v132, v133
v_exp_f32_e32 v134, v134
v_mfma_f32_16x16x32_bf16 v[148:151], a[28:31], a[88:91], v[148:151]
ds_read_b128 a[20:23], v201 offset:12800
v_exp_f32_e32 v135, v135
v_mfma_f32_16x16x32_bf16 v[156:159], a[28:31], a[92:95], v[156:159]
s_waitcnt lgkmcnt(4)
v_cvt_pk_bf16_f32 v187, v134, v135
v_add_f32_e32 v244, v244, v130
v_mfma_f32_16x16x32_bf16 v[160:163], a[0:3], a[64:67], v[192:195]
ds_read_b128 a[24:27], v201 offset:13312
v_exp_f32_e32 v136, v136
v_mfma_f32_16x16x32_bf16 v[168:171], a[0:3], a[68:71], v[196:199]
v_exp_f32_e32 v137, v137
v_mfma_f32_16x16x32_bf16 v[160:163], a[4:7], a[72:75], v[160:163]
ds_read_b128 a[28:31], v201 offset:13824
v_cvt_pk_bf16_f32 v188, v136, v137
v_add_f32_e32 v245, v245, v136
v_mfma_f32_16x16x32_bf16 v[168:171], a[4:7], a[76:79], v[168:171]
s_barrier
s_waitcnt lgkmcnt(4)
v_mfma_f32_16x16x32_bf16 v[160:163], a[8:11], a[80:83], v[160:163]
ds_read_b64_tr_b16 a[0:1], v205 offset:32768
v_exp_f32_e32 v138, v138
v_mfma_f32_16x16x32_bf16 v[168:171], a[8:11], a[84:87], v[168:171]
ds_read_b64_tr_b16 a[2:3], v205 offset:36864
v_exp_f32_e32 v139, v139
v_mfma_f32_16x16x32_bf16 v[160:163], a[12:15], a[88:91], v[160:163]
ds_read_b64_tr_b16 a[4:5], v206 offset:32768
v_cvt_pk_bf16_f32 v189, v138, v139
v_mfma_f32_16x16x32_bf16 v[168:171], a[12:15], a[92:95], v[168:171]
s_waitcnt lgkmcnt(5)
ds_read_b64_tr_b16 a[6:7], v206 offset:36864
v_exp_f32_e32 v140, v140
v_mfma_f32_16x16x32_bf16 v[164:167], a[16:19], a[64:67], v[192:195]
ds_read_b64_tr_b16 a[8:9], v205 offset:33280
v_exp_f32_e32 v141, v141
v_mfma_f32_16x16x32_bf16 v[172:175], a[16:19], a[68:71], v[196:199]
ds_read_b64_tr_b16 a[10:11], v205 offset:37376
v_cvt_pk_bf16_f32 v190, v140, v141
v_add_f32_e32 v245, v245, v137
v_mfma_f32_16x16x32_bf16 v[164:167], a[20:23], a[72:75], v[164:167]
ds_read_b64_tr_b16 a[12:13], v206 offset:33280
v_exp_f32_e32 v142, v142
v_mfma_f32_16x16x32_bf16 v[172:175], a[20:23], a[76:79], v[172:175]
s_waitcnt lgkmcnt(7)
ds_read_b64_tr_b16 a[14:15], v206 offset:37376
v_add_f32_e32 v245, v245, v138
v_mfma_f32_16x16x32_bf16 v[164:167], a[24:27], a[80:83], v[164:167]
ds_read_b64_tr_b16 a[16:17], v205 offset:33792
v_exp_f32_e32 v143, v143
v_mfma_f32_16x16x32_bf16 v[172:175], a[24:27], a[84:87], v[172:175]
ds_read_b64_tr_b16 a[18:19], v205 offset:37888
v_cvt_pk_bf16_f32 v191, v142, v143
v_mfma_f32_16x16x32_bf16 v[164:167], a[28:31], a[88:91], v[164:167]
ds_read_b64_tr_b16 a[20:21], v206 offset:33792
v_add_f32_e32 v244, v244, v131
v_add_f32_e32 v245, v245, v139
v_mfma_f32_16x16x32_bf16 v[172:175], a[28:31], a[92:95], v[172:175]
s_waitcnt lgkmcnt(7)
ds_read_b64_tr_b16 a[22:23], v206 offset:37888
v_add_f32_e32 v244, v244, v132
v_add_f32_e32 v245, v245, v140
v_mfma_f32_16x16x32_bf16 v[0:3], a[0:3], v[176:179], v[0:3]
ds_read_b64_tr_b16 a[24:25], v205 offset:34304
v_add_f32_e32 v244, v244, v133
v_mfma_f32_16x16x32_bf16 v[8:11], a[0:3], v[180:183], v[8:11]
ds_read_b64_tr_b16 a[26:27], v205 offset:38400
v_add_f32_e32 v245, v245, v141
v_add_f32_e32 v244, v244, v134
v_mfma_f32_16x16x32_bf16 v[4:7], a[4:7], v[176:179], v[4:7]
ds_read_b64_tr_b16 a[28:29], v206 offset:34304
v_add_f32_e32 v245, v245, v142
v_add_f32_e32 v244, v244, v135
v_mfma_f32_16x16x32_bf16 v[12:15], a[4:7], v[180:183], v[12:15]
s_waitcnt lgkmcnt(7)
ds_read_b64_tr_b16 a[30:31], v206 offset:38400
v_add_f32_e32 v245, v245, v143
v_max3_f32 v236, v144, v145, v146
v_mfma_f32_16x16x32_bf16 v[16:19], a[8:11], v[176:179], v[16:19]
ds_read_b64_tr_b16 a[0:1], v205 offset:40960
v_max3_f32 v237, v152, v153, v154
v_mfma_f32_16x16x32_bf16 v[24:27], a[8:11], v[180:183], v[24:27]
ds_read_b64_tr_b16 a[2:3], v205 offset:45056
v_max3_f32 v236, v236, v147, v148
v_max3_f32 v237, v237, v155, v156
v_mfma_f32_16x16x32_bf16 v[20:23], a[12:15], v[176:179], v[20:23]
ds_read_b64_tr_b16 a[4:5], v206 offset:40960
v_max3_f32 v236, v236, v149, v150
v_max3_f32 v237, v237, v157, v158
v_mfma_f32_16x16x32_bf16 v[28:31], a[12:15], v[180:183], v[28:31]
s_waitcnt lgkmcnt(7)
ds_read_b64_tr_b16 a[6:7], v206 offset:45056
v_max3_f32 v236, v236, v151, v160
v_max3_f32 v237, v237, v159, v168
v_mfma_f32_16x16x32_bf16 v[32:35], a[16:19], v[176:179], v[32:35]
ds_read_b64_tr_b16 a[8:9], v205 offset:41472
v_max3_f32 v236, v236, v161, v162
v_max3_f32 v237, v237, v169, v170
v_mfma_f32_16x16x32_bf16 v[40:43], a[16:19], v[180:183], v[40:43]
ds_read_b64_tr_b16 a[10:11], v205 offset:45568
v_max3_f32 v236, v236, v163, v164
v_mfma_f32_16x16x32_bf16 v[36:39], a[20:23], v[176:179], v[36:39]
ds_read_b64_tr_b16 a[12:13], v206 offset:41472
v_max3_f32 v237, v237, v171, v172
v_max3_f32 v236, v236, v165, v166
v_mfma_f32_16x16x32_bf16 v[44:47], a[20:23], v[180:183], v[44:47]
s_waitcnt lgkmcnt(7)
ds_read_b64_tr_b16 a[14:15], v206 offset:45568
v_max3_f32 v237, v237, v173, v174
v_max3_f32 v248, v236, v167, v175
v_mfma_f32_16x16x32_bf16 v[48:51], a[24:27], v[176:179], v[48:51]
ds_read_b64_tr_b16 a[16:17], v205 offset:41984
v_max_f32_e32 v248, v248, v237
v_cmp_lt_f32_e32 vcc, s70, v248
v_mfma_f32_16x16x32_bf16 v[56:59], a[24:27], v[180:183], v[56:59]
ds_read_b64_tr_b16 a[18:19], v205 offset:46080
v_mfma_f32_16x16x32_bf16 v[52:55], a[28:31], v[176:179], v[52:55]
s_cbranch_vccnz .Lk_ara11
.Lk_aba11:
ds_read_b64_tr_b16 a[20:21], v206 offset:41984
v_mfma_f32_16x16x32_bf16 v[60:63], a[28:31], v[180:183], v[60:63]
s_waitcnt lgkmcnt(7)
ds_read_b64_tr_b16 a[22:23], v206 offset:46080
v_exp_f32_e32 v144, v144
v_mfma_f32_16x16x32_bf16 v[0:3], a[0:3], v[184:187], v[0:3]
ds_read_b64_tr_b16 a[24:25], v205 offset:42496
v_add_f32_e32 v244, v244, v144
v_mfma_f32_16x16x32_bf16 v[8:11], a[0:3], v[188:191], v[8:11]
ds_read_b64_tr_b16 a[26:27], v205 offset:46592
v_exp_f32_e32 v145, v145
s_nop 0
v_add_f32_e32 v244, v244, v145
v_mfma_f32_16x16x32_bf16 v[4:7], a[4:7], v[184:187], v[4:7]
ds_read_b64_tr_b16 a[28:29], v206 offset:42496
v_cvt_pk_bf16_f32 v176, v144, v145
v_mfma_f32_16x16x32_bf16 v[12:15], a[4:7], v[188:191], v[12:15]
s_barrier
ds_read_b64_tr_b16 a[30:31], v206 offset:46592
s_waitcnt lgkmcnt(8)
v_mfma_f32_16x16x32_bf16 v[16:19], a[8:11], v[184:187], v[16:19]
ds_read_b128 a[0:3], v202 offset:16384
v_exp_f32_e32 v146, v146
v_mfma_f32_16x16x32_bf16 v[24:27], a[8:11], v[188:191], v[24:27]
v_add_f32_e32 v244, v244, v146
v_exp_f32_e32 v147, v147
v_mfma_f32_16x16x32_bf16 v[20:23], a[12:15], v[184:187], v[20:23]
ds_read_b128 a[4:7], v202 offset:16896
v_add_f32_e32 v244, v244, v147
v_cvt_pk_bf16_f32 v177, v146, v147
v_mfma_f32_16x16x32_bf16 v[28:31], a[12:15], v[188:191], v[28:31]
s_waitcnt lgkmcnt(6)
v_exp_f32_e32 v152, v152
s_nop 0
v_add_f32_e32 v245, v245, v152
v_mfma_f32_16x16x32_bf16 v[32:35], a[16:19], v[184:187], v[32:35]
ds_read_b128 a[8:11], v202 offset:17408
v_exp_f32_e32 v153, v153
v_mfma_f32_16x16x32_bf16 v[40:43], a[16:19], v[188:191], v[40:43]
v_add_f32_e32 v245, v245, v153
v_cvt_pk_bf16_f32 v180, v152, v153
v_mfma_f32_16x16x32_bf16 v[36:39], a[20:23], v[184:187], v[36:39]
ds_read_b128 a[12:15], v202 offset:17920
v_exp_f32_e32 v154, v154
v_mfma_f32_16x16x32_bf16 v[44:47], a[20:23], v[188:191], v[44:47]
s_waitcnt lgkmcnt(4)
v_add_f32_e32 v245, v245, v154
v_exp_f32_e32 v155, v155
v_mfma_f32_16x16x32_bf16 v[48:51], a[24:27], v[184:187], v[48:51]
ds_read_b128 a[16:19], v202 offset:20480
v_add_f32_e32 v245, v245, v155
v_cvt_pk_bf16_f32 v181, v154, v155
v_mfma_f32_16x16x32_bf16 v[56:59], a[24:27], v[188:191], v[56:59]
v_exp_f32_e32 v148, v148
s_nop 0
v_add_f32_e32 v244, v244, v148
v_mfma_f32_16x16x32_bf16 v[52:55], a[28:31], v[184:187], v[52:55]
ds_read_b128 a[20:23], v202 offset:20992
v_exp_f32_e32 v149, v149
v_mfma_f32_16x16x32_bf16 v[60:63], a[28:31], v[188:191], v[60:63]
s_waitcnt lgkmcnt(4)
v_add_f32_e32 v244, v244, v149
v_cvt_pk_bf16_f32 v178, v148, v149
v_mfma_f32_16x16x32_bf16 v[128:131], a[0:3], a[64:67], v[192:195]
ds_read_b128 a[24:27], v202 offset:21504
v_exp_f32_e32 v150, v150
v_mfma_f32_16x16x32_bf16 v[136:139], a[0:3], a[68:71], v[196:199]
v_add_f32_e32 v244, v244, v150
v_exp_f32_e32 v151, v151
v_mfma_f32_16x16x32_bf16 v[128:131], a[4:7], a[72:75], v[128:131]
ds_read_b128 a[28:31], v202 offset:22016
v_add_f32_e32 v244, v244, v151
v_cvt_pk_bf16_f32 v179, v150, v151
v_mfma_f32_16x16x32_bf16 v[136:139], a[4:7], a[76:79], v[136:139]
s_waitcnt lgkmcnt(4)
v_exp_f32_e32 v156, v156
s_nop 0
v_add_f32_e32 v245, v245, v156
v_mfma_f32_16x16x32_bf16 v[128:131], a[8:11], a[80:83], v[128:131]
)"
R"(
ds_read_b128 a[0:3], v202 offset:24576
v_exp_f32_e32 v157, v157
v_mfma_f32_16x16x32_bf16 v[136:139], a[8:11], a[84:87], v[136:139]
v_add_f32_e32 v245, v245, v157
v_cvt_pk_bf16_f32 v182, v156, v157
v_mfma_f32_16x16x32_bf16 v[128:131], a[12:15], a[88:91], v[128:131]
ds_read_b128 a[4:7], v202 offset:25088
v_exp_f32_e32 v158, v158
v_mfma_f32_16x16x32_bf16 v[136:139], a[12:15], a[92:95], v[136:139]
s_waitcnt lgkmcnt(4)
v_add_f32_e32 v245, v245, v158
v_exp_f32_e32 v159, v159
v_mfma_f32_16x16x32_bf16 v[132:135], a[16:19], a[64:67], v[192:195]
ds_read_b128 a[8:11], v202 offset:25600
v_add_f32_e32 v245, v245, v159
v_cvt_pk_bf16_f32 v183, v158, v159
v_mfma_f32_16x16x32_bf16 v[140:143], a[16:19], a[68:71], v[196:199]
s_cbranch_vccnz .Lk_arb11
.Lk_abb11:
v_mfma_f32_16x16x32_bf16 v[132:135], a[20:23], a[72:75], v[132:135]
ds_read_b128 a[12:15], v202 offset:26112
v_exp_f32_e32 v160, v160
v_mfma_f32_16x16x32_bf16 v[140:143], a[20:23], a[76:79], v[140:143]
s_waitcnt lgkmcnt(4)
v_exp_f32_e32 v161, v161
v_add_f32_e32 v244, v244, v160
v_mfma_f32_16x16x32_bf16 v[132:135], a[24:27], a[80:83], v[132:135]
ds_read_b128 a[16:19], v202 offset:28672
v_cvt_pk_bf16_f32 v184, v160, v161
v_add_f32_e32 v244, v244, v161
v_mfma_f32_16x16x32_bf16 v[140:143], a[24:27], a[84:87], v[140:143]
v_exp_f32_e32 v162, v162
v_max3_f32 v236, v128, v129, v130
v_mfma_f32_16x16x32_bf16 v[132:135], a[28:31], a[88:91], v[132:135]
ds_read_b128 a[20:23], v202 offset:29184
v_exp_f32_e32 v163, v163
v_mfma_f32_16x16x32_bf16 v[140:143], a[28:31], a[92:95], v[140:143]
s_waitcnt lgkmcnt(4)
v_cvt_pk_bf16_f32 v185, v162, v163
v_exp_f32_e32 v164, v164
v_mfma_f32_16x16x32_bf16 v[144:147], a[0:3], a[64:67], v[192:195]
ds_read_b128 a[24:27], v202 offset:29696
v_exp_f32_e32 v165, v165
v_mfma_f32_16x16x32_bf16 v[152:155], a[0:3], a[68:71], v[196:199]
v_cvt_pk_bf16_f32 v186, v164, v165
v_exp_f32_e32 v166, v166
v_mfma_f32_16x16x32_bf16 v[144:147], a[4:7], a[72:75], v[144:147]
ds_read_b128 a[28:31], v202 offset:30208
v_add_f32_e32 v244, v244, v162
v_mfma_f32_16x16x32_bf16 v[152:155], a[4:7], a[76:79], v[152:155]
s_barrier
v_add_f32_e32 v244, v244, v163
s_waitcnt lgkmcnt(4)
v_mfma_f32_16x16x32_bf16 v[144:147], a[8:11], a[80:83], v[144:147]
ds_read_b64_tr_b16 a[0:1], v203
v_exp_f32_e32 v167, v167
v_mfma_f32_16x16x32_bf16 v[152:155], a[8:11], a[84:87], v[152:155]
ds_read_b64_tr_b16 a[2:3], v203 offset:4096
v_cvt_pk_bf16_f32 v187, v166, v167
v_add_f32_e32 v244, v244, v164
v_mfma_f32_16x16x32_bf16 v[144:147], a[12:15], a[88:91], v[144:147]
ds_read_b64_tr_b16 a[4:5], v204
v_exp_f32_e32 v168, v168
v_mfma_f32_16x16x32_bf16 v[152:155], a[12:15], a[92:95], v[152:155]
s_waitcnt lgkmcnt(5)
ds_read_b64_tr_b16 a[6:7], v204 offset:4096
v_exp_f32_e32 v169, v169
v_mfma_f32_16x16x32_bf16 v[148:151], a[16:19], a[64:67], v[192:195]
ds_read_b64_tr_b16 a[8:9], v203 offset:512
v_cvt_pk_bf16_f32 v188, v168, v169
v_mfma_f32_16x16x32_bf16 v[156:159], a[16:19], a[68:71], v[196:199]
ds_read_b64_tr_b16 a[10:11], v203 offset:4608
v_exp_f32_e32 v170, v170
v_mfma_f32_16x16x32_bf16 v[148:151], a[20:23], a[72:75], v[148:151]
ds_read_b64_tr_b16 a[12:13], v204 offset:512
v_exp_f32_e32 v171, v171
v_mfma_f32_16x16x32_bf16 v[156:159], a[20:23], a[76:79], v[156:159]
s_waitcnt lgkmcnt(7)
ds_read_b64_tr_b16 a[14:15], v204 offset:4608
v_cvt_pk_bf16_f32 v189, v170, v171
v_add_f32_e32 v245, v245, v168
v_mfma_f32_16x16x32_bf16 v[148:151], a[24:27], a[80:83], v[148:151]
ds_read_b64_tr_b16 a[16:17], v203 offset:1024
v_exp_f32_e32 v172, v172
v_mfma_f32_16x16x32_bf16 v[156:159], a[24:27], a[84:87], v[156:159]
ds_read_b64_tr_b16 a[18:19], v203 offset:5120
v_exp_f32_e32 v173, v173
v_mfma_f32_16x16x32_bf16 v[148:151], a[28:31], a[88:91], v[148:151]
ds_read_b64_tr_b16 a[20:21], v204 offset:1024
v_cvt_pk_bf16_f32 v190, v172, v173
v_mfma_f32_16x16x32_bf16 v[156:159], a[28:31], a[92:95], v[156:159]
s_waitcnt lgkmcnt(7)
ds_read_b64_tr_b16 a[22:23], v204 offset:5120
v_exp_f32_e32 v174, v174
v_mfma_f32_16x16x32_bf16 v[0:3], a[0:3], v[176:179], v[0:3]
ds_read_b64_tr_b16 a[24:25], v203 offset:1536
v_exp_f32_e32 v175, v175
v_mfma_f32_16x16x32_bf16 v[8:11], a[0:3], v[180:183], v[8:11]
ds_read_b64_tr_b16 a[26:27], v203 offset:5632
v_cvt_pk_bf16_f32 v191, v174, v175
v_add_f32_e32 v245, v245, v169
v_mfma_f32_16x16x32_bf16 v[4:7], a[4:7], v[176:179], v[4:7]
ds_read_b64_tr_b16 a[28:29], v204 offset:1536
v_add_f32_e32 v245, v245, v170
v_add_f32_e32 v245, v245, v171
v_mfma_f32_16x16x32_bf16 v[12:15], a[4:7], v[180:183], v[12:15]
s_waitcnt lgkmcnt(7)
ds_read_b64_tr_b16 a[30:31], v204 offset:5632
v_add_f32_e32 v245, v245, v172
v_add_f32_e32 v244, v244, v165
v_mfma_f32_16x16x32_bf16 v[16:19], a[8:11], v[176:179], v[16:19]
ds_read_b64_tr_b16 a[0:1], v203 offset:8192
v_add_f32_e32 v245, v245, v173
v_add_f32_e32 v244, v244, v166
v_mfma_f32_16x16x32_bf16 v[24:27], a[8:11], v[180:183], v[24:27]
ds_read_b64_tr_b16 a[2:3], v203 offset:12288
v_add_f32_e32 v245, v245, v174
v_add_f32_e32 v244, v244, v167
v_mfma_f32_16x16x32_bf16 v[20:23], a[12:15], v[176:179], v[20:23]
ds_read_b64_tr_b16 a[4:5], v204 offset:8192
v_add_f32_e32 v245, v245, v175
v_max3_f32 v237, v136, v137, v138
v_mfma_f32_16x16x32_bf16 v[28:31], a[12:15], v[180:183], v[28:31]
s_waitcnt lgkmcnt(7)
ds_read_b64_tr_b16 a[6:7], v204 offset:12288
v_max3_f32 v236, v236, v131, v132
v_max3_f32 v237, v237, v139, v140
v_mfma_f32_16x16x32_bf16 v[32:35], a[16:19], v[176:179], v[32:35]
ds_read_b64_tr_b16 a[8:9], v203 offset:8704
v_max3_f32 v236, v236, v133, v134
v_max3_f32 v237, v237, v141, v142
v_mfma_f32_16x16x32_bf16 v[40:43], a[16:19], v[180:183], v[40:43]
ds_read_b64_tr_b16 a[10:11], v203 offset:12800
v_max3_f32 v236, v236, v135, v144
v_max3_f32 v237, v237, v143, v152
v_mfma_f32_16x16x32_bf16 v[36:39], a[20:23], v[176:179], v[36:39]
ds_read_b64_tr_b16 a[12:13], v204 offset:8704
v_max3_f32 v236, v236, v145, v146
v_mfma_f32_16x16x32_bf16 v[44:47], a[20:23], v[180:183], v[44:47]
s_waitcnt lgkmcnt(7)
ds_read_b64_tr_b16 a[14:15], v204 offset:12800
v_max3_f32 v237, v237, v153, v154
v_max3_f32 v236, v236, v147, v148
v_mfma_f32_16x16x32_bf16 v[48:51], a[24:27], v[176:179], v[48:51]
ds_read_b64_tr_b16 a[16:17], v203 offset:9216
v_max3_f32 v237, v237, v155, v156
v_max3_f32 v236, v236, v149, v150
v_mfma_f32_16x16x32_bf16 v[56:59], a[24:27], v[180:183], v[56:59]
ds_read_b64_tr_b16 a[18:19], v203 offset:13312
v_max3_f32 v237, v237, v157, v158
v_max3_f32 v248, v236, v151, v159
v_mfma_f32_16x16x32_bf16 v[52:55], a[28:31], v[176:179], v[52:55]
ds_read_b64_tr_b16 a[20:21], v204 offset:9216
v_max_f32_e32 v248, v248, v237
v_cmp_lt_f32_e32 vcc, s70, v248
v_mfma_f32_16x16x32_bf16 v[60:63], a[28:31], v[180:183], v[60:63]
s_nop 0
s_cbranch_vccnz .Lk_ara12
.Lk_aba12:
ds_read_b64_tr_b16 a[22:23], v204 offset:13312
s_waitcnt lgkmcnt(8)
v_mfma_f32_16x16x32_bf16 v[0:3], a[0:3], v[184:187], v[0:3]
ds_read_b64_tr_b16 a[24:25], v203 offset:9728
v_mfma_f32_16x16x32_bf16 v[8:11], a[0:3], v[188:191], v[8:11]
ds_read_b64_tr_b16 a[26:27], v203 offset:13824
v_exp_f32_e32 v128, v128
s_nop 0
v_add_f32_e32 v244, v244, v128
v_mfma_f32_16x16x32_bf16 v[4:7], a[4:7], v[184:187], v[4:7]
ds_read_b64_tr_b16 a[28:29], v204 offset:9728
v_exp_f32_e32 v129, v129
v_mfma_f32_16x16x32_bf16 v[12:15], a[4:7], v[188:191], v[12:15]
s_barrier
ds_read_b64_tr_b16 a[30:31], v204 offset:13824
s_waitcnt lgkmcnt(8)
v_mfma_f32_16x16x32_bf16 v[16:19], a[8:11], v[184:187], v[16:19]
ds_read_b128 a[0:3], v200 offset:49152
v_add_f32_e32 v244, v244, v129
v_cvt_pk_bf16_f32 v176, v128, v129
v_mfma_f32_16x16x32_bf16 v[24:27], a[8:11], v[188:191], v[24:27]
v_exp_f32_e32 v130, v130
s_nop 0
v_add_f32_e32 v244, v244, v130
v_mfma_f32_16x16x32_bf16 v[20:23], a[12:15], v[184:187], v[20:23]
ds_read_b128 a[4:7], v200 offset:49664
v_exp_f32_e32 v131, v131
v_mfma_f32_16x16x32_bf16 v[28:31], a[12:15], v[188:191], v[28:31]
s_waitcnt lgkmcnt(6)
v_add_f32_e32 v244, v244, v131
v_cvt_pk_bf16_f32 v177, v130, v131
v_mfma_f32_16x16x32_bf16 v[32:35], a[16:19], v[184:187], v[32:35]
ds_read_b128 a[8:11], v200 offset:50176
v_exp_f32_e32 v136, v136
v_mfma_f32_16x16x32_bf16 v[40:43], a[16:19], v[188:191], v[40:43]
v_add_f32_e32 v245, v245, v136
v_exp_f32_e32 v137, v137
v_mfma_f32_16x16x32_bf16 v[36:39], a[20:23], v[184:187], v[36:39]
ds_read_b128 a[12:15], v200 offset:50688
v_add_f32_e32 v245, v245, v137
v_cvt_pk_bf16_f32 v180, v136, v137
)"
R"(
v_mfma_f32_16x16x32_bf16 v[44:47], a[20:23], v[188:191], v[44:47]
s_waitcnt lgkmcnt(4)
v_exp_f32_e32 v138, v138
v_exp_f32_e32 v139, v139
v_mfma_f32_16x16x32_bf16 v[48:51], a[24:27], v[184:187], v[48:51]
ds_read_b128 a[16:19], v200 offset:53248
v_add_f32_e32 v245, v245, v138
v_mfma_f32_16x16x32_bf16 v[56:59], a[24:27], v[188:191], v[56:59]
v_add_f32_e32 v245, v245, v139
v_cvt_pk_bf16_f32 v181, v138, v139
v_mfma_f32_16x16x32_bf16 v[52:55], a[28:31], v[184:187], v[52:55]
ds_read_b128 a[20:23], v200 offset:53760
v_exp_f32_e32 v132, v132
s_nop 0
v_add_f32_e32 v244, v244, v132
v_mfma_f32_16x16x32_bf16 v[60:63], a[28:31], v[188:191], v[60:63]
s_waitcnt lgkmcnt(4)
v_exp_f32_e32 v133, v133
s_nop 0
v_add_f32_e32 v244, v244, v133
v_mfma_f32_16x16x32_bf16 v[160:163], a[0:3], a[64:67], v[192:195]
ds_read_b128 a[24:27], v200 offset:54272
v_cvt_pk_bf16_f32 v178, v132, v133
v_mfma_f32_16x16x32_bf16 v[168:171], a[0:3], a[68:71], v[196:199]
v_exp_f32_e32 v134, v134
v_exp_f32_e32 v135, v135
v_mfma_f32_16x16x32_bf16 v[160:163], a[4:7], a[72:75], v[160:163]
ds_read_b128 a[28:31], v200 offset:54784
v_add_f32_e32 v244, v244, v134
v_mfma_f32_16x16x32_bf16 v[168:171], a[4:7], a[76:79], v[168:171]
s_waitcnt lgkmcnt(4)
v_add_f32_e32 v244, v244, v135
v_cvt_pk_bf16_f32 v179, v134, v135
v_exp_f32_e32 v140, v140
v_mfma_f32_16x16x32_bf16 v[160:163], a[8:11], a[80:83], v[160:163]
ds_read_b128 a[0:3], v200 offset:57344
v_add_f32_e32 v245, v245, v140
v_mfma_f32_16x16x32_bf16 v[168:171], a[8:11], a[84:87], v[168:171]
s_mov_b32 exec_lo, 0
s_mov_b32 exec_hi, 0xff0000
buffer_load_dword v251, v252, s[52:55], 0 offen
s_mov_b64 exec, -1
v_mfma_f32_16x16x32_bf16 v[160:163], a[12:15], a[88:91], v[160:163]
ds_read_b128 a[4:7], v200 offset:57856
v_mfma_f32_16x16x32_bf16 v[168:171], a[12:15], a[92:95], v[168:171]
s_waitcnt lgkmcnt(4)
v_exp_f32_e32 v141, v141
s_nop 0
v_add_f32_e32 v245, v245, v141
v_mfma_f32_16x16x32_bf16 v[164:167], a[16:19], a[64:67], v[192:195]
ds_read_b128 a[8:11], v200 offset:58368
v_cvt_pk_bf16_f32 v182, v140, v141
v_mfma_f32_16x16x32_bf16 v[172:175], a[16:19], a[68:71], v[196:199]
v_exp_f32_e32 v142, v142
v_exp_f32_e32 v143, v143
v_mfma_f32_16x16x32_bf16 v[164:167], a[20:23], a[72:75], v[164:167]
ds_read_b128 a[12:15], v200 offset:58880
v_add_f32_e32 v245, v245, v142
v_add_f32_e32 v245, v245, v143
v_mfma_f32_16x16x32_bf16 v[172:175], a[20:23], a[76:79], v[172:175]
s_waitcnt lgkmcnt(4)
v_cvt_pk_bf16_f32 v183, v142, v143
v_exp_f32_e32 v144, v144
v_mfma_f32_16x16x32_bf16 v[164:167], a[24:27], a[80:83], v[164:167]
ds_read_b128 a[16:19], v200 offset:61440
v_exp_f32_e32 v145, v145
v_mfma_f32_16x16x32_bf16 v[172:175], a[24:27], a[84:87], v[172:175]
s_cbranch_vccnz .Lk_arb12
.Lk_abb12:
v_mfma_f32_16x16x32_bf16 v[164:167], a[28:31], a[88:91], v[164:167]
ds_read_b128 a[20:23], v200 offset:61952
v_cvt_pk_bf16_f32 v184, v144, v145
v_mfma_f32_16x16x32_bf16 v[172:175], a[28:31], a[92:95], v[172:175]
s_waitcnt lgkmcnt(4)
v_exp_f32_e32 v146, v146
v_exp_f32_e32 v147, v147
v_mfma_f32_16x16x32_bf16 v[128:131], a[0:3], a[64:67], v[192:195]
ds_read_b128 a[24:27], v200 offset:62464
v_cvt_pk_bf16_f32 v185, v146, v147
v_mfma_f32_16x16x32_bf16 v[136:139], a[0:3], a[68:71], v[196:199]
v_exp_f32_e32 v148, v148
v_add_f32_e32 v244, v244, v144
v_mfma_f32_16x16x32_bf16 v[128:131], a[4:7], a[72:75], v[128:131]
ds_read_b128 a[28:31], v200 offset:62976
v_exp_f32_e32 v149, v149
v_mfma_f32_16x16x32_bf16 v[136:139], a[4:7], a[76:79], v[136:139]
s_barrier
v_cvt_pk_bf16_f32 v186, v148, v149
s_waitcnt lgkmcnt(4)
v_mfma_f32_16x16x32_bf16 v[128:131], a[8:11], a[80:83], v[128:131]
ds_read_b64_tr_b16 a[0:1], v203 offset:32768
v_exp_f32_e32 v150, v150
v_mfma_f32_16x16x32_bf16 v[136:139], a[8:11], a[84:87], v[136:139]
ds_read_b64_tr_b16 a[2:3], v203 offset:36864
v_exp_f32_e32 v151, v151
v_mfma_f32_16x16x32_bf16 v[128:131], a[12:15], a[88:91], v[128:131]
ds_read_b64_tr_b16 a[4:5], v204 offset:32768
v_cvt_pk_bf16_f32 v187, v150, v151
v_add_f32_e32 v244, v244, v145
v_mfma_f32_16x16x32_bf16 v[136:139], a[12:15], a[92:95], v[136:139]
s_waitcnt lgkmcnt(5)
ds_read_b64_tr_b16 a[6:7], v204 offset:36864
v_exp_f32_e32 v152, v152
v_mfma_f32_16x16x32_bf16 v[132:135], a[16:19], a[64:67], v[192:195]
ds_read_b64_tr_b16 a[8:9], v203 offset:33280
v_exp_f32_e32 v153, v153
v_mfma_f32_16x16x32_bf16 v[140:143], a[16:19], a[68:71], v[196:199]
ds_read_b64_tr_b16 a[10:11], v203 offset:37376
v_cvt_pk_bf16_f32 v188, v152, v153
v_mfma_f32_16x16x32_bf16 v[132:135], a[20:23], a[72:75], v[132:135]
ds_read_b64_tr_b16 a[12:13], v204 offset:33280
v_exp_f32_e32 v154, v154
v_mfma_f32_16x16x32_bf16 v[140:143], a[20:23], a[76:79], v[140:143]
s_waitcnt lgkmcnt(7)
ds_read_b64_tr_b16 a[14:15], v204 offset:37376
v_exp_f32_e32 v155, v155
v_mfma_f32_16x16x32_bf16 v[132:135], a[24:27], a[80:83], v[132:135]
ds_read_b64_tr_b16 a[16:17], v203 offset:33792
v_cvt_pk_bf16_f32 v189, v154, v155
v_add_f32_e32 v245, v245, v152
v_mfma_f32_16x16x32_bf16 v[140:143], a[24:27], a[84:87], v[140:143]
ds_read_b64_tr_b16 a[18:19], v203 offset:37888
v_exp_f32_e32 v156, v156
v_mfma_f32_16x16x32_bf16 v[132:135], a[28:31], a[88:91], v[132:135]
ds_read_b64_tr_b16 a[20:21], v204 offset:33792
v_exp_f32_e32 v157, v157
v_mfma_f32_16x16x32_bf16 v[140:143], a[28:31], a[92:95], v[140:143]
s_waitcnt lgkmcnt(7)
ds_read_b64_tr_b16 a[22:23], v204 offset:37888
v_cvt_pk_bf16_f32 v190, v156, v157
v_add_f32_e32 v245, v245, v153
v_mfma_f32_16x16x32_bf16 v[0:3], a[0:3], v[176:179], v[0:3]
ds_read_b64_tr_b16 a[24:25], v203 offset:34304
v_exp_f32_e32 v158, v158
v_mfma_f32_16x16x32_bf16 v[8:11], a[0:3], v[180:183], v[8:11]
ds_read_b64_tr_b16 a[26:27], v203 offset:38400
v_exp_f32_e32 v159, v159
v_mfma_f32_16x16x32_bf16 v[4:7], a[4:7], v[176:179], v[4:7]
ds_read_b64_tr_b16 a[28:29], v204 offset:34304
v_cvt_pk_bf16_f32 v191, v158, v159
v_add_f32_e32 v244, v244, v146
v_mfma_f32_16x16x32_bf16 v[12:15], a[4:7], v[180:183], v[12:15]
s_waitcnt lgkmcnt(7)
ds_read_b64_tr_b16 a[30:31], v204 offset:38400
v_add_f32_e32 v245, v245, v154
v_add_f32_e32 v244, v244, v147
v_add_f32_e32 v245, v245, v155
v_mfma_f32_16x16x32_bf16 v[16:19], a[8:11], v[176:179], v[16:19]
ds_read_b64_tr_b16 a[0:1], v203 offset:40960
v_add_f32_e32 v244, v244, v148
v_add_f32_e32 v245, v245, v156
v_mfma_f32_16x16x32_bf16 v[24:27], a[8:11], v[180:183], v[24:27]
ds_read_b64_tr_b16 a[2:3], v203 offset:45056
v_add_f32_e32 v244, v244, v149
v_add_f32_e32 v245, v245, v157
v_add_f32_e32 v244, v244, v150
v_mfma_f32_16x16x32_bf16 v[20:23], a[12:15], v[176:179], v[20:23]
ds_read_b64_tr_b16 a[4:5], v204 offset:40960
v_add_f32_e32 v245, v245, v158
v_add_f32_e32 v244, v244, v151
v_mfma_f32_16x16x32_bf16 v[28:31], a[12:15], v[180:183], v[28:31]
s_waitcnt lgkmcnt(7)
ds_read_b64_tr_b16 a[6:7], v204 offset:45056
v_add_f32_e32 v245, v245, v159
v_max3_f32 v236, v160, v161, v162
v_max3_f32 v237, v168, v169, v170
v_mfma_f32_16x16x32_bf16 v[32:35], a[16:19], v[176:179], v[32:35]
ds_read_b64_tr_b16 a[8:9], v203 offset:41472
v_max3_f32 v236, v236, v163, v164
v_max3_f32 v237, v237, v171, v172
v_mfma_f32_16x16x32_bf16 v[40:43], a[16:19], v[180:183], v[40:43]
ds_read_b64_tr_b16 a[10:11], v203 offset:45568
v_max3_f32 v236, v236, v165, v166
v_max3_f32 v237, v237, v173, v174
v_max3_f32 v236, v236, v167, v128
v_mfma_f32_16x16x32_bf16 v[36:39], a[20:23], v[176:179], v[36:39]
ds_read_b64_tr_b16 a[12:13], v204 offset:41472
v_max3_f32 v237, v237, v175, v136
v_max3_f32 v236, v236, v129, v130
v_mfma_f32_16x16x32_bf16 v[44:47], a[20:23], v[180:183], v[44:47]
s_waitcnt lgkmcnt(7)
ds_read_b64_tr_b16 a[14:15], v204 offset:45568
v_max3_f32 v237, v237, v137, v138
v_max3_f32 v236, v236, v131, v132
v_max3_f32 v237, v237, v139, v140
v_mfma_f32_16x16x32_bf16 v[48:51], a[24:27], v[176:179], v[48:51]
ds_read_b64_tr_b16 a[16:17], v203 offset:41984
v_max3_f32 v236, v236, v133, v134
v_max3_f32 v237, v237, v141, v142
v_mfma_f32_16x16x32_bf16 v[56:59], a[24:27], v[180:183], v[56:59]
ds_read_b64_tr_b16 a[18:19], v203 offset:46080
v_max3_f32 v248, v236, v135, v143
v_max_f32_e32 v248, v248, v237
v_cmp_lt_f32_e32 vcc, s70, v248
v_mfma_f32_16x16x32_bf16 v[52:55], a[28:31], v[176:179], v[52:55]
s_nop 0
s_cbranch_vccnz .Lk_ara13
.Lk_aba13:
ds_read_b64_tr_b16 a[20:21], v204 offset:41984
v_mfma_f32_16x16x32_bf16 v[60:63], a[28:31], v[180:183], v[60:63]
)"
R"(
s_waitcnt lgkmcnt(7)
ds_read_b64_tr_b16 a[22:23], v204 offset:46080
v_exp_f32_e32 v160, v160
v_mfma_f32_16x16x32_bf16 v[0:3], a[0:3], v[184:187], v[0:3]
ds_read_b64_tr_b16 a[24:25], v203 offset:42496
v_add_f32_e32 v244, v244, v160
v_mfma_f32_16x16x32_bf16 v[8:11], a[0:3], v[188:191], v[8:11]
ds_read_b64_tr_b16 a[26:27], v203 offset:46592
v_exp_f32_e32 v161, v161
v_exp_f32_e32 v162, v162
v_mfma_f32_16x16x32_bf16 v[4:7], a[4:7], v[184:187], v[4:7]
ds_read_b64_tr_b16 a[28:29], v204 offset:42496
v_add_f32_e32 v244, v244, v161
v_cvt_pk_bf16_f32 v176, v160, v161
v_mfma_f32_16x16x32_bf16 v[12:15], a[4:7], v[188:191], v[12:15]
s_barrier
ds_read_b64_tr_b16 a[30:31], v204 offset:46592
s_waitcnt lgkmcnt(8)
v_mfma_f32_16x16x32_bf16 v[16:19], a[8:11], v[184:187], v[16:19]
s_add_u32 s71, s81, 0
buffer_store_dwordx4 a[32:35], v229, s[56:59], s71 offen sc1
ds_read_b128 a[0:3], v201 offset:49152
v_mfma_f32_16x16x32_bf16 v[24:27], a[8:11], v[188:191], v[24:27]
v_add_f32_e32 v244, v244, v162
v_exp_f32_e32 v163, v163
v_mfma_f32_16x16x32_bf16 v[20:23], a[12:15], v[184:187], v[20:23]
ds_read_b128 a[4:7], v201 offset:49664
buffer_store_dwordx4 a[36:39], v229, s[56:59], s71 offen offset:1024 sc1
v_mfma_f32_16x16x32_bf16 v[28:31], a[12:15], v[188:191], v[28:31]
s_waitcnt lgkmcnt(6)
v_add_f32_e32 v244, v244, v163
v_cvt_pk_bf16_f32 v177, v162, v163
v_exp_f32_e32 v168, v168
v_mfma_f32_16x16x32_bf16 v[32:35], a[16:19], v[184:187], v[32:35]
ds_read_b128 a[8:11], v201 offset:50176
buffer_store_dwordx4 a[40:43], v229, s[56:59], s71 offen offset:2048 sc1
v_mfma_f32_16x16x32_bf16 v[40:43], a[16:19], v[188:191], v[40:43]
v_add_f32_e32 v245, v245, v168
v_exp_f32_e32 v169, v169
v_mfma_f32_16x16x32_bf16 v[36:39], a[20:23], v[184:187], v[36:39]
ds_read_b128 a[12:15], v201 offset:50688
buffer_store_dwordx4 a[44:47], v229, s[56:59], s71 offen offset:3072 sc1
v_mfma_f32_16x16x32_bf16 v[44:47], a[20:23], v[188:191], v[44:47]
s_waitcnt lgkmcnt(4)
v_add_f32_e32 v245, v245, v169
v_cvt_pk_bf16_f32 v180, v168, v169
v_mfma_f32_16x16x32_bf16 v[48:51], a[24:27], v[184:187], v[48:51]
s_add_u32 s71, s81, 0x1000
buffer_store_dwordx4 a[48:51], v229, s[56:59], s71 offen sc1
ds_read_b128 a[16:19], v201 offset:53248
v_mfma_f32_16x16x32_bf16 v[56:59], a[24:27], v[188:191], v[56:59]
v_exp_f32_e32 v170, v170
v_exp_f32_e32 v171, v171
v_mfma_f32_16x16x32_bf16 v[52:55], a[28:31], v[184:187], v[52:55]
ds_read_b128 a[20:23], v201 offset:53760
buffer_store_dwordx4 a[52:55], v229, s[56:59], s71 offen offset:1024 sc1
v_mfma_f32_16x16x32_bf16 v[60:63], a[28:31], v[188:191], v[60:63]
s_waitcnt lgkmcnt(4)
v_add_f32_e32 v245, v245, v170
v_add_f32_e32 v245, v245, v171
v_cvt_pk_bf16_f32 v181, v170, v171
v_mfma_f32_16x16x32_bf16 v[144:147], a[0:3], a[64:67], v[192:195]
ds_read_b128 a[24:27], v201 offset:54272
buffer_store_dwordx4 a[56:59], v229, s[56:59], s71 offen offset:2048 sc1
v_mfma_f32_16x16x32_bf16 v[152:155], a[0:3], a[68:71], v[196:199]
v_exp_f32_e32 v164, v164
v_exp_f32_e32 v165, v165
v_mfma_f32_16x16x32_bf16 v[144:147], a[4:7], a[72:75], v[144:147]
ds_read_b128 a[28:31], v201 offset:54784
buffer_store_dwordx4 a[60:63], v229, s[56:59], s71 offen offset:3072 sc1
v_mfma_f32_16x16x32_bf16 v[152:155], a[4:7], a[76:79], v[152:155]
s_waitcnt lgkmcnt(4)
v_add_f32_e32 v244, v244, v164
v_add_f32_e32 v244, v244, v165
v_cvt_pk_bf16_f32 v178, v164, v165
v_mfma_f32_16x16x32_bf16 v[144:147], a[8:11], a[80:83], v[144:147]
ds_read_b128 a[0:3], v201 offset:57344
v_exp_f32_e32 v166, v166
s_nop 0
v_add_f32_e32 v244, v244, v166
v_mfma_f32_16x16x32_bf16 v[152:155], a[8:11], a[84:87], v[152:155]
v_exp_f32_e32 v167, v167
v_exp_f32_e32 v172, v172
v_mfma_f32_16x16x32_bf16 v[144:147], a[12:15], a[88:91], v[144:147]
ds_read_b128 a[4:7], v201 offset:57856
v_add_f32_e32 v244, v244, v167
v_cvt_pk_bf16_f32 v179, v166, v167
v_mfma_f32_16x16x32_bf16 v[152:155], a[12:15], a[92:95], v[152:155]
s_waitcnt lgkmcnt(4)
v_add_f32_e32 v245, v245, v172
v_exp_f32_e32 v173, v173
s_nop 0
v_add_f32_e32 v245, v245, v173
v_mfma_f32_16x16x32_bf16 v[148:151], a[16:19], a[64:67], v[192:195]
ds_read_b128 a[8:11], v201 offset:58368
v_cvt_pk_bf16_f32 v182, v172, v173
v_exp_f32_e32 v174, v174
v_mfma_f32_16x16x32_bf16 v[156:159], a[16:19], a[68:71], v[196:199]
v_add_f32_e32 v245, v245, v174
v_exp_f32_e32 v175, v175
v_mfma_f32_16x16x32_bf16 v[148:151], a[20:23], a[72:75], v[148:151]
ds_read_b128 a[12:15], v201 offset:58880
v_add_f32_e32 v245, v245, v175
v_cvt_pk_bf16_f32 v183, v174, v175
v_max3_f32 v236, v144, v145, v146
v_mfma_f32_16x16x32_bf16 v[156:159], a[20:23], a[76:79], v[156:159]
s_cbranch_vccnz .Lk_arb13
.Lk_abb13:
v_max3_f32 v237, v152, v153, v154
s_waitcnt lgkmcnt(4)
v_mfma_f32_16x16x32_bf16 v[148:151], a[24:27], a[80:83], v[148:151]
ds_read_b128 a[16:19], v201 offset:61440
v_exp_f32_e32 v128, v128
v_mfma_f32_16x16x32_bf16 v[156:159], a[24:27], a[84:87], v[156:159]
v_exp_f32_e32 v129, v129
v_exp_f32_e32 v130, v130
v_mfma_f32_16x16x32_bf16 v[148:151], a[28:31], a[88:91], v[148:151]
ds_read_b128 a[20:23], v201 offset:61952
v_cvt_pk_bf16_f32 v184, v128, v129
v_exp_f32_e32 v131, v131
v_mfma_f32_16x16x32_bf16 v[156:159], a[28:31], a[92:95], v[156:159]
s_waitcnt lgkmcnt(4)
v_cvt_pk_bf16_f32 v185, v130, v131
v_exp_f32_e32 v132, v132
v_add_f32_e32 v244, v244, v128
v_mfma_f32_16x16x32_bf16 v[160:163], a[0:3], a[64:67], v[192:195]
ds_read_b128 a[24:27], v201 offset:62464
v_exp_f32_e32 v133, v133
v_mfma_f32_16x16x32_bf16 v[168:171], a[0:3], a[68:71], v[196:199]
v_cvt_pk_bf16_f32 v186, v132, v133
v_exp_f32_e32 v134, v134
v_exp_f32_e32 v135, v135
v_mfma_f32_16x16x32_bf16 v[160:163], a[4:7], a[72:75], v[160:163]
ds_read_b128 a[28:31], v201 offset:62976
v_cvt_pk_bf16_f32 v187, v134, v135
v_exp_f32_e32 v136, v136
v_mfma_f32_16x16x32_bf16 v[168:171], a[4:7], a[76:79], v[168:171]
s_barrier
v_add_f32_e32 v245, v245, v136
s_waitcnt lgkmcnt(4)
v_mfma_f32_16x16x32_bf16 v[160:163], a[8:11], a[80:83], v[160:163]
ds_read_b64_tr_b16 a[0:1], v205
v_exp_f32_e32 v137, v137
v_add_f32_e32 v244, v244, v129
v_mfma_f32_16x16x32_bf16 v[168:171], a[8:11], a[84:87], v[168:171]
ds_read_b64_tr_b16 a[2:3], v205 offset:4096
v_cvt_pk_bf16_f32 v188, v136, v137
v_exp_f32_e32 v138, v138
v_mfma_f32_16x16x32_bf16 v[160:163], a[12:15], a[88:91], v[160:163]
ds_read_b64_tr_b16 a[4:5], v206
v_exp_f32_e32 v139, v139
v_add_f32_e32 v245, v245, v137
v_mfma_f32_16x16x32_bf16 v[168:171], a[12:15], a[92:95], v[168:171]
s_waitcnt lgkmcnt(5)
ds_read_b64_tr_b16 a[6:7], v206 offset:4096
v_cvt_pk_bf16_f32 v189, v138, v139
v_exp_f32_e32 v140, v140
v_mfma_f32_16x16x32_bf16 v[164:167], a[16:19], a[64:67], v[192:195]
ds_read_b64_tr_b16 a[8:9], v205 offset:512
v_exp_f32_e32 v141, v141
v_add_f32_e32 v244, v244, v130
v_mfma_f32_16x16x32_bf16 v[172:175], a[16:19], a[68:71], v[196:199]
ds_read_b64_tr_b16 a[10:11], v205 offset:4608
v_cvt_pk_bf16_f32 v190, v140, v141
v_exp_f32_e32 v142, v142
v_mfma_f32_16x16x32_bf16 v[164:167], a[20:23], a[72:75], v[164:167]
ds_read_b64_tr_b16 a[12:13], v206 offset:512
v_exp_f32_e32 v143, v143
v_add_f32_e32 v245, v245, v138
v_mfma_f32_16x16x32_bf16 v[172:175], a[20:23], a[76:79], v[172:175]
s_waitcnt lgkmcnt(7)
ds_read_b64_tr_b16 a[14:15], v206 offset:4608
v_cvt_pk_bf16_f32 v191, v142, v143
v_add_f32_e32 v244, v244, v131
v_mfma_f32_16x16x32_bf16 v[164:167], a[24:27], a[80:83], v[164:167]
ds_read_b64_tr_b16 a[16:17], v205 offset:1024
v_add_f32_e32 v245, v245, v139
v_add_f32_e32 v244, v244, v132
v_add_f32_e32 v245, v245, v140
v_mfma_f32_16x16x32_bf16 v[172:175], a[24:27], a[84:87], v[172:175]
ds_read_b64_tr_b16 a[18:19], v205 offset:5120
v_add_f32_e32 v244, v244, v133
v_add_f32_e32 v245, v245, v141
v_add_f32_e32 v244, v244, v134
v_mfma_f32_16x16x32_bf16 v[164:167], a[28:31], a[88:91], v[164:167]
ds_read_b64_tr_b16 a[20:21], v206 offset:1024
v_add_f32_e32 v245, v245, v142
v_add_f32_e32 v244, v244, v135
v_add_f32_e32 v245, v245, v143
v_mfma_f32_16x16x32_bf16 v[172:175], a[28:31], a[92:95], v[172:175]
s_waitcnt lgkmcnt(7)
ds_read_b64_tr_b16 a[22:23], v206 offset:5120
v_max3_f32 v236, v236, v147, v148
v_max3_f32 v237, v237, v155, v156
v_max3_f32 v236, v236, v149, v150
v_mfma_f32_16x16x32_bf16 v[0:3], a[0:3], v[176:179], v[0:3]
ds_read_b64_tr_b16 a[24:25], v205 offset:1536
v_max3_f32 v237, v237, v157, v158
v_max3_f32 v236, v236, v151, v160
v_max3_f32 v237, v237, v159, v168
)"
R"(
v_mfma_f32_16x16x32_bf16 v[8:11], a[0:3], v[180:183], v[8:11]
ds_read_b64_tr_b16 a[26:27], v205 offset:5632
v_max3_f32 v236, v236, v161, v162
v_max3_f32 v237, v237, v169, v170
v_mfma_f32_16x16x32_bf16 v[4:7], a[4:7], v[176:179], v[4:7]
ds_read_b64_tr_b16 a[28:29], v206 offset:1536
v_max3_f32 v236, v236, v163, v164
v_max3_f32 v236, v236, v165, v166
v_mfma_f32_16x16x32_bf16 v[12:15], a[4:7], v[180:183], v[12:15]
s_waitcnt lgkmcnt(7)
ds_read_b64_tr_b16 a[30:31], v206 offset:5632
v_max3_f32 v237, v237, v171, v172
v_max3_f32 v237, v237, v173, v174
v_max3_f32 v248, v236, v167, v175
v_max_f32_e32 v248, v248, v237
v_cmp_lt_f32_e32 vcc, s70, v248
v_mfma_f32_16x16x32_bf16 v[16:19], a[8:11], v[176:179], v[16:19]
s_nop 0
s_cbranch_vccnz .Lk_ara14
.Lk_aba14:
ds_read_b64_tr_b16 a[0:1], v205 offset:8192
v_mfma_f32_16x16x32_bf16 v[24:27], a[8:11], v[180:183], v[24:27]
ds_read_b64_tr_b16 a[2:3], v205 offset:12288
v_exp_f32_e32 v144, v144
s_nop 0
v_add_f32_e32 v244, v244, v144
v_mfma_f32_16x16x32_bf16 v[20:23], a[12:15], v[176:179], v[20:23]
ds_read_b64_tr_b16 a[4:5], v206 offset:8192
v_exp_f32_e32 v145, v145
s_nop 0
v_add_f32_e32 v244, v244, v145
v_mfma_f32_16x16x32_bf16 v[28:31], a[12:15], v[180:183], v[28:31]
s_waitcnt lgkmcnt(7)
ds_read_b64_tr_b16 a[6:7], v206 offset:12288
v_exp_f32_e32 v146, v146
s_nop 0
v_add_f32_e32 v244, v244, v146
v_mfma_f32_16x16x32_bf16 v[32:35], a[16:19], v[176:179], v[32:35]
ds_read_b64_tr_b16 a[8:9], v205 offset:8704
v_exp_f32_e32 v147, v147
s_nop 0
v_add_f32_e32 v244, v244, v147
v_mfma_f32_16x16x32_bf16 v[40:43], a[16:19], v[180:183], v[40:43]
ds_read_b64_tr_b16 a[10:11], v205 offset:12800
v_exp_f32_e32 v152, v152
s_nop 0
v_add_f32_e32 v245, v245, v152
v_mfma_f32_16x16x32_bf16 v[36:39], a[20:23], v[176:179], v[36:39]
ds_read_b64_tr_b16 a[12:13], v206 offset:8704
v_exp_f32_e32 v153, v153
s_nop 0
v_add_f32_e32 v245, v245, v153
v_mfma_f32_16x16x32_bf16 v[44:47], a[20:23], v[180:183], v[44:47]
s_waitcnt lgkmcnt(7)
ds_read_b64_tr_b16 a[14:15], v206 offset:12800
v_exp_f32_e32 v154, v154
s_nop 0
v_add_f32_e32 v245, v245, v154
v_mfma_f32_16x16x32_bf16 v[48:51], a[24:27], v[176:179], v[48:51]
ds_read_b64_tr_b16 a[16:17], v205 offset:9216
v_exp_f32_e32 v155, v155
v_mfma_f32_16x16x32_bf16 v[56:59], a[24:27], v[180:183], v[56:59]
ds_read_b64_tr_b16 a[18:19], v205 offset:13312
v_add_f32_e32 v245, v245, v155
v_exp_f32_e32 v148, v148
v_mfma_f32_16x16x32_bf16 v[52:55], a[28:31], v[176:179], v[52:55]
ds_read_b64_tr_b16 a[20:21], v206 offset:9216
v_cvt_pk_bf16_f32 v176, v144, v145
v_cvt_pk_bf16_f32 v177, v146, v147
v_add_f32_e32 v244, v244, v148
v_mfma_f32_16x16x32_bf16 v[60:63], a[28:31], v[180:183], v[60:63]
s_waitcnt lgkmcnt(7)
ds_read_b64_tr_b16 a[22:23], v206 offset:13312
v_cvt_pk_bf16_f32 v180, v152, v153
v_cvt_pk_bf16_f32 v181, v154, v155
v_exp_f32_e32 v149, v149
v_mfma_f32_16x16x32_bf16 v[0:3], a[0:3], v[184:187], v[0:3]
ds_read_b64_tr_b16 a[24:25], v205 offset:9728
v_add_f32_e32 v244, v244, v149
v_cvt_pk_bf16_f32 v178, v148, v149
v_mfma_f32_16x16x32_bf16 v[8:11], a[0:3], v[188:191], v[8:11]
s_waitcnt vmcnt(0)
s_mov_b64 exec, 14
buffer_store_dword v234, v232, s[56:59], 0 offen sc1
s_mov_b64 exec, -1
ds_read_b64_tr_b16 a[26:27], v205 offset:13824
v_mfma_f32_16x16x32_bf16 v[4:7], a[4:7], v[184:187], v[4:7]
ds_read_b64_tr_b16 a[28:29], v206 offset:9728
v_exp_f32_e32 v150, v150
v_exp_f32_e32 v151, v151
v_mfma_f32_16x16x32_bf16 v[12:15], a[4:7], v[188:191], v[12:15]
s_barrier
ds_read_b64_tr_b16 a[30:31], v206 offset:13824
s_waitcnt lgkmcnt(8)
v_mfma_f32_16x16x32_bf16 v[16:19], a[8:11], v[184:187], v[16:19]
ds_read_b128 a[0:3], v201 offset:32768
v_add_f32_e32 v244, v244, v150
v_add_f32_e32 v244, v244, v151
v_cvt_pk_bf16_f32 v179, v150, v151
v_mfma_f32_16x16x32_bf16 v[24:27], a[8:11], v[188:191], v[24:27]
v_exp_f32_e32 v156, v156
v_exp_f32_e32 v157, v157
v_mfma_f32_16x16x32_bf16 v[20:23], a[12:15], v[184:187], v[20:23]
ds_read_b128 a[4:7], v201 offset:33280
v_add_f32_e32 v245, v245, v156
v_add_f32_e32 v245, v245, v157
v_cvt_pk_bf16_f32 v182, v156, v157
v_mfma_f32_16x16x32_bf16 v[28:31], a[12:15], v[188:191], v[28:31]
s_waitcnt lgkmcnt(6)
v_exp_f32_e32 v158, v158
v_exp_f32_e32 v159, v159
v_mfma_f32_16x16x32_bf16 v[32:35], a[16:19], v[184:187], v[32:35]
ds_read_b128 a[8:11], v201 offset:33792
v_add_f32_e32 v245, v245, v158
v_add_f32_e32 v245, v245, v159
v_cvt_pk_bf16_f32 v183, v158, v159
v_mfma_f32_16x16x32_bf16 v[40:43], a[16:19], v[188:191], v[40:43]
v_exp_f32_e32 v160, v160
v_exp_f32_e32 v161, v161
v_mfma_f32_16x16x32_bf16 v[36:39], a[20:23], v[184:187], v[36:39]
ds_read_b128 a[12:15], v201 offset:34304
v_exp_f32_e32 v162, v162
v_add_f32_e32 v244, v244, v160
v_mfma_f32_16x16x32_bf16 v[44:47], a[20:23], v[188:191], v[44:47]
s_waitcnt lgkmcnt(4)
v_exp_f32_e32 v163, v163
v_exp_f32_e32 v164, v164
v_mfma_f32_16x16x32_bf16 v[48:51], a[24:27], v[184:187], v[48:51]
ds_read_b128 a[16:19], v201 offset:36864
v_exp_f32_e32 v165, v165
v_mfma_f32_16x16x32_bf16 v[56:59], a[24:27], v[188:191], v[56:59]
v_exp_f32_e32 v166, v166
v_exp_f32_e32 v167, v167
v_mfma_f32_16x16x32_bf16 v[52:55], a[28:31], v[184:187], v[52:55]
ds_read_b128 a[20:23], v201 offset:37376
v_cvt_pk_bf16_f32 v184, v160, v161
v_cvt_pk_bf16_f32 v185, v162, v163
v_cvt_pk_bf16_f32 v186, v164, v165
v_mfma_f32_16x16x32_bf16 v[60:63], a[28:31], v[188:191], v[60:63]
s_cbranch_vccnz .Lk_arb14
.Lk_abb14:
v_cvt_pk_bf16_f32 v187, v166, v167
s_waitcnt lgkmcnt(4)
v_mfma_f32_16x16x32_bf16 v[128:131], a[0:3], a[64:67], v[192:195]
ds_read_b128 a[24:27], v201 offset:37888
v_exp_f32_e32 v168, v168
v_exp_f32_e32 v169, v169
v_mfma_f32_16x16x32_bf16 v[136:139], a[0:3], a[68:71], v[196:199]
v_cvt_pk_bf16_f32 v188, v168, v169
v_exp_f32_e32 v170, v170
v_mfma_f32_16x16x32_bf16 v[128:131], a[4:7], a[72:75], v[128:131]
ds_read_b128 a[28:31], v201 offset:38400
v_exp_f32_e32 v171, v171
v_exp_f32_e32 v172, v172
v_mfma_f32_16x16x32_bf16 v[136:139], a[4:7], a[76:79], v[136:139]
s_waitcnt lgkmcnt(4)
v_cvt_pk_bf16_f32 v189, v170, v171
v_exp_f32_e32 v173, v173
v_mfma_f32_16x16x32_bf16 v[128:131], a[8:11], a[80:83], v[128:131]
ds_read_b128 a[0:3], v201 offset:40960
v_cvt_pk_bf16_f32 v190, v172, v173
v_exp_f32_e32 v174, v174
v_mfma_f32_16x16x32_bf16 v[136:139], a[8:11], a[84:87], v[136:139]
v_exp_f32_e32 v175, v175
v_add_f32_e32 v245, v245, v168
v_cvt_pk_bf16_f32 v191, v174, v175
v_mfma_f32_16x16x32_bf16 v[128:131], a[12:15], a[88:91], v[128:131]
ds_read_b128 a[4:7], v201 offset:41472
v_add_f32_e32 v244, v244, v161
v_add_f32_e32 v245, v245, v169
v_add_f32_e32 v244, v244, v162
v_mfma_f32_16x16x32_bf16 v[136:139], a[12:15], a[92:95], v[136:139]
s_waitcnt lgkmcnt(4)
v_add_f32_e32 v245, v245, v170
v_add_f32_e32 v244, v244, v163
v_add_f32_e32 v245, v245, v171
v_add_f32_e32 v244, v244, v164
v_mfma_f32_16x16x32_bf16 v[132:135], a[16:19], a[64:67], v[192:195]
ds_read_b128 a[8:11], v201 offset:41984
v_add_f32_e32 v245, v245, v172
v_add_f32_e32 v244, v244, v165
v_add_f32_e32 v245, v245, v173
v_mfma_f32_16x16x32_bf16 v[140:143], a[16:19], a[68:71], v[196:199]
v_add_f32_e32 v244, v244, v166
v_add_f32_e32 v245, v245, v174
v_add_f32_e32 v244, v244, v167
v_add_f32_e32 v245, v245, v175
v_mfma_f32_16x16x32_bf16 v[132:135], a[20:23], a[72:75], v[132:135]
ds_read_b128 a[12:15], v201 offset:42496
v_max3_f32 v236, v128, v129, v130
v_mfma_f32_16x16x32_bf16 v[140:143], a[20:23], a[76:79], v[140:143]
s_waitcnt lgkmcnt(4)
v_max3_f32 v237, v136, v137, v138
v_mfma_f32_16x16x32_bf16 v[132:135], a[24:27], a[80:83], v[132:135]
ds_read_b128 a[16:19], v201 offset:45056
v_mfma_f32_16x16x32_bf16 v[140:143], a[24:27], a[84:87], v[140:143]
v_mfma_f32_16x16x32_bf16 v[132:135], a[28:31], a[88:91], v[132:135]
ds_read_b128 a[20:23], v201 offset:45568
v_mfma_f32_16x16x32_bf16 v[140:143], a[28:31], a[92:95], v[140:143]
s_waitcnt lgkmcnt(4)
)"
R"(
v_mfma_f32_16x16x32_bf16 v[144:147], a[0:3], a[64:67], v[192:195]
ds_read_b128 a[24:27], v201 offset:46080
v_mfma_f32_16x16x32_bf16 v[152:155], a[0:3], a[68:71], v[196:199]
v_mfma_f32_16x16x32_bf16 v[144:147], a[4:7], a[72:75], v[144:147]
ds_read_b128 a[28:31], v201 offset:46592
v_max3_f32 v236, v236, v131, v132
v_max3_f32 v236, v236, v133, v134
v_mfma_f32_16x16x32_bf16 v[152:155], a[4:7], a[76:79], v[152:155]
s_barrier
v_max3_f32 v237, v237, v139, v140
v_max3_f32 v237, v237, v141, v142
s_waitcnt lgkmcnt(4)
v_mfma_f32_16x16x32_bf16 v[144:147], a[8:11], a[80:83], v[144:147]
ds_read_b64_tr_b16 a[0:1], v203
v_mfma_f32_16x16x32_bf16 v[152:155], a[8:11], a[84:87], v[152:155]
ds_read_b64_tr_b16 a[2:3], v203 offset:4096
v_mfma_f32_16x16x32_bf16 v[144:147], a[12:15], a[88:91], v[144:147]
ds_read_b64_tr_b16 a[4:5], v204
v_mfma_f32_16x16x32_bf16 v[152:155], a[12:15], a[92:95], v[152:155]
s_waitcnt lgkmcnt(5)
ds_read_b64_tr_b16 a[6:7], v204 offset:4096
v_mfma_f32_16x16x32_bf16 v[148:151], a[16:19], a[64:67], v[192:195]
ds_read_b64_tr_b16 a[8:9], v203 offset:512
v_mfma_f32_16x16x32_bf16 v[156:159], a[16:19], a[68:71], v[196:199]
ds_read_b64_tr_b16 a[10:11], v203 offset:4608
v_mfma_f32_16x16x32_bf16 v[148:151], a[20:23], a[72:75], v[148:151]
ds_read_b64_tr_b16 a[12:13], v204 offset:512
v_max3_f32 v236, v236, v135, v144
v_max3_f32 v236, v236, v145, v146
v_mfma_f32_16x16x32_bf16 v[156:159], a[20:23], a[76:79], v[156:159]
s_waitcnt lgkmcnt(7)
ds_read_b64_tr_b16 a[14:15], v204 offset:4608
v_max3_f32 v237, v237, v143, v152
v_max3_f32 v237, v237, v153, v154
v_mfma_f32_16x16x32_bf16 v[148:151], a[24:27], a[80:83], v[148:151]
ds_read_b64_tr_b16 a[16:17], v203 offset:1024
v_mfma_f32_16x16x32_bf16 v[156:159], a[24:27], a[84:87], v[156:159]
ds_read_b64_tr_b16 a[18:19], v203 offset:5120
v_mfma_f32_16x16x32_bf16 v[148:151], a[28:31], a[88:91], v[148:151]
ds_read_b64_tr_b16 a[20:21], v204 offset:1024
v_mfma_f32_16x16x32_bf16 v[156:159], a[28:31], a[92:95], v[156:159]
s_barrier
ds_read_b64_tr_b16 a[22:23], v204 offset:5120
s_waitcnt lgkmcnt(8)
v_mfma_f32_16x16x32_bf16 v[0:3], a[0:3], v[176:179], v[0:3]
ds_read_b64_tr_b16 a[24:25], v203 offset:1536
v_mfma_f32_16x16x32_bf16 v[8:11], a[0:3], v[180:183], v[8:11]
ds_read_b64_tr_b16 a[26:27], v203 offset:5632
v_mfma_f32_16x16x32_bf16 v[4:7], a[4:7], v[176:179], v[4:7]
ds_read_b64_tr_b16 a[28:29], v204 offset:1536
v_max3_f32 v236, v236, v147, v148
v_max3_f32 v236, v236, v149, v150
v_mfma_f32_16x16x32_bf16 v[12:15], a[4:7], v[180:183], v[12:15]
s_waitcnt lgkmcnt(7)
ds_read_b64_tr_b16 a[30:31], v204 offset:5632
v_max3_f32 v237, v237, v155, v156
v_max3_f32 v237, v237, v157, v158
v_max3_f32 v248, v236, v151, v159
v_max_f32_e32 v248, v248, v237
v_cmp_lt_f32_e32 vcc, s70, v248
v_mfma_f32_16x16x32_bf16 v[16:19], a[8:11], v[176:179], v[16:19]
s_nop 0
s_cbranch_vccnz .Lk_ara15
.Lk_aba15:
ds_read_b64_tr_b16 a[0:1], v203 offset:8192
v_exp_f32_e32 v128, v128
v_mfma_f32_16x16x32_bf16 v[24:27], a[8:11], v[180:183], v[24:27]
ds_read_b64_tr_b16 a[2:3], v203 offset:12288
v_exp_f32_e32 v129, v129
v_exp_f32_e32 v130, v130
v_mfma_f32_16x16x32_bf16 v[20:23], a[12:15], v[176:179], v[20:23]
ds_read_b64_tr_b16 a[4:5], v204 offset:8192
v_exp_f32_e32 v131, v131
v_add_f32_e32 v244, v244, v128
v_mfma_f32_16x16x32_bf16 v[28:31], a[12:15], v[180:183], v[28:31]
s_waitcnt lgkmcnt(7)
ds_read_b64_tr_b16 a[6:7], v204 offset:12288
v_exp_f32_e32 v132, v132
v_exp_f32_e32 v133, v133
v_mfma_f32_16x16x32_bf16 v[32:35], a[16:19], v[176:179], v[32:35]
ds_read_b64_tr_b16 a[8:9], v203 offset:8704
v_exp_f32_e32 v134, v134
v_add_f32_e32 v244, v244, v129
v_mfma_f32_16x16x32_bf16 v[40:43], a[16:19], v[180:183], v[40:43]
ds_read_b64_tr_b16 a[10:11], v203 offset:12800
v_exp_f32_e32 v135, v135
v_exp_f32_e32 v136, v136
v_mfma_f32_16x16x32_bf16 v[36:39], a[20:23], v[176:179], v[36:39]
ds_read_b64_tr_b16 a[12:13], v204 offset:8704
v_exp_f32_e32 v137, v137
v_exp_f32_e32 v138, v138
v_mfma_f32_16x16x32_bf16 v[44:47], a[20:23], v[180:183], v[44:47]
s_waitcnt lgkmcnt(7)
ds_read_b64_tr_b16 a[14:15], v204 offset:12800
v_exp_f32_e32 v139, v139
v_add_f32_e32 v244, v244, v130
v_mfma_f32_16x16x32_bf16 v[48:51], a[24:27], v[176:179], v[48:51]
ds_read_b64_tr_b16 a[16:17], v203 offset:9216
v_exp_f32_e32 v140, v140
v_exp_f32_e32 v141, v141
v_mfma_f32_16x16x32_bf16 v[56:59], a[24:27], v[180:183], v[56:59]
ds_read_b64_tr_b16 a[18:19], v203 offset:13312
v_exp_f32_e32 v142, v142
v_exp_f32_e32 v143, v143
v_mfma_f32_16x16x32_bf16 v[52:55], a[28:31], v[176:179], v[52:55]
ds_read_b64_tr_b16 a[20:21], v204 offset:9216
v_cvt_pk_bf16_f32 v176, v128, v129
v_cvt_pk_bf16_f32 v177, v130, v131
v_cvt_pk_bf16_f32 v178, v132, v133
v_mfma_f32_16x16x32_bf16 v[60:63], a[28:31], v[180:183], v[60:63]
s_waitcnt lgkmcnt(7)
ds_read_b64_tr_b16 a[22:23], v204 offset:13312
v_cvt_pk_bf16_f32 v179, v134, v135
v_cvt_pk_bf16_f32 v180, v136, v137
v_cvt_pk_bf16_f32 v181, v138, v139
v_cvt_pk_bf16_f32 v182, v140, v141
v_mfma_f32_16x16x32_bf16 v[0:3], a[0:3], v[184:187], v[0:3]
ds_read_b64_tr_b16 a[24:25], v203 offset:9728
v_cvt_pk_bf16_f32 v183, v142, v143
v_exp_f32_e32 v144, v144
v_mfma_f32_16x16x32_bf16 v[8:11], a[0:3], v[188:191], v[8:11]
ds_read_b64_tr_b16 a[26:27], v203 offset:13824
v_exp_f32_e32 v145, v145
v_exp_f32_e32 v146, v146
v_mfma_f32_16x16x32_bf16 v[4:7], a[4:7], v[184:187], v[4:7]
ds_read_b64_tr_b16 a[28:29], v204 offset:9728
v_exp_f32_e32 v147, v147
v_exp_f32_e32 v148, v148
v_mfma_f32_16x16x32_bf16 v[12:15], a[4:7], v[188:191], v[12:15]
s_waitcnt lgkmcnt(7)
ds_read_b64_tr_b16 a[30:31], v204 offset:13824
v_exp_f32_e32 v149, v149
v_exp_f32_e32 v150, v150
v_mfma_f32_16x16x32_bf16 v[16:19], a[8:11], v[184:187], v[16:19]
v_exp_f32_e32 v151, v151
v_exp_f32_e32 v152, v152
v_mfma_f32_16x16x32_bf16 v[24:27], a[8:11], v[188:191], v[24:27]
v_exp_f32_e32 v153, v153
v_exp_f32_e32 v154, v154
v_add_f32_e32 v244, v244, v131
v_mfma_f32_16x16x32_bf16 v[20:23], a[12:15], v[184:187], v[20:23]
v_exp_f32_e32 v155, v155
v_exp_f32_e32 v156, v156
v_mfma_f32_16x16x32_bf16 v[28:31], a[12:15], v[188:191], v[28:31]
s_waitcnt lgkmcnt(4)
v_exp_f32_e32 v157, v157
v_exp_f32_e32 v158, v158
v_add_f32_e32 v244, v244, v132
v_mfma_f32_16x16x32_bf16 v[32:35], a[16:19], v[184:187], v[32:35]
v_exp_f32_e32 v159, v159
v_add_f32_e32 v244, v244, v133
v_add_f32_e32 v244, v244, v134
v_add_f32_e32 v244, v244, v135
v_mfma_f32_16x16x32_bf16 v[40:43], a[16:19], v[188:191], v[40:43]
v_add_f32_e32 v244, v244, v144
v_add_f32_e32 v244, v244, v145
v_add_f32_e32 v244, v244, v146
v_add_f32_e32 v244, v244, v147
v_mfma_f32_16x16x32_bf16 v[36:39], a[20:23], v[184:187], v[36:39]
buffer_load_dword v235, v231, s[56:59], 0 offen sc1
v_add_f32_e32 v244, v244, v148
v_add_f32_e32 v244, v244, v149
v_mfma_f32_16x16x32_bf16 v[44:47], a[20:23], v[188:191], v[44:47]
s_waitcnt lgkmcnt(0)
v_add_f32_e32 v244, v244, v150
v_add_f32_e32 v244, v244, v151
v_mov_b32_e32 v248, v244
v_add_f32_e32 v245, v245, v136
s_nop 0
v_permlane16_swap_b32_e32 v244, v248
v_mfma_f32_16x16x32_bf16 v[48:51], a[24:27], v[184:187], v[48:51]
v_add_f32_e32 v244, v244, v248
v_mov_b32_e32 v248, v244
v_add_f32_e32 v245, v245, v137
s_nop 0
v_permlane32_swap_b32_e32 v244, v248
v_add_f32_e32 v244, v244, v248
v_mfma_f32_16x16x32_bf16 v[56:59], a[24:27], v[188:191], v[56:59]
v_rcp_f32_e32 v240, v244
v_add_f32_e32 v245, v245, v138
v_add_f32_e32 v245, v245, v139
v_add_f32_e32 v245, v245, v140
v_mfma_f32_16x16x32_bf16 v[52:55], a[28:31], v[184:187], v[52:55]
v_cvt_pk_bf16_f32 v184, v144, v145
v_cvt_pk_bf16_f32 v185, v146, v147
v_cvt_pk_bf16_f32 v186, v148, v149
v_cvt_pk_bf16_f32 v187, v150, v151
v_add_f32_e32 v245, v245, v141
v_add_f32_e32 v245, v245, v142
v_add_f32_e32 v245, v245, v143
v_add_f32_e32 v245, v245, v152
v_add_f32_e32 v245, v245, v153
v_mfma_f32_16x16x32_bf16 v[60:63], a[28:31], v[188:191], v[60:63]
s_cbranch_vccnz .Lk_arb15
.Lk_abb15:
s_barrier
ds_read_b64_tr_b16 a[0:1], v203 offset:49152
ds_read_b64_tr_b16 a[2:3], v203 offset:53248
ds_read_b64_tr_b16 a[4:5], v203 offset:57344
ds_read_b64_tr_b16 a[6:7], v203 offset:61440
)"
R"(
ds_read_b64_tr_b16 a[8:9], v204 offset:49152
ds_read_b64_tr_b16 a[10:11], v204 offset:53248
ds_read_b64_tr_b16 a[12:13], v204 offset:57344
ds_read_b64_tr_b16 a[14:15], v204 offset:61440
ds_read_b64_tr_b16 a[16:17], v203 offset:49664
ds_read_b64_tr_b16 a[18:19], v203 offset:53760
ds_read_b64_tr_b16 a[20:21], v203 offset:57856
ds_read_b64_tr_b16 a[22:23], v203 offset:61952
s_waitcnt lgkmcnt(8)
v_mfma_f32_16x16x32_bf16 v[0:3], a[0:3], v[176:179], v[0:3]
ds_read_b64_tr_b16 a[24:25], v204 offset:49664
v_cvt_pk_bf16_f32 v188, v152, v153
v_cvt_pk_bf16_f32 v189, v154, v155
v_cvt_pk_bf16_f32 v190, v156, v157
v_mfma_f32_16x16x32_bf16 v[8:11], a[0:3], v[180:183], v[8:11]
ds_read_b64_tr_b16 a[26:27], v204 offset:53760
v_cvt_pk_bf16_f32 v191, v158, v159
v_add_f32_e32 v245, v245, v154
v_add_f32_e32 v245, v245, v155
v_add_f32_e32 v245, v245, v156
v_mfma_f32_16x16x32_bf16 v[0:3], a[4:7], v[184:187], v[0:3]
ds_read_b64_tr_b16 a[28:29], v204 offset:57856
v_add_f32_e32 v245, v245, v157
v_add_f32_e32 v245, v245, v158
v_add_f32_e32 v245, v245, v159
v_mfma_f32_16x16x32_bf16 v[8:11], a[4:7], v[188:191], v[8:11]
s_waitcnt lgkmcnt(7)
ds_read_b64_tr_b16 a[30:31], v204 offset:61952
v_mov_b32_e32 v249, v245
s_nop 1
v_permlane16_swap_b32_e32 v245, v249
v_add_f32_e32 v245, v245, v249
v_mfma_f32_16x16x32_bf16 v[4:7], a[8:11], v[176:179], v[4:7]
ds_read_b64_tr_b16 a[0:1], v203 offset:50176
v_mov_b32_e32 v249, v245
s_nop 1
v_permlane32_swap_b32_e32 v245, v249
v_add_f32_e32 v245, v245, v249
v_mfma_f32_16x16x32_bf16 v[12:15], a[8:11], v[180:183], v[12:15]
ds_read_b64_tr_b16 a[2:3], v203 offset:54272
v_rcp_f32_e32 v241, v245
v_mfma_f32_16x16x32_bf16 v[4:7], a[12:15], v[184:187], v[4:7]
ds_read_b64_tr_b16 a[4:5], v203 offset:58368
v_mul_f32_e32 v0, v240, v0
v_mul_f32_e32 v1, v240, v1
v_mul_f32_e32 v2, v240, v2
v_mul_f32_e32 v3, v240, v3
v_cvt_pk_bf16_f32 v0, v0, v1
v_mfma_f32_16x16x32_bf16 v[12:15], a[12:15], v[188:191], v[12:15]
s_waitcnt lgkmcnt(7)
ds_read_b64_tr_b16 a[6:7], v203 offset:62464
v_cvt_pk_bf16_f32 v1, v2, v3
v_mul_f32_e32 v8, v241, v8
v_mul_f32_e32 v9, v241, v9
v_mul_f32_e32 v10, v241, v10
v_mfma_f32_16x16x32_bf16 v[16:19], a[16:19], v[176:179], v[16:19]
ds_read_b64_tr_b16 a[8:9], v204 offset:50176
v_mul_f32_e32 v11, v241, v11
v_cvt_pk_bf16_f32 v8, v8, v9
v_cvt_pk_bf16_f32 v9, v10, v11
v_mfma_f32_16x16x32_bf16 v[24:27], a[16:19], v[180:183], v[24:27]
ds_read_b64_tr_b16 a[10:11], v204 offset:54272
v_mfma_f32_16x16x32_bf16 v[16:19], a[20:23], v[184:187], v[16:19]
ds_read_b64_tr_b16 a[12:13], v204 offset:58368
v_mul_f32_e32 v4, v240, v4
v_mul_f32_e32 v5, v240, v5
v_mul_f32_e32 v6, v240, v6
v_mul_f32_e32 v7, v240, v7
v_cvt_pk_bf16_f32 v2, v4, v5
v_mfma_f32_16x16x32_bf16 v[24:27], a[20:23], v[188:191], v[24:27]
s_waitcnt lgkmcnt(7)
ds_read_b64_tr_b16 a[14:15], v204 offset:62464
v_cvt_pk_bf16_f32 v3, v6, v7
v_mul_f32_e32 v12, v241, v12
v_mul_f32_e32 v13, v241, v13
v_mul_f32_e32 v14, v241, v14
v_mfma_f32_16x16x32_bf16 v[20:23], a[24:27], v[176:179], v[20:23]
ds_read_b64_tr_b16 a[16:17], v203 offset:50688
v_mul_f32_e32 v15, v241, v15
v_cvt_pk_bf16_f32 v10, v12, v13
v_cvt_pk_bf16_f32 v11, v14, v15
v_mfma_f32_16x16x32_bf16 v[28:31], a[24:27], v[180:183], v[28:31]
ds_read_b64_tr_b16 a[18:19], v203 offset:54784
v_mfma_f32_16x16x32_bf16 v[20:23], a[28:31], v[184:187], v[20:23]
ds_read_b64_tr_b16 a[20:21], v203 offset:58880
v_mul_f32_e32 v16, v240, v16
v_mul_f32_e32 v17, v240, v17
v_mul_f32_e32 v18, v240, v18
v_mul_f32_e32 v19, v240, v19
v_cvt_pk_bf16_f32 v16, v16, v17
v_mfma_f32_16x16x32_bf16 v[28:31], a[28:31], v[188:191], v[28:31]
s_waitcnt lgkmcnt(7)
ds_read_b64_tr_b16 a[22:23], v203 offset:62976
v_cvt_pk_bf16_f32 v17, v18, v19
v_mul_f32_e32 v24, v241, v24
v_mul_f32_e32 v25, v241, v25
v_mul_f32_e32 v26, v241, v26
v_mfma_f32_16x16x32_bf16 v[32:35], a[0:3], v[176:179], v[32:35]
ds_read_b64_tr_b16 a[24:25], v204 offset:50688
v_mul_f32_e32 v27, v241, v27
v_cvt_pk_bf16_f32 v24, v24, v25
v_cvt_pk_bf16_f32 v25, v26, v27
v_mfma_f32_16x16x32_bf16 v[40:43], a[0:3], v[180:183], v[40:43]
ds_read_b64_tr_b16 a[26:27], v204 offset:54784
v_mfma_f32_16x16x32_bf16 v[32:35], a[4:7], v[184:187], v[32:35]
ds_read_b64_tr_b16 a[28:29], v204 offset:58880
v_mul_f32_e32 v20, v240, v20
v_mul_f32_e32 v21, v240, v21
v_mul_f32_e32 v22, v240, v22
v_mul_f32_e32 v23, v240, v23
v_cvt_pk_bf16_f32 v18, v20, v21
v_cvt_pk_bf16_f32 v19, v22, v23
v_mfma_f32_16x16x32_bf16 v[40:43], a[4:7], v[188:191], v[40:43]
s_barrier
ds_read_b64_tr_b16 a[30:31], v204 offset:62976
v_mul_f32_e32 v28, v241, v28
v_mul_f32_e32 v29, v241, v29
s_waitcnt lgkmcnt(8)
v_mfma_f32_16x16x32_bf16 v[36:39], a[8:11], v[176:179], v[36:39]
ds_read_b64_tr_b16 a[0:1], v203 offset:16896
v_mul_f32_e32 v30, v241, v30
v_mul_f32_e32 v31, v241, v31
v_cvt_pk_bf16_f32 v26, v28, v29
v_cvt_pk_bf16_f32 v27, v30, v31
v_mfma_f32_16x16x32_bf16 v[44:47], a[8:11], v[180:183], v[44:47]
ds_read_b64_tr_b16 a[2:3], v203 offset:20992
s_waitcnt vmcnt(0)
v_cmp_eq_u32_e64 s[74:75], v235, v233
s_nop 1
s_and_b32 s79, s74, 14
s_cmp_eq_u32 s79, 14
s_cbranch_scc0 .Lk_aslow1
.Lk_aok1:
buffer_load_dword v234, v232, s[56:59], 0 offen offset:128 sc1
v_mfma_f32_16x16x32_bf16 v[36:39], a[12:15], v[184:187], v[36:39]
s_xor_b32 s86, s96, 0
s_add_u32 s87, s86, 0x1000
buffer_load_dwordx4 a[96:99], v229, s[56:59], s86 offen sc1
ds_read_b64_tr_b16 a[4:5], v204 offset:16896
v_mul_f32_e32 v32, v240, v32
v_mfma_f32_16x16x32_bf16 v[44:47], a[12:15], v[188:191], v[44:47]
s_waitcnt lgkmcnt(7)
buffer_load_dwordx4 a[100:103], v229, s[56:59], s86 offen offset:1024 sc1
ds_read_b64_tr_b16 a[6:7], v204 offset:20992
v_mfma_f32_16x16x32_bf16 v[48:51], a[16:19], v[176:179], v[48:51]
ds_read_b64_tr_b16 a[8:9], v203 offset:17408
buffer_load_dwordx4 a[104:107], v229, s[56:59], s86 offen offset:2048 sc1
v_mul_f32_e32 v33, v240, v33
v_mfma_f32_16x16x32_bf16 v[56:59], a[16:19], v[180:183], v[56:59]
buffer_load_dwordx4 a[108:111], v229, s[56:59], s86 offen offset:3072 sc1
ds_read_b64_tr_b16 a[10:11], v203 offset:21504
v_mfma_f32_16x16x32_bf16 v[48:51], a[20:23], v[184:187], v[48:51]
ds_read_b64_tr_b16 a[12:13], v204 offset:17408
buffer_load_dwordx4 a[112:115], v229, s[56:59], s87 offen sc1
v_mul_f32_e32 v34, v240, v34
v_mfma_f32_16x16x32_bf16 v[56:59], a[20:23], v[188:191], v[56:59]
s_waitcnt lgkmcnt(7)
ds_read_b64_tr_b16 a[14:15], v204 offset:21504
buffer_load_dwordx4 a[116:119], v229, s[56:59], s87 offen offset:1024 sc1
v_mul_f32_e32 v35, v240, v35
v_mfma_f32_16x16x32_bf16 v[52:55], a[24:27], v[176:179], v[52:55]
ds_read_b64_tr_b16 a[16:17], v203 offset:17920
buffer_load_dwordx4 a[120:123], v229, s[56:59], s87 offen offset:2048 sc1
v_mfma_f32_16x16x32_bf16 v[60:63], a[24:27], v[180:183], v[60:63]
ds_read_b64_tr_b16 a[18:19], v203 offset:22016
buffer_load_dwordx4 a[124:127], v229, s[56:59], s87 offen offset:3072 sc1
v_mul_f32_e32 v36, v240, v36
v_mfma_f32_16x16x32_bf16 v[52:55], a[28:31], v[184:187], v[52:55]
ds_read_b64_tr_b16 a[20:21], v204 offset:17920
v_mul_f32_e32 v37, v240, v37
v_mul_f32_e32 v38, v240, v38
v_mul_f32_e32 v39, v240, v39
v_mfma_f32_16x16x32_bf16 v[60:63], a[28:31], v[188:191], v[60:63]
s_waitcnt lgkmcnt(7)
ds_read_b64_tr_b16 a[22:23], v204 offset:22016
v_cvt_pk_bf16_f32 v32, v32, v33
v_cvt_pk_bf16_f32 v33, v34, v35
v_cvt_pk_bf16_f32 v34, v36, v37
v_mfma_f32_16x16x32_bf16 v[80:83], a[0:3], v[0:3], v[80:83]
s_add_u32 s84, s81, 0x800000
buffer_store_dwordx4 v[0:3], v229, s[56:59], s84 offen sc1
ds_read_b64_tr_b16 a[24:25], v203 offset:16384
v_mfma_f32_16x16x32_bf16 v[88:91], a[0:3], v[8:11], v[88:91]
ds_read_b64_tr_b16 a[26:27], v203 offset:20480
v_cvt_pk_bf16_f32 v35, v38, v39
v_mul_f32_e32 v40, v241, v40
v_mfma_f32_16x16x32_bf16 v[84:87], a[4:7], v[0:3], v[84:87]
ds_read_b64_tr_b16 a[28:29], v204 offset:16384
buffer_store_dwordx4 v[8:11], v229, s[56:59], s84 offen offset:1024 sc1
v_mul_f32_e32 v41, v241, v41
v_mfma_f32_16x16x32_bf16 v[92:95], a[4:7], v[8:11], v[92:95]
s_waitcnt lgkmcnt(7)
ds_read_b64_tr_b16 a[30:31], v204 offset:20480
v_mul_f32_e32 v42, v241, v42
v_mul_f32_e32 v43, v241, v43
v_mul_f32_e32 v44, v241, v44
v_mfma_f32_16x16x32_bf16 v[96:99], a[8:11], v[0:3], v[96:99]
ds_read_b64_tr_b16 a[0:1], v203 offset:25088
buffer_store_dwordx4 v[16:19], v229, s[56:59], s84 offen offset:2048 sc1
v_mul_f32_e32 v45, v241, v45
)"
R"(
v_mfma_f32_16x16x32_bf16 v[104:107], a[8:11], v[8:11], v[104:107]
ds_read_b64_tr_b16 a[2:3], v203 offset:29184
v_mul_f32_e32 v46, v241, v46
v_mul_f32_e32 v47, v241, v47
v_cvt_pk_bf16_f32 v40, v40, v41
v_mfma_f32_16x16x32_bf16 v[100:103], a[12:15], v[0:3], v[100:103]
ds_read_b64_tr_b16 a[4:5], v204 offset:25088
buffer_store_dwordx4 v[24:27], v229, s[56:59], s84 offen offset:3072 sc1
v_mfma_f32_16x16x32_bf16 v[108:111], a[12:15], v[8:11], v[108:111]
s_waitcnt lgkmcnt(7)
ds_read_b64_tr_b16 a[6:7], v204 offset:29184
v_cvt_pk_bf16_f32 v41, v42, v43
v_cvt_pk_bf16_f32 v42, v44, v45
v_cvt_pk_bf16_f32 v43, v46, v47
v_mfma_f32_16x16x32_bf16 v[112:115], a[16:19], v[0:3], v[112:115]
s_add_u32 s84, s81, 0x801000
buffer_store_dwordx4 v[32:35], v229, s[56:59], s84 offen sc1
ds_read_b64_tr_b16 a[8:9], v203 offset:25600
v_mfma_f32_16x16x32_bf16 v[120:123], a[16:19], v[8:11], v[120:123]
ds_read_b64_tr_b16 a[10:11], v203 offset:29696
v_mul_f32_e32 v48, v240, v48
v_mul_f32_e32 v49, v240, v49
v_mul_f32_e32 v50, v240, v50
v_mfma_f32_16x16x32_bf16 v[116:119], a[20:23], v[0:3], v[116:119]
ds_read_b64_tr_b16 a[12:13], v204 offset:25600
buffer_store_dwordx4 v[40:43], v229, s[56:59], s84 offen offset:1024 sc1
v_mfma_f32_16x16x32_bf16 v[124:127], a[20:23], v[8:11], v[124:127]
s_waitcnt lgkmcnt(7)
ds_read_b64_tr_b16 a[14:15], v204 offset:29696
v_mul_f32_e32 v51, v240, v51
v_mul_f32_e32 v52, v240, v52
v_mul_f32_e32 v53, v240, v53
v_mfma_f32_16x16x32_bf16 v[64:67], a[24:27], v[0:3], v[64:67]
ds_read_b64_tr_b16 a[16:17], v203 offset:26112
v_mul_f32_e32 v54, v240, v54
v_mul_f32_e32 v55, v240, v55
v_cvt_pk_bf16_f32 v48, v48, v49
v_cvt_pk_bf16_f32 v49, v50, v51
v_mfma_f32_16x16x32_bf16 v[72:75], a[24:27], v[8:11], v[72:75]
ds_read_b64_tr_b16 a[18:19], v203 offset:30208
v_cvt_pk_bf16_f32 v50, v52, v53
v_cvt_pk_bf16_f32 v51, v54, v55
v_mul_f32_e32 v56, v241, v56
v_mfma_f32_16x16x32_bf16 v[68:71], a[28:31], v[0:3], v[68:71]
ds_read_b64_tr_b16 a[20:21], v204 offset:26112
buffer_store_dwordx4 v[48:51], v229, s[56:59], s84 offen offset:2048 sc1
v_mfma_f32_16x16x32_bf16 v[76:79], a[28:31], v[8:11], v[76:79]
s_waitcnt lgkmcnt(7)
ds_read_b64_tr_b16 a[22:23], v204 offset:30208
v_mul_f32_e32 v57, v241, v57
v_mul_f32_e32 v58, v241, v58
v_mul_f32_e32 v59, v241, v59
v_mfma_f32_16x16x32_bf16 v[80:83], a[0:3], v[16:19], v[80:83]
ds_read_b64_tr_b16 a[24:25], v203 offset:24576
v_mul_f32_e32 v60, v241, v60
v_mul_f32_e32 v61, v241, v61
v_mul_f32_e32 v62, v241, v62
v_mfma_f32_16x16x32_bf16 v[88:91], a[0:3], v[24:27], v[88:91]
ds_read_b64_tr_b16 a[26:27], v203 offset:28672
v_mul_f32_e32 v63, v241, v63
v_cvt_pk_bf16_f32 v56, v56, v57
v_cvt_pk_bf16_f32 v57, v58, v59
v_cvt_pk_bf16_f32 v58, v60, v61
v_mfma_f32_16x16x32_bf16 v[84:87], a[4:7], v[16:19], v[84:87]
ds_read_b64_tr_b16 a[28:29], v204 offset:24576
v_cvt_pk_bf16_f32 v59, v62, v63
buffer_store_dwordx4 v[56:59], v229, s[56:59], s84 offen offset:3072 sc1
v_mfma_f32_16x16x32_bf16 v[92:95], a[4:7], v[24:27], v[92:95]
s_barrier
ds_read_b64_tr_b16 a[30:31], v204 offset:28672
s_waitcnt lgkmcnt(8)
v_mfma_f32_16x16x32_bf16 v[96:99], a[8:11], v[16:19], v[96:99]
ds_read_b64_tr_b16 a[0:1], v205 offset:16896
v_mfma_f32_16x16x32_bf16 v[104:107], a[8:11], v[24:27], v[104:107]
ds_read_b64_tr_b16 a[2:3], v205 offset:20992
v_mfma_f32_16x16x32_bf16 v[100:103], a[12:15], v[16:19], v[100:103]
s_mov_b32 exec_lo, 0xff000000
s_mov_b32 exec_hi, 0xff00ff00
buffer_load_dword v251, v252, s[52:55], 0 offen
s_mov_b64 exec, -1
ds_read_b64_tr_b16 a[4:5], v206 offset:16896
v_mfma_f32_16x16x32_bf16 v[108:111], a[12:15], v[24:27], v[108:111]
s_waitcnt lgkmcnt(7)
ds_read_b64_tr_b16 a[6:7], v206 offset:20992
v_mfma_f32_16x16x32_bf16 v[112:115], a[16:19], v[16:19], v[112:115]
ds_read_b64_tr_b16 a[8:9], v205 offset:17408
v_mfma_f32_16x16x32_bf16 v[120:123], a[16:19], v[24:27], v[120:123]
ds_read_b64_tr_b16 a[10:11], v205 offset:21504
v_mfma_f32_16x16x32_bf16 v[116:119], a[20:23], v[16:19], v[116:119]
ds_read_b64_tr_b16 a[12:13], v206 offset:17408
v_mfma_f32_16x16x32_bf16 v[124:127], a[20:23], v[24:27], v[124:127]
s_waitcnt lgkmcnt(7)
ds_read_b64_tr_b16 a[14:15], v206 offset:21504
v_mfma_f32_16x16x32_bf16 v[64:67], a[24:27], v[16:19], v[64:67]
ds_read_b64_tr_b16 a[16:17], v205 offset:17920
v_mfma_f32_16x16x32_bf16 v[72:75], a[24:27], v[24:27], v[72:75]
ds_read_b64_tr_b16 a[18:19], v205 offset:22016
v_mfma_f32_16x16x32_bf16 v[68:71], a[28:31], v[16:19], v[68:71]
ds_read_b64_tr_b16 a[20:21], v206 offset:17920
v_mfma_f32_16x16x32_bf16 v[76:79], a[28:31], v[24:27], v[76:79]
s_waitcnt lgkmcnt(7)
ds_read_b64_tr_b16 a[22:23], v206 offset:22016
v_mfma_f32_16x16x32_bf16 v[80:83], a[0:3], v[32:35], v[80:83]
ds_read_b64_tr_b16 a[24:25], v205 offset:16384
v_mfma_f32_16x16x32_bf16 v[88:91], a[0:3], v[40:43], v[88:91]
ds_read_b64_tr_b16 a[26:27], v205 offset:20480
v_mfma_f32_16x16x32_bf16 v[84:87], a[4:7], v[32:35], v[84:87]
ds_read_b64_tr_b16 a[28:29], v206 offset:16384
v_mfma_f32_16x16x32_bf16 v[92:95], a[4:7], v[40:43], v[92:95]
s_waitcnt lgkmcnt(7)
ds_read_b64_tr_b16 a[30:31], v206 offset:20480
v_mfma_f32_16x16x32_bf16 v[96:99], a[8:11], v[32:35], v[96:99]
ds_read_b64_tr_b16 a[0:1], v205 offset:25088
v_mfma_f32_16x16x32_bf16 v[104:107], a[8:11], v[40:43], v[104:107]
ds_read_b64_tr_b16 a[2:3], v205 offset:29184
v_mfma_f32_16x16x32_bf16 v[100:103], a[12:15], v[32:35], v[100:103]
ds_read_b64_tr_b16 a[4:5], v206 offset:25088
v_mfma_f32_16x16x32_bf16 v[108:111], a[12:15], v[40:43], v[108:111]
s_waitcnt lgkmcnt(7)
ds_read_b64_tr_b16 a[6:7], v206 offset:29184
v_mfma_f32_16x16x32_bf16 v[112:115], a[16:19], v[32:35], v[112:115]
ds_read_b64_tr_b16 a[8:9], v205 offset:25600
v_mfma_f32_16x16x32_bf16 v[120:123], a[16:19], v[40:43], v[120:123]
ds_read_b64_tr_b16 a[10:11], v205 offset:29696
v_mfma_f32_16x16x32_bf16 v[116:119], a[20:23], v[32:35], v[116:119]
ds_read_b64_tr_b16 a[12:13], v206 offset:25600
v_mfma_f32_16x16x32_bf16 v[124:127], a[20:23], v[40:43], v[124:127]
s_waitcnt lgkmcnt(7)
ds_read_b64_tr_b16 a[14:15], v206 offset:29696
v_mfma_f32_16x16x32_bf16 v[64:67], a[24:27], v[32:35], v[64:67]
ds_read_b64_tr_b16 a[16:17], v205 offset:26112
v_mfma_f32_16x16x32_bf16 v[72:75], a[24:27], v[40:43], v[72:75]
ds_read_b64_tr_b16 a[18:19], v205 offset:30208
v_mfma_f32_16x16x32_bf16 v[68:71], a[28:31], v[32:35], v[68:71]
ds_read_b64_tr_b16 a[20:21], v206 offset:26112
v_mfma_f32_16x16x32_bf16 v[76:79], a[28:31], v[40:43], v[76:79]
s_waitcnt lgkmcnt(7)
ds_read_b64_tr_b16 a[22:23], v206 offset:30208
v_mfma_f32_16x16x32_bf16 v[80:83], a[0:3], v[48:51], v[80:83]
ds_read_b64_tr_b16 a[24:25], v205 offset:24576
v_mfma_f32_16x16x32_bf16 v[88:91], a[0:3], v[56:59], v[88:91]
ds_read_b64_tr_b16 a[26:27], v205 offset:28672
v_mfma_f32_16x16x32_bf16 v[84:87], a[4:7], v[48:51], v[84:87]
ds_read_b64_tr_b16 a[28:29], v206 offset:24576
v_mfma_f32_16x16x32_bf16 v[92:95], a[4:7], v[56:59], v[92:95]
s_barrier
ds_read_b64_tr_b16 a[30:31], v206 offset:28672
s_waitcnt lgkmcnt(8)
v_mfma_f32_16x16x32_bf16 v[96:99], a[8:11], v[48:51], v[96:99]
ds_read_b64_tr_b16 a[0:1], v207 offset:512
v_mfma_f32_16x16x32_bf16 v[104:107], a[8:11], v[56:59], v[104:107]
ds_read_b64_tr_b16 a[2:3], v207 offset:4608
v_mfma_f32_16x16x32_bf16 v[100:103], a[12:15], v[48:51], v[100:103]
ds_read_b64_tr_b16 a[4:5], v208 offset:512
v_mfma_f32_16x16x32_bf16 v[108:111], a[12:15], v[56:59], v[108:111]
s_waitcnt lgkmcnt(7)
ds_read_b64_tr_b16 a[6:7], v208 offset:4608
v_mfma_f32_16x16x32_bf16 v[112:115], a[16:19], v[48:51], v[112:115]
ds_read_b64_tr_b16 a[8:9], v207 offset:1024
v_mfma_f32_16x16x32_bf16 v[120:123], a[16:19], v[56:59], v[120:123]
ds_read_b64_tr_b16 a[10:11], v207 offset:5120
v_mfma_f32_16x16x32_bf16 v[116:119], a[20:23], v[48:51], v[116:119]
ds_read_b64_tr_b16 a[12:13], v208 offset:1024
v_mfma_f32_16x16x32_bf16 v[124:127], a[20:23], v[56:59], v[124:127]
s_waitcnt lgkmcnt(7)
ds_read_b64_tr_b16 a[14:15], v208 offset:5120
v_mfma_f32_16x16x32_bf16 v[64:67], a[24:27], v[48:51], v[64:67]
ds_read_b64_tr_b16 a[16:17], v207 offset:1536
v_mfma_f32_16x16x32_bf16 v[72:75], a[24:27], v[56:59], v[72:75]
ds_read_b64_tr_b16 a[18:19], v207 offset:5632
v_mfma_f32_16x16x32_bf16 v[68:71], a[28:31], v[48:51], v[68:71]
ds_read_b64_tr_b16 a[20:21], v208 offset:1536
v_mfma_f32_16x16x32_bf16 v[76:79], a[28:31], v[56:59], v[76:79]
s_waitcnt vmcnt(15)
ds_read_b64_tr_b16 a[22:23], v208 offset:5632
s_waitcnt lgkmcnt(8)
v_mfma_f32_16x16x32_bf16 v[80:83], a[0:3], a[96:99], v[80:83]
ds_read_b64_tr_b16 a[24:25], v207
v_mfma_f32_16x16x32_bf16 v[88:91], a[0:3], a[100:103], v[88:91]
ds_read_b64_tr_b16 a[26:27], v207 offset:4096
v_mfma_f32_16x16x32_bf16 v[84:87], a[4:7], a[96:99], v[84:87]
ds_read_b64_tr_b16 a[28:29], v208
v_mfma_f32_16x16x32_bf16 v[92:95], a[4:7], a[100:103], v[92:95]
s_waitcnt lgkmcnt(7)
ds_read_b64_tr_b16 a[30:31], v208 offset:4096
v_mfma_f32_16x16x32_bf16 v[96:99], a[8:11], a[96:99], v[96:99]
ds_read_b64_tr_b16 a[0:1], v207 offset:8704
v_mfma_f32_16x16x32_bf16 v[104:107], a[8:11], a[100:103], v[104:107]
ds_read_b64_tr_b16 a[2:3], v207 offset:12800
v_mfma_f32_16x16x32_bf16 v[100:103], a[12:15], a[96:99], v[100:103]
ds_read_b64_tr_b16 a[4:5], v208 offset:8704
v_mfma_f32_16x16x32_bf16 v[108:111], a[12:15], a[100:103], v[108:111]
s_waitcnt lgkmcnt(7)
ds_read_b64_tr_b16 a[6:7], v208 offset:12800
v_mfma_f32_16x16x32_bf16 v[112:115], a[16:19], a[96:99], v[112:115]
ds_read_b64_tr_b16 a[8:9], v207 offset:9216
v_mfma_f32_16x16x32_bf16 v[120:123], a[16:19], a[100:103], v[120:123]
ds_read_b64_tr_b16 a[10:11], v207 offset:13312
v_mfma_f32_16x16x32_bf16 v[116:119], a[20:23], a[96:99], v[116:119]
ds_read_b64_tr_b16 a[12:13], v208 offset:9216
)"
R"(
v_mfma_f32_16x16x32_bf16 v[124:127], a[20:23], a[100:103], v[124:127]
s_waitcnt lgkmcnt(7)
ds_read_b64_tr_b16 a[14:15], v208 offset:13312
v_mfma_f32_16x16x32_bf16 v[64:67], a[24:27], a[96:99], v[64:67]
ds_read_b64_tr_b16 a[16:17], v207 offset:9728
v_mfma_f32_16x16x32_bf16 v[72:75], a[24:27], a[100:103], v[72:75]
ds_read_b64_tr_b16 a[18:19], v207 offset:13824
v_mfma_f32_16x16x32_bf16 v[68:71], a[28:31], a[96:99], v[68:71]
ds_read_b64_tr_b16 a[20:21], v208 offset:9728
v_mfma_f32_16x16x32_bf16 v[76:79], a[28:31], a[100:103], v[76:79]
s_waitcnt vmcnt(13)
ds_read_b64_tr_b16 a[22:23], v208 offset:13824
s_waitcnt lgkmcnt(8)
v_mfma_f32_16x16x32_bf16 v[80:83], a[0:3], a[104:107], v[80:83]
ds_read_b64_tr_b16 a[24:25], v207 offset:8192
v_mfma_f32_16x16x32_bf16 v[88:91], a[0:3], a[108:111], v[88:91]
ds_read_b64_tr_b16 a[26:27], v207 offset:12288
v_mfma_f32_16x16x32_bf16 v[84:87], a[4:7], a[104:107], v[84:87]
ds_read_b64_tr_b16 a[28:29], v208 offset:8192
v_mfma_f32_16x16x32_bf16 v[92:95], a[4:7], a[108:111], v[92:95]
s_barrier
ds_read_b64_tr_b16 a[30:31], v208 offset:12288
s_waitcnt lgkmcnt(8)
v_mfma_f32_16x16x32_bf16 v[96:99], a[8:11], a[104:107], v[96:99]
ds_read_b64_tr_b16 a[0:1], v207 offset:16896
v_mfma_f32_16x16x32_bf16 v[104:107], a[8:11], a[108:111], v[104:107]
s_waitcnt vmcnt(1)
s_mov_b64 exec, 0xee
buffer_store_dword v234, v232, s[56:59], 0 offen sc1
s_mov_b64 exec, -1
ds_read_b64_tr_b16 a[2:3], v207 offset:20992
v_mfma_f32_16x16x32_bf16 v[100:103], a[12:15], a[104:107], v[100:103]
s_xor_b32 s86, s97, 0
s_add_u32 s87, s86, 0x1000
buffer_load_dwordx4 a[128:131], v229, s[56:59], s86 offen sc1
ds_read_b64_tr_b16 a[4:5], v208 offset:16896
v_mfma_f32_16x16x32_bf16 v[108:111], a[12:15], a[108:111], v[108:111]
s_waitcnt lgkmcnt(7)
ds_read_b64_tr_b16 a[6:7], v208 offset:20992
v_mfma_f32_16x16x32_bf16 v[112:115], a[16:19], a[104:107], v[112:115]
ds_read_b64_tr_b16 a[8:9], v207 offset:17408
buffer_load_dwordx4 a[132:135], v229, s[56:59], s86 offen offset:1024 sc1
v_mfma_f32_16x16x32_bf16 v[120:123], a[16:19], a[108:111], v[120:123]
ds_read_b64_tr_b16 a[10:11], v207 offset:21504
v_mfma_f32_16x16x32_bf16 v[116:119], a[20:23], a[104:107], v[116:119]
ds_read_b64_tr_b16 a[12:13], v208 offset:17408
v_mfma_f32_16x16x32_bf16 v[124:127], a[20:23], a[108:111], v[124:127]
s_waitcnt lgkmcnt(7)
ds_read_b64_tr_b16 a[14:15], v208 offset:21504
v_mfma_f32_16x16x32_bf16 v[64:67], a[24:27], a[104:107], v[64:67]
ds_read_b64_tr_b16 a[16:17], v207 offset:17920
buffer_load_dwordx4 a[136:139], v229, s[56:59], s86 offen offset:2048 sc1
v_mfma_f32_16x16x32_bf16 v[72:75], a[24:27], a[108:111], v[72:75]
ds_read_b64_tr_b16 a[18:19], v207 offset:22016
v_mfma_f32_16x16x32_bf16 v[68:71], a[28:31], a[104:107], v[68:71]
ds_read_b64_tr_b16 a[20:21], v208 offset:17920
buffer_load_dwordx4 a[140:143], v229, s[56:59], s86 offen offset:3072 sc1
v_mfma_f32_16x16x32_bf16 v[76:79], a[28:31], a[108:111], v[76:79]
s_waitcnt vmcnt(16)
ds_read_b64_tr_b16 a[22:23], v208 offset:22016
s_waitcnt lgkmcnt(8)
v_mfma_f32_16x16x32_bf16 v[80:83], a[0:3], a[112:115], v[80:83]
ds_read_b64_tr_b16 a[24:25], v207 offset:16384
v_mfma_f32_16x16x32_bf16 v[88:91], a[0:3], a[116:119], v[88:91]
ds_read_b64_tr_b16 a[26:27], v207 offset:20480
v_mfma_f32_16x16x32_bf16 v[84:87], a[4:7], a[112:115], v[84:87]
ds_read_b64_tr_b16 a[28:29], v208 offset:16384
v_mfma_f32_16x16x32_bf16 v[92:95], a[4:7], a[116:119], v[92:95]
s_waitcnt lgkmcnt(7)
ds_read_b64_tr_b16 a[30:31], v208 offset:20480
v_mfma_f32_16x16x32_bf16 v[96:99], a[8:11], a[112:115], v[96:99]
ds_read_b64_tr_b16 a[0:1], v207 offset:25088
buffer_load_dwordx4 a[144:147], v229, s[56:59], s87 offen sc1
v_mfma_f32_16x16x32_bf16 v[104:107], a[8:11], a[116:119], v[104:107]
ds_read_b64_tr_b16 a[2:3], v207 offset:29184
v_mfma_f32_16x16x32_bf16 v[100:103], a[12:15], a[112:115], v[100:103]
ds_read_b64_tr_b16 a[4:5], v208 offset:25088
buffer_load_dwordx4 a[148:151], v229, s[56:59], s87 offen offset:1024 sc1
v_mfma_f32_16x16x32_bf16 v[108:111], a[12:15], a[116:119], v[108:111]
s_waitcnt lgkmcnt(7)
ds_read_b64_tr_b16 a[6:7], v208 offset:29184
v_mfma_f32_16x16x32_bf16 v[112:115], a[16:19], a[112:115], v[112:115]
ds_read_b64_tr_b16 a[8:9], v207 offset:25600
v_mfma_f32_16x16x32_bf16 v[120:123], a[16:19], a[116:119], v[120:123]
ds_read_b64_tr_b16 a[10:11], v207 offset:29696
v_mfma_f32_16x16x32_bf16 v[116:119], a[20:23], a[112:115], v[116:119]
ds_read_b64_tr_b16 a[12:13], v208 offset:25600
buffer_load_dwordx4 a[152:155], v229, s[56:59], s87 offen offset:2048 sc1
v_mfma_f32_16x16x32_bf16 v[124:127], a[20:23], a[116:119], v[124:127]
s_waitcnt lgkmcnt(7)
ds_read_b64_tr_b16 a[14:15], v208 offset:29696
v_mfma_f32_16x16x32_bf16 v[64:67], a[24:27], a[112:115], v[64:67]
ds_read_b64_tr_b16 a[16:17], v207 offset:26112
v_mfma_f32_16x16x32_bf16 v[72:75], a[24:27], a[116:119], v[72:75]
ds_read_b64_tr_b16 a[18:19], v207 offset:30208
v_mfma_f32_16x16x32_bf16 v[68:71], a[28:31], a[112:115], v[68:71]
ds_read_b64_tr_b16 a[20:21], v208 offset:26112
buffer_load_dwordx4 a[156:159], v229, s[56:59], s87 offen offset:3072 sc1
v_mfma_f32_16x16x32_bf16 v[76:79], a[28:31], a[116:119], v[76:79]
s_waitcnt vmcnt(18)
ds_read_b64_tr_b16 a[22:23], v208 offset:30208
s_waitcnt lgkmcnt(8)
v_mfma_f32_16x16x32_bf16 v[80:83], a[0:3], a[120:123], v[80:83]
ds_read_b64_tr_b16 a[24:25], v207 offset:24576
v_mfma_f32_16x16x32_bf16 v[88:91], a[0:3], a[124:127], v[88:91]
ds_read_b64_tr_b16 a[26:27], v207 offset:28672
v_mfma_f32_16x16x32_bf16 v[84:87], a[4:7], a[120:123], v[84:87]
ds_read_b64_tr_b16 a[28:29], v208 offset:24576
v_mfma_f32_16x16x32_bf16 v[92:95], a[4:7], a[124:127], v[92:95]
s_barrier
ds_read_b64_tr_b16 a[30:31], v208 offset:28672
s_waitcnt lgkmcnt(8)
v_mfma_f32_16x16x32_bf16 v[96:99], a[8:11], a[120:123], v[96:99]
ds_read_b64_tr_b16 a[0:1], v203 offset:33280
v_mfma_f32_16x16x32_bf16 v[104:107], a[8:11], a[124:127], v[104:107]
ds_read_b64_tr_b16 a[2:3], v203 offset:37376
v_mfma_f32_16x16x32_bf16 v[100:103], a[12:15], a[120:123], v[100:103]
ds_read_b64_tr_b16 a[4:5], v204 offset:33280
v_mfma_f32_16x16x32_bf16 v[108:111], a[12:15], a[124:127], v[108:111]
s_waitcnt lgkmcnt(7)
ds_read_b64_tr_b16 a[6:7], v204 offset:37376
v_mfma_f32_16x16x32_bf16 v[112:115], a[16:19], a[120:123], v[112:115]
ds_read_b64_tr_b16 a[8:9], v203 offset:33792
v_mfma_f32_16x16x32_bf16 v[120:123], a[16:19], a[124:127], v[120:123]
s_xor_b32 s86, s98, 0
s_add_u32 s87, s86, 0x1000
buffer_load_dwordx4 a[160:163], v229, s[56:59], s86 offen sc1
ds_read_b64_tr_b16 a[10:11], v203 offset:37888
v_mfma_f32_16x16x32_bf16 v[116:119], a[20:23], a[120:123], v[116:119]
ds_read_b64_tr_b16 a[12:13], v204 offset:33792
v_mfma_f32_16x16x32_bf16 v[124:127], a[20:23], a[124:127], v[124:127]
s_waitcnt lgkmcnt(7)
ds_read_b64_tr_b16 a[14:15], v204 offset:37888
v_mfma_f32_16x16x32_bf16 v[64:67], a[24:27], a[120:123], v[64:67]
ds_read_b64_tr_b16 a[16:17], v203 offset:34304
v_mfma_f32_16x16x32_bf16 v[72:75], a[24:27], a[124:127], v[72:75]
ds_read_b64_tr_b16 a[18:19], v203 offset:38400
v_mfma_f32_16x16x32_bf16 v[68:71], a[28:31], a[120:123], v[68:71]
ds_read_b64_tr_b16 a[20:21], v204 offset:34304
buffer_load_dwordx4 a[164:167], v229, s[56:59], s86 offen offset:1024 sc1
v_mfma_f32_16x16x32_bf16 v[76:79], a[28:31], a[124:127], v[76:79]
s_waitcnt vmcnt(8)
ds_read_b64_tr_b16 a[22:23], v204 offset:38400
s_waitcnt lgkmcnt(8)
v_mfma_f32_16x16x32_bf16 v[80:83], a[0:3], a[128:131], v[80:83]
ds_read_b64_tr_b16 a[24:25], v203 offset:32768
v_mfma_f32_16x16x32_bf16 v[88:91], a[0:3], a[132:135], v[88:91]
ds_read_b64_tr_b16 a[26:27], v203 offset:36864
v_mfma_f32_16x16x32_bf16 v[84:87], a[4:7], a[128:131], v[84:87]
ds_read_b64_tr_b16 a[28:29], v204 offset:32768
v_mfma_f32_16x16x32_bf16 v[92:95], a[4:7], a[132:135], v[92:95]
s_waitcnt lgkmcnt(7)
ds_read_b64_tr_b16 a[30:31], v204 offset:36864
v_mfma_f32_16x16x32_bf16 v[96:99], a[8:11], a[128:131], v[96:99]
ds_read_b64_tr_b16 a[0:1], v203 offset:41472
v_mfma_f32_16x16x32_bf16 v[104:107], a[8:11], a[132:135], v[104:107]
ds_read_b64_tr_b16 a[2:3], v203 offset:45568
v_mfma_f32_16x16x32_bf16 v[100:103], a[12:15], a[128:131], v[100:103]
ds_read_b64_tr_b16 a[4:5], v204 offset:41472
buffer_load_dwordx4 a[168:171], v229, s[56:59], s86 offen offset:2048 sc1
v_mfma_f32_16x16x32_bf16 v[108:111], a[12:15], a[132:135], v[108:111]
s_waitcnt lgkmcnt(7)
ds_read_b64_tr_b16 a[6:7], v204 offset:45568
v_mfma_f32_16x16x32_bf16 v[112:115], a[16:19], a[128:131], v[112:115]
ds_read_b64_tr_b16 a[8:9], v203 offset:41984
v_mfma_f32_16x16x32_bf16 v[120:123], a[16:19], a[132:135], v[120:123]
ds_read_b64_tr_b16 a[10:11], v203 offset:46080
v_mfma_f32_16x16x32_bf16 v[116:119], a[20:23], a[128:131], v[116:119]
ds_read_b64_tr_b16 a[12:13], v204 offset:41984
v_mfma_f32_16x16x32_bf16 v[124:127], a[20:23], a[132:135], v[124:127]
s_waitcnt lgkmcnt(7)
ds_read_b64_tr_b16 a[14:15], v204 offset:46080
v_mfma_f32_16x16x32_bf16 v[64:67], a[24:27], a[128:131], v[64:67]
ds_read_b64_tr_b16 a[16:17], v203 offset:42496
buffer_load_dwordx4 a[172:175], v229, s[56:59], s86 offen offset:3072 sc1
v_mfma_f32_16x16x32_bf16 v[72:75], a[24:27], a[132:135], v[72:75]
ds_read_b64_tr_b16 a[18:19], v203 offset:46592
v_mfma_f32_16x16x32_bf16 v[68:71], a[28:31], a[128:131], v[68:71]
ds_read_b64_tr_b16 a[20:21], v204 offset:42496
v_mfma_f32_16x16x32_bf16 v[76:79], a[28:31], a[132:135], v[76:79]
s_waitcnt vmcnt(8)
ds_read_b64_tr_b16 a[22:23], v204 offset:46592
s_waitcnt lgkmcnt(8)
v_mfma_f32_16x16x32_bf16 v[80:83], a[0:3], a[136:139], v[80:83]
ds_read_b64_tr_b16 a[24:25], v203 offset:40960
v_mfma_f32_16x16x32_bf16 v[88:91], a[0:3], a[140:143], v[88:91]
ds_read_b64_tr_b16 a[26:27], v203 offset:45056
buffer_load_dwordx4 a[176:179], v229, s[56:59], s87 offen sc1
v_mfma_f32_16x16x32_bf16 v[84:87], a[4:7], a[136:139], v[84:87]
ds_read_b64_tr_b16 a[28:29], v204 offset:40960
v_mfma_f32_16x16x32_bf16 v[92:95], a[4:7], a[140:143], v[92:95]
s_barrier
ds_read_b64_tr_b16 a[30:31], v204 offset:45056
s_waitcnt lgkmcnt(8)
v_mfma_f32_16x16x32_bf16 v[96:99], a[8:11], a[136:139], v[96:99]
ds_read_b64_tr_b16 a[0:1], v205 offset:49664
v_mfma_f32_16x16x32_bf16 v[104:107], a[8:11], a[140:143], v[104:107]
ds_read_b64_tr_b16 a[2:3], v205 offset:53760
v_mfma_f32_16x16x32_bf16 v[100:103], a[12:15], a[136:139], v[100:103]
ds_read_b64_tr_b16 a[4:5], v206 offset:49664
)"
R"(
v_mfma_f32_16x16x32_bf16 v[108:111], a[12:15], a[140:143], v[108:111]
s_waitcnt lgkmcnt(7)
ds_read_b64_tr_b16 a[6:7], v206 offset:53760
v_mfma_f32_16x16x32_bf16 v[112:115], a[16:19], a[136:139], v[112:115]
ds_read_b64_tr_b16 a[8:9], v205 offset:50176
buffer_load_dwordx4 a[180:183], v229, s[56:59], s87 offen offset:1024 sc1
v_mfma_f32_16x16x32_bf16 v[120:123], a[16:19], a[140:143], v[120:123]
ds_read_b64_tr_b16 a[10:11], v205 offset:54272
v_mfma_f32_16x16x32_bf16 v[116:119], a[20:23], a[136:139], v[116:119]
ds_read_b64_tr_b16 a[12:13], v206 offset:50176
v_mfma_f32_16x16x32_bf16 v[124:127], a[20:23], a[140:143], v[124:127]
s_waitcnt lgkmcnt(7)
ds_read_b64_tr_b16 a[14:15], v206 offset:54272
v_mfma_f32_16x16x32_bf16 v[64:67], a[24:27], a[136:139], v[64:67]
ds_read_b64_tr_b16 a[16:17], v205 offset:50688
v_mfma_f32_16x16x32_bf16 v[72:75], a[24:27], a[140:143], v[72:75]
ds_read_b64_tr_b16 a[18:19], v205 offset:54784
v_mfma_f32_16x16x32_bf16 v[68:71], a[28:31], a[136:139], v[68:71]
ds_read_b64_tr_b16 a[20:21], v206 offset:50688
buffer_load_dwordx4 a[184:187], v229, s[56:59], s87 offen offset:2048 sc1
v_mfma_f32_16x16x32_bf16 v[76:79], a[28:31], a[140:143], v[76:79]
s_waitcnt vmcnt(9)
ds_read_b64_tr_b16 a[22:23], v206 offset:54784
s_waitcnt lgkmcnt(8)
v_mfma_f32_16x16x32_bf16 v[80:83], a[0:3], a[144:147], v[80:83]
ds_read_b64_tr_b16 a[24:25], v205 offset:49152
v_mfma_f32_16x16x32_bf16 v[88:91], a[0:3], a[148:151], v[88:91]
ds_read_b64_tr_b16 a[26:27], v205 offset:53248
v_mfma_f32_16x16x32_bf16 v[84:87], a[4:7], a[144:147], v[84:87]
ds_read_b64_tr_b16 a[28:29], v206 offset:49152
v_mfma_f32_16x16x32_bf16 v[92:95], a[4:7], a[148:151], v[92:95]
s_waitcnt lgkmcnt(7)
ds_read_b64_tr_b16 a[30:31], v206 offset:53248
v_mfma_f32_16x16x32_bf16 v[96:99], a[8:11], a[144:147], v[96:99]
ds_read_b64_tr_b16 a[0:1], v205 offset:57856
buffer_load_dwordx4 a[188:191], v229, s[56:59], s87 offen offset:3072 sc1
v_mfma_f32_16x16x32_bf16 v[104:107], a[8:11], a[148:151], v[104:107]
ds_read_b64_tr_b16 a[2:3], v205 offset:61952
v_mfma_f32_16x16x32_bf16 v[100:103], a[12:15], a[144:147], v[100:103]
ds_read_b64_tr_b16 a[4:5], v206 offset:57856
v_mfma_f32_16x16x32_bf16 v[108:111], a[12:15], a[148:151], v[108:111]
s_waitcnt lgkmcnt(7)
ds_read_b64_tr_b16 a[6:7], v206 offset:61952
v_mfma_f32_16x16x32_bf16 v[112:115], a[16:19], a[144:147], v[112:115]
ds_read_b64_tr_b16 a[8:9], v205 offset:58368
v_mfma_f32_16x16x32_bf16 v[120:123], a[16:19], a[148:151], v[120:123]
ds_read_b64_tr_b16 a[10:11], v205 offset:62464
v_mfma_f32_16x16x32_bf16 v[116:119], a[20:23], a[144:147], v[116:119]
ds_read_b64_tr_b16 a[12:13], v206 offset:58368
v_mfma_f32_16x16x32_bf16 v[124:127], a[20:23], a[148:151], v[124:127]
s_waitcnt lgkmcnt(7)
ds_read_b64_tr_b16 a[14:15], v206 offset:62464
v_mfma_f32_16x16x32_bf16 v[64:67], a[24:27], a[144:147], v[64:67]
ds_read_b64_tr_b16 a[16:17], v205 offset:58880
v_mfma_f32_16x16x32_bf16 v[72:75], a[24:27], a[148:151], v[72:75]
ds_read_b64_tr_b16 a[18:19], v205 offset:62976
v_mfma_f32_16x16x32_bf16 v[68:71], a[28:31], a[144:147], v[68:71]
ds_read_b64_tr_b16 a[20:21], v206 offset:58880
buffer_load_dword v235, v231, s[56:59], 0 offen sc1
v_mfma_f32_16x16x32_bf16 v[76:79], a[28:31], a[148:151], v[76:79]
s_waitcnt vmcnt(9)
ds_read_b64_tr_b16 a[22:23], v206 offset:62976
s_waitcnt lgkmcnt(8)
v_mfma_f32_16x16x32_bf16 v[80:83], a[0:3], a[152:155], v[80:83]
ds_read_b64_tr_b16 a[24:25], v205 offset:57344
v_mfma_f32_16x16x32_bf16 v[88:91], a[0:3], a[156:159], v[88:91]
ds_read_b64_tr_b16 a[26:27], v205 offset:61440
v_mfma_f32_16x16x32_bf16 v[84:87], a[4:7], a[152:155], v[84:87]
ds_read_b64_tr_b16 a[28:29], v206 offset:57344
v_mfma_f32_16x16x32_bf16 v[92:95], a[4:7], a[156:159], v[92:95]
s_barrier
ds_read_b64_tr_b16 a[30:31], v206 offset:61440
s_waitcnt lgkmcnt(8)
v_mfma_f32_16x16x32_bf16 v[96:99], a[8:11], a[152:155], v[96:99]
ds_read_b64_tr_b16 a[0:1], v205 offset:512
v_mfma_f32_16x16x32_bf16 v[104:107], a[8:11], a[156:159], v[104:107]
ds_read_b64_tr_b16 a[2:3], v205 offset:4608
v_mfma_f32_16x16x32_bf16 v[100:103], a[12:15], a[152:155], v[100:103]
ds_read_b64_tr_b16 a[4:5], v206 offset:512
v_mfma_f32_16x16x32_bf16 v[108:111], a[12:15], a[156:159], v[108:111]
s_waitcnt lgkmcnt(7)
ds_read_b64_tr_b16 a[6:7], v206 offset:4608
v_mfma_f32_16x16x32_bf16 v[112:115], a[16:19], a[152:155], v[112:115]
ds_read_b64_tr_b16 a[8:9], v205 offset:1024
v_mfma_f32_16x16x32_bf16 v[120:123], a[16:19], a[156:159], v[120:123]
ds_read_b64_tr_b16 a[10:11], v205 offset:5120
v_mfma_f32_16x16x32_bf16 v[116:119], a[20:23], a[152:155], v[116:119]
ds_read_b64_tr_b16 a[12:13], v206 offset:1024
v_mfma_f32_16x16x32_bf16 v[124:127], a[20:23], a[156:159], v[124:127]
s_waitcnt lgkmcnt(7)
ds_read_b64_tr_b16 a[14:15], v206 offset:5120
v_mfma_f32_16x16x32_bf16 v[64:67], a[24:27], a[152:155], v[64:67]
ds_read_b64_tr_b16 a[16:17], v205 offset:1536
v_mfma_f32_16x16x32_bf16 v[72:75], a[24:27], a[156:159], v[72:75]
ds_read_b64_tr_b16 a[18:19], v205 offset:5632
v_mfma_f32_16x16x32_bf16 v[68:71], a[28:31], a[152:155], v[68:71]
ds_read_b64_tr_b16 a[20:21], v206 offset:1536
v_mfma_f32_16x16x32_bf16 v[76:79], a[28:31], a[156:159], v[76:79]
s_waitcnt vmcnt(7)
ds_read_b64_tr_b16 a[22:23], v206 offset:5632
s_waitcnt lgkmcnt(8)
v_mfma_f32_16x16x32_bf16 v[80:83], a[0:3], a[160:163], v[80:83]
ds_read_b64_tr_b16 a[24:25], v205 offset:8704
v_mfma_f32_16x16x32_bf16 v[88:91], a[0:3], a[164:167], v[88:91]
ds_read_b64_tr_b16 a[26:27], v205 offset:12800
s_waitcnt vmcnt(0)
v_cmp_eq_u32_e64 s[74:75], v235, v233
s_nop 1
s_and_b32 s79, s74, 0xe0
s_cmp_eq_u32 s79, 0xe0
s_cbranch_scc0 .Lk_aslow2
.Lk_aok2:
v_mfma_f32_16x16x32_bf16 v[84:87], a[4:7], a[160:163], v[84:87]
s_xor_b32 s86, s96, 0x800000
s_add_u32 s87, s86, 0x1000
buffer_load_dwordx4 a[192:195], v229, s[56:59], s86 offen sc1
ds_read_b64_tr_b16 a[28:29], v206 offset:8704
v_mfma_f32_16x16x32_bf16 v[92:95], a[4:7], a[164:167], v[92:95]
s_waitcnt lgkmcnt(7)
ds_read_b64_tr_b16 a[30:31], v206 offset:12800
v_mfma_f32_16x16x32_bf16 v[96:99], a[8:11], a[160:163], v[96:99]
ds_read_b64_tr_b16 a[0:1], v205 offset:9216
buffer_load_dwordx4 a[196:199], v229, s[56:59], s86 offen offset:1024 sc1
v_mfma_f32_16x16x32_bf16 v[104:107], a[8:11], a[164:167], v[104:107]
ds_read_b64_tr_b16 a[2:3], v205 offset:13312
v_mfma_f32_16x16x32_bf16 v[100:103], a[12:15], a[160:163], v[100:103]
ds_read_b64_tr_b16 a[4:5], v206 offset:9216
buffer_load_dwordx4 a[200:203], v229, s[56:59], s86 offen offset:2048 sc1
v_mfma_f32_16x16x32_bf16 v[108:111], a[12:15], a[164:167], v[108:111]
s_waitcnt lgkmcnt(7)
ds_read_b64_tr_b16 a[6:7], v206 offset:13312
v_mfma_f32_16x16x32_bf16 v[112:115], a[16:19], a[160:163], v[112:115]
ds_read_b64_tr_b16 a[8:9], v205 offset:9728
v_mfma_f32_16x16x32_bf16 v[120:123], a[16:19], a[164:167], v[120:123]
ds_read_b64_tr_b16 a[10:11], v205 offset:13824
v_mfma_f32_16x16x32_bf16 v[116:119], a[20:23], a[160:163], v[116:119]
ds_read_b64_tr_b16 a[12:13], v206 offset:9728
buffer_load_dwordx4 a[204:207], v229, s[56:59], s86 offen offset:3072 sc1
v_mfma_f32_16x16x32_bf16 v[124:127], a[20:23], a[164:167], v[124:127]
s_barrier
s_waitcnt vmcnt(9)
ds_read_b64_tr_b16 a[14:15], v206 offset:13824
s_waitcnt lgkmcnt(8)
v_mfma_f32_16x16x32_bf16 v[80:83], a[24:27], a[168:171], v[80:83]
ds_read_b64_tr_b16 a[16:17], v205 offset:33280
v_mfma_f32_16x16x32_bf16 v[88:91], a[24:27], a[172:175], v[88:91]
ds_read_b64_tr_b16 a[18:19], v205 offset:37376
v_mfma_f32_16x16x32_bf16 v[84:87], a[28:31], a[168:171], v[84:87]
ds_read_b64_tr_b16 a[20:21], v206 offset:33280
v_mfma_f32_16x16x32_bf16 v[92:95], a[28:31], a[172:175], v[92:95]
s_waitcnt lgkmcnt(7)
ds_read_b64_tr_b16 a[22:23], v206 offset:37376
v_mfma_f32_16x16x32_bf16 v[96:99], a[0:3], a[168:171], v[96:99]
ds_read_b64_tr_b16 a[24:25], v205 offset:33792
buffer_load_dwordx4 a[208:211], v229, s[56:59], s87 offen sc1
v_mfma_f32_16x16x32_bf16 v[104:107], a[0:3], a[172:175], v[104:107]
ds_read_b64_tr_b16 a[26:27], v205 offset:37888
v_mfma_f32_16x16x32_bf16 v[100:103], a[4:7], a[168:171], v[100:103]
ds_read_b64_tr_b16 a[28:29], v206 offset:33792
v_mfma_f32_16x16x32_bf16 v[108:111], a[4:7], a[172:175], v[108:111]
s_waitcnt lgkmcnt(7)
ds_read_b64_tr_b16 a[30:31], v206 offset:37888
buffer_load_dwordx4 a[212:215], v229, s[56:59], s87 offen offset:1024 sc1
v_mfma_f32_16x16x32_bf16 v[112:115], a[8:11], a[168:171], v[112:115]
ds_read_b64_tr_b16 a[0:1], v205 offset:34304
v_mfma_f32_16x16x32_bf16 v[120:123], a[8:11], a[172:175], v[120:123]
ds_read_b64_tr_b16 a[2:3], v205 offset:38400
v_mfma_f32_16x16x32_bf16 v[116:119], a[12:15], a[168:171], v[116:119]
ds_read_b64_tr_b16 a[4:5], v206 offset:34304
buffer_load_dwordx4 a[216:219], v229, s[56:59], s87 offen offset:2048 sc1
v_mfma_f32_16x16x32_bf16 v[124:127], a[12:15], a[172:175], v[124:127]
s_waitcnt vmcnt(10)
ds_read_b64_tr_b16 a[6:7], v206 offset:38400
s_waitcnt lgkmcnt(8)
v_mfma_f32_16x16x32_bf16 v[80:83], a[16:19], a[176:179], v[80:83]
ds_read_b64_tr_b16 a[8:9], v205 offset:41472
buffer_load_dwordx4 a[220:223], v229, s[56:59], s87 offen offset:3072 sc1
v_mfma_f32_16x16x32_bf16 v[88:91], a[16:19], a[180:183], v[88:91]
ds_read_b64_tr_b16 a[10:11], v205 offset:45568
v_mfma_f32_16x16x32_bf16 v[84:87], a[20:23], a[176:179], v[84:87]
ds_read_b64_tr_b16 a[12:13], v206 offset:41472
v_mfma_f32_16x16x32_bf16 v[92:95], a[20:23], a[180:183], v[92:95]
s_waitcnt lgkmcnt(7)
ds_read_b64_tr_b16 a[14:15], v206 offset:45568
v_mfma_f32_16x16x32_bf16 v[96:99], a[24:27], a[176:179], v[96:99]
s_xor_b32 s86, s97, 0x800000
s_add_u32 s87, s86, 0x1000
buffer_load_dwordx4 a[224:227], v229, s[56:59], s86 offen sc1
ds_read_b64_tr_b16 a[16:17], v205 offset:41984
v_mfma_f32_16x16x32_bf16 v[104:107], a[24:27], a[180:183], v[104:107]
ds_read_b64_tr_b16 a[18:19], v205 offset:46080
v_mfma_f32_16x16x32_bf16 v[100:103], a[28:31], a[176:179], v[100:103]
ds_read_b64_tr_b16 a[20:21], v206 offset:41984
v_mfma_f32_16x16x32_bf16 v[108:111], a[28:31], a[180:183], v[108:111]
s_waitcnt lgkmcnt(7)
ds_read_b64_tr_b16 a[22:23], v206 offset:46080
v_mfma_f32_16x16x32_bf16 v[112:115], a[0:3], a[176:179], v[112:115]
ds_read_b64_tr_b16 a[24:25], v205 offset:42496
buffer_load_dwordx4 a[228:231], v229, s[56:59], s86 offen offset:1024 sc1
v_mfma_f32_16x16x32_bf16 v[120:123], a[0:3], a[180:183], v[120:123]
)"
R"(
ds_read_b64_tr_b16 a[26:27], v205 offset:46592
v_mfma_f32_16x16x32_bf16 v[116:119], a[4:7], a[176:179], v[116:119]
ds_read_b64_tr_b16 a[28:29], v206 offset:42496
v_mfma_f32_16x16x32_bf16 v[124:127], a[4:7], a[180:183], v[124:127]
s_barrier
s_waitcnt vmcnt(11)
ds_read_b64_tr_b16 a[30:31], v206 offset:46592
s_waitcnt lgkmcnt(8)
v_mfma_f32_16x16x32_bf16 v[80:83], a[8:11], a[184:187], v[80:83]
ds_read_b64_tr_b16 a[0:1], v203 offset:512
v_mfma_f32_16x16x32_bf16 v[88:91], a[8:11], a[188:191], v[88:91]
ds_read_b64_tr_b16 a[2:3], v203 offset:4608
v_mfma_f32_16x16x32_bf16 v[84:87], a[12:15], a[184:187], v[84:87]
ds_read_b64_tr_b16 a[4:5], v204 offset:512
buffer_load_dwordx4 a[232:235], v229, s[56:59], s86 offen offset:2048 sc1
v_mfma_f32_16x16x32_bf16 v[92:95], a[12:15], a[188:191], v[92:95]
s_waitcnt lgkmcnt(7)
ds_read_b64_tr_b16 a[6:7], v204 offset:4608
v_mfma_f32_16x16x32_bf16 v[96:99], a[16:19], a[184:187], v[96:99]
ds_read_b64_tr_b16 a[8:9], v203 offset:1024
v_mfma_f32_16x16x32_bf16 v[104:107], a[16:19], a[188:191], v[104:107]
ds_read_b64_tr_b16 a[10:11], v203 offset:5120
buffer_load_dwordx4 a[236:239], v229, s[56:59], s86 offen offset:3072 sc1
v_mfma_f32_16x16x32_bf16 v[100:103], a[20:23], a[184:187], v[100:103]
ds_read_b64_tr_b16 a[12:13], v204 offset:1024
v_mfma_f32_16x16x32_bf16 v[108:111], a[20:23], a[188:191], v[108:111]
s_waitcnt lgkmcnt(7)
ds_read_b64_tr_b16 a[14:15], v204 offset:5120
v_mfma_f32_16x16x32_bf16 v[112:115], a[24:27], a[184:187], v[112:115]
ds_read_b64_tr_b16 a[16:17], v203 offset:1536
buffer_load_dwordx4 a[240:243], v229, s[56:59], s87 offen sc1
v_mfma_f32_16x16x32_bf16 v[120:123], a[24:27], a[188:191], v[120:123]
ds_read_b64_tr_b16 a[18:19], v203 offset:5632
v_mfma_f32_16x16x32_bf16 v[116:119], a[28:31], a[184:187], v[116:119]
ds_read_b64_tr_b16 a[20:21], v204 offset:1536
buffer_load_dwordx4 a[244:247], v229, s[56:59], s87 offen offset:1024 sc1
v_mfma_f32_16x16x32_bf16 v[124:127], a[28:31], a[188:191], v[124:127]
s_waitcnt vmcnt(12)
ds_read_b64_tr_b16 a[22:23], v204 offset:5632
s_waitcnt lgkmcnt(8)
v_mfma_f32_16x16x32_bf16 v[80:83], a[0:3], a[192:195], v[80:83]
ds_read_b64_tr_b16 a[24:25], v203 offset:8704
v_mfma_f32_16x16x32_bf16 v[88:91], a[0:3], a[196:199], v[88:91]
ds_read_b64_tr_b16 a[26:27], v203 offset:12800
v_mfma_f32_16x16x32_bf16 v[84:87], a[4:7], a[192:195], v[84:87]
ds_read_b64_tr_b16 a[28:29], v204 offset:8704
buffer_load_dwordx4 a[248:251], v229, s[56:59], s87 offen offset:2048 sc1
v_mfma_f32_16x16x32_bf16 v[92:95], a[4:7], a[196:199], v[92:95]
s_waitcnt lgkmcnt(7)
ds_read_b64_tr_b16 a[30:31], v204 offset:12800
v_mfma_f32_16x16x32_bf16 v[96:99], a[8:11], a[192:195], v[96:99]
ds_read_b64_tr_b16 a[0:1], v203 offset:9216
v_mfma_f32_16x16x32_bf16 v[104:107], a[8:11], a[196:199], v[104:107]
ds_read_b64_tr_b16 a[2:3], v203 offset:13312
v_mfma_f32_16x16x32_bf16 v[100:103], a[12:15], a[192:195], v[100:103]
ds_read_b64_tr_b16 a[4:5], v204 offset:9216
buffer_load_dwordx4 a[252:255], v229, s[56:59], s87 offen offset:3072 sc1
v_mfma_f32_16x16x32_bf16 v[108:111], a[12:15], a[196:199], v[108:111]
s_waitcnt lgkmcnt(7)
ds_read_b64_tr_b16 a[6:7], v204 offset:13312
v_mfma_f32_16x16x32_bf16 v[112:115], a[16:19], a[192:195], v[112:115]
ds_read_b64_tr_b16 a[8:9], v203 offset:9728
v_mfma_f32_16x16x32_bf16 v[120:123], a[16:19], a[196:199], v[120:123]
ds_read_b64_tr_b16 a[10:11], v203 offset:13824
v_mfma_f32_16x16x32_bf16 v[116:119], a[20:23], a[192:195], v[116:119]
s_xor_b32 s86, s98, 0x800000
s_add_u32 s87, s86, 0x1000
buffer_load_dwordx4 a[64:67], v229, s[56:59], s86 offen sc1
ds_read_b64_tr_b16 a[12:13], v204 offset:9728
v_mfma_f32_16x16x32_bf16 v[124:127], a[20:23], a[196:199], v[124:127]
s_barrier
s_waitcnt vmcnt(13)
ds_read_b64_tr_b16 a[14:15], v204 offset:13824
s_waitcnt lgkmcnt(8)
v_mfma_f32_16x16x32_bf16 v[80:83], a[24:27], a[200:203], v[80:83]
ds_read_b64_tr_b16 a[16:17], v203 offset:49664
v_mfma_f32_16x16x32_bf16 v[88:91], a[24:27], a[204:207], v[88:91]
ds_read_b64_tr_b16 a[18:19], v203 offset:53760
v_mfma_f32_16x16x32_bf16 v[84:87], a[28:31], a[200:203], v[84:87]
ds_read_b64_tr_b16 a[20:21], v204 offset:49664
v_mfma_f32_16x16x32_bf16 v[92:95], a[28:31], a[204:207], v[92:95]
s_waitcnt lgkmcnt(7)
ds_read_b64_tr_b16 a[22:23], v204 offset:53760
v_mfma_f32_16x16x32_bf16 v[96:99], a[0:3], a[200:203], v[96:99]
ds_read_b64_tr_b16 a[24:25], v203 offset:50176
buffer_load_dwordx4 a[68:71], v229, s[56:59], s86 offen offset:1024 sc1
v_mfma_f32_16x16x32_bf16 v[104:107], a[0:3], a[204:207], v[104:107]
ds_read_b64_tr_b16 a[26:27], v203 offset:54272
v_mfma_f32_16x16x32_bf16 v[100:103], a[4:7], a[200:203], v[100:103]
ds_read_b64_tr_b16 a[28:29], v204 offset:50176
v_mfma_f32_16x16x32_bf16 v[108:111], a[4:7], a[204:207], v[108:111]
s_waitcnt lgkmcnt(7)
ds_read_b64_tr_b16 a[30:31], v204 offset:54272
v_mfma_f32_16x16x32_bf16 v[112:115], a[8:11], a[200:203], v[112:115]
ds_read_b64_tr_b16 a[0:1], v203 offset:50688
buffer_load_dwordx4 a[72:75], v229, s[56:59], s86 offen offset:2048 sc1
v_mfma_f32_16x16x32_bf16 v[120:123], a[8:11], a[204:207], v[120:123]
ds_read_b64_tr_b16 a[2:3], v203 offset:54784
buffer_load_dwordx4 a[76:79], v229, s[56:59], s86 offen offset:3072 sc1
v_mfma_f32_16x16x32_bf16 v[116:119], a[12:15], a[200:203], v[116:119]
ds_read_b64_tr_b16 a[4:5], v204 offset:50688
v_mfma_f32_16x16x32_bf16 v[124:127], a[12:15], a[204:207], v[124:127]
s_waitcnt vmcnt(14)
ds_read_b64_tr_b16 a[6:7], v204 offset:54784
s_waitcnt lgkmcnt(8)
v_mfma_f32_16x16x32_bf16 v[80:83], a[16:19], a[208:211], v[80:83]
ds_read_b64_tr_b16 a[8:9], v203 offset:57856
v_mfma_f32_16x16x32_bf16 v[88:91], a[16:19], a[212:215], v[88:91]
ds_read_b64_tr_b16 a[10:11], v203 offset:61952
v_mfma_f32_16x16x32_bf16 v[84:87], a[20:23], a[208:211], v[84:87]
ds_read_b64_tr_b16 a[12:13], v204 offset:57856
buffer_load_dwordx4 a[80:83], v229, s[56:59], s87 offen sc1
v_mfma_f32_16x16x32_bf16 v[92:95], a[20:23], a[212:215], v[92:95]
s_waitcnt lgkmcnt(7)
ds_read_b64_tr_b16 a[14:15], v204 offset:61952
v_mfma_f32_16x16x32_bf16 v[96:99], a[24:27], a[208:211], v[96:99]
ds_read_b64_tr_b16 a[16:17], v203 offset:58368
v_mfma_f32_16x16x32_bf16 v[104:107], a[24:27], a[212:215], v[104:107]
ds_read_b64_tr_b16 a[18:19], v203 offset:62464
v_mfma_f32_16x16x32_bf16 v[100:103], a[28:31], a[208:211], v[100:103]
ds_read_b64_tr_b16 a[20:21], v204 offset:58368
buffer_load_dwordx4 a[84:87], v229, s[56:59], s87 offen offset:1024 sc1
v_mfma_f32_16x16x32_bf16 v[108:111], a[28:31], a[212:215], v[108:111]
s_waitcnt lgkmcnt(7)
ds_read_b64_tr_b16 a[22:23], v204 offset:62464
v_mfma_f32_16x16x32_bf16 v[112:115], a[0:3], a[208:211], v[112:115]
ds_read_b64_tr_b16 a[24:25], v203 offset:58880
buffer_load_dwordx4 a[88:91], v229, s[56:59], s87 offen offset:2048 sc1
v_mfma_f32_16x16x32_bf16 v[120:123], a[0:3], a[212:215], v[120:123]
ds_read_b64_tr_b16 a[26:27], v203 offset:62976
v_mfma_f32_16x16x32_bf16 v[116:119], a[4:7], a[208:211], v[116:119]
ds_read_b64_tr_b16 a[28:29], v204 offset:58880
v_mfma_f32_16x16x32_bf16 v[124:127], a[4:7], a[212:215], v[124:127]
s_barrier
s_waitcnt vmcnt(15)
ds_read_b64_tr_b16 a[30:31], v204 offset:62976
s_waitcnt lgkmcnt(8)
v_mfma_f32_16x16x32_bf16 v[80:83], a[8:11], a[216:219], v[80:83]
ds_read_b64_tr_b16 a[0:1], v203 offset:16896
v_mfma_f32_16x16x32_bf16 v[88:91], a[8:11], a[220:223], v[88:91]
ds_read_b64_tr_b16 a[2:3], v203 offset:20992
v_mfma_f32_16x16x32_bf16 v[84:87], a[12:15], a[216:219], v[84:87]
ds_read_b64_tr_b16 a[4:5], v204 offset:16896
buffer_load_dwordx4 a[92:95], v229, s[56:59], s87 offen offset:3072 sc1
v_mfma_f32_16x16x32_bf16 v[92:95], a[12:15], a[220:223], v[92:95]
s_waitcnt lgkmcnt(7)
ds_read_b64_tr_b16 a[6:7], v204 offset:20992
v_mfma_f32_16x16x32_bf16 v[96:99], a[16:19], a[216:219], v[96:99]
ds_read_b64_tr_b16 a[8:9], v203 offset:17408
v_mfma_f32_16x16x32_bf16 v[104:107], a[16:19], a[220:223], v[104:107]
ds_read_b64_tr_b16 a[10:11], v203 offset:21504
v_mfma_f32_16x16x32_bf16 v[100:103], a[20:23], a[216:219], v[100:103]
ds_read_b64_tr_b16 a[12:13], v204 offset:17408
v_mfma_f32_16x16x32_bf16 v[108:111], a[20:23], a[220:223], v[108:111]
s_waitcnt lgkmcnt(7)
ds_read_b64_tr_b16 a[14:15], v204 offset:21504
v_mfma_f32_16x16x32_bf16 v[112:115], a[24:27], a[216:219], v[112:115]
ds_read_b64_tr_b16 a[16:17], v203 offset:17920
v_mfma_f32_16x16x32_bf16 v[120:123], a[24:27], a[220:223], v[120:123]
ds_read_b64_tr_b16 a[18:19], v203 offset:22016
v_mfma_f32_16x16x32_bf16 v[116:119], a[28:31], a[216:219], v[116:119]
ds_read_b64_tr_b16 a[20:21], v204 offset:17920
v_mfma_f32_16x16x32_bf16 v[124:127], a[28:31], a[220:223], v[124:127]
s_waitcnt vmcnt(14)
ds_read_b64_tr_b16 a[22:23], v204 offset:22016
s_waitcnt lgkmcnt(8)
v_mfma_f32_16x16x32_bf16 v[80:83], a[0:3], a[224:227], v[80:83]
ds_read_b64_tr_b16 a[24:25], v203 offset:25088
v_mfma_f32_16x16x32_bf16 v[88:91], a[0:3], a[228:231], v[88:91]
ds_read_b64_tr_b16 a[26:27], v203 offset:29184
v_mfma_f32_16x16x32_bf16 v[84:87], a[4:7], a[224:227], v[84:87]
ds_read_b64_tr_b16 a[28:29], v204 offset:25088
v_mfma_f32_16x16x32_bf16 v[92:95], a[4:7], a[228:231], v[92:95]
s_waitcnt lgkmcnt(7)
ds_read_b64_tr_b16 a[30:31], v204 offset:29184
v_mfma_f32_16x16x32_bf16 v[96:99], a[8:11], a[224:227], v[96:99]
ds_read_b64_tr_b16 a[0:1], v203 offset:25600
v_mfma_f32_16x16x32_bf16 v[104:107], a[8:11], a[228:231], v[104:107]
ds_read_b64_tr_b16 a[2:3], v203 offset:29696
v_mfma_f32_16x16x32_bf16 v[100:103], a[12:15], a[224:227], v[100:103]
ds_read_b64_tr_b16 a[4:5], v204 offset:25600
v_mfma_f32_16x16x32_bf16 v[108:111], a[12:15], a[228:231], v[108:111]
s_waitcnt lgkmcnt(7)
ds_read_b64_tr_b16 a[6:7], v204 offset:29696
v_mfma_f32_16x16x32_bf16 v[112:115], a[16:19], a[224:227], v[112:115]
ds_read_b64_tr_b16 a[8:9], v203 offset:26112
v_mfma_f32_16x16x32_bf16 v[120:123], a[16:19], a[228:231], v[120:123]
ds_read_b64_tr_b16 a[10:11], v203 offset:30208
v_mfma_f32_16x16x32_bf16 v[116:119], a[20:23], a[224:227], v[116:119]
ds_read_b64_tr_b16 a[12:13], v204 offset:26112
v_mfma_f32_16x16x32_bf16 v[124:127], a[20:23], a[228:231], v[124:127]
s_barrier
s_waitcnt vmcnt(12)
ds_read_b64_tr_b16 a[14:15], v204 offset:30208
s_waitcnt lgkmcnt(8)
v_mfma_f32_16x16x32_bf16 v[80:83], a[24:27], a[232:235], v[80:83]
ds_read_b64_tr_b16 a[16:17], v205 offset:16896
v_mfma_f32_16x16x32_bf16 v[88:91], a[24:27], a[236:239], v[88:91]
ds_read_b64_tr_b16 a[18:19], v205 offset:20992
v_mfma_f32_16x16x32_bf16 v[84:87], a[28:31], a[232:235], v[84:87]
)"
R"(
ds_read_b64_tr_b16 a[20:21], v206 offset:16896
v_mfma_f32_16x16x32_bf16 v[92:95], a[28:31], a[236:239], v[92:95]
s_waitcnt lgkmcnt(7)
ds_read_b64_tr_b16 a[22:23], v206 offset:20992
v_mfma_f32_16x16x32_bf16 v[96:99], a[0:3], a[232:235], v[96:99]
ds_read_b64_tr_b16 a[24:25], v205 offset:17408
v_mfma_f32_16x16x32_bf16 v[104:107], a[0:3], a[236:239], v[104:107]
ds_read_b64_tr_b16 a[26:27], v205 offset:21504
v_mfma_f32_16x16x32_bf16 v[100:103], a[4:7], a[232:235], v[100:103]
ds_read_b64_tr_b16 a[28:29], v206 offset:17408
v_mfma_f32_16x16x32_bf16 v[108:111], a[4:7], a[236:239], v[108:111]
s_waitcnt lgkmcnt(7)
ds_read_b64_tr_b16 a[30:31], v206 offset:21504
v_mfma_f32_16x16x32_bf16 v[112:115], a[8:11], a[232:235], v[112:115]
ds_read_b64_tr_b16 a[0:1], v205 offset:17920
v_mfma_f32_16x16x32_bf16 v[120:123], a[8:11], a[236:239], v[120:123]
ds_read_b64_tr_b16 a[2:3], v205 offset:22016
v_mfma_f32_16x16x32_bf16 v[116:119], a[12:15], a[232:235], v[116:119]
ds_read_b64_tr_b16 a[4:5], v206 offset:17920
v_mfma_f32_16x16x32_bf16 v[124:127], a[12:15], a[236:239], v[124:127]
s_waitcnt vmcnt(10)
ds_read_b64_tr_b16 a[6:7], v206 offset:22016
s_waitcnt lgkmcnt(8)
v_mfma_f32_16x16x32_bf16 v[80:83], a[16:19], a[240:243], v[80:83]
ds_read_b64_tr_b16 a[8:9], v205 offset:25088
v_mfma_f32_16x16x32_bf16 v[88:91], a[16:19], a[244:247], v[88:91]
ds_read_b64_tr_b16 a[10:11], v205 offset:29184
v_mfma_f32_16x16x32_bf16 v[84:87], a[20:23], a[240:243], v[84:87]
ds_read_b64_tr_b16 a[12:13], v206 offset:25088
v_mfma_f32_16x16x32_bf16 v[92:95], a[20:23], a[244:247], v[92:95]
s_waitcnt lgkmcnt(7)
ds_read_b64_tr_b16 a[14:15], v206 offset:29184
v_mfma_f32_16x16x32_bf16 v[96:99], a[24:27], a[240:243], v[96:99]
ds_read_b64_tr_b16 a[16:17], v205 offset:25600
v_mfma_f32_16x16x32_bf16 v[104:107], a[24:27], a[244:247], v[104:107]
ds_read_b64_tr_b16 a[18:19], v205 offset:29696
v_mfma_f32_16x16x32_bf16 v[100:103], a[28:31], a[240:243], v[100:103]
ds_read_b64_tr_b16 a[20:21], v206 offset:25600
v_mfma_f32_16x16x32_bf16 v[108:111], a[28:31], a[244:247], v[108:111]
s_waitcnt lgkmcnt(7)
ds_read_b64_tr_b16 a[22:23], v206 offset:29696
v_mfma_f32_16x16x32_bf16 v[112:115], a[0:3], a[240:243], v[112:115]
ds_read_b64_tr_b16 a[24:25], v205 offset:26112
v_mfma_f32_16x16x32_bf16 v[120:123], a[0:3], a[244:247], v[120:123]
ds_read_b64_tr_b16 a[26:27], v205 offset:30208
v_mfma_f32_16x16x32_bf16 v[116:119], a[4:7], a[240:243], v[116:119]
ds_read_b64_tr_b16 a[28:29], v206 offset:26112
v_mfma_f32_16x16x32_bf16 v[124:127], a[4:7], a[244:247], v[124:127]
s_barrier
s_waitcnt vmcnt(8)
ds_read_b64_tr_b16 a[30:31], v206 offset:30208
s_waitcnt lgkmcnt(8)
v_mfma_f32_16x16x32_bf16 v[80:83], a[8:11], a[248:251], v[80:83]
ds_read_b64_tr_b16 a[0:1], v207 offset:512
v_mfma_f32_16x16x32_bf16 v[88:91], a[8:11], a[252:255], v[88:91]
ds_read_b64_tr_b16 a[2:3], v207 offset:4608
v_mfma_f32_16x16x32_bf16 v[84:87], a[12:15], a[248:251], v[84:87]
ds_read_b64_tr_b16 a[4:5], v208 offset:512
v_mfma_f32_16x16x32_bf16 v[92:95], a[12:15], a[252:255], v[92:95]
s_waitcnt lgkmcnt(7)
ds_read_b64_tr_b16 a[6:7], v208 offset:4608
v_mfma_f32_16x16x32_bf16 v[96:99], a[16:19], a[248:251], v[96:99]
ds_read_b64_tr_b16 a[8:9], v207 offset:8704
v_mfma_f32_16x16x32_bf16 v[104:107], a[16:19], a[252:255], v[104:107]
ds_read_b64_tr_b16 a[10:11], v207 offset:12800
v_mfma_f32_16x16x32_bf16 v[100:103], a[20:23], a[248:251], v[100:103]
ds_read_b64_tr_b16 a[12:13], v208 offset:8704
v_mfma_f32_16x16x32_bf16 v[108:111], a[20:23], a[252:255], v[108:111]
s_barrier
ds_read_b64_tr_b16 a[14:15], v208 offset:12800
s_waitcnt lgkmcnt(8)
v_mfma_f32_16x16x32_bf16 v[112:115], a[24:27], a[248:251], v[112:115]
ds_read_b64_tr_b16 a[16:17], v207 offset:16896
v_mfma_f32_16x16x32_bf16 v[120:123], a[24:27], a[252:255], v[120:123]
ds_read_b64_tr_b16 a[18:19], v207 offset:20992
v_mfma_f32_16x16x32_bf16 v[116:119], a[28:31], a[248:251], v[116:119]
ds_read_b64_tr_b16 a[20:21], v208 offset:16896
v_mfma_f32_16x16x32_bf16 v[124:127], a[28:31], a[252:255], v[124:127]
s_waitcnt vmcnt(6)
ds_read_b64_tr_b16 a[22:23], v208 offset:20992
s_waitcnt lgkmcnt(8)
v_mfma_f32_16x16x32_bf16 v[80:83], a[0:3], a[64:67], v[80:83]
ds_read_b64_tr_b16 a[24:25], v207 offset:25088
v_mfma_f32_16x16x32_bf16 v[88:91], a[0:3], a[68:71], v[88:91]
ds_read_b64_tr_b16 a[26:27], v207 offset:29184
v_mfma_f32_16x16x32_bf16 v[84:87], a[4:7], a[64:67], v[84:87]
ds_read_b64_tr_b16 a[28:29], v208 offset:25088
v_mfma_f32_16x16x32_bf16 v[92:95], a[4:7], a[68:71], v[92:95]
s_waitcnt vmcnt(4)
ds_read_b64_tr_b16 a[30:31], v208 offset:29184
s_waitcnt lgkmcnt(8)
v_mfma_f32_16x16x32_bf16 v[80:83], a[8:11], a[72:75], v[80:83]
ds_read_b64_tr_b16 a[0:1], v207 offset:1024
v_mfma_f32_16x16x32_bf16 v[88:91], a[8:11], a[76:79], v[88:91]
ds_read_b64_tr_b16 a[2:3], v207 offset:5120
v_mfma_f32_16x16x32_bf16 v[84:87], a[12:15], a[72:75], v[84:87]
ds_read_b64_tr_b16 a[4:5], v208 offset:1024
v_mfma_f32_16x16x32_bf16 v[92:95], a[12:15], a[76:79], v[92:95]
s_waitcnt vmcnt(2)
ds_read_b64_tr_b16 a[6:7], v208 offset:5120
s_waitcnt lgkmcnt(8)
v_mfma_f32_16x16x32_bf16 v[80:83], a[16:19], a[80:83], v[80:83]
ds_read_b64_tr_b16 a[8:9], v207 offset:9216
v_mfma_f32_16x16x32_bf16 v[88:91], a[16:19], a[84:87], v[88:91]
ds_read_b64_tr_b16 a[10:11], v207 offset:13312
v_mfma_f32_16x16x32_bf16 v[84:87], a[20:23], a[80:83], v[84:87]
ds_read_b64_tr_b16 a[12:13], v208 offset:9216
v_mfma_f32_16x16x32_bf16 v[92:95], a[20:23], a[84:87], v[92:95]
s_waitcnt vmcnt(0)
ds_read_b64_tr_b16 a[14:15], v208 offset:13312
s_waitcnt lgkmcnt(8)
v_mfma_f32_16x16x32_bf16 v[80:83], a[24:27], a[88:91], v[80:83]
ds_read_b64_tr_b16 a[16:17], v207 offset:17408
v_mfma_f32_16x16x32_bf16 v[88:91], a[24:27], a[92:95], v[88:91]
ds_read_b64_tr_b16 a[18:19], v207 offset:21504
v_mfma_f32_16x16x32_bf16 v[84:87], a[28:31], a[88:91], v[84:87]
ds_read_b64_tr_b16 a[20:21], v208 offset:17408
v_mfma_f32_16x16x32_bf16 v[92:95], a[28:31], a[92:95], v[92:95]
s_waitcnt lgkmcnt(7)
ds_read_b64_tr_b16 a[22:23], v208 offset:21504
v_mfma_f32_16x16x32_bf16 v[96:99], a[0:3], a[64:67], v[96:99]
ds_read_b64_tr_b16 a[24:25], v207 offset:25600
v_cvt_pk_bf16_f32 v136, v80, v81
v_cvt_pk_bf16_f32 v137, v82, v83
v_mfma_f32_16x16x32_bf16 v[104:107], a[0:3], a[68:71], v[104:107]
ds_read_b64_tr_b16 a[26:27], v207 offset:29696
v_cvt_pk_bf16_f32 v140, v88, v89
v_cvt_pk_bf16_f32 v141, v90, v91
v_mfma_f32_16x16x32_bf16 v[100:103], a[4:7], a[64:67], v[100:103]
ds_read_b64_tr_b16 a[28:29], v208 offset:25600
v_cvt_pk_bf16_f32 v138, v84, v85
v_cvt_pk_bf16_f32 v139, v86, v87
v_mfma_f32_16x16x32_bf16 v[108:111], a[4:7], a[68:71], v[108:111]
s_waitcnt lgkmcnt(7)
ds_read_b64_tr_b16 a[30:31], v208 offset:29696
v_cvt_pk_bf16_f32 v142, v92, v93
v_mfma_f32_16x16x32_bf16 v[96:99], a[8:11], a[72:75], v[96:99]
ds_read_b64_tr_b16 a[0:1], v207 offset:1536
v_cvt_pk_bf16_f32 v143, v94, v95
v_mfma_f32_16x16x32_bf16 v[104:107], a[8:11], a[76:79], v[104:107]
ds_read_b64_tr_b16 a[2:3], v207 offset:5632
v_mfma_f32_16x16x32_bf16 v[100:103], a[12:15], a[72:75], v[100:103]
s_add_u32 s84, s82, 0x200000
buffer_store_dwordx4 v[136:139], v229, s[56:59], s84 offen sc1
ds_read_b64_tr_b16 a[4:5], v208 offset:1536
v_mfma_f32_16x16x32_bf16 v[108:111], a[12:15], a[76:79], v[108:111]
s_waitcnt lgkmcnt(7)
ds_read_b64_tr_b16 a[6:7], v208 offset:5632
v_mfma_f32_16x16x32_bf16 v[96:99], a[16:19], a[80:83], v[96:99]
ds_read_b64_tr_b16 a[8:9], v207 offset:9728
buffer_store_dwordx4 v[140:143], v229, s[56:59], s84 offen offset:1024 sc1
v_mfma_f32_16x16x32_bf16 v[104:107], a[16:19], a[84:87], v[104:107]
ds_read_b64_tr_b16 a[10:11], v207 offset:13824
v_mfma_f32_16x16x32_bf16 v[100:103], a[20:23], a[80:83], v[100:103]
ds_read_b64_tr_b16 a[12:13], v208 offset:9728
v_mfma_f32_16x16x32_bf16 v[108:111], a[20:23], a[84:87], v[108:111]
s_waitcnt lgkmcnt(7)
ds_read_b64_tr_b16 a[14:15], v208 offset:13824
v_mfma_f32_16x16x32_bf16 v[96:99], a[24:27], a[88:91], v[96:99]
ds_read_b64_tr_b16 a[16:17], v207 offset:17920
v_mfma_f32_16x16x32_bf16 v[104:107], a[24:27], a[92:95], v[104:107]
ds_read_b64_tr_b16 a[18:19], v207 offset:22016
v_mfma_f32_16x16x32_bf16 v[100:103], a[28:31], a[88:91], v[100:103]
ds_read_b64_tr_b16 a[20:21], v208 offset:17920
v_mfma_f32_16x16x32_bf16 v[108:111], a[28:31], a[92:95], v[108:111]
s_waitcnt lgkmcnt(7)
ds_read_b64_tr_b16 a[22:23], v208 offset:22016
v_mfma_f32_16x16x32_bf16 v[112:115], a[0:3], a[64:67], v[112:115]
ds_read_b64_tr_b16 a[24:25], v207 offset:26112
v_cvt_pk_bf16_f32 v144, v96, v97
v_cvt_pk_bf16_f32 v145, v98, v99
v_mfma_f32_16x16x32_bf16 v[120:123], a[0:3], a[68:71], v[120:123]
ds_read_b64_tr_b16 a[26:27], v207 offset:30208
v_cvt_pk_bf16_f32 v148, v104, v105
v_cvt_pk_bf16_f32 v149, v106, v107
v_mfma_f32_16x16x32_bf16 v[116:119], a[4:7], a[64:67], v[116:119]
ds_read_b64_tr_b16 a[28:29], v208 offset:26112
v_cvt_pk_bf16_f32 v146, v100, v101
v_cvt_pk_bf16_f32 v147, v102, v103
v_mfma_f32_16x16x32_bf16 v[124:127], a[4:7], a[68:71], v[124:127]
s_waitcnt lgkmcnt(7)
ds_read_b64_tr_b16 a[30:31], v208 offset:30208
v_cvt_pk_bf16_f32 v150, v108, v109
v_mfma_f32_16x16x32_bf16 v[112:115], a[8:11], a[72:75], v[112:115]
ds_read_b64_tr_b16 a[0:1], v205
v_cvt_pk_bf16_f32 v151, v110, v111
v_mfma_f32_16x16x32_bf16 v[120:123], a[8:11], a[76:79], v[120:123]
ds_read_b64_tr_b16 a[2:3], v205 offset:4096
v_mfma_f32_16x16x32_bf16 v[116:119], a[12:15], a[72:75], v[116:119]
s_add_u32 s84, s82, 0x400000
buffer_store_dwordx4 v[144:147], v229, s[56:59], s84 offen sc1
ds_read_b64_tr_b16 a[4:5], v206
v_mfma_f32_16x16x32_bf16 v[124:127], a[12:15], a[76:79], v[124:127]
s_waitcnt lgkmcnt(7)
ds_read_b64_tr_b16 a[6:7], v206 offset:4096
v_mfma_f32_16x16x32_bf16 v[112:115], a[16:19], a[80:83], v[112:115]
ds_read_b64_tr_b16 a[8:9], v205 offset:8192
buffer_store_dwordx4 v[148:151], v229, s[56:59], s84 offen offset:1024 sc1
v_mfma_f32_16x16x32_bf16 v[120:123], a[16:19], a[84:87], v[120:123]
ds_read_b64_tr_b16 a[10:11], v205 offset:12288
)"
R"(
v_mfma_f32_16x16x32_bf16 v[116:119], a[20:23], a[80:83], v[116:119]
ds_read_b64_tr_b16 a[12:13], v206 offset:8192
v_mfma_f32_16x16x32_bf16 v[124:127], a[20:23], a[84:87], v[124:127]
s_waitcnt lgkmcnt(7)
ds_read_b64_tr_b16 a[14:15], v206 offset:12288
v_mfma_f32_16x16x32_bf16 v[112:115], a[24:27], a[88:91], v[112:115]
ds_read_b64_tr_b16 a[16:17], v205 offset:32768
v_mfma_f32_16x16x32_bf16 v[120:123], a[24:27], a[92:95], v[120:123]
ds_read_b64_tr_b16 a[18:19], v205 offset:36864
v_mfma_f32_16x16x32_bf16 v[116:119], a[28:31], a[88:91], v[116:119]
ds_read_b64_tr_b16 a[20:21], v206 offset:32768
v_mfma_f32_16x16x32_bf16 v[124:127], a[28:31], a[92:95], v[124:127]
s_waitcnt lgkmcnt(7)
ds_read_b64_tr_b16 a[22:23], v206 offset:36864
v_mfma_f32_16x16x32_bf16 v[64:67], a[0:3], a[160:163], v[64:67]
ds_read_b64_tr_b16 a[24:25], v205 offset:40960
v_cvt_pk_bf16_f32 v152, v112, v113
v_cvt_pk_bf16_f32 v153, v114, v115
v_mfma_f32_16x16x32_bf16 v[72:75], a[0:3], a[164:167], v[72:75]
ds_read_b64_tr_b16 a[26:27], v205 offset:45056
v_cvt_pk_bf16_f32 v156, v120, v121
v_cvt_pk_bf16_f32 v157, v122, v123
v_mfma_f32_16x16x32_bf16 v[68:71], a[4:7], a[160:163], v[68:71]
ds_read_b64_tr_b16 a[28:29], v206 offset:40960
v_cvt_pk_bf16_f32 v154, v116, v117
v_cvt_pk_bf16_f32 v155, v118, v119
v_mfma_f32_16x16x32_bf16 v[76:79], a[4:7], a[164:167], v[76:79]
s_waitcnt lgkmcnt(7)
ds_read_b64_tr_b16 a[30:31], v206 offset:45056
v_cvt_pk_bf16_f32 v158, v124, v125
v_mfma_f32_16x16x32_bf16 v[64:67], a[8:11], a[168:171], v[64:67]
ds_read_b64_tr_b16 a[0:1], v203
v_cvt_pk_bf16_f32 v159, v126, v127
v_mfma_f32_16x16x32_bf16 v[72:75], a[8:11], a[172:175], v[72:75]
ds_read_b64_tr_b16 a[2:3], v203 offset:4096
v_mfma_f32_16x16x32_bf16 v[68:71], a[12:15], a[168:171], v[68:71]
s_add_u32 s84, s82, 0x600000
buffer_store_dwordx4 v[152:155], v229, s[56:59], s84 offen sc1
ds_read_b64_tr_b16 a[4:5], v204
v_mfma_f32_16x16x32_bf16 v[76:79], a[12:15], a[172:175], v[76:79]
s_waitcnt lgkmcnt(7)
ds_read_b64_tr_b16 a[6:7], v204 offset:4096
v_mfma_f32_16x16x32_bf16 v[64:67], a[16:19], a[176:179], v[64:67]
ds_read_b64_tr_b16 a[8:9], v203 offset:8192
buffer_store_dwordx4 v[156:159], v229, s[56:59], s84 offen offset:1024 sc1
v_mfma_f32_16x16x32_bf16 v[72:75], a[16:19], a[180:183], v[72:75]
ds_read_b64_tr_b16 a[10:11], v203 offset:12288
v_mfma_f32_16x16x32_bf16 v[68:71], a[20:23], a[176:179], v[68:71]
ds_read_b64_tr_b16 a[12:13], v204 offset:8192
v_mfma_f32_16x16x32_bf16 v[76:79], a[20:23], a[180:183], v[76:79]
s_waitcnt lgkmcnt(7)
ds_read_b64_tr_b16 a[14:15], v204 offset:12288
v_mfma_f32_16x16x32_bf16 v[64:67], a[24:27], a[184:187], v[64:67]
ds_read_b64_tr_b16 a[16:17], v203 offset:49152
v_mfma_f32_16x16x32_bf16 v[72:75], a[24:27], a[188:191], v[72:75]
ds_read_b64_tr_b16 a[18:19], v203 offset:53248
v_mfma_f32_16x16x32_bf16 v[68:71], a[28:31], a[184:187], v[68:71]
ds_read_b64_tr_b16 a[20:21], v204 offset:49152
v_mfma_f32_16x16x32_bf16 v[76:79], a[28:31], a[188:191], v[76:79]
s_waitcnt lgkmcnt(7)
ds_read_b64_tr_b16 a[22:23], v204 offset:53248
v_mfma_f32_16x16x32_bf16 v[64:67], a[0:3], a[192:195], v[64:67]
ds_read_b64_tr_b16 a[24:25], v203 offset:57344
v_mfma_f32_16x16x32_bf16 v[72:75], a[0:3], a[196:199], v[72:75]
ds_read_b64_tr_b16 a[26:27], v203 offset:61440
v_mfma_f32_16x16x32_bf16 v[68:71], a[4:7], a[192:195], v[68:71]
ds_read_b64_tr_b16 a[28:29], v204 offset:57344
v_mfma_f32_16x16x32_bf16 v[76:79], a[4:7], a[196:199], v[76:79]
s_waitcnt lgkmcnt(7)
ds_read_b64_tr_b16 a[30:31], v204 offset:61440
v_mfma_f32_16x16x32_bf16 v[64:67], a[8:11], a[200:203], v[64:67]
ds_read_b64_tr_b16 a[0:1], v203 offset:16384
v_mfma_f32_16x16x32_bf16 v[72:75], a[8:11], a[204:207], v[72:75]
ds_read_b64_tr_b16 a[2:3], v203 offset:20480
v_mfma_f32_16x16x32_bf16 v[68:71], a[12:15], a[200:203], v[68:71]
ds_read_b64_tr_b16 a[4:5], v204 offset:16384
v_mfma_f32_16x16x32_bf16 v[76:79], a[12:15], a[204:207], v[76:79]
s_waitcnt lgkmcnt(7)
ds_read_b64_tr_b16 a[6:7], v204 offset:20480
v_mfma_f32_16x16x32_bf16 v[64:67], a[16:19], a[208:211], v[64:67]
ds_read_b64_tr_b16 a[8:9], v203 offset:24576
v_mfma_f32_16x16x32_bf16 v[72:75], a[16:19], a[212:215], v[72:75]
ds_read_b64_tr_b16 a[10:11], v203 offset:28672
v_mfma_f32_16x16x32_bf16 v[68:71], a[20:23], a[208:211], v[68:71]
ds_read_b64_tr_b16 a[12:13], v204 offset:24576
buffer_load_dword v235, v231, s[56:59], 0 offen sc1
v_mfma_f32_16x16x32_bf16 v[76:79], a[20:23], a[212:215], v[76:79]
s_waitcnt lgkmcnt(7)
ds_read_b64_tr_b16 a[14:15], v204 offset:28672
v_mfma_f32_16x16x32_bf16 v[64:67], a[24:27], a[216:219], v[64:67]
ds_read_b64_tr_b16 a[16:17], v205 offset:16384
v_mfma_f32_16x16x32_bf16 v[72:75], a[24:27], a[220:223], v[72:75]
ds_read_b64_tr_b16 a[18:19], v205 offset:20480
v_mfma_f32_16x16x32_bf16 v[68:71], a[28:31], a[216:219], v[68:71]
ds_read_b64_tr_b16 a[20:21], v206 offset:16384
v_mfma_f32_16x16x32_bf16 v[76:79], a[28:31], a[220:223], v[76:79]
s_waitcnt lgkmcnt(7)
ds_read_b64_tr_b16 a[22:23], v206 offset:20480
v_mfma_f32_16x16x32_bf16 v[64:67], a[0:3], a[224:227], v[64:67]
ds_read_b64_tr_b16 a[24:25], v205 offset:24576
v_mfma_f32_16x16x32_bf16 v[72:75], a[0:3], a[228:231], v[72:75]
ds_read_b64_tr_b16 a[26:27], v205 offset:28672
v_mfma_f32_16x16x32_bf16 v[68:71], a[4:7], a[224:227], v[68:71]
ds_read_b64_tr_b16 a[28:29], v206 offset:24576
v_mfma_f32_16x16x32_bf16 v[76:79], a[4:7], a[228:231], v[76:79]
s_waitcnt lgkmcnt(7)
ds_read_b64_tr_b16 a[30:31], v206 offset:28672
v_mfma_f32_16x16x32_bf16 v[64:67], a[8:11], a[232:235], v[64:67]
ds_read_b64_tr_b16 a[0:1], v207
v_mfma_f32_16x16x32_bf16 v[72:75], a[8:11], a[236:239], v[72:75]
ds_read_b64_tr_b16 a[2:3], v207 offset:4096
v_mfma_f32_16x16x32_bf16 v[68:71], a[12:15], a[232:235], v[68:71]
ds_read_b64_tr_b16 a[4:5], v208
v_mfma_f32_16x16x32_bf16 v[76:79], a[12:15], a[236:239], v[76:79]
s_waitcnt lgkmcnt(7)
ds_read_b64_tr_b16 a[6:7], v208 offset:4096
v_mfma_f32_16x16x32_bf16 v[64:67], a[16:19], a[240:243], v[64:67]
ds_read_b64_tr_b16 a[8:9], v207 offset:8192
v_mfma_f32_16x16x32_bf16 v[72:75], a[16:19], a[244:247], v[72:75]
s_waitcnt vmcnt(1)
s_mov_b64 exec, 0xe00
buffer_store_dword v234, v232, s[56:59], 0 offen sc1
s_mov_b64 exec, -1
ds_read_b64_tr_b16 a[10:11], v207 offset:12288
v_mfma_f32_16x16x32_bf16 v[68:71], a[20:23], a[240:243], v[68:71]
ds_read_b64_tr_b16 a[12:13], v208 offset:8192
v_mfma_f32_16x16x32_bf16 v[76:79], a[20:23], a[244:247], v[76:79]
s_waitcnt lgkmcnt(7)
ds_read_b64_tr_b16 a[14:15], v208 offset:12288
v_mfma_f32_16x16x32_bf16 v[64:67], a[24:27], a[248:251], v[64:67]
ds_read_b64_tr_b16 a[16:17], v207 offset:16384
v_mfma_f32_16x16x32_bf16 v[72:75], a[24:27], a[252:255], v[72:75]
ds_read_b64_tr_b16 a[18:19], v207 offset:20480
v_mfma_f32_16x16x32_bf16 v[68:71], a[28:31], a[248:251], v[68:71]
ds_read_b64_tr_b16 a[20:21], v208 offset:16384
v_mfma_f32_16x16x32_bf16 v[76:79], a[28:31], a[252:255], v[76:79]
s_waitcnt lgkmcnt(7)
ds_read_b64_tr_b16 a[22:23], v208 offset:20480
v_mfma_f32_16x16x32_bf16 v[64:67], a[0:3], a[64:67], v[64:67]
ds_read_b64_tr_b16 a[24:25], v207 offset:24576
v_mfma_f32_16x16x32_bf16 v[72:75], a[0:3], a[68:71], v[72:75]
ds_read_b64_tr_b16 a[26:27], v207 offset:28672
v_mfma_f32_16x16x32_bf16 v[68:71], a[4:7], a[64:67], v[68:71]
ds_read_b64_tr_b16 a[28:29], v208 offset:24576
v_mfma_f32_16x16x32_bf16 v[76:79], a[4:7], a[68:71], v[76:79]
s_waitcnt lgkmcnt(7)
ds_read_b64_tr_b16 a[30:31], v208 offset:28672
v_mfma_f32_16x16x32_bf16 v[64:67], a[8:11], a[72:75], v[64:67]
v_mfma_f32_16x16x32_bf16 v[72:75], a[8:11], a[76:79], v[72:75]
v_mfma_f32_16x16x32_bf16 v[68:71], a[12:15], a[72:75], v[68:71]
v_mfma_f32_16x16x32_bf16 v[76:79], a[12:15], a[76:79], v[76:79]
s_waitcnt lgkmcnt(4)
v_mfma_f32_16x16x32_bf16 v[64:67], a[16:19], a[80:83], v[64:67]
v_mfma_f32_16x16x32_bf16 v[72:75], a[16:19], a[84:87], v[72:75]
v_mfma_f32_16x16x32_bf16 v[68:71], a[20:23], a[80:83], v[68:71]
v_mfma_f32_16x16x32_bf16 v[76:79], a[20:23], a[84:87], v[76:79]
s_waitcnt lgkmcnt(0)
v_mfma_f32_16x16x32_bf16 v[64:67], a[24:27], a[88:91], v[64:67]
v_mfma_f32_16x16x32_bf16 v[72:75], a[24:27], a[92:95], v[72:75]
v_mfma_f32_16x16x32_bf16 v[68:71], a[28:31], a[88:91], v[68:71]
v_mfma_f32_16x16x32_bf16 v[76:79], a[28:31], a[92:95], v[76:79]
s_waitcnt vmcnt(1)
v_cmp_eq_u32_e64 s[74:75], v235, v233
s_nop 1
s_and_b32 s79, s74, 0xe00
s_cmp_eq_u32 s79, 0xe00
s_cbranch_scc0 .Lk_aslow3
.Lk_aok3:
s_xor_b32 s78, s82, 0x202000
buffer_load_dwordx4 v[136:139], v229, s[56:59], s78 offen sc1
buffer_load_dwordx4 v[140:143], v229, s[56:59], s78 offen offset:1024 sc1
s_xor_b32 s78, s82, 0x404000
buffer_load_dwordx4 v[144:147], v229, s[56:59], s78 offen sc1
buffer_load_dwordx4 v[148:151], v229, s[56:59], s78 offen offset:1024 sc1
s_xor_b32 s78, s82, 0x606000
buffer_load_dwordx4 v[152:155], v229, s[56:59], s78 offen sc1
buffer_load_dwordx4 v[156:159], v229, s[56:59], s78 offen offset:1024 sc1
buffer_load_dword v234, v232, s[56:59], 0 offen offset:128 sc1
s_mov_b32 s79, 0xffff0000
s_mov_b32 s84, 0x8000
s_waitcnt vmcnt(6)
v_lshlrev_b32_e32 v246, 16, v136
v_and_b32_e32 v247, s79, v136
v_pk_add_f32 v[64:65], v[64:65], v[246:247]
v_lshlrev_b32_e32 v246, 16, v137
v_and_b32_e32 v247, s79, v137
v_pk_add_f32 v[66:67], v[66:67], v[246:247]
v_lshlrev_b32_e32 v246, 16, v138
v_and_b32_e32 v247, s79, v138
v_pk_add_f32 v[68:69], v[68:69], v[246:247]
v_lshlrev_b32_e32 v246, 16, v139
v_and_b32_e32 v247, s79, v139
v_pk_add_f32 v[70:71], v[70:71], v[246:247]
s_waitcnt vmcnt(5)
v_lshlrev_b32_e32 v246, 16, v140
v_and_b32_e32 v247, s79, v140
v_pk_add_f32 v[72:73], v[72:73], v[246:247]
v_lshlrev_b32_e32 v246, 16, v141
v_and_b32_e32 v247, s79, v141
v_pk_add_f32 v[74:75], v[74:75], v[246:247]
)"
R"(
v_lshlrev_b32_e32 v246, 16, v142
v_and_b32_e32 v247, s79, v142
v_pk_add_f32 v[76:77], v[76:77], v[246:247]
v_lshlrev_b32_e32 v246, 16, v143
v_and_b32_e32 v247, s79, v143
v_pk_add_f32 v[78:79], v[78:79], v[246:247]
s_waitcnt vmcnt(4)
v_lshlrev_b32_e32 v246, 16, v144
v_and_b32_e32 v247, s79, v144
v_pk_add_f32 v[64:65], v[64:65], v[246:247]
v_lshlrev_b32_e32 v246, 16, v145
v_and_b32_e32 v247, s79, v145
v_pk_add_f32 v[66:67], v[66:67], v[246:247]
v_lshlrev_b32_e32 v246, 16, v146
v_and_b32_e32 v247, s79, v146
v_pk_add_f32 v[68:69], v[68:69], v[246:247]
v_lshlrev_b32_e32 v246, 16, v147
v_and_b32_e32 v247, s79, v147
v_pk_add_f32 v[70:71], v[70:71], v[246:247]
s_waitcnt vmcnt(3)
v_lshlrev_b32_e32 v246, 16, v148
v_and_b32_e32 v247, s79, v148
v_pk_add_f32 v[72:73], v[72:73], v[246:247]
v_lshlrev_b32_e32 v246, 16, v149
v_and_b32_e32 v247, s79, v149
v_pk_add_f32 v[74:75], v[74:75], v[246:247]
v_lshlrev_b32_e32 v246, 16, v150
v_and_b32_e32 v247, s79, v150
v_pk_add_f32 v[76:77], v[76:77], v[246:247]
v_lshlrev_b32_e32 v246, 16, v151
v_and_b32_e32 v247, s79, v151
v_pk_add_f32 v[78:79], v[78:79], v[246:247]
s_waitcnt vmcnt(2)
v_lshlrev_b32_e32 v246, 16, v152
v_and_b32_e32 v247, s79, v152
v_pk_add_f32 v[64:65], v[64:65], v[246:247]
v_lshlrev_b32_e32 v246, 16, v153
v_and_b32_e32 v247, s79, v153
v_pk_add_f32 v[66:67], v[66:67], v[246:247]
v_lshlrev_b32_e32 v246, 16, v154
v_and_b32_e32 v247, s79, v154
v_pk_add_f32 v[68:69], v[68:69], v[246:247]
v_lshlrev_b32_e32 v246, 16, v155
v_and_b32_e32 v247, s79, v155
v_pk_add_f32 v[70:71], v[70:71], v[246:247]
s_waitcnt vmcnt(1)
v_lshlrev_b32_e32 v246, 16, v156
v_and_b32_e32 v247, s79, v156
v_pk_add_f32 v[72:73], v[72:73], v[246:247]
v_lshlrev_b32_e32 v246, 16, v157
v_and_b32_e32 v247, s79, v157
v_pk_add_f32 v[74:75], v[74:75], v[246:247]
v_lshlrev_b32_e32 v246, 16, v158
v_and_b32_e32 v247, s79, v158
v_pk_add_f32 v[76:77], v[76:77], v[246:247]
v_lshlrev_b32_e32 v246, 16, v159
v_and_b32_e32 v247, s79, v159
v_pk_add_f32 v[78:79], v[78:79], v[246:247]
v_cvt_pk_bf16_f32 v0, v64, v65
v_cvt_pk_bf16_f32 v2, v68, v69
v_cvt_pk_bf16_f32 v1, v66, v67
v_cvt_pk_bf16_f32 v3, v70, v71
v_permlane16_swap_b32_e32 v0, v2
s_nop 0
v_permlane16_swap_b32_e32 v1, v3
buffer_store_dwordx4 v[0:3], v230, s[60:63], 0 offen sc1
v_cvt_pk_bf16_f32 v4, v72, v73
v_cvt_pk_bf16_f32 v6, v76, v77
v_cvt_pk_bf16_f32 v5, v74, v75
v_cvt_pk_bf16_f32 v7, v78, v79
v_permlane16_swap_b32_e32 v4, v6
s_nop 0
v_permlane16_swap_b32_e32 v5, v7
v_add_u32_e32 v230, s84, v230
buffer_store_dwordx4 v[4:7], v230, s[60:63], 0 offen sc1
s_waitcnt vmcnt(2)
s_mov_b64 exec, 0xe00
buffer_store_dword v234, v232, s[56:59], 0 offen sc1
s_mov_b64 exec, -1
s_endpgm
.Lk_aslow1:
s_mov_b32 s83, 0
.Lk_aspin1:
s_add_u32 s83, s83, 1
s_cmp_gt_u32 s83, 0x30d40
s_cbranch_scc1 .Lk_aok1
s_and_b32 s79, s83, 63
s_cbranch_scc1 .Lk_anore1
buffer_load_dword v234, v232, s[56:59], 0 offen offset:128 sc1
s_waitcnt vmcnt(0)
s_mov_b64 exec, 14
buffer_store_dword v234, v232, s[56:59], 0 offen sc1
s_mov_b64 exec, -1
.Lk_anore1:
buffer_load_dword v235, v231, s[56:59], 0 offen sc1
s_waitcnt vmcnt(0)
v_cmp_eq_u32_e64 s[74:75], v235, v233
s_nop 1
s_and_b32 s79, s74, 14
s_cmp_eq_u32 s79, 14
s_cbranch_scc0 .Lk_aspin1
s_branch .Lk_aok1
.Lk_aslow2:
s_mov_b32 s83, 0
.Lk_aspin2:
s_add_u32 s83, s83, 1
s_cmp_gt_u32 s83, 0x30d40
s_cbranch_scc1 .Lk_aok2
s_and_b32 s79, s83, 63
s_cbranch_scc1 .Lk_anore2
buffer_load_dword v234, v232, s[56:59], 0 offen offset:128 sc1
s_waitcnt vmcnt(0)
s_mov_b64 exec, 0xee
buffer_store_dword v234, v232, s[56:59], 0 offen sc1
s_mov_b64 exec, -1
.Lk_anore2:
buffer_load_dword v235, v231, s[56:59], 0 offen sc1
s_waitcnt vmcnt(0)
v_cmp_eq_u32_e64 s[74:75], v235, v233
s_nop 1
s_and_b32 s79, s74, 0xe0
s_cmp_eq_u32 s79, 0xe0
s_cbranch_scc0 .Lk_aspin2
s_branch .Lk_aok2
.Lk_aslow3:
s_mov_b32 s83, 0
.Lk_aspin3:
s_add_u32 s83, s83, 1
s_cmp_gt_u32 s83, 0x30d40
s_cbranch_scc1 .Lk_aok3
s_and_b32 s79, s83, 63
s_cbranch_scc1 .Lk_anore3
buffer_load_dword v234, v232, s[56:59], 0 offen offset:128 sc1
s_waitcnt vmcnt(0)
s_mov_b64 exec, 0xeee
buffer_store_dword v234, v232, s[56:59], 0 offen sc1
s_mov_b64 exec, -1
.Lk_anore3:
buffer_load_dword v235, v231, s[56:59], 0 offen sc1
s_waitcnt vmcnt(0)
v_cmp_eq_u32_e64 s[74:75], v235, v233
s_nop 1
s_and_b32 s79, s74, 0xe00
s_cmp_eq_u32 s79, 0xe00
s_cbranch_scc0 .Lk_aspin3
s_branch .Lk_aok3
.Lk_w3:
s_waitcnt vmcnt(9)
s_barrier
ds_read_b128 a[96:99], v180
ds_read_b64_tr_b16 a[0:1], v209 offset:16384
ds_read_b64_tr_b16 a[2:3], v209 offset:16640
ds_read_b64_tr_b16 a[4:5], v210 offset:16384
ds_read_b64_tr_b16 a[6:7], v210 offset:16640
ds_read_b64_tr_b16 a[8:9], v209 offset:16896
ds_read_b64_tr_b16 a[10:11], v209 offset:17152
ds_read_b64_tr_b16 a[12:13], v210 offset:16896
ds_read_b64_tr_b16 a[14:15], v210 offset:17152
ds_read_b64_tr_b16 a[16:17], v209 offset:17408
ds_read_b64_tr_b16 a[18:19], v209 offset:17664
ds_read_b64_tr_b16 a[20:21], v210 offset:17408
ds_read_b64_tr_b16 a[22:23], v210 offset:17664
s_waitcnt lgkmcnt(10)
v_mfma_f32_16x16x32_bf16 v[0:3], a[0:3], a[96:99], 0
s_add_u32 m0, s68, 0x10000
s_nop 0
buffer_load_dwordx4 v221, s[40:43], s77 offen lds
ds_read_b64_tr_b16 a[24:25], v209 offset:17920
ds_read_b64_tr_b16 a[26:27], v209 offset:18176
s_waitcnt lgkmcnt(8)
v_mfma_f32_16x16x32_bf16 v[4:7], a[4:7], a[96:99], 0
ds_read_b64_tr_b16 a[28:29], v210 offset:17920
ds_read_b64_tr_b16 a[30:31], v210 offset:18176
buffer_load_dwordx4 v222, s[40:43], s77 offen offset:1024 lds
v_mfma_f32_16x16x32_bf16 v[16:19], a[8:11], a[96:99], 0
s_waitcnt lgkmcnt(6)
ds_read_b128 a[104:107], v180 offset:512
ds_read_b64_tr_b16 a[0:1], v209 offset:24576
ds_read_b64_tr_b16 a[2:3], v209 offset:24832
v_mfma_f32_16x16x32_bf16 v[20:23], a[12:15], a[96:99], 0
ds_read_b64_tr_b16 a[4:5], v210 offset:24576
ds_read_b64_tr_b16 a[6:7], v210 offset:24832
buffer_load_dwordx4 v223, s[40:43], s77 offen offset:2048 lds
v_subrev_u32_e32 v8, 0x3000, v215
v_mfma_f32_16x16x32_bf16 v[32:35], a[16:19], a[96:99], 0
s_waitcnt lgkmcnt(7)
ds_read_b64_tr_b16 a[8:9], v209 offset:25088
ds_read_b64_tr_b16 a[10:11], v209 offset:25344
v_subrev_u32_e32 v9, 0x3000, v216
v_subrev_u32_e32 v10, 0x3000, v217
v_mfma_f32_16x16x32_bf16 v[36:39], a[20:23], a[96:99], 0
buffer_load_dwordx4 v224, s[40:43], s77 offen offset:3072 lds
s_add_u32 s77, s77, 0x80000
s_and_b32 s77, s77, 0x3fffff
ds_read_b64_tr_b16 a[12:13], v210 offset:25088
ds_read_b64_tr_b16 a[14:15], v210 offset:25344
v_mfma_f32_16x16x32_bf16 v[48:51], a[24:27], a[96:99], 0
s_add_u32 m0, s68, 0x14000
s_nop 0
buffer_load_dwordx4 v221, s[40:43], s85 offen lds
)"
R"(
ds_read_b64_tr_b16 a[16:17], v209 offset:25600
ds_read_b64_tr_b16 a[18:19], v209 offset:25856
s_waitcnt lgkmcnt(9)
v_mfma_f32_16x16x32_bf16 v[52:55], a[28:31], a[96:99], 0
s_waitcnt lgkmcnt(6)
ds_read_b64_tr_b16 a[20:21], v210 offset:25600
ds_read_b64_tr_b16 a[22:23], v210 offset:25856
buffer_load_dwordx4 v222, s[40:43], s85 offen offset:1024 lds
v_mfma_f32_16x16x32_bf16 v[0:3], a[0:3], a[104:107], v[0:3]
ds_read_b64_tr_b16 a[24:25], v209 offset:26112
ds_read_b64_tr_b16 a[26:27], v209 offset:26368
v_subrev_u32_e32 v11, 0x3000, v218
v_subrev_u32_e32 v28, 0x60000, v221
v_subrev_u32_e32 v29, 0x60000, v222
v_mfma_f32_16x16x32_bf16 v[4:7], a[4:7], a[104:107], v[4:7]
buffer_load_dwordx4 v223, s[40:43], s85 offen offset:2048 lds
s_waitcnt vmcnt(12)
s_barrier
ds_read_b64_tr_b16 a[28:29], v210 offset:26112
ds_read_b64_tr_b16 a[30:31], v210 offset:26368
s_waitcnt lgkmcnt(8)
v_mfma_f32_16x16x32_bf16 v[16:19], a[8:11], a[104:107], v[16:19]
ds_read_b64_tr_b16 a[0:1], v209 offset:32768
ds_read_b64_tr_b16 a[2:3], v209 offset:33024
v_xor_b32_e32 v24, 32, v28
v_xor_b32_e32 v25, 32, v29
v_mfma_f32_16x16x32_bf16 v[20:23], a[12:15], a[104:107], v[20:23]
s_waitcnt lgkmcnt(6)
buffer_load_dwordx4 v224, s[40:43], s85 offen offset:3072 lds
s_add_u32 s85, s85, 0x80000
s_and_b32 s85, s85, 0x3fffff
ds_read_b64_tr_b16 a[4:5], v210 offset:32768
ds_read_b64_tr_b16 a[6:7], v210 offset:33024
v_mfma_f32_16x16x32_bf16 v[32:35], a[16:19], a[104:107], v[32:35]
s_add_u32 m0, s68, 0x18000
s_nop 0
buffer_load_dwordx4 v176, s[36:39], s76 offen lds
ds_read_b64_tr_b16 a[8:9], v209 offset:33280
ds_read_b64_tr_b16 a[10:11], v209 offset:33536
v_mfma_f32_16x16x32_bf16 v[36:39], a[20:23], a[104:107], v[36:39]
s_waitcnt lgkmcnt(6)
ds_read_b64_tr_b16 a[12:13], v210 offset:33280
ds_read_b64_tr_b16 a[14:15], v210 offset:33536
v_subrev_u32_e32 v30, 0x60000, v223
v_mfma_f32_16x16x32_bf16 v[48:51], a[24:27], a[104:107], v[48:51]
ds_read_b64_tr_b16 a[16:17], v209 offset:33792
ds_read_b64_tr_b16 a[18:19], v209 offset:34048
v_xor_b32_e32 v26, 32, v30
v_subrev_u32_e32 v31, 0x60000, v224
v_xor_b32_e32 v27, 32, v31
v_subrev_u32_e32 v12, 0x3000, v219
v_mfma_f32_16x16x32_bf16 v[52:55], a[28:31], a[104:107], v[52:55]
s_waitcnt lgkmcnt(6)
buffer_load_dwordx4 v177, s[36:39], s76 offen offset:1024 lds
s_add_u32 s76, s76, 0x80
s_and_b32 s76, s76, 0x3ff
s_barrier
ds_read_b64_tr_b16 a[20:21], v210 offset:33792
ds_read_b64_tr_b16 a[22:23], v210 offset:34048
v_mfma_f32_16x16x32_bf16 v[64:67], a[0:3], a[96:99], 0
ds_read_b64_tr_b16 a[24:25], v209 offset:34304
ds_read_b64_tr_b16 a[26:27], v209 offset:34560
v_subrev_u32_e32 v13, 0x3000, v220
v_subrev_u32_e32 v14, 0x3000, v219
v_mfma_f32_16x16x32_bf16 v[68:71], a[4:7], a[96:99], 0
ds_read_b64_tr_b16 a[28:29], v210 offset:34304
ds_read_b64_tr_b16 a[30:31], v210 offset:34560
s_waitcnt vmcnt(14)
v_add_u32_e32 v233, 1, v233
buffer_store_dword v233, v231, s[56:59], 0 offen offset:128 sc1
s_waitcnt lgkmcnt(8)
v_mfma_f32_16x16x32_bf16 v[80:83], a[8:11], a[96:99], 0
s_add_u32 m0, s68, 0x1c000
s_nop 0
buffer_load_dwordx4 v221, s[40:43], s77 offen lds
ds_read_b64_tr_b16 a[0:1], v209 offset:40960
ds_read_b64_tr_b16 a[2:3], v209 offset:41216
v_mfma_f32_16x16x32_bf16 v[84:87], a[12:15], a[96:99], 0
s_waitcnt lgkmcnt(6)
ds_read_b64_tr_b16 a[4:5], v210 offset:40960
ds_read_b64_tr_b16 a[6:7], v210 offset:41216
buffer_load_dwordx4 v222, s[40:43], s77 offen offset:1024 lds
v_mfma_f32_16x16x32_bf16 v[96:99], a[16:19], a[96:99], 0
ds_read_b64_tr_b16 a[8:9], v209 offset:41472
ds_read_b64_tr_b16 a[10:11], v209 offset:41728
v_subrev_u32_e32 v15, 0x3000, v220
v_mfma_f32_16x16x32_bf16 v[100:103], a[20:23], a[96:99], 0
s_waitcnt lgkmcnt(6)
ds_read_b64_tr_b16 a[12:13], v210 offset:41472
ds_read_b64_tr_b16 a[14:15], v210 offset:41728
buffer_load_dwordx4 v223, s[40:43], s77 offen offset:2048 lds
v_mov_b32_e32 v242, 0
v_mfma_f32_16x16x32_bf16 v[112:115], a[24:27], a[96:99], 0
ds_read_b64_tr_b16 a[16:17], v209 offset:41984
ds_read_b64_tr_b16 a[18:19], v209 offset:42240
buffer_load_dwordx4 v224, s[40:43], s77 offen offset:3072 lds
s_add_u32 s77, s77, 0x80000
s_and_b32 s77, s77, 0x3fffff
v_mfma_f32_16x16x32_bf16 v[116:119], a[28:31], a[96:99], 0
s_waitcnt lgkmcnt(6)
ds_read_b64_tr_b16 a[20:21], v210 offset:41984
ds_read_b64_tr_b16 a[22:23], v210 offset:42240
v_subrev_u32_e32 v40, 0xc000, v225
v_mfma_f32_16x16x32_bf16 v[64:67], a[0:3], a[104:107], v[64:67]
s_add_u32 m0, s68, 0x20000
s_nop 0
buffer_load_dwordx4 v176, s[36:39], s76 offen lds
ds_read_b64_tr_b16 a[24:25], v209 offset:42496
ds_read_b64_tr_b16 a[26:27], v209 offset:42752
v_mfma_f32_16x16x32_bf16 v[68:71], a[4:7], a[104:107], v[68:71]
s_waitcnt vmcnt(12)
s_barrier
ds_read_b64_tr_b16 a[28:29], v210 offset:42496
ds_read_b64_tr_b16 a[30:31], v210 offset:42752
s_waitcnt lgkmcnt(8)
v_mfma_f32_16x16x32_bf16 v[80:83], a[8:11], a[104:107], v[80:83]
ds_read_b128 a[112:115], v180 offset:49152
ds_read_b64_tr_b16 a[0:1], v211
ds_read_b64_tr_b16 a[2:3], v211 offset:256
v_subrev_u32_e32 v41, 0xc000, v226
v_mfma_f32_16x16x32_bf16 v[84:87], a[12:15], a[104:107], v[84:87]
s_waitcnt lgkmcnt(7)
ds_read_b64_tr_b16 a[4:5], v212
ds_read_b64_tr_b16 a[6:7], v212 offset:256
buffer_load_dwordx4 v177, s[36:39], s76 offen offset:1024 lds
s_add_u32 s76, s76, 0x80
s_and_b32 s76, s76, 0x3ff
v_mfma_f32_16x16x32_bf16 v[96:99], a[16:19], a[104:107], v[96:99]
ds_read_b64_tr_b16 a[8:9], v211 offset:512
ds_read_b64_tr_b16 a[10:11], v211 offset:768
v_subrev_u32_e32 v42, 0xc000, v227
v_mfma_f32_16x16x32_bf16 v[100:103], a[20:23], a[104:107], v[100:103]
s_waitcnt lgkmcnt(7)
ds_read_b64_tr_b16 a[12:13], v212 offset:512
ds_read_b64_tr_b16 a[14:15], v212 offset:768
v_subrev_u32_e32 v43, 0xc000, v228
v_mov_b32_e32 v244, 0
v_mfma_f32_16x16x32_bf16 v[112:115], a[24:27], a[104:107], v[112:115]
s_add_u32 m0, s68, 0x24000
s_nop 0
buffer_load_dwordx4 v221, s[40:43], s77 offen lds
ds_read_b64_tr_b16 a[16:17], v211 offset:1024
ds_read_b64_tr_b16 a[18:19], v211 offset:1280
v_mfma_f32_16x16x32_bf16 v[116:119], a[28:31], a[104:107], v[116:119]
s_waitcnt lgkmcnt(6)
buffer_load_dwordx4 v222, s[40:43], s77 offen offset:1024 lds
s_barrier
ds_read_b64_tr_b16 a[20:21], v212 offset:1024
ds_read_b64_tr_b16 a[22:23], v212 offset:1280
v_mfma_f32_16x16x32_bf16 v[0:3], a[0:3], a[112:115], v[0:3]
ds_read_b64_tr_b16 a[24:25], v211 offset:1536
ds_read_b64_tr_b16 a[26:27], v211 offset:1792
v_mfma_f32_16x16x32_bf16 v[4:7], a[4:7], a[112:115], v[4:7]
s_waitcnt lgkmcnt(6)
ds_read_b64_tr_b16 a[28:29], v212 offset:1536
ds_read_b64_tr_b16 a[30:31], v212 offset:1792
buffer_load_dwordx4 v223, s[40:43], s77 offen offset:2048 lds
v_mfma_f32_16x16x32_bf16 v[16:19], a[8:11], a[112:115], v[16:19]
ds_read_b128 a[120:123], v180 offset:49664
ds_read_b64_tr_b16 a[0:1], v211 offset:8192
ds_read_b64_tr_b16 a[2:3], v211 offset:8448
buffer_load_dwordx4 v224, s[40:43], s77 offen offset:3072 lds
s_add_u32 s77, s77, 0x80000
s_and_b32 s77, s77, 0x3fffff
v_mfma_f32_16x16x32_bf16 v[20:23], a[12:15], a[112:115], v[20:23]
s_waitcnt lgkmcnt(7)
ds_read_b64_tr_b16 a[4:5], v212 offset:8192
ds_read_b64_tr_b16 a[6:7], v212 offset:8448
v_mfma_f32_16x16x32_bf16 v[32:35], a[16:19], a[112:115], v[32:35]
s_mov_b32 m0, s68
s_nop 0
buffer_load_dwordx4 v176, s[36:39], s76 offen lds
ds_read_b64_tr_b16 a[8:9], v211 offset:8704
ds_read_b64_tr_b16 a[10:11], v211 offset:8960
v_mfma_f32_16x16x32_bf16 v[36:39], a[20:23], a[112:115], v[36:39]
s_waitcnt lgkmcnt(7)
ds_read_b64_tr_b16 a[12:13], v212 offset:8704
ds_read_b64_tr_b16 a[14:15], v212 offset:8960
v_mfma_f32_16x16x32_bf16 v[48:51], a[24:27], a[112:115], v[48:51]
ds_read_b64_tr_b16 a[16:17], v211 offset:9216
ds_read_b64_tr_b16 a[18:19], v211 offset:9472
v_mfma_f32_16x16x32_bf16 v[52:55], a[28:31], a[112:115], v[52:55]
s_waitcnt lgkmcnt(6)
ds_read_b64_tr_b16 a[20:21], v212 offset:9216
ds_read_b64_tr_b16 a[22:23], v212 offset:9472
buffer_load_dwordx4 v177, s[36:39], s76 offen offset:1024 lds
s_add_u32 s76, s76, 0x80
s_and_b32 s76, s76, 0x3ff
v_mfma_f32_16x16x32_bf16 v[0:3], a[0:3], a[120:123], v[0:3]
ds_read_b64_tr_b16 a[24:25], v211 offset:9728
ds_read_b64_tr_b16 a[26:27], v211 offset:9984
v_mfma_f32_16x16x32_bf16 v[4:7], a[4:7], a[120:123], v[4:7]
s_waitcnt vmcnt(15)
s_barrier
ds_read_b64_tr_b16 a[28:29], v212 offset:9728
ds_read_b64_tr_b16 a[30:31], v212 offset:9984
s_waitcnt lgkmcnt(8)
v_mfma_f32_16x16x32_bf16 v[16:19], a[8:11], a[120:123], v[16:19]
s_add_u32 m0, s68, 0x4000
s_nop 0
)"
R"(
buffer_load_dwordx4 v221, s[40:43], s77 offen lds
ds_read_b64_tr_b16 a[0:1], v211 offset:16384
ds_read_b64_tr_b16 a[2:3], v211 offset:16640
v_mfma_f32_16x16x32_bf16 v[20:23], a[12:15], a[120:123], v[20:23]
s_waitcnt lgkmcnt(6)
ds_read_b64_tr_b16 a[4:5], v212 offset:16384
ds_read_b64_tr_b16 a[6:7], v212 offset:16640
buffer_load_dwordx4 v222, s[40:43], s77 offen offset:1024 lds
v_mfma_f32_16x16x32_bf16 v[32:35], a[16:19], a[120:123], v[32:35]
ds_read_b64_tr_b16 a[8:9], v211 offset:16896
ds_read_b64_tr_b16 a[10:11], v211 offset:17152
v_mfma_f32_16x16x32_bf16 v[36:39], a[20:23], a[120:123], v[36:39]
s_waitcnt lgkmcnt(6)
ds_read_b64_tr_b16 a[12:13], v212 offset:16896
ds_read_b64_tr_b16 a[14:15], v212 offset:17152
buffer_load_dwordx4 v223, s[40:43], s77 offen offset:2048 lds
v_mfma_f32_16x16x32_bf16 v[48:51], a[24:27], a[120:123], v[48:51]
ds_read_b64_tr_b16 a[16:17], v211 offset:17408
ds_read_b64_tr_b16 a[18:19], v211 offset:17664
buffer_load_dwordx4 v224, s[40:43], s77 offen offset:3072 lds
s_add_u32 s77, s77, 0x80000
s_and_b32 s77, s77, 0x3fffff
v_mfma_f32_16x16x32_bf16 v[52:55], a[28:31], a[120:123], v[52:55]
s_barrier
ds_read_b64_tr_b16 a[20:21], v212 offset:17408
ds_read_b64_tr_b16 a[22:23], v212 offset:17664
s_waitcnt lgkmcnt(8)
v_mfma_f32_16x16x32_bf16 v[64:67], a[0:3], a[112:115], v[64:67]
s_add_u32 m0, s68, 0x8000
s_nop 0
buffer_load_dwordx4 v176, s[36:39], s76 offen lds
ds_read_b64_tr_b16 a[24:25], v211 offset:17920
ds_read_b64_tr_b16 a[26:27], v211 offset:18176
v_mfma_f32_16x16x32_bf16 v[68:71], a[4:7], a[112:115], v[68:71]
s_waitcnt lgkmcnt(6)
ds_read_b64_tr_b16 a[28:29], v212 offset:17920
ds_read_b64_tr_b16 a[30:31], v212 offset:18176
v_mfma_f32_16x16x32_bf16 v[80:83], a[8:11], a[112:115], v[80:83]
s_mov_b32 exec_lo, 0xffff
s_mov_b32 exec_hi, 0
buffer_load_dword v251, v252, s[52:55], 0 offen
s_mov_b64 exec, -1
ds_read_b64_tr_b16 a[0:1], v211 offset:24576
ds_read_b64_tr_b16 a[2:3], v211 offset:24832
v_mfma_f32_16x16x32_bf16 v[84:87], a[12:15], a[112:115], v[84:87]
s_waitcnt lgkmcnt(6)
ds_read_b64_tr_b16 a[4:5], v212 offset:24576
ds_read_b64_tr_b16 a[6:7], v212 offset:24832
buffer_load_dwordx4 v177, s[36:39], s76 offen offset:1024 lds
s_add_u32 s76, s76, 0x80
s_and_b32 s76, s76, 0x3ff
v_mfma_f32_16x16x32_bf16 v[96:99], a[16:19], a[112:115], v[96:99]
ds_read_b64_tr_b16 a[8:9], v211 offset:25088
ds_read_b64_tr_b16 a[10:11], v211 offset:25344
v_mfma_f32_16x16x32_bf16 v[100:103], a[20:23], a[112:115], v[100:103]
s_waitcnt lgkmcnt(6)
ds_read_b64_tr_b16 a[12:13], v212 offset:25088
ds_read_b64_tr_b16 a[14:15], v212 offset:25344
v_mfma_f32_16x16x32_bf16 v[112:115], a[24:27], a[112:115], v[112:115]
s_add_u32 m0, s68, 0xc000
s_nop 0
buffer_load_dwordx4 v221, s[40:43], s77 offen lds
ds_read_b64_tr_b16 a[16:17], v211 offset:25600
ds_read_b64_tr_b16 a[18:19], v211 offset:25856
v_mfma_f32_16x16x32_bf16 v[116:119], a[28:31], a[112:115], v[116:119]
s_waitcnt lgkmcnt(6)
ds_read_b64_tr_b16 a[20:21], v212 offset:25600
ds_read_b64_tr_b16 a[22:23], v212 offset:25856
buffer_load_dwordx4 v222, s[40:43], s77 offen offset:1024 lds
v_mfma_f32_16x16x32_bf16 v[64:67], a[0:3], a[120:123], v[64:67]
ds_read_b64_tr_b16 a[24:25], v211 offset:26112
ds_read_b64_tr_b16 a[26:27], v211 offset:26368
v_mfma_f32_16x16x32_bf16 v[68:71], a[4:7], a[120:123], v[68:71]
buffer_load_dwordx4 v223, s[40:43], s77 offen offset:2048 lds
s_waitcnt vmcnt(18)
s_barrier
ds_read_b64_tr_b16 a[28:29], v212 offset:26112
ds_read_b64_tr_b16 a[30:31], v212 offset:26368
s_waitcnt lgkmcnt(8)
v_mfma_f32_16x16x32_bf16 v[80:83], a[8:11], a[120:123], v[80:83]
ds_read_b128 a[128:131], v181 offset:32768
ds_read_b64_tr_b16 a[0:1], v211 offset:49152
ds_read_b64_tr_b16 a[2:3], v211 offset:49408
buffer_load_dwordx4 v224, s[40:43], s77 offen offset:3072 lds
s_add_u32 s77, s77, 0x80000
s_and_b32 s77, s77, 0x3fffff
v_mfma_f32_16x16x32_bf16 v[84:87], a[12:15], a[120:123], v[84:87]
s_waitcnt lgkmcnt(7)
ds_read_b64_tr_b16 a[4:5], v212 offset:49152
ds_read_b64_tr_b16 a[6:7], v212 offset:49408
v_mfma_f32_16x16x32_bf16 v[96:99], a[16:19], a[120:123], v[96:99]
s_add_u32 m0, s68, 0x10000
s_nop 0
buffer_load_dwordx4 v176, s[36:39], s76 offen lds
ds_read_b64_tr_b16 a[8:9], v211 offset:49664
ds_read_b64_tr_b16 a[10:11], v211 offset:49920
v_mfma_f32_16x16x32_bf16 v[100:103], a[20:23], a[120:123], v[100:103]
s_waitcnt lgkmcnt(7)
ds_read_b64_tr_b16 a[12:13], v212 offset:49664
ds_read_b64_tr_b16 a[14:15], v212 offset:49920
v_mfma_f32_16x16x32_bf16 v[112:115], a[24:27], a[120:123], v[112:115]
ds_read_b64_tr_b16 a[16:17], v211 offset:50176
ds_read_b64_tr_b16 a[18:19], v211 offset:50432
v_mfma_f32_16x16x32_bf16 v[116:119], a[28:31], a[120:123], v[116:119]
s_waitcnt lgkmcnt(6)
buffer_load_dwordx4 v177, s[36:39], s76 offen offset:1024 lds
s_add_u32 s76, s76, 0x80
s_and_b32 s76, s76, 0x3ff
s_barrier
ds_read_b64_tr_b16 a[20:21], v212 offset:50176
ds_read_b64_tr_b16 a[22:23], v212 offset:50432
v_mfma_f32_16x16x32_bf16 v[0:3], a[0:3], a[128:131], v[0:3]
ds_read_b64_tr_b16 a[24:25], v211 offset:50688
ds_read_b64_tr_b16 a[26:27], v211 offset:50944
v_mfma_f32_16x16x32_bf16 v[4:7], a[4:7], a[128:131], v[4:7]
s_waitcnt lgkmcnt(6)
ds_read_b64_tr_b16 a[28:29], v212 offset:50688
ds_read_b64_tr_b16 a[30:31], v212 offset:50944
v_mfma_f32_16x16x32_bf16 v[16:19], a[8:11], a[128:131], v[16:19]
s_add_u32 m0, s68, 0x14000
s_nop 0
buffer_load_dwordx4 v221, s[40:43], s77 offen lds
ds_read_b128 a[136:139], v181 offset:33280
ds_read_b64_tr_b16 a[0:1], v211 offset:57344
ds_read_b64_tr_b16 a[2:3], v211 offset:57600
v_mfma_f32_16x16x32_bf16 v[20:23], a[12:15], a[128:131], v[20:23]
s_waitcnt lgkmcnt(7)
ds_read_b64_tr_b16 a[4:5], v212 offset:57344
ds_read_b64_tr_b16 a[6:7], v212 offset:57600
v_mfma_f32_16x16x32_bf16 v[32:35], a[16:19], a[128:131], v[32:35]
ds_read_b64_tr_b16 a[8:9], v211 offset:57856
ds_read_b64_tr_b16 a[10:11], v211 offset:58112
buffer_load_dwordx4 v222, s[40:43], s77 offen offset:1024 lds
v_mfma_f32_16x16x32_bf16 v[36:39], a[20:23], a[128:131], v[36:39]
s_waitcnt lgkmcnt(7)
ds_read_b64_tr_b16 a[12:13], v212 offset:57856
ds_read_b64_tr_b16 a[14:15], v212 offset:58112
v_mfma_f32_16x16x32_bf16 v[48:51], a[24:27], a[128:131], v[48:51]
ds_read_b64_tr_b16 a[16:17], v211 offset:58368
ds_read_b64_tr_b16 a[18:19], v211 offset:58624
buffer_load_dwordx4 v223, s[40:43], s77 offen offset:2048 lds
v_mfma_f32_16x16x32_bf16 v[52:55], a[28:31], a[128:131], v[52:55]
s_waitcnt lgkmcnt(6)
ds_read_b64_tr_b16 a[20:21], v212 offset:58368
ds_read_b64_tr_b16 a[22:23], v212 offset:58624
v_mfma_f32_16x16x32_bf16 v[0:3], a[0:3], a[136:139], v[0:3]
ds_read_b64_tr_b16 a[24:25], v211 offset:58880
ds_read_b64_tr_b16 a[26:27], v211 offset:59136
buffer_load_dwordx4 v224, s[40:43], s77 offen offset:3072 lds
s_add_u32 s77, s77, 0x80000
s_and_b32 s77, s77, 0x3fffff
v_mfma_f32_16x16x32_bf16 v[4:7], a[4:7], a[136:139], v[4:7]
s_waitcnt vmcnt(19)
s_barrier
ds_read_b64_tr_b16 a[28:29], v212 offset:58880
ds_read_b64_tr_b16 a[30:31], v212 offset:59136
s_waitcnt lgkmcnt(8)
v_mfma_f32_16x16x32_bf16 v[16:19], a[8:11], a[136:139], v[16:19]
s_add_u32 m0, s68, 0x18000
s_nop 0
buffer_load_dwordx4 v176, s[36:39], s76 offen lds
ds_read_b128 a[144:147], v182
ds_read_b64_tr_b16 a[0:1], v213 offset:16384
ds_read_b64_tr_b16 a[2:3], v213 offset:16640
v_mfma_f32_16x16x32_bf16 v[20:23], a[12:15], a[136:139], v[20:23]
s_waitcnt lgkmcnt(7)
ds_read_b64_tr_b16 a[4:5], v214 offset:16384
ds_read_b64_tr_b16 a[6:7], v214 offset:16640
v_mfma_f32_16x16x32_bf16 v[32:35], a[16:19], a[136:139], v[32:35]
ds_read_b64_tr_b16 a[8:9], v213 offset:16896
ds_read_b64_tr_b16 a[10:11], v213 offset:17152
v_mfma_f32_16x16x32_bf16 v[36:39], a[20:23], a[136:139], v[36:39]
s_waitcnt lgkmcnt(7)
ds_read_b64_tr_b16 a[12:13], v214 offset:16896
ds_read_b64_tr_b16 a[14:15], v214 offset:17152
buffer_load_dwordx4 v177, s[36:39], s76 offen offset:1024 lds
s_add_u32 s76, s76, 0x80
s_and_b32 s76, s76, 0x3ff
v_mfma_f32_16x16x32_bf16 v[48:51], a[24:27], a[136:139], v[48:51]
ds_read_b64_tr_b16 a[16:17], v213 offset:17408
ds_read_b64_tr_b16 a[18:19], v213 offset:17664
v_mfma_f32_16x16x32_bf16 v[52:55], a[28:31], a[136:139], v[52:55]
s_barrier
ds_read_b64_tr_b16 a[20:21], v214 offset:17408
ds_read_b64_tr_b16 a[22:23], v214 offset:17664
s_waitcnt lgkmcnt(8)
v_mfma_f32_16x16x32_bf16 v[0:3], a[0:3], a[144:147], v[0:3]
s_add_u32 m0, s68, 0x1c000
s_nop 0
buffer_load_dwordx4 v221, s[40:43], s77 offen lds
ds_read_b64_tr_b16 a[24:25], v213 offset:17920
ds_read_b64_tr_b16 a[26:27], v213 offset:18176
v_mfma_f32_16x16x32_bf16 v[4:7], a[4:7], a[144:147], v[4:7]
s_waitcnt lgkmcnt(6)
ds_read_b64_tr_b16 a[28:29], v214 offset:17920
ds_read_b64_tr_b16 a[30:31], v214 offset:18176
v_mfma_f32_16x16x32_bf16 v[16:19], a[8:11], a[144:147], v[16:19]
ds_read_b128 a[152:155], v182 offset:512
ds_read_b64_tr_b16 a[0:1], v213 offset:24576
ds_read_b64_tr_b16 a[2:3], v213 offset:24832
)"
R"(
buffer_load_dwordx4 v222, s[40:43], s77 offen offset:1024 lds
s_mov_b32 exec_lo, 0xff0000
s_mov_b32 exec_hi, 0xff
buffer_load_dword v251, v252, s[52:55], 0 offen
s_mov_b64 exec, -1
v_mfma_f32_16x16x32_bf16 v[20:23], a[12:15], a[144:147], v[20:23]
s_waitcnt lgkmcnt(7)
ds_read_b64_tr_b16 a[4:5], v214 offset:24576
ds_read_b64_tr_b16 a[6:7], v214 offset:24832
v_mfma_f32_16x16x32_bf16 v[32:35], a[16:19], a[144:147], v[32:35]
ds_read_b64_tr_b16 a[8:9], v213 offset:25088
ds_read_b64_tr_b16 a[10:11], v213 offset:25344
v_mfma_f32_16x16x32_bf16 v[36:39], a[20:23], a[144:147], v[36:39]
s_waitcnt lgkmcnt(7)
ds_read_b64_tr_b16 a[12:13], v214 offset:25088
ds_read_b64_tr_b16 a[14:15], v214 offset:25344
buffer_load_dwordx4 v223, s[40:43], s77 offen offset:2048 lds
v_mfma_f32_16x16x32_bf16 v[48:51], a[24:27], a[144:147], v[48:51]
ds_read_b64_tr_b16 a[16:17], v213 offset:25600
ds_read_b64_tr_b16 a[18:19], v213 offset:25856
v_mfma_f32_16x16x32_bf16 v[52:55], a[28:31], a[144:147], v[52:55]
s_waitcnt lgkmcnt(6)
ds_read_b64_tr_b16 a[20:21], v214 offset:25600
ds_read_b64_tr_b16 a[22:23], v214 offset:25856
buffer_load_dwordx4 v224, s[40:43], s77 offen offset:3072 lds
s_add_u32 s77, s77, 0x80000
s_and_b32 s77, s77, 0x3fffff
v_mfma_f32_16x16x32_bf16 v[0:3], a[0:3], a[152:155], v[0:3]
ds_read_b64_tr_b16 a[24:25], v213 offset:26112
ds_read_b64_tr_b16 a[26:27], v213 offset:26368
v_mfma_f32_16x16x32_bf16 v[4:7], a[4:7], a[152:155], v[4:7]
s_waitcnt vmcnt(20)
s_barrier
ds_read_b64_tr_b16 a[28:29], v214 offset:26112
ds_read_b64_tr_b16 a[30:31], v214 offset:26368
s_waitcnt lgkmcnt(8)
v_mfma_f32_16x16x32_bf16 v[16:19], a[8:11], a[152:155], v[16:19]
s_mov_b32 m0, 0x20000
s_mov_b32 s78, 0
buffer_load_dwordx4 v8, s[44:47], s78 offen nt lds
ds_read_b128 a[160:163], v180
ds_read_b64_tr_b16 a[0:1], v209 offset:16384
ds_read_b64_tr_b16 a[2:3], v209 offset:16640
buffer_load_dwordx4 v9, s[44:47], s78 offen offset:1024 nt lds
v_mfma_f32_16x16x32_bf16 v[20:23], a[12:15], a[152:155], v[20:23]
s_waitcnt lgkmcnt(7)
ds_read_b64_tr_b16 a[4:5], v210 offset:16384
ds_read_b64_tr_b16 a[6:7], v210 offset:16640
buffer_load_dwordx4 v10, s[44:47], s78 offen offset:2048 nt lds
v_mfma_f32_16x16x32_bf16 v[32:35], a[16:19], a[152:155], v[32:35]
ds_read_b64_tr_b16 a[8:9], v209 offset:16896
ds_read_b64_tr_b16 a[10:11], v209 offset:17152
buffer_load_dwordx4 v11, s[44:47], s78 offen offset:3072 nt lds
s_mov_b32 m0, 0x21000
s_mov_b32 s78, 0x1000
buffer_load_dwordx4 v8, s[44:47], s78 offen nt lds
v_mfma_f32_16x16x32_bf16 v[36:39], a[20:23], a[152:155], v[36:39]
s_waitcnt lgkmcnt(7)
ds_read_b64_tr_b16 a[12:13], v210 offset:16896
ds_read_b64_tr_b16 a[14:15], v210 offset:17152
buffer_load_dwordx4 v9, s[44:47], s78 offen offset:1024 nt lds
v_mfma_f32_16x16x32_bf16 v[48:51], a[24:27], a[152:155], v[48:51]
ds_read_b64_tr_b16 a[16:17], v209 offset:17408
ds_read_b64_tr_b16 a[18:19], v209 offset:17664
buffer_load_dwordx4 v10, s[44:47], s78 offen offset:2048 nt lds
buffer_load_dwordx4 v11, s[44:47], s78 offen offset:3072 nt lds
v_mfma_f32_16x16x32_bf16 v[52:55], a[28:31], a[152:155], v[52:55]
s_mov_b32 m0, 0x22000
s_mov_b32 s78, 0x2000
buffer_load_dwordx4 v8, s[44:47], s78 offen nt lds
s_barrier
ds_read_b64_tr_b16 a[20:21], v210 offset:17408
ds_read_b64_tr_b16 a[22:23], v210 offset:17664
s_waitcnt lgkmcnt(8)
v_mfma_f32_16x16x32_bf16 v[0:3], a[0:3], a[160:163], v[0:3]
ds_read_b64_tr_b16 a[24:25], v209 offset:17920
ds_read_b64_tr_b16 a[26:27], v209 offset:18176
buffer_load_dwordx4 v9, s[44:47], s78 offen offset:1024 nt lds
buffer_load_dwordx4 v10, s[44:47], s78 offen offset:2048 nt lds
v_mfma_f32_16x16x32_bf16 v[4:7], a[4:7], a[160:163], v[4:7]
s_waitcnt lgkmcnt(6)
ds_read_b64_tr_b16 a[28:29], v210 offset:17920
ds_read_b64_tr_b16 a[30:31], v210 offset:18176
buffer_load_dwordx4 v11, s[44:47], s78 offen offset:3072 nt lds
v_mfma_f32_16x16x32_bf16 v[16:19], a[8:11], a[160:163], v[16:19]
s_mov_b32 m0, 0x23000
s_mov_b32 s78, 0x3000
buffer_load_dwordx4 v8, s[44:47], s78 offen nt lds
ds_read_b128 a[168:171], v180 offset:512
ds_read_b64_tr_b16 a[0:1], v209 offset:24576
ds_read_b64_tr_b16 a[2:3], v209 offset:24832
buffer_load_dwordx4 v9, s[44:47], s78 offen offset:1024 nt lds
v_mfma_f32_16x16x32_bf16 v[20:23], a[12:15], a[160:163], v[20:23]
s_waitcnt lgkmcnt(7)
ds_read_b64_tr_b16 a[4:5], v210 offset:24576
ds_read_b64_tr_b16 a[6:7], v210 offset:24832
buffer_load_dwordx4 v10, s[44:47], s78 offen offset:2048 nt lds
v_mfma_f32_16x16x32_bf16 v[32:35], a[16:19], a[160:163], v[32:35]
ds_read_b64_tr_b16 a[8:9], v209 offset:25088
ds_read_b64_tr_b16 a[10:11], v209 offset:25344
buffer_load_dwordx4 v11, s[44:47], s78 offen offset:3072 nt lds
v_mfma_f32_16x16x32_bf16 v[36:39], a[20:23], a[160:163], v[36:39]
s_mov_b32 m0, 0x24000
s_add_u32 s78, s85, 0
buffer_load_dwordx4 v24, s[40:43], s78 offen lds
ds_read_b64_tr_b16 a[12:13], v210 offset:25088
ds_read_b64_tr_b16 a[14:15], v210 offset:25344
buffer_load_dwordx4 v25, s[40:43], s78 offen offset:1024 lds
s_waitcnt lgkmcnt(9)
v_mfma_f32_16x16x32_bf16 v[48:51], a[24:27], a[160:163], v[48:51]
ds_read_b64_tr_b16 a[16:17], v209 offset:25600
ds_read_b64_tr_b16 a[18:19], v209 offset:25856
buffer_load_dwordx4 v26, s[40:43], s78 offen offset:2048 lds
v_mfma_f32_16x16x32_bf16 v[52:55], a[28:31], a[160:163], v[52:55]
s_waitcnt lgkmcnt(6)
ds_read_b64_tr_b16 a[20:21], v210 offset:25600
ds_read_b64_tr_b16 a[22:23], v210 offset:25856
buffer_load_dwordx4 v27, s[40:43], s78 offen offset:3072 lds
s_mov_b32 m0, 0x25000
s_add_u32 s78, s85, 0x20000
buffer_load_dwordx4 v28, s[40:43], s78 offen lds
v_mfma_f32_16x16x32_bf16 v[0:3], a[0:3], a[168:171], v[0:3]
ds_read_b64_tr_b16 a[24:25], v209 offset:26112
ds_read_b64_tr_b16 a[26:27], v209 offset:26368
buffer_load_dwordx4 v29, s[40:43], s78 offen offset:1024 lds
v_mfma_f32_16x16x32_bf16 v[4:7], a[4:7], a[168:171], v[4:7]
buffer_load_dwordx4 v30, s[40:43], s78 offen offset:2048 lds
buffer_load_dwordx4 v31, s[40:43], s78 offen offset:3072 lds
s_waitcnt vmcnt(37)
s_barrier
ds_read_b64_tr_b16 a[28:29], v210 offset:26112
ds_read_b64_tr_b16 a[30:31], v210 offset:26368
s_waitcnt lgkmcnt(8)
v_mfma_f32_16x16x32_bf16 v[16:19], a[8:11], a[168:171], v[16:19]
s_mov_b32 m0, 0x26000
s_add_u32 s78, s85, 0x40000
buffer_load_dwordx4 v24, s[40:43], s78 offen lds
ds_read_b128 a[176:179], v180 offset:32768
ds_read_b64_tr_b16 a[0:1], v209 offset:49152
ds_read_b64_tr_b16 a[2:3], v209 offset:49408
v_mfma_f32_16x16x32_bf16 v[20:23], a[12:15], a[168:171], v[20:23]
s_waitcnt lgkmcnt(7)
ds_read_b64_tr_b16 a[4:5], v210 offset:49152
ds_read_b64_tr_b16 a[6:7], v210 offset:49408
buffer_load_dwordx4 v25, s[40:43], s78 offen offset:1024 lds
buffer_load_dwordx4 v26, s[40:43], s78 offen offset:2048 lds
v_mfma_f32_16x16x32_bf16 v[32:35], a[16:19], a[168:171], v[32:35]
ds_read_b64_tr_b16 a[8:9], v209 offset:49664
ds_read_b64_tr_b16 a[10:11], v209 offset:49920
buffer_load_dwordx4 v27, s[40:43], s78 offen offset:3072 lds
v_mfma_f32_16x16x32_bf16 v[36:39], a[20:23], a[168:171], v[36:39]
s_mov_b32 m0, 0x27000
s_add_u32 s78, s85, 0x60000
buffer_load_dwordx4 v28, s[40:43], s78 offen lds
ds_read_b64_tr_b16 a[12:13], v210 offset:49664
ds_read_b64_tr_b16 a[14:15], v210 offset:49920
buffer_load_dwordx4 v29, s[40:43], s78 offen offset:1024 lds
s_waitcnt lgkmcnt(9)
v_mfma_f32_16x16x32_bf16 v[48:51], a[24:27], a[168:171], v[48:51]
ds_read_b64_tr_b16 a[16:17], v209 offset:50176
ds_read_b64_tr_b16 a[18:19], v209 offset:50432
buffer_load_dwordx4 v30, s[40:43], s78 offen offset:2048 lds
v_mfma_f32_16x16x32_bf16 v[52:55], a[28:31], a[168:171], v[52:55]
s_waitcnt lgkmcnt(6)
buffer_load_dwordx4 v31, s[40:43], s78 offen offset:3072 lds
s_add_u32 s85, s85, 0x80000
s_and_b32 s85, s85, 0x3fffff
s_barrier
ds_read_b64_tr_b16 a[20:21], v210 offset:50176
ds_read_b64_tr_b16 a[22:23], v210 offset:50432
v_mfma_f32_16x16x32_bf16 v[0:3], a[0:3], a[176:179], v[0:3]
s_mov_b32 m0, 0
s_mov_b32 s78, 0x4000
buffer_load_dwordx4 v8, s[44:47], s78 offen nt lds
ds_read_b64_tr_b16 a[24:25], v209 offset:50688
ds_read_b64_tr_b16 a[26:27], v209 offset:50944
v_mfma_f32_16x16x32_bf16 v[4:7], a[4:7], a[176:179], v[4:7]
s_waitcnt lgkmcnt(6)
ds_read_b64_tr_b16 a[28:29], v210 offset:50688
ds_read_b64_tr_b16 a[30:31], v210 offset:50944
buffer_load_dwordx4 v9, s[44:47], s78 offen offset:1024 nt lds
v_mfma_f32_16x16x32_bf16 v[16:19], a[8:11], a[176:179], v[16:19]
ds_read_b128 a[184:187], v180 offset:33280
ds_read_b64_tr_b16 a[0:1], v209 offset:57344
ds_read_b64_tr_b16 a[2:3], v209 offset:57600
buffer_load_dwordx4 v10, s[44:47], s78 offen offset:2048 nt lds
v_mfma_f32_16x16x32_bf16 v[20:23], a[12:15], a[176:179], v[20:23]
s_waitcnt lgkmcnt(7)
ds_read_b64_tr_b16 a[4:5], v210 offset:57344
ds_read_b64_tr_b16 a[6:7], v210 offset:57600
buffer_load_dwordx4 v11, s[44:47], s78 offen offset:3072 nt lds
v_mfma_f32_16x16x32_bf16 v[32:35], a[16:19], a[176:179], v[32:35]
s_mov_b32 m0, 0x1000
s_mov_b32 s78, 0x5000
buffer_load_dwordx4 v8, s[44:47], s78 offen nt lds
ds_read_b64_tr_b16 a[8:9], v209 offset:57856
ds_read_b64_tr_b16 a[10:11], v209 offset:58112
v_mfma_f32_16x16x32_bf16 v[36:39], a[20:23], a[176:179], v[36:39]
s_waitcnt lgkmcnt(7)
ds_read_b64_tr_b16 a[12:13], v210 offset:57856
)"
R"(
ds_read_b64_tr_b16 a[14:15], v210 offset:58112
buffer_load_dwordx4 v9, s[44:47], s78 offen offset:1024 nt lds
v_mfma_f32_16x16x32_bf16 v[48:51], a[24:27], a[176:179], v[48:51]
ds_read_b64_tr_b16 a[16:17], v209 offset:58368
ds_read_b64_tr_b16 a[18:19], v209 offset:58624
buffer_load_dwordx4 v10, s[44:47], s78 offen offset:2048 nt lds
v_mfma_f32_16x16x32_bf16 v[52:55], a[28:31], a[176:179], v[52:55]
s_waitcnt lgkmcnt(6)
ds_read_b64_tr_b16 a[20:21], v210 offset:58368
ds_read_b64_tr_b16 a[22:23], v210 offset:58624
buffer_load_dwordx4 v11, s[44:47], s78 offen offset:3072 nt lds
v_mfma_f32_16x16x32_bf16 v[0:3], a[0:3], a[184:187], v[0:3]
ds_read_b64_tr_b16 a[24:25], v209 offset:58880
ds_read_b64_tr_b16 a[26:27], v209 offset:59136
v_mfma_f32_16x16x32_bf16 v[4:7], a[4:7], a[184:187], v[4:7]
s_waitcnt vmcnt(38)
s_mov_b32 m0, 0x2000
s_mov_b32 s78, 0x6000
buffer_load_dwordx4 v8, s[44:47], s78 offen nt lds
s_waitcnt vmcnt(48)
s_barrier
ds_read_b64_tr_b16 a[28:29], v210 offset:58880
ds_read_b64_tr_b16 a[30:31], v210 offset:59136
s_waitcnt lgkmcnt(8)
v_mfma_f32_16x16x32_bf16 v[16:19], a[8:11], a[184:187], v[16:19]
ds_read_b128 a[192:195], v181
ds_read_b64_tr_b16 a[0:1], v211 offset:16384
ds_read_b64_tr_b16 a[2:3], v211 offset:16640
buffer_load_dwordx4 v9, s[44:47], s78 offen offset:1024 nt lds
v_mfma_f32_16x16x32_bf16 v[20:23], a[12:15], a[184:187], v[20:23]
s_waitcnt vmcnt(38)
buffer_load_dwordx4 v10, s[44:47], s78 offen offset:2048 nt lds
ds_read_b64_tr_b16 a[4:5], v212 offset:16384
ds_read_b64_tr_b16 a[6:7], v212 offset:16640
s_waitcnt lgkmcnt(9)
v_mfma_f32_16x16x32_bf16 v[32:35], a[16:19], a[184:187], v[32:35]
ds_read_b64_tr_b16 a[8:9], v211 offset:16896
ds_read_b64_tr_b16 a[10:11], v211 offset:17152
buffer_load_dwordx4 v11, s[44:47], s78 offen offset:3072 nt lds
v_mfma_f32_16x16x32_bf16 v[36:39], a[20:23], a[184:187], v[36:39]
s_waitcnt vmcnt(38)
s_mov_b32 m0, 0x3000
s_mov_b32 s78, 0x7000
buffer_load_dwordx4 v8, s[44:47], s78 offen nt lds
ds_read_b64_tr_b16 a[12:13], v212 offset:16896
ds_read_b64_tr_b16 a[14:15], v212 offset:17152
s_waitcnt lgkmcnt(9)
v_mfma_f32_16x16x32_bf16 v[48:51], a[24:27], a[184:187], v[48:51]
ds_read_b64_tr_b16 a[16:17], v211 offset:17408
ds_read_b64_tr_b16 a[18:19], v211 offset:17664
buffer_load_dwordx4 v9, s[44:47], s78 offen offset:1024 nt lds
v_mfma_f32_16x16x32_bf16 v[52:55], a[28:31], a[184:187], v[52:55]
s_waitcnt vmcnt(38)
buffer_load_dwordx4 v10, s[44:47], s78 offen offset:2048 nt lds
s_barrier
ds_read_b64_tr_b16 a[20:21], v212 offset:17408
ds_read_b64_tr_b16 a[22:23], v212 offset:17664
s_waitcnt lgkmcnt(8)
v_mfma_f32_16x16x32_bf16 v[0:3], a[0:3], a[192:195], v[0:3]
ds_read_b64_tr_b16 a[24:25], v211 offset:17920
ds_read_b64_tr_b16 a[26:27], v211 offset:18176
buffer_load_dwordx4 v11, s[44:47], s78 offen offset:3072 nt lds
v_mfma_f32_16x16x32_bf16 v[4:7], a[4:7], a[192:195], v[4:7]
s_waitcnt lgkmcnt(6)
ds_read_b64_tr_b16 a[28:29], v212 offset:17920
ds_read_b64_tr_b16 a[30:31], v212 offset:18176
v_mfma_f32_16x16x32_bf16 v[16:19], a[8:11], a[192:195], v[16:19]
s_mov_b32 m0, 0x4000
s_add_u32 s78, s85, 0
buffer_load_dwordx4 v24, s[40:43], s78 offen lds
ds_read_b128 a[200:203], v181 offset:512
ds_read_b64_tr_b16 a[0:1], v211 offset:24576
ds_read_b64_tr_b16 a[2:3], v211 offset:24832
buffer_load_dwordx4 v25, s[40:43], s78 offen offset:1024 lds
v_mfma_f32_16x16x32_bf16 v[20:23], a[12:15], a[192:195], v[20:23]
s_waitcnt lgkmcnt(7)
ds_read_b64_tr_b16 a[4:5], v212 offset:24576
ds_read_b64_tr_b16 a[6:7], v212 offset:24832
buffer_load_dwordx4 v26, s[40:43], s78 offen offset:2048 lds
v_mfma_f32_16x16x32_bf16 v[32:35], a[16:19], a[192:195], v[32:35]
ds_read_b64_tr_b16 a[8:9], v211 offset:25088
ds_read_b64_tr_b16 a[10:11], v211 offset:25344
buffer_load_dwordx4 v27, s[40:43], s78 offen offset:3072 lds
s_mov_b32 m0, 0x5000
s_add_u32 s78, s85, 0x20000
buffer_load_dwordx4 v28, s[40:43], s78 offen lds
v_mfma_f32_16x16x32_bf16 v[36:39], a[20:23], a[192:195], v[36:39]
s_waitcnt lgkmcnt(7)
ds_read_b64_tr_b16 a[12:13], v212 offset:25088
ds_read_b64_tr_b16 a[14:15], v212 offset:25344
buffer_load_dwordx4 v29, s[40:43], s78 offen offset:1024 lds
v_mfma_f32_16x16x32_bf16 v[48:51], a[24:27], a[192:195], v[48:51]
ds_read_b64_tr_b16 a[16:17], v211 offset:25600
ds_read_b64_tr_b16 a[18:19], v211 offset:25856
buffer_load_dwordx4 v30, s[40:43], s78 offen offset:2048 lds
buffer_load_dwordx4 v31, s[40:43], s78 offen offset:3072 lds
v_mfma_f32_16x16x32_bf16 v[52:55], a[28:31], a[192:195], v[52:55]
s_mov_b32 m0, 0x6000
s_add_u32 s78, s85, 0x40000
buffer_load_dwordx4 v24, s[40:43], s78 offen lds
ds_read_b64_tr_b16 a[20:21], v212 offset:25600
ds_read_b64_tr_b16 a[22:23], v212 offset:25856
s_waitcnt lgkmcnt(8)
v_mfma_f32_16x16x32_bf16 v[0:3], a[0:3], a[200:203], v[0:3]
ds_read_b64_tr_b16 a[24:25], v211 offset:26112
ds_read_b64_tr_b16 a[26:27], v211 offset:26368
buffer_load_dwordx4 v25, s[40:43], s78 offen offset:1024 lds
buffer_load_dwordx4 v26, s[40:43], s78 offen offset:2048 lds
v_mfma_f32_16x16x32_bf16 v[4:7], a[4:7], a[200:203], v[4:7]
buffer_load_dwordx4 v27, s[40:43], s78 offen offset:3072 lds
s_waitcnt vmcnt(60)
s_barrier
ds_read_b64_tr_b16 a[28:29], v212 offset:26112
ds_read_b64_tr_b16 a[30:31], v212 offset:26368
s_waitcnt lgkmcnt(8)
v_mfma_f32_16x16x32_bf16 v[16:19], a[8:11], a[200:203], v[16:19]
s_mov_b32 m0, 0x7000
s_add_u32 s78, s85, 0x60000
buffer_load_dwordx4 v28, s[40:43], s78 offen lds
ds_read_b128 a[208:211], v181 offset:32768
ds_read_b64_tr_b16 a[0:1], v211 offset:49152
ds_read_b64_tr_b16 a[2:3], v211 offset:49408
buffer_load_dwordx4 v29, s[40:43], s78 offen offset:1024 lds
v_mfma_f32_16x16x32_bf16 v[20:23], a[12:15], a[200:203], v[20:23]
s_waitcnt lgkmcnt(7)
ds_read_b64_tr_b16 a[4:5], v212 offset:49152
ds_read_b64_tr_b16 a[6:7], v212 offset:49408
buffer_load_dwordx4 v30, s[40:43], s78 offen offset:2048 lds
v_mfma_f32_16x16x32_bf16 v[32:35], a[16:19], a[200:203], v[32:35]
ds_read_b64_tr_b16 a[8:9], v211 offset:49664
ds_read_b64_tr_b16 a[10:11], v211 offset:49920
buffer_load_dwordx4 v31, s[40:43], s78 offen offset:3072 lds
s_add_u32 s85, s85, 0x80000
s_and_b32 s85, s85, 0x3fffff
v_mfma_f32_16x16x32_bf16 v[36:39], a[20:23], a[200:203], v[36:39]
s_waitcnt vmcnt(54)
s_mov_b32 m0, 0x8000
s_mov_b32 s78, 0
buffer_load_dwordx4 v12, s[48:51], s78 offen nt lds
ds_read_b64_tr_b16 a[12:13], v212 offset:49664
ds_read_b64_tr_b16 a[14:15], v212 offset:49920
buffer_load_dwordx4 v13, s[48:51], s78 offen offset:1024 nt lds
s_waitcnt lgkmcnt(9)
v_mfma_f32_16x16x32_bf16 v[48:51], a[24:27], a[200:203], v[48:51]
s_waitcnt vmcnt(54)
buffer_load_dwordx4 v14, s[48:51], s78 offen offset:2048 nt lds
ds_read_b64_tr_b16 a[16:17], v211 offset:50176
ds_read_b64_tr_b16 a[18:19], v211 offset:50432
v_mfma_f32_16x16x32_bf16 v[52:55], a[28:31], a[200:203], v[52:55]
s_waitcnt lgkmcnt(6)
buffer_load_dwordx4 v15, s[48:51], s78 offen offset:3072 nt lds
s_barrier
ds_read_b64_tr_b16 a[20:21], v212 offset:50176
ds_read_b64_tr_b16 a[22:23], v212 offset:50432
v_mfma_f32_16x16x32_bf16 v[0:3], a[0:3], a[208:211], v[0:3]
s_waitcnt vmcnt(54)
s_mov_b32 m0, 0x9000
s_mov_b32 s78, 0x1000
buffer_load_dwordx4 v12, s[48:51], s78 offen nt lds
ds_read_b64_tr_b16 a[24:25], v211 offset:50688
ds_read_b64_tr_b16 a[26:27], v211 offset:50944
v_mfma_f32_16x16x32_bf16 v[4:7], a[4:7], a[208:211], v[4:7]
s_waitcnt lgkmcnt(6)
ds_read_b64_tr_b16 a[28:29], v212 offset:50688
ds_read_b64_tr_b16 a[30:31], v212 offset:50944
buffer_load_dwordx4 v13, s[48:51], s78 offen offset:1024 nt lds
v_mfma_f32_16x16x32_bf16 v[16:19], a[8:11], a[208:211], v[16:19]
s_waitcnt vmcnt(54)
buffer_load_dwordx4 v14, s[48:51], s78 offen offset:2048 nt lds
ds_read_b128 a[216:219], v181 offset:33280
ds_read_b64_tr_b16 a[0:1], v211 offset:57344
ds_read_b64_tr_b16 a[2:3], v211 offset:57600
v_mfma_f32_16x16x32_bf16 v[20:23], a[12:15], a[208:211], v[20:23]
s_waitcnt lgkmcnt(7)
ds_read_b64_tr_b16 a[4:5], v212 offset:57344
ds_read_b64_tr_b16 a[6:7], v212 offset:57600
buffer_load_dwordx4 v15, s[48:51], s78 offen offset:3072 nt lds
v_mfma_f32_16x16x32_bf16 v[32:35], a[16:19], a[208:211], v[32:35]
s_waitcnt vmcnt(38)
s_mov_b32 m0, 0xa000
s_mov_b32 s78, 0x2000
buffer_load_dwordx4 v12, s[48:51], s78 offen nt lds
ds_read_b64_tr_b16 a[8:9], v211 offset:57856
ds_read_b64_tr_b16 a[10:11], v211 offset:58112
buffer_load_dwordx4 v13, s[48:51], s78 offen offset:1024 nt lds
v_mfma_f32_16x16x32_bf16 v[36:39], a[20:23], a[208:211], v[36:39]
s_waitcnt vmcnt(38)
buffer_load_dwordx4 v14, s[48:51], s78 offen offset:2048 nt lds
ds_read_b64_tr_b16 a[12:13], v212 offset:57856
ds_read_b64_tr_b16 a[14:15], v212 offset:58112
s_waitcnt lgkmcnt(9)
v_mfma_f32_16x16x32_bf16 v[48:51], a[24:27], a[208:211], v[48:51]
ds_read_b64_tr_b16 a[16:17], v211 offset:58368
ds_read_b64_tr_b16 a[18:19], v211 offset:58624
buffer_load_dwordx4 v15, s[48:51], s78 offen offset:3072 nt lds
v_mfma_f32_16x16x32_bf16 v[52:55], a[28:31], a[208:211], v[52:55]
s_waitcnt vmcnt(38)
s_mov_b32 m0, 0xb000
s_mov_b32 s78, 0x3000
buffer_load_dwordx4 v12, s[48:51], s78 offen nt lds
)"
R"(
ds_read_b64_tr_b16 a[20:21], v212 offset:58368
ds_read_b64_tr_b16 a[22:23], v212 offset:58624
s_waitcnt lgkmcnt(8)
v_mfma_f32_16x16x32_bf16 v[0:3], a[0:3], a[216:219], v[0:3]
ds_read_b64_tr_b16 a[24:25], v211 offset:58880
ds_read_b64_tr_b16 a[26:27], v211 offset:59136
buffer_load_dwordx4 v13, s[48:51], s78 offen offset:1024 nt lds
v_mfma_f32_16x16x32_bf16 v[4:7], a[4:7], a[216:219], v[4:7]
s_waitcnt vmcnt(38)
buffer_load_dwordx4 v14, s[48:51], s78 offen offset:2048 nt lds
s_waitcnt vmcnt(47)
s_barrier
ds_read_b64_tr_b16 a[28:29], v212 offset:58880
ds_read_b64_tr_b16 a[30:31], v212 offset:59136
s_waitcnt lgkmcnt(8)
v_mfma_f32_16x16x32_bf16 v[16:19], a[8:11], a[216:219], v[16:19]
ds_read_b64_tr_b16 a[0:1], v213 offset:16384
ds_read_b64_tr_b16 a[2:3], v213 offset:16640
buffer_load_dwordx4 v15, s[48:51], s78 offen offset:3072 nt lds
v_mfma_f32_16x16x32_bf16 v[20:23], a[12:15], a[216:219], v[20:23]
s_waitcnt vmcnt(38)
s_mov_b32 m0, 0xc000
s_mov_b32 s78, 0x8000
buffer_load_dwordx4 v8, s[44:47], s78 offen nt lds
ds_read_b64_tr_b16 a[4:5], v214 offset:16384
ds_read_b64_tr_b16 a[6:7], v214 offset:16640
v_mul_f32_e32 v0, s69, v0
v_mul_f32_e32 v1, s69, v1
s_waitcnt lgkmcnt(8)
v_mfma_f32_16x16x32_bf16 v[32:35], a[16:19], a[216:219], v[32:35]
ds_read_b64_tr_b16 a[8:9], v213 offset:16896
ds_read_b64_tr_b16 a[10:11], v213 offset:17152
buffer_load_dwordx4 v9, s[44:47], s78 offen offset:1024 nt lds
v_mul_f32_e32 v2, s69, v2
v_mul_f32_e32 v3, s69, v3
v_mfma_f32_16x16x32_bf16 v[36:39], a[20:23], a[216:219], v[36:39]
s_waitcnt vmcnt(38)
buffer_load_dwordx4 v10, s[44:47], s78 offen offset:2048 nt lds
ds_read_b64_tr_b16 a[12:13], v214 offset:16896
ds_read_b64_tr_b16 a[14:15], v214 offset:17152
v_mul_f32_e32 v4, s69, v4
v_mul_f32_e32 v5, s69, v5
s_waitcnt lgkmcnt(8)
v_mfma_f32_16x16x32_bf16 v[48:51], a[24:27], a[216:219], v[48:51]
ds_read_b64_tr_b16 a[16:17], v213 offset:17408
ds_read_b64_tr_b16 a[18:19], v213 offset:17664
buffer_load_dwordx4 v11, s[44:47], s78 offen offset:3072 nt lds
v_mul_f32_e32 v6, s69, v6
v_mul_f32_e32 v7, s69, v7
v_cvt_pk_bf16_f32 v0, v0, v1
v_mfma_f32_16x16x32_bf16 v[52:55], a[28:31], a[216:219], v[52:55]
s_barrier
ds_read_b64_tr_b16 a[20:21], v214 offset:17408
ds_read_b64_tr_b16 a[22:23], v214 offset:17664
v_cvt_pk_bf16_f32 v1, v2, v3
v_cvt_pk_bf16_f32 v2, v4, v5
v_cvt_pk_bf16_f32 v3, v6, v7
s_waitcnt lgkmcnt(8)
v_mfma_f32_16x16x32_bf16 v[64:67], a[0:3], a[128:131], v[64:67]
s_waitcnt vmcnt(38)
s_mov_b32 m0, 0xd000
s_mov_b32 s78, 0x9000
buffer_load_dwordx4 v8, s[44:47], s78 offen nt lds
ds_read_b64_tr_b16 a[24:25], v213 offset:17920
ds_read_b64_tr_b16 a[26:27], v213 offset:18176
v_mfma_f32_16x16x32_bf16 v[68:71], a[4:7], a[128:131], v[68:71]
s_waitcnt lgkmcnt(6)
ds_read_b64_tr_b16 a[28:29], v214 offset:17920
ds_read_b64_tr_b16 a[30:31], v214 offset:18176
buffer_load_dwordx4 v9, s[44:47], s78 offen offset:1024 nt lds
v_permlane16_swap_b32_e32 v0, v2
v_permlane16_swap_b32_e32 v1, v3
v_mfma_f32_16x16x32_bf16 v[80:83], a[8:11], a[128:131], v[80:83]
s_waitcnt vmcnt(38)
buffer_load_dwordx4 v10, s[44:47], s78 offen offset:2048 nt lds
ds_read_b64_tr_b16 a[0:1], v213 offset:24576
ds_read_b64_tr_b16 a[2:3], v213 offset:24832
v_accvgpr_write_b32 a32, v0
v_accvgpr_write_b32 a33, v1
v_mfma_f32_16x16x32_bf16 v[84:87], a[12:15], a[128:131], v[84:87]
s_waitcnt lgkmcnt(6)
ds_read_b64_tr_b16 a[4:5], v214 offset:24576
ds_read_b64_tr_b16 a[6:7], v214 offset:24832
buffer_load_dwordx4 v11, s[44:47], s78 offen offset:3072 nt lds
v_accvgpr_write_b32 a34, v2
v_accvgpr_write_b32 a35, v3
v_mul_f32_e32 v16, s69, v16
v_mfma_f32_16x16x32_bf16 v[96:99], a[16:19], a[128:131], v[96:99]
ds_read_b64_tr_b16 a[8:9], v213 offset:25088
ds_read_b64_tr_b16 a[10:11], v213 offset:25344
v_mul_f32_e32 v17, s69, v17
v_mul_f32_e32 v18, s69, v18
v_mul_f32_e32 v19, s69, v19
v_mul_f32_e32 v20, s69, v20
v_mul_f32_e32 v21, s69, v21
v_mfma_f32_16x16x32_bf16 v[100:103], a[20:23], a[128:131], v[100:103]
s_waitcnt vmcnt(22)
s_mov_b32 m0, 0xe000
s_mov_b32 s78, 0xa000
buffer_load_dwordx4 v8, s[44:47], s78 offen nt lds
ds_read_b64_tr_b16 a[12:13], v214 offset:25088
ds_read_b64_tr_b16 a[14:15], v214 offset:25344
s_waitcnt lgkmcnt(8)
v_mfma_f32_16x16x32_bf16 v[112:115], a[24:27], a[128:131], v[112:115]
ds_read_b64_tr_b16 a[16:17], v213 offset:25600
ds_read_b64_tr_b16 a[18:19], v213 offset:25856
buffer_load_dwordx4 v9, s[44:47], s78 offen offset:1024 nt lds
v_mul_f32_e32 v22, s69, v22
v_mul_f32_e32 v23, s69, v23
v_mfma_f32_16x16x32_bf16 v[116:119], a[28:31], a[128:131], v[116:119]
s_waitcnt vmcnt(22)
buffer_load_dwordx4 v10, s[44:47], s78 offen offset:2048 nt lds
ds_read_b64_tr_b16 a[20:21], v214 offset:25600
ds_read_b64_tr_b16 a[22:23], v214 offset:25856
v_cvt_pk_bf16_f32 v16, v16, v17
v_cvt_pk_bf16_f32 v17, v18, v19
s_waitcnt lgkmcnt(8)
v_mfma_f32_16x16x32_bf16 v[64:67], a[0:3], a[136:139], v[64:67]
ds_read_b64_tr_b16 a[24:25], v213 offset:26112
ds_read_b64_tr_b16 a[26:27], v213 offset:26368
buffer_load_dwordx4 v11, s[44:47], s78 offen offset:3072 nt lds
v_cvt_pk_bf16_f32 v18, v20, v21
v_cvt_pk_bf16_f32 v19, v22, v23
v_mfma_f32_16x16x32_bf16 v[68:71], a[4:7], a[136:139], v[68:71]
s_waitcnt vmcnt(63)
s_barrier
ds_read_b64_tr_b16 a[28:29], v214 offset:26112
ds_read_b64_tr_b16 a[30:31], v214 offset:26368
v_permlane16_swap_b32_e32 v16, v18
v_permlane16_swap_b32_e32 v17, v19
s_waitcnt lgkmcnt(8)
v_mfma_f32_16x16x32_bf16 v[80:83], a[8:11], a[136:139], v[80:83]
s_waitcnt vmcnt(22)
s_mov_b32 m0, 0xf000
s_mov_b32 s78, 0xb000
buffer_load_dwordx4 v8, s[44:47], s78 offen nt lds
ds_read_b128 a[0:3], v202
v_accvgpr_write_b32 a40, v16
v_mfma_f32_16x16x32_bf16 v[84:87], a[12:15], a[136:139], v[84:87]
s_waitcnt lgkmcnt(5)
ds_read_b128 a[4:7], v202 offset:512
buffer_load_dwordx4 v9, s[44:47], s78 offen offset:1024 nt lds
v_accvgpr_write_b32 a41, v17
v_accvgpr_write_b32 a42, v18
v_accvgpr_write_b32 a43, v19
v_mul_f32_e32 v32, s69, v32
v_mfma_f32_16x16x32_bf16 v[96:99], a[16:19], a[136:139], v[96:99]
s_waitcnt vmcnt(22)
buffer_load_dwordx4 v10, s[44:47], s78 offen offset:2048 nt lds
ds_read_b128 a[8:11], v202 offset:1024
v_mul_f32_e32 v33, s69, v33
v_mul_f32_e32 v34, s69, v34
v_mfma_f32_16x16x32_bf16 v[100:103], a[20:23], a[136:139], v[100:103]
s_waitcnt lgkmcnt(3)
ds_read_b128 a[12:15], v202 offset:1536
buffer_load_dwordx4 v11, s[44:47], s78 offen offset:3072 nt lds
v_mul_f32_e32 v35, s69, v35
v_mul_f32_e32 v36, s69, v36
v_mul_f32_e32 v37, s69, v37
v_mul_f32_e32 v38, s69, v38
v_mfma_f32_16x16x32_bf16 v[112:115], a[24:27], a[136:139], v[112:115]
ds_read_b128 a[16:19], v202 offset:4096
v_mul_f32_e32 v39, s69, v39
v_cvt_pk_bf16_f32 v32, v32, v33
v_cvt_pk_bf16_f32 v33, v34, v35
v_cvt_pk_bf16_f32 v34, v36, v37
v_cvt_pk_bf16_f32 v35, v38, v39
s_nop 0
v_permlane16_swap_b32_e32 v32, v34
v_mfma_f32_16x16x32_bf16 v[116:119], a[28:31], a[136:139], v[116:119]
s_barrier
ds_read_b128 a[20:23], v202 offset:4608
v_permlane16_swap_b32_e32 v33, v35
v_accvgpr_write_b32 a48, v32
v_accvgpr_write_b32 a49, v33
v_accvgpr_write_b32 a50, v34
v_accvgpr_write_b32 a51, v35
s_waitcnt lgkmcnt(4)
v_mfma_f32_16x16x32_bf16 v[128:131], a[0:3], a[32:35], 0
ds_read_b128 a[24:27], v202 offset:5120
v_mul_f32_e32 v48, s69, v48
v_mul_f32_e32 v49, s69, v49
v_mul_f32_e32 v50, s69, v50
v_mul_f32_e32 v51, s69, v51
v_mul_f32_e32 v52, s69, v52
v_mul_f32_e32 v53, s69, v53
v_mfma_f32_16x16x32_bf16 v[128:131], a[4:7], a[40:43], v[128:131]
s_waitcnt lgkmcnt(3)
ds_read_b128 a[28:31], v202 offset:5632
v_mul_f32_e32 v54, s69, v54
v_mul_f32_e32 v55, s69, v55
v_cvt_pk_bf16_f32 v48, v48, v49
v_cvt_pk_bf16_f32 v49, v50, v51
v_cvt_pk_bf16_f32 v50, v52, v53
v_cvt_pk_bf16_f32 v51, v54, v55
v_mfma_f32_16x16x32_bf16 v[128:131], a[8:11], a[48:51], v[128:131]
ds_read_b128 a[0:3], v202 offset:8192
v_permlane16_swap_b32_e32 v48, v50
v_permlane16_swap_b32_e32 v49, v51
v_accvgpr_write_b32 a56, v48
)"
R"(
v_accvgpr_write_b32 a57, v49
v_accvgpr_write_b32 a58, v50
v_accvgpr_write_b32 a59, v51
s_nop 1
v_mfma_f32_16x16x32_bf16 v[128:131], a[12:15], a[56:59], v[128:131]
s_waitcnt vmcnt(22)
s_mov_b32 m0, 0x10000
s_mov_b32 s78, 0x4000
buffer_load_dwordx4 v12, s[48:51], s78 offen nt lds
ds_read_b128 a[4:7], v202 offset:8704
s_waitcnt lgkmcnt(4)
v_mfma_f32_16x16x32_bf16 v[132:135], a[16:19], a[32:35], 0
buffer_load_dwordx4 v13, s[48:51], s78 offen offset:1024 nt lds
s_waitcnt vmcnt(22)
buffer_load_dwordx4 v14, s[48:51], s78 offen offset:2048 nt lds
ds_read_b128 a[8:11], v202 offset:9216
v_mfma_f32_16x16x32_bf16 v[132:135], a[20:23], a[40:43], v[132:135]
s_waitcnt lgkmcnt(3)
ds_read_b128 a[12:15], v202 offset:9728
buffer_load_dwordx4 v15, s[48:51], s78 offen offset:3072 nt lds
v_mfma_f32_16x16x32_bf16 v[132:135], a[24:27], a[48:51], v[132:135]
ds_read_b128 a[16:19], v202 offset:12288
v_max3_f32 v236, v128, v129, v130
v_mfma_f32_16x16x32_bf16 v[132:135], a[28:31], a[56:59], v[132:135]
s_waitcnt vmcnt(22)
s_mov_b32 m0, 0x11000
s_mov_b32 s78, 0x5000
buffer_load_dwordx4 v12, s[48:51], s78 offen nt lds
ds_read_b128 a[20:23], v202 offset:12800
s_waitcnt lgkmcnt(4)
v_mfma_f32_16x16x32_bf16 v[144:147], a[0:3], a[32:35], 0
ds_read_b128 a[24:27], v202 offset:13312
buffer_load_dwordx4 v13, s[48:51], s78 offen offset:1024 nt lds
v_mfma_f32_16x16x32_bf16 v[144:147], a[4:7], a[40:43], v[144:147]
s_waitcnt vmcnt(22)
buffer_load_dwordx4 v14, s[48:51], s78 offen offset:2048 nt lds
s_waitcnt vmcnt(39)
s_barrier
ds_read_b128 a[28:31], v202 offset:13824
s_waitcnt lgkmcnt(4)
v_mfma_f32_16x16x32_bf16 v[144:147], a[8:11], a[48:51], v[144:147]
ds_read_b64_tr_b16 a[0:1], v209 offset:16384
ds_read_b64_tr_b16 a[2:3], v209 offset:16640
buffer_load_dwordx4 v15, s[48:51], s78 offen offset:3072 nt lds
v_max3_f32 v236, v236, v131, v132
v_max3_f32 v236, v236, v133, v134
v_mfma_f32_16x16x32_bf16 v[144:147], a[12:15], a[56:59], v[144:147]
s_waitcnt lgkmcnt(4)
ds_read_b64_tr_b16 a[4:5], v210 offset:16384
ds_read_b64_tr_b16 a[6:7], v210 offset:16640
v_mfma_f32_16x16x32_bf16 v[148:151], a[16:19], a[32:35], 0
s_waitcnt vmcnt(22)
s_mov_b32 m0, 0x12000
s_mov_b32 s78, 0x6000
buffer_load_dwordx4 v12, s[48:51], s78 offen nt lds
ds_read_b64_tr_b16 a[8:9], v209 offset:16896
ds_read_b64_tr_b16 a[10:11], v209 offset:17152
v_mfma_f32_16x16x32_bf16 v[148:151], a[20:23], a[40:43], v[148:151]
s_waitcnt lgkmcnt(6)
ds_read_b64_tr_b16 a[12:13], v210 offset:16896
ds_read_b64_tr_b16 a[14:15], v210 offset:17152
buffer_load_dwordx4 v13, s[48:51], s78 offen offset:1024 nt lds
v_mfma_f32_16x16x32_bf16 v[148:151], a[24:27], a[48:51], v[148:151]
s_waitcnt vmcnt(22)
buffer_load_dwordx4 v14, s[48:51], s78 offen offset:2048 nt lds
ds_read_b64_tr_b16 a[16:17], v209 offset:17408
ds_read_b64_tr_b16 a[18:19], v209 offset:17664
v_max3_f32 v236, v236, v135, v144
v_max3_f32 v236, v236, v145, v146
v_mfma_f32_16x16x32_bf16 v[148:151], a[28:31], a[56:59], v[148:151]
s_waitcnt lgkmcnt(6)
ds_read_b64_tr_b16 a[20:21], v210 offset:17408
ds_read_b64_tr_b16 a[22:23], v210 offset:17664
buffer_load_dwordx4 v15, s[48:51], s78 offen offset:3072 nt lds
v_mfma_f32_16x16x32_bf16 v[64:67], a[0:3], a[144:147], v[64:67]
ds_read_b64_tr_b16 a[24:25], v209 offset:17920
ds_read_b64_tr_b16 a[26:27], v209 offset:18176
v_mfma_f32_16x16x32_bf16 v[68:71], a[4:7], a[144:147], v[68:71]
s_waitcnt vmcnt(22)
s_mov_b32 m0, 0x13000
s_mov_b32 s78, 0x7000
buffer_load_dwordx4 v12, s[48:51], s78 offen nt lds
ds_read_b64_tr_b16 a[28:29], v210 offset:17920
ds_read_b64_tr_b16 a[30:31], v210 offset:18176
s_waitcnt lgkmcnt(8)
v_mfma_f32_16x16x32_bf16 v[80:83], a[8:11], a[144:147], v[80:83]
ds_read_b64_tr_b16 a[0:1], v209 offset:24576
ds_read_b64_tr_b16 a[2:3], v209 offset:24832
buffer_load_dwordx4 v13, s[48:51], s78 offen offset:1024 nt lds
v_max3_f32 v236, v236, v147, v148
v_max3_f32 v236, v236, v149, v150
v_max_f32_e32 v236, v236, v151
v_mov_b32_e32 v246, v236
s_nop 1
v_permlane16_swap_b32_e32 v236, v246
v_mfma_f32_16x16x32_bf16 v[84:87], a[12:15], a[144:147], v[84:87]
s_waitcnt vmcnt(22)
buffer_load_dwordx4 v14, s[48:51], s78 offen offset:2048 nt lds
ds_read_b64_tr_b16 a[4:5], v210 offset:24576
ds_read_b64_tr_b16 a[6:7], v210 offset:24832
v_max_f32_e32 v236, v236, v246
v_mov_b32_e32 v246, v236
s_waitcnt lgkmcnt(8)
v_mfma_f32_16x16x32_bf16 v[96:99], a[16:19], a[144:147], v[96:99]
ds_read_b64_tr_b16 a[8:9], v209 offset:25088
ds_read_b64_tr_b16 a[10:11], v209 offset:25344
buffer_load_dwordx4 v15, s[48:51], s78 offen offset:3072 nt lds
v_permlane32_swap_b32_e32 v236, v246
v_max_f32_e32 v236, v236, v246
v_sub_f32_e32 v192, 0, v236
v_mfma_f32_16x16x32_bf16 v[100:103], a[20:23], a[144:147], v[100:103]
s_waitcnt lgkmcnt(6)
ds_read_b64_tr_b16 a[12:13], v210 offset:25088
ds_read_b64_tr_b16 a[14:15], v210 offset:25344
v_sub_f32_e32 v193, 0, v236
v_sub_f32_e32 v194, 0, v236
v_sub_f32_e32 v195, 0, v236
v_sub_f32_e32 v128, v128, v236
v_sub_f32_e32 v129, v129, v236
v_sub_f32_e32 v130, v130, v236
v_mfma_f32_16x16x32_bf16 v[112:115], a[24:27], a[144:147], v[112:115]
ds_read_b64_tr_b16 a[16:17], v209 offset:25600
ds_read_b64_tr_b16 a[18:19], v209 offset:25856
v_sub_f32_e32 v131, v131, v236
v_exp_f32_e32 v128, v128
v_exp_f32_e32 v129, v129
v_add_f32_e32 v242, v242, v128
v_mfma_f32_16x16x32_bf16 v[116:119], a[28:31], a[144:147], v[116:119]
s_mov_b32 m0, 0x14000
s_add_u32 s78, s85, 0
buffer_load_dwordx4 v24, s[40:43], s78 offen lds
ds_read_b64_tr_b16 a[20:21], v210 offset:25600
ds_read_b64_tr_b16 a[22:23], v210 offset:25856
buffer_load_dwordx4 v25, s[40:43], s78 offen offset:1024 lds
s_waitcnt lgkmcnt(8)
v_mfma_f32_16x16x32_bf16 v[64:67], a[0:3], a[152:155], v[64:67]
ds_read_b64_tr_b16 a[24:25], v209 offset:26112
ds_read_b64_tr_b16 a[26:27], v209 offset:26368
buffer_load_dwordx4 v26, s[40:43], s78 offen offset:2048 lds
v_add_f32_e32 v242, v242, v129
v_cvt_pk_bf16_f32 v176, v128, v129
v_sub_f32_e32 v132, v132, v236
v_mfma_f32_16x16x32_bf16 v[68:71], a[4:7], a[152:155], v[68:71]
buffer_load_dwordx4 v27, s[40:43], s78 offen offset:3072 lds
s_mov_b32 m0, 0x15000
s_add_u32 s78, s85, 0x20000
buffer_load_dwordx4 v28, s[40:43], s78 offen lds
s_waitcnt vmcnt(63)
s_barrier
ds_read_b64_tr_b16 a[28:29], v210 offset:26112
ds_read_b64_tr_b16 a[30:31], v210 offset:26368
s_waitcnt lgkmcnt(8)
v_mfma_f32_16x16x32_bf16 v[80:83], a[8:11], a[152:155], v[80:83]
ds_read_b128 a[0:3], v200
buffer_load_dwordx4 v29, s[40:43], s78 offen offset:1024 lds
v_exp_f32_e32 v130, v130
v_exp_f32_e32 v131, v131
v_mfma_f32_16x16x32_bf16 v[84:87], a[12:15], a[152:155], v[84:87]
s_waitcnt lgkmcnt(5)
ds_read_b128 a[4:7], v200 offset:512
buffer_load_dwordx4 v30, s[40:43], s78 offen offset:2048 lds
buffer_load_dwordx4 v31, s[40:43], s78 offen offset:3072 lds
v_add_f32_e32 v242, v242, v130
v_mfma_f32_16x16x32_bf16 v[96:99], a[16:19], a[152:155], v[96:99]
s_mov_b32 m0, 0x16000
s_add_u32 s78, s85, 0x40000
buffer_load_dwordx4 v24, s[40:43], s78 offen lds
ds_read_b128 a[8:11], v200 offset:1024
v_add_f32_e32 v242, v242, v131
v_cvt_pk_bf16_f32 v177, v130, v131
v_mfma_f32_16x16x32_bf16 v[100:103], a[20:23], a[152:155], v[100:103]
s_waitcnt lgkmcnt(3)
ds_read_b128 a[12:15], v200 offset:1536
buffer_load_dwordx4 v25, s[40:43], s78 offen offset:1024 lds
buffer_load_dwordx4 v26, s[40:43], s78 offen offset:2048 lds
v_sub_f32_e32 v133, v133, v236
v_mfma_f32_16x16x32_bf16 v[112:115], a[24:27], a[152:155], v[112:115]
ds_read_b128 a[16:19], v200 offset:4096
buffer_load_dwordx4 v27, s[40:43], s78 offen offset:3072 lds
v_sub_f32_e32 v134, v134, v236
v_sub_f32_e32 v135, v135, v236
v_exp_f32_e32 v132, v132
v_mfma_f32_16x16x32_bf16 v[116:119], a[28:31], a[152:155], v[116:119]
s_mov_b32 m0, 0x17000
s_add_u32 s78, s85, 0x60000
buffer_load_dwordx4 v28, s[40:43], s78 offen lds
ds_read_b128 a[20:23], v200 offset:4608
buffer_load_dwordx4 v29, s[40:43], s78 offen offset:1024 lds
s_waitcnt lgkmcnt(4)
v_mfma_f32_16x16x32_bf16 v[160:163], a[0:3], a[32:35], v[192:195]
ds_read_b128 a[24:27], v200 offset:5120
buffer_load_dwordx4 v30, s[40:43], s78 offen offset:2048 lds
v_add_f32_e32 v242, v242, v132
v_exp_f32_e32 v133, v133
v_mfma_f32_16x16x32_bf16 v[160:163], a[4:7], a[40:43], v[160:163]
s_waitcnt lgkmcnt(3)
ds_read_b128 a[28:31], v200 offset:5632
buffer_load_dwordx4 v31, s[40:43], s78 offen offset:3072 lds
s_add_u32 s85, s85, 0x80000
s_and_b32 s85, s85, 0x3fffff
)"
R"(
v_add_f32_e32 v242, v242, v133
v_cvt_pk_bf16_f32 v178, v132, v133
v_mfma_f32_16x16x32_bf16 v[160:163], a[8:11], a[48:51], v[160:163]
s_waitcnt vmcnt(38)
s_mov_b32 m0, 0x18000
s_mov_b32 s78, 0xc000
buffer_load_dwordx4 v8, s[44:47], s78 offen nt lds
ds_read_b128 a[0:3], v200 offset:8192
v_sub_f32_e32 v144, v144, v236
v_mfma_f32_16x16x32_bf16 v[160:163], a[12:15], a[56:59], v[160:163]
buffer_load_dwordx4 v9, s[44:47], s78 offen offset:1024 nt lds
s_waitcnt vmcnt(38)
buffer_load_dwordx4 v10, s[44:47], s78 offen offset:2048 nt lds
ds_read_b128 a[4:7], v200 offset:8704
s_waitcnt lgkmcnt(4)
v_mfma_f32_16x16x32_bf16 v[164:167], a[16:19], a[32:35], v[192:195]
ds_read_b128 a[8:11], v200 offset:9216
buffer_load_dwordx4 v11, s[44:47], s78 offen offset:3072 nt lds
s_waitcnt vmcnt(38)
s_mov_b32 m0, 0x19000
s_mov_b32 s78, 0xd000
buffer_load_dwordx4 v8, s[44:47], s78 offen nt lds
v_mfma_f32_16x16x32_bf16 v[164:167], a[20:23], a[40:43], v[164:167]
s_waitcnt lgkmcnt(3)
ds_read_b128 a[12:15], v200 offset:9728
buffer_load_dwordx4 v9, s[44:47], s78 offen offset:1024 nt lds
v_exp_f32_e32 v134, v134
v_mfma_f32_16x16x32_bf16 v[164:167], a[24:27], a[48:51], v[164:167]
s_waitcnt vmcnt(38)
buffer_load_dwordx4 v10, s[44:47], s78 offen offset:2048 nt lds
ds_read_b128 a[16:19], v200 offset:12288
buffer_load_dwordx4 v11, s[44:47], s78 offen offset:3072 nt lds
v_add_f32_e32 v242, v242, v134
v_mfma_f32_16x16x32_bf16 v[164:167], a[28:31], a[56:59], v[164:167]
s_waitcnt vmcnt(38)
s_mov_b32 m0, 0x1a000
s_mov_b32 s78, 0xe000
buffer_load_dwordx4 v8, s[44:47], s78 offen nt lds
ds_read_b128 a[20:23], v200 offset:12800
v_sub_f32_e32 v145, v145, v236
s_waitcnt lgkmcnt(4)
v_mfma_f32_16x16x32_bf16 v[128:131], a[0:3], a[32:35], v[192:195]
ds_read_b128 a[24:27], v200 offset:13312
buffer_load_dwordx4 v9, s[44:47], s78 offen offset:1024 nt lds
s_waitcnt vmcnt(38)
buffer_load_dwordx4 v10, s[44:47], s78 offen offset:2048 nt lds
v_mfma_f32_16x16x32_bf16 v[128:131], a[4:7], a[40:43], v[128:131]
buffer_load_dwordx4 v11, s[44:47], s78 offen offset:3072 nt lds
s_waitcnt vmcnt(60)
s_barrier
ds_read_b128 a[28:31], v200 offset:13824
v_exp_f32_e32 v135, v135
s_waitcnt lgkmcnt(4)
v_mfma_f32_16x16x32_bf16 v[128:131], a[8:11], a[48:51], v[128:131]
ds_read_b64_tr_b16 a[0:1], v203 offset:32768
ds_read_b64_tr_b16 a[2:3], v203 offset:36864
v_add_f32_e32 v242, v242, v135
v_cvt_pk_bf16_f32 v179, v134, v135
v_sub_f32_e32 v146, v146, v236
v_sub_f32_e32 v147, v147, v236
v_mfma_f32_16x16x32_bf16 v[128:131], a[12:15], a[56:59], v[128:131]
s_waitcnt vmcnt(38)
s_mov_b32 m0, 0x1b000
s_mov_b32 s78, 0xf000
buffer_load_dwordx4 v8, s[44:47], s78 offen nt lds
buffer_load_dwordx4 v9, s[44:47], s78 offen offset:1024 nt lds
ds_read_b64_tr_b16 a[4:5], v204 offset:32768
ds_read_b64_tr_b16 a[6:7], v204 offset:36864
s_waitcnt lgkmcnt(6)
v_mfma_f32_16x16x32_bf16 v[132:135], a[16:19], a[32:35], v[192:195]
s_waitcnt vmcnt(38)
buffer_load_dwordx4 v10, s[44:47], s78 offen offset:2048 nt lds
ds_read_b64_tr_b16 a[8:9], v203 offset:33280
ds_read_b64_tr_b16 a[10:11], v203 offset:37376
buffer_load_dwordx4 v11, s[44:47], s78 offen offset:3072 nt lds
v_mfma_f32_16x16x32_bf16 v[132:135], a[20:23], a[40:43], v[132:135]
s_waitcnt vmcnt(38)
s_mov_b32 m0, 0x1c000
s_mov_b32 s78, 0x8000
buffer_load_dwordx4 v12, s[48:51], s78 offen nt lds
ds_read_b64_tr_b16 a[12:13], v204 offset:33280
ds_read_b64_tr_b16 a[14:15], v204 offset:37376
s_waitcnt lgkmcnt(8)
v_mfma_f32_16x16x32_bf16 v[132:135], a[24:27], a[48:51], v[132:135]
ds_read_b64_tr_b16 a[16:17], v203 offset:33792
ds_read_b64_tr_b16 a[18:19], v203 offset:37888
buffer_load_dwordx4 v13, s[48:51], s78 offen offset:1024 nt lds
v_sub_f32_e32 v148, v148, v236
v_sub_f32_e32 v149, v149, v236
v_mfma_f32_16x16x32_bf16 v[132:135], a[28:31], a[56:59], v[132:135]
s_waitcnt vmcnt(38)
buffer_load_dwordx4 v14, s[48:51], s78 offen offset:2048 nt lds
ds_read_b64_tr_b16 a[20:21], v204 offset:33792
ds_read_b64_tr_b16 a[22:23], v204 offset:37888
v_sub_f32_e32 v150, v150, v236
s_waitcnt lgkmcnt(8)
v_mfma_f32_16x16x32_bf16 v[0:3], a[0:3], v[176:179], 0
ds_read_b64_tr_b16 a[24:25], v203 offset:34304
ds_read_b64_tr_b16 a[26:27], v203 offset:38400
buffer_load_dwordx4 v15, s[48:51], s78 offen offset:3072 nt lds
v_sub_f32_e32 v151, v151, v236
v_exp_f32_e32 v144, v144
v_mfma_f32_16x16x32_bf16 v[4:7], a[4:7], v[176:179], 0
s_waitcnt lgkmcnt(6)
ds_read_b64_tr_b16 a[28:29], v204 offset:34304
ds_read_b64_tr_b16 a[30:31], v204 offset:38400
v_exp_f32_e32 v145, v145
v_exp_f32_e32 v146, v146
v_cvt_pk_bf16_f32 v184, v144, v145
v_mfma_f32_16x16x32_bf16 v[16:19], a[8:11], v[176:179], 0
s_waitcnt vmcnt(38)
s_mov_b32 m0, 0x1d000
s_mov_b32 s78, 0x9000
buffer_load_dwordx4 v12, s[48:51], s78 offen nt lds
ds_read_b64_tr_b16 a[0:1], v203 offset:40960
ds_read_b64_tr_b16 a[2:3], v203 offset:45056
v_mfma_f32_16x16x32_bf16 v[20:23], a[12:15], v[176:179], 0
s_waitcnt lgkmcnt(6)
ds_read_b64_tr_b16 a[4:5], v204 offset:40960
ds_read_b64_tr_b16 a[6:7], v204 offset:45056
buffer_load_dwordx4 v13, s[48:51], s78 offen offset:1024 nt lds
v_exp_f32_e32 v147, v147
v_add_f32_e32 v242, v242, v144
v_mfma_f32_16x16x32_bf16 v[32:35], a[16:19], v[176:179], 0
s_waitcnt vmcnt(38)
buffer_load_dwordx4 v14, s[48:51], s78 offen offset:2048 nt lds
ds_read_b64_tr_b16 a[8:9], v203 offset:41472
ds_read_b64_tr_b16 a[10:11], v203 offset:45568
v_cvt_pk_bf16_f32 v185, v146, v147
v_mfma_f32_16x16x32_bf16 v[36:39], a[20:23], v[176:179], 0
s_waitcnt lgkmcnt(6)
ds_read_b64_tr_b16 a[12:13], v204 offset:41472
ds_read_b64_tr_b16 a[14:15], v204 offset:45568
buffer_load_dwordx4 v15, s[48:51], s78 offen offset:3072 nt lds
v_exp_f32_e32 v148, v148
v_exp_f32_e32 v149, v149
v_mfma_f32_16x16x32_bf16 v[48:51], a[24:27], v[176:179], 0
ds_read_b64_tr_b16 a[16:17], v203 offset:41984
ds_read_b64_tr_b16 a[18:19], v203 offset:46080
v_cvt_pk_bf16_f32 v186, v148, v149
v_exp_f32_e32 v150, v150
v_exp_f32_e32 v151, v151
v_mfma_f32_16x16x32_bf16 v[52:55], a[28:31], v[176:179], 0
s_waitcnt vmcnt(22)
s_mov_b32 m0, 0x1e000
s_mov_b32 s78, 0xa000
buffer_load_dwordx4 v12, s[48:51], s78 offen nt lds
ds_read_b64_tr_b16 a[20:21], v204 offset:41984
ds_read_b64_tr_b16 a[22:23], v204 offset:46080
v_cvt_pk_bf16_f32 v187, v150, v151
s_waitcnt lgkmcnt(8)
s_nop 0
v_mfma_f32_16x16x32_bf16 v[0:3], a[0:3], v[184:187], v[0:3]
ds_read_b64_tr_b16 a[24:25], v203 offset:42496
ds_read_b64_tr_b16 a[26:27], v203 offset:46592
buffer_load_dwordx4 v13, s[48:51], s78 offen offset:1024 nt lds
v_mfma_f32_16x16x32_bf16 v[4:7], a[4:7], v[184:187], v[4:7]
s_waitcnt vmcnt(22)
buffer_load_dwordx4 v14, s[48:51], s78 offen offset:2048 nt lds
s_waitcnt vmcnt(59)
s_barrier
ds_read_b64_tr_b16 a[28:29], v204 offset:42496
ds_read_b64_tr_b16 a[30:31], v204 offset:46592
s_waitcnt lgkmcnt(8)
v_mfma_f32_16x16x32_bf16 v[16:19], a[8:11], v[184:187], v[16:19]
ds_read_b128 a[0:3], v200 offset:49152
buffer_load_dwordx4 v15, s[48:51], s78 offen offset:3072 nt lds
v_add_f32_e32 v242, v242, v145
v_add_f32_e32 v242, v242, v146
v_add_f32_e32 v242, v242, v147
v_add_f32_e32 v242, v242, v148
v_mfma_f32_16x16x32_bf16 v[20:23], a[12:15], v[184:187], v[20:23]
s_waitcnt lgkmcnt(5)
ds_read_b128 a[4:7], v200 offset:49664
v_add_f32_e32 v242, v242, v149
v_add_f32_e32 v242, v242, v150
v_add_f32_e32 v242, v242, v151
v_max3_f32 v236, v160, v161, v162
v_max3_f32 v236, v236, v163, v164
v_max3_f32 v236, v236, v165, v166
v_mfma_f32_16x16x32_bf16 v[32:35], a[16:19], v[184:187], v[32:35]
s_waitcnt vmcnt(22)
s_mov_b32 m0, 0x1f000
s_mov_b32 s78, 0xb000
buffer_load_dwordx4 v12, s[48:51], s78 offen nt lds
ds_read_b128 a[8:11], v200 offset:50176
v_mfma_f32_16x16x32_bf16 v[36:39], a[20:23], v[184:187], v[36:39]
s_waitcnt lgkmcnt(3)
ds_read_b128 a[12:15], v200 offset:50688
buffer_load_dwordx4 v13, s[48:51], s78 offen offset:1024 nt lds
v_max3_f32 v236, v236, v167, v128
v_max3_f32 v236, v236, v129, v130
v_max3_f32 v236, v236, v131, v132
v_max3_f32 v236, v236, v133, v134
v_mfma_f32_16x16x32_bf16 v[48:51], a[24:27], v[184:187], v[48:51]
s_waitcnt vmcnt(22)
buffer_load_dwordx4 v14, s[48:51], s78 offen offset:2048 nt lds
ds_read_b128 a[16:19], v200 offset:53248
v_max_f32_e32 v248, v236, v135
v_cmp_lt_f32_e32 vcc, s70, v248
)"
R"(
v_mfma_f32_16x16x32_bf16 v[52:55], a[28:31], v[184:187], v[52:55]
s_nop 0
s_cbranch_vccnz .Lk_bra1
.Lk_bba1:
ds_read_b128 a[20:23], v200 offset:53760
buffer_load_dwordx4 v15, s[48:51], s78 offen offset:3072 nt lds
s_waitcnt lgkmcnt(4)
v_mfma_f32_16x16x32_bf16 v[144:147], a[0:3], a[32:35], v[192:195]
ds_read_b128 a[24:27], v200 offset:54272
v_exp_f32_e32 v160, v160
v_exp_f32_e32 v161, v161
v_add_f32_e32 v242, v242, v160
v_add_f32_e32 v242, v242, v161
v_cvt_pk_bf16_f32 v176, v160, v161
v_mfma_f32_16x16x32_bf16 v[144:147], a[4:7], a[40:43], v[144:147]
s_waitcnt lgkmcnt(3)
ds_read_b128 a[28:31], v200 offset:54784
v_exp_f32_e32 v162, v162
v_exp_f32_e32 v163, v163
v_add_f32_e32 v242, v242, v162
v_add_f32_e32 v242, v242, v163
v_mfma_f32_16x16x32_bf16 v[144:147], a[8:11], a[48:51], v[144:147]
ds_read_b128 a[0:3], v200 offset:57344
v_cvt_pk_bf16_f32 v177, v162, v163
v_exp_f32_e32 v164, v164
v_exp_f32_e32 v165, v165
v_add_f32_e32 v242, v242, v164
v_mfma_f32_16x16x32_bf16 v[144:147], a[12:15], a[56:59], v[144:147]
s_waitcnt vmcnt(22)
s_mov_b32 m0, 0x24000
s_mov_b32 s78, 0x10000
buffer_load_dwordx4 v8, s[44:47], s78 offen nt lds
ds_read_b128 a[4:7], v200 offset:57856
s_waitcnt lgkmcnt(4)
v_mfma_f32_16x16x32_bf16 v[148:151], a[16:19], a[32:35], v[192:195]
ds_read_b128 a[8:11], v200 offset:58368
buffer_load_dwordx4 v9, s[44:47], s78 offen offset:1024 nt lds
v_add_f32_e32 v242, v242, v165
v_cvt_pk_bf16_f32 v178, v164, v165
v_exp_f32_e32 v166, v166
v_mfma_f32_16x16x32_bf16 v[148:151], a[20:23], a[40:43], v[148:151]
s_waitcnt vmcnt(22)
buffer_load_dwordx4 v10, s[44:47], s78 offen offset:2048 nt lds
ds_read_b128 a[12:15], v200 offset:58880
v_add_f32_e32 v242, v242, v166
s_waitcnt lgkmcnt(4)
v_mfma_f32_16x16x32_bf16 v[148:151], a[24:27], a[48:51], v[148:151]
s_cbranch_vccnz .Lk_brb1
.Lk_bbb1:
ds_read_b128 a[16:19], v200 offset:61440
buffer_load_dwordx4 v11, s[44:47], s78 offen offset:3072 nt lds
v_exp_f32_e32 v167, v167
v_mfma_f32_16x16x32_bf16 v[148:151], a[28:31], a[56:59], v[148:151]
s_waitcnt lgkmcnt(3)
ds_read_b128 a[20:23], v200 offset:61952
v_add_f32_e32 v242, v242, v167
v_cvt_pk_bf16_f32 v179, v166, v167
v_exp_f32_e32 v128, v128
v_exp_f32_e32 v129, v129
v_mfma_f32_16x16x32_bf16 v[160:163], a[0:3], a[32:35], v[192:195]
s_waitcnt vmcnt(22)
s_mov_b32 m0, 0x25000
s_mov_b32 s78, 0x11000
buffer_load_dwordx4 v8, s[44:47], s78 offen nt lds
ds_read_b128 a[24:27], v200 offset:62464
v_mfma_f32_16x16x32_bf16 v[160:163], a[4:7], a[40:43], v[160:163]
buffer_load_dwordx4 v9, s[44:47], s78 offen offset:1024 nt lds
s_waitcnt vmcnt(38)
s_barrier
ds_read_b128 a[28:31], v200 offset:62976
s_waitcnt lgkmcnt(4)
v_mfma_f32_16x16x32_bf16 v[160:163], a[8:11], a[48:51], v[160:163]
s_waitcnt vmcnt(22)
buffer_load_dwordx4 v10, s[44:47], s78 offen offset:2048 nt lds
ds_read_b64_tr_b16 a[0:1], v211 offset:16384
ds_read_b64_tr_b16 a[2:3], v211 offset:16640
v_cvt_pk_bf16_f32 v184, v128, v129
v_add_f32_e32 v242, v242, v128
v_mfma_f32_16x16x32_bf16 v[160:163], a[12:15], a[56:59], v[160:163]
s_waitcnt lgkmcnt(4)
ds_read_b64_tr_b16 a[4:5], v212 offset:16384
ds_read_b64_tr_b16 a[6:7], v212 offset:16640
buffer_load_dwordx4 v11, s[44:47], s78 offen offset:3072 nt lds
v_exp_f32_e32 v130, v130
v_mfma_f32_16x16x32_bf16 v[164:167], a[16:19], a[32:35], v[192:195]
ds_read_b64_tr_b16 a[8:9], v211 offset:16896
ds_read_b64_tr_b16 a[10:11], v211 offset:17152
v_exp_f32_e32 v131, v131
v_exp_f32_e32 v132, v132
v_cvt_pk_bf16_f32 v185, v130, v131
v_mfma_f32_16x16x32_bf16 v[164:167], a[20:23], a[40:43], v[164:167]
s_waitcnt vmcnt(22)
s_mov_b32 m0, 0x26000
s_mov_b32 s78, 0x12000
buffer_load_dwordx4 v8, s[44:47], s78 offen nt lds
ds_read_b64_tr_b16 a[12:13], v212 offset:16896
ds_read_b64_tr_b16 a[14:15], v212 offset:17152
s_waitcnt lgkmcnt(8)
v_mfma_f32_16x16x32_bf16 v[164:167], a[24:27], a[48:51], v[164:167]
ds_read_b64_tr_b16 a[16:17], v211 offset:17408
ds_read_b64_tr_b16 a[18:19], v211 offset:17664
buffer_load_dwordx4 v9, s[44:47], s78 offen offset:1024 nt lds
v_exp_f32_e32 v133, v133
v_mfma_f32_16x16x32_bf16 v[164:167], a[28:31], a[56:59], v[164:167]
s_waitcnt vmcnt(22)
buffer_load_dwordx4 v10, s[44:47], s78 offen offset:2048 nt lds
ds_read_b64_tr_b16 a[20:21], v212 offset:17408
ds_read_b64_tr_b16 a[22:23], v212 offset:17664
v_cvt_pk_bf16_f32 v186, v132, v133
v_add_f32_e32 v242, v242, v129
s_waitcnt lgkmcnt(8)
v_mfma_f32_16x16x32_bf16 v[64:67], a[0:3], a[160:163], v[64:67]
ds_read_b64_tr_b16 a[24:25], v211 offset:17920
ds_read_b64_tr_b16 a[26:27], v211 offset:18176
buffer_load_dwordx4 v11, s[44:47], s78 offen offset:3072 nt lds
v_exp_f32_e32 v134, v134
v_mfma_f32_16x16x32_bf16 v[68:71], a[4:7], a[160:163], v[68:71]
s_waitcnt lgkmcnt(6)
ds_read_b64_tr_b16 a[28:29], v212 offset:17920
ds_read_b64_tr_b16 a[30:31], v212 offset:18176
v_exp_f32_e32 v135, v135
v_add_f32_e32 v242, v242, v130
v_cvt_pk_bf16_f32 v187, v134, v135
v_add_f32_e32 v242, v242, v131
v_mfma_f32_16x16x32_bf16 v[80:83], a[8:11], a[160:163], v[80:83]
s_waitcnt vmcnt(22)
s_mov_b32 m0, 0x27000
s_mov_b32 s78, 0x13000
buffer_load_dwordx4 v8, s[44:47], s78 offen nt lds
ds_read_b64_tr_b16 a[0:1], v211 offset:24576
ds_read_b64_tr_b16 a[2:3], v211 offset:24832
v_mfma_f32_16x16x32_bf16 v[84:87], a[12:15], a[160:163], v[84:87]
s_waitcnt lgkmcnt(6)
ds_read_b64_tr_b16 a[4:5], v212 offset:24576
ds_read_b64_tr_b16 a[6:7], v212 offset:24832
buffer_load_dwordx4 v9, s[44:47], s78 offen offset:1024 nt lds
v_add_f32_e32 v242, v242, v132
v_add_f32_e32 v242, v242, v133
v_mfma_f32_16x16x32_bf16 v[96:99], a[16:19], a[160:163], v[96:99]
s_waitcnt vmcnt(22)
buffer_load_dwordx4 v10, s[44:47], s78 offen offset:2048 nt lds
ds_read_b64_tr_b16 a[8:9], v211 offset:25088
ds_read_b64_tr_b16 a[10:11], v211 offset:25344
v_add_f32_e32 v242, v242, v134
v_add_f32_e32 v242, v242, v135
v_mfma_f32_16x16x32_bf16 v[100:103], a[20:23], a[160:163], v[100:103]
s_waitcnt lgkmcnt(6)
ds_read_b64_tr_b16 a[12:13], v212 offset:25088
ds_read_b64_tr_b16 a[14:15], v212 offset:25344
buffer_load_dwordx4 v11, s[44:47], s78 offen offset:3072 nt lds
v_max3_f32 v236, v144, v145, v146
v_max3_f32 v236, v236, v147, v148
v_mfma_f32_16x16x32_bf16 v[112:115], a[24:27], a[160:163], v[112:115]
ds_read_b64_tr_b16 a[16:17], v211 offset:25600
ds_read_b64_tr_b16 a[18:19], v211 offset:25856
v_max3_f32 v236, v236, v149, v150
v_max3_f32 v236, v236, v151, v160
v_max3_f32 v236, v236, v161, v162
v_max3_f32 v236, v236, v163, v164
v_max3_f32 v236, v236, v165, v166
v_mfma_f32_16x16x32_bf16 v[116:119], a[28:31], a[160:163], v[116:119]
s_mov_b32 m0, 0x20000
s_add_u32 s78, s85, 0
buffer_load_dwordx4 v24, s[40:43], s78 offen lds
ds_read_b64_tr_b16 a[20:21], v212 offset:25600
ds_read_b64_tr_b16 a[22:23], v212 offset:25856
buffer_load_dwordx4 v25, s[40:43], s78 offen offset:1024 lds
s_waitcnt lgkmcnt(8)
v_mfma_f32_16x16x32_bf16 v[64:67], a[0:3], a[168:171], v[64:67]
ds_read_b64_tr_b16 a[24:25], v211 offset:26112
ds_read_b64_tr_b16 a[26:27], v211 offset:26368
buffer_load_dwordx4 v26, s[40:43], s78 offen offset:2048 lds
v_max_f32_e32 v248, v236, v167
v_cmp_lt_f32_e32 vcc, s70, v248
v_mfma_f32_16x16x32_bf16 v[68:71], a[4:7], a[168:171], v[68:71]
buffer_load_dwordx4 v27, s[40:43], s78 offen offset:3072 lds
s_mov_b32 m0, 0x21000
s_add_u32 s78, s85, 0x20000
buffer_load_dwordx4 v28, s[40:43], s78 offen lds
s_waitcnt vmcnt(63)
s_barrier
ds_read_b64_tr_b16 a[28:29], v212 offset:26112
ds_read_b64_tr_b16 a[30:31], v212 offset:26368
s_waitcnt lgkmcnt(8)
v_mfma_f32_16x16x32_bf16 v[80:83], a[8:11], a[168:171], v[80:83]
s_cbranch_vccnz .Lk_bra2
.Lk_bba2:
ds_read_b64_tr_b16 a[0:1], v205
ds_read_b64_tr_b16 a[2:3], v205 offset:4096
buffer_load_dwordx4 v29, s[40:43], s78 offen offset:1024 lds
v_mfma_f32_16x16x32_bf16 v[84:87], a[12:15], a[168:171], v[84:87]
s_waitcnt lgkmcnt(6)
ds_read_b64_tr_b16 a[4:5], v206
ds_read_b64_tr_b16 a[6:7], v206 offset:4096
buffer_load_dwordx4 v30, s[40:43], s78 offen offset:2048 lds
buffer_load_dwordx4 v31, s[40:43], s78 offen offset:3072 lds
v_mfma_f32_16x16x32_bf16 v[96:99], a[16:19], a[168:171], v[96:99]
s_mov_b32 m0, 0x22000
s_add_u32 s78, s85, 0x40000
buffer_load_dwordx4 v24, s[40:43], s78 offen lds
)"
R"(
ds_read_b64_tr_b16 a[8:9], v205 offset:512
ds_read_b64_tr_b16 a[10:11], v205 offset:4608
v_mfma_f32_16x16x32_bf16 v[100:103], a[20:23], a[168:171], v[100:103]
s_waitcnt lgkmcnt(6)
ds_read_b64_tr_b16 a[12:13], v206 offset:512
ds_read_b64_tr_b16 a[14:15], v206 offset:4608
buffer_load_dwordx4 v25, s[40:43], s78 offen offset:1024 lds
buffer_load_dwordx4 v26, s[40:43], s78 offen offset:2048 lds
v_mfma_f32_16x16x32_bf16 v[112:115], a[24:27], a[168:171], v[112:115]
ds_read_b64_tr_b16 a[16:17], v205 offset:1024
ds_read_b64_tr_b16 a[18:19], v205 offset:5120
buffer_load_dwordx4 v27, s[40:43], s78 offen offset:3072 lds
v_exp_f32_e32 v144, v144
v_exp_f32_e32 v145, v145
v_mfma_f32_16x16x32_bf16 v[116:119], a[28:31], a[168:171], v[116:119]
s_mov_b32 m0, 0x23000
s_add_u32 s78, s85, 0x60000
buffer_load_dwordx4 v28, s[40:43], s78 offen lds
ds_read_b64_tr_b16 a[20:21], v206 offset:1024
ds_read_b64_tr_b16 a[22:23], v206 offset:5120
s_waitcnt lgkmcnt(8)
v_mfma_f32_16x16x32_bf16 v[0:3], a[0:3], v[176:179], v[0:3]
buffer_load_dwordx4 v29, s[40:43], s78 offen offset:1024 lds
ds_read_b64_tr_b16 a[24:25], v205 offset:1536
ds_read_b64_tr_b16 a[26:27], v205 offset:5632
buffer_load_dwordx4 v30, s[40:43], s78 offen offset:2048 lds
v_mfma_f32_16x16x32_bf16 v[4:7], a[4:7], v[176:179], v[4:7]
s_waitcnt lgkmcnt(6)
ds_read_b64_tr_b16 a[28:29], v206 offset:1536
ds_read_b64_tr_b16 a[30:31], v206 offset:5632
buffer_load_dwordx4 v31, s[40:43], s78 offen offset:3072 lds
s_add_u32 s85, s85, 0x80000
s_and_b32 s85, s85, 0x3fffff
v_add_f32_e32 v242, v242, v144
v_mfma_f32_16x16x32_bf16 v[16:19], a[8:11], v[176:179], v[16:19]
s_waitcnt vmcnt(38)
s_mov_b32 m0, 0x4000
s_mov_b32 s78, 0xc000
buffer_load_dwordx4 v12, s[48:51], s78 offen nt lds
ds_read_b64_tr_b16 a[0:1], v205 offset:8192
ds_read_b64_tr_b16 a[2:3], v205 offset:12288
v_mfma_f32_16x16x32_bf16 v[20:23], a[12:15], v[176:179], v[20:23]
buffer_load_dwordx4 v13, s[48:51], s78 offen offset:1024 nt lds
s_waitcnt vmcnt(38)
buffer_load_dwordx4 v14, s[48:51], s78 offen offset:2048 nt lds
ds_read_b64_tr_b16 a[4:5], v206 offset:8192
ds_read_b64_tr_b16 a[6:7], v206 offset:12288
s_waitcnt lgkmcnt(8)
v_mfma_f32_16x16x32_bf16 v[32:35], a[16:19], v[176:179], v[32:35]
ds_read_b64_tr_b16 a[8:9], v205 offset:8704
ds_read_b64_tr_b16 a[10:11], v205 offset:12800
buffer_load_dwordx4 v15, s[48:51], s78 offen offset:3072 nt lds
v_add_f32_e32 v242, v242, v145
v_mfma_f32_16x16x32_bf16 v[36:39], a[20:23], v[176:179], v[36:39]
s_waitcnt vmcnt(38)
s_mov_b32 m0, 0x5000
s_mov_b32 s78, 0xd000
buffer_load_dwordx4 v12, s[48:51], s78 offen nt lds
ds_read_b64_tr_b16 a[12:13], v206 offset:8704
ds_read_b64_tr_b16 a[14:15], v206 offset:12800
s_waitcnt lgkmcnt(8)
v_mfma_f32_16x16x32_bf16 v[48:51], a[24:27], v[176:179], v[48:51]
buffer_load_dwordx4 v13, s[48:51], s78 offen offset:1024 nt lds
s_waitcnt vmcnt(38)
buffer_load_dwordx4 v14, s[48:51], s78 offen offset:2048 nt lds
ds_read_b64_tr_b16 a[16:17], v205 offset:9216
ds_read_b64_tr_b16 a[18:19], v205 offset:13312
v_mfma_f32_16x16x32_bf16 v[52:55], a[28:31], v[176:179], v[52:55]
s_waitcnt lgkmcnt(6)
ds_read_b64_tr_b16 a[20:21], v206 offset:9216
ds_read_b64_tr_b16 a[22:23], v206 offset:13312
buffer_load_dwordx4 v15, s[48:51], s78 offen offset:3072 nt lds
v_cvt_pk_bf16_f32 v176, v144, v145
v_exp_f32_e32 v146, v146
s_nop 0
v_add_f32_e32 v242, v242, v146
v_mfma_f32_16x16x32_bf16 v[0:3], a[0:3], v[184:187], v[0:3]
s_waitcnt vmcnt(38)
s_mov_b32 m0, 0x6000
s_mov_b32 s78, 0xe000
buffer_load_dwordx4 v12, s[48:51], s78 offen nt lds
ds_read_b64_tr_b16 a[24:25], v205 offset:9728
ds_read_b64_tr_b16 a[26:27], v205 offset:13824
buffer_load_dwordx4 v13, s[48:51], s78 offen offset:1024 nt lds
v_mfma_f32_16x16x32_bf16 v[4:7], a[4:7], v[184:187], v[4:7]
s_waitcnt vmcnt(38)
buffer_load_dwordx4 v14, s[48:51], s78 offen offset:2048 nt lds
s_waitcnt vmcnt(59)
s_barrier
ds_read_b64_tr_b16 a[28:29], v206 offset:9728
ds_read_b64_tr_b16 a[30:31], v206 offset:13824
s_waitcnt lgkmcnt(8)
v_mfma_f32_16x16x32_bf16 v[16:19], a[8:11], v[184:187], v[16:19]
ds_read_b128 a[0:3], v201 offset:32768
buffer_load_dwordx4 v15, s[48:51], s78 offen offset:3072 nt lds
v_exp_f32_e32 v147, v147
v_mfma_f32_16x16x32_bf16 v[20:23], a[12:15], v[184:187], v[20:23]
s_waitcnt vmcnt(38)
s_mov_b32 m0, 0x7000
s_mov_b32 s78, 0xf000
buffer_load_dwordx4 v12, s[48:51], s78 offen nt lds
ds_read_b128 a[4:7], v201 offset:33280
s_waitcnt lgkmcnt(6)
v_mfma_f32_16x16x32_bf16 v[32:35], a[16:19], v[184:187], v[32:35]
buffer_load_dwordx4 v13, s[48:51], s78 offen offset:1024 nt lds
s_waitcnt vmcnt(38)
buffer_load_dwordx4 v14, s[48:51], s78 offen offset:2048 nt lds
ds_read_b128 a[8:11], v201 offset:33792
v_mfma_f32_16x16x32_bf16 v[36:39], a[20:23], v[184:187], v[36:39]
s_waitcnt lgkmcnt(3)
ds_read_b128 a[12:15], v201 offset:34304
buffer_load_dwordx4 v15, s[48:51], s78 offen offset:3072 nt lds
v_add_f32_e32 v242, v242, v147
v_cvt_pk_bf16_f32 v177, v146, v147
v_mfma_f32_16x16x32_bf16 v[48:51], a[24:27], v[184:187], v[48:51]
s_waitcnt vmcnt(38)
s_mov_b32 m0, 0
s_mov_b32 s78, 0x14000
buffer_load_dwordx4 v8, s[44:47], s78 offen nt lds
ds_read_b128 a[16:19], v201 offset:36864
v_mfma_f32_16x16x32_bf16 v[52:55], a[28:31], v[184:187], v[52:55]
s_waitcnt lgkmcnt(3)
ds_read_b128 a[20:23], v201 offset:37376
buffer_load_dwordx4 v9, s[44:47], s78 offen offset:1024 nt lds
v_exp_f32_e32 v148, v148
v_exp_f32_e32 v149, v149
v_mfma_f32_16x16x32_bf16 v[128:131], a[0:3], a[32:35], v[192:195]
s_waitcnt vmcnt(38)
buffer_load_dwordx4 v10, s[44:47], s78 offen offset:2048 nt lds
ds_read_b128 a[24:27], v201 offset:37888
v_add_f32_e32 v242, v242, v148
v_add_f32_e32 v242, v242, v149
v_mfma_f32_16x16x32_bf16 v[128:131], a[4:7], a[40:43], v[128:131]
s_waitcnt lgkmcnt(3)
ds_read_b128 a[28:31], v201 offset:38400
buffer_load_dwordx4 v11, s[44:47], s78 offen offset:3072 nt lds
v_cvt_pk_bf16_f32 v178, v148, v149
v_exp_f32_e32 v150, v150
v_mfma_f32_16x16x32_bf16 v[128:131], a[8:11], a[48:51], v[128:131]
s_cbranch_vccnz .Lk_brb2
.Lk_bbb2:
ds_read_b128 a[0:3], v201 offset:40960
v_add_f32_e32 v242, v242, v150
v_exp_f32_e32 v151, v151
v_mfma_f32_16x16x32_bf16 v[128:131], a[12:15], a[56:59], v[128:131]
s_waitcnt vmcnt(38)
s_mov_b32 m0, 0x1000
s_mov_b32 s78, 0x15000
buffer_load_dwordx4 v8, s[44:47], s78 offen nt lds
ds_read_b128 a[4:7], v201 offset:41472
s_waitcnt lgkmcnt(4)
v_mfma_f32_16x16x32_bf16 v[132:135], a[16:19], a[32:35], v[192:195]
ds_read_b128 a[8:11], v201 offset:41984
buffer_load_dwordx4 v9, s[44:47], s78 offen offset:1024 nt lds
v_add_f32_e32 v242, v242, v151
v_cvt_pk_bf16_f32 v179, v150, v151
v_mfma_f32_16x16x32_bf16 v[132:135], a[20:23], a[40:43], v[132:135]
s_waitcnt vmcnt(38)
buffer_load_dwordx4 v10, s[44:47], s78 offen offset:2048 nt lds
ds_read_b128 a[12:15], v201 offset:42496
v_exp_f32_e32 v160, v160
s_nop 0
v_add_f32_e32 v242, v242, v160
s_waitcnt lgkmcnt(4)
v_mfma_f32_16x16x32_bf16 v[132:135], a[24:27], a[48:51], v[132:135]
ds_read_b128 a[16:19], v201 offset:45056
buffer_load_dwordx4 v11, s[44:47], s78 offen offset:3072 nt lds
v_exp_f32_e32 v161, v161
v_max3_f32 v236, v128, v129, v130
v_mfma_f32_16x16x32_bf16 v[132:135], a[28:31], a[56:59], v[132:135]
s_waitcnt lgkmcnt(3)
ds_read_b128 a[20:23], v201 offset:45568
v_cvt_pk_bf16_f32 v184, v160, v161
v_exp_f32_e32 v162, v162
v_exp_f32_e32 v163, v163
v_add_f32_e32 v242, v242, v161
v_mfma_f32_16x16x32_bf16 v[144:147], a[0:3], a[32:35], v[192:195]
s_waitcnt vmcnt(22)
s_mov_b32 m0, 0x2000
s_mov_b32 s78, 0x16000
buffer_load_dwordx4 v8, s[44:47], s78 offen nt lds
ds_read_b128 a[24:27], v201 offset:46080
v_mfma_f32_16x16x32_bf16 v[144:147], a[4:7], a[40:43], v[144:147]
buffer_load_dwordx4 v9, s[44:47], s78 offen offset:1024 nt lds
s_waitcnt vmcnt(58)
s_barrier
ds_read_b128 a[28:31], v201 offset:46592
s_waitcnt lgkmcnt(4)
v_mfma_f32_16x16x32_bf16 v[144:147], a[8:11], a[48:51], v[144:147]
s_waitcnt vmcnt(22)
buffer_load_dwordx4 v10, s[44:47], s78 offen offset:2048 nt lds
ds_read_b64_tr_b16 a[0:1], v205 offset:49152
ds_read_b64_tr_b16 a[2:3], v205 offset:53248
v_cvt_pk_bf16_f32 v185, v162, v163
v_mfma_f32_16x16x32_bf16 v[144:147], a[12:15], a[56:59], v[144:147]
s_waitcnt lgkmcnt(4)
ds_read_b64_tr_b16 a[4:5], v206 offset:49152
ds_read_b64_tr_b16 a[6:7], v206 offset:53248
buffer_load_dwordx4 v11, s[44:47], s78 offen offset:3072 nt lds
v_exp_f32_e32 v164, v164
)"
R"(
v_mfma_f32_16x16x32_bf16 v[148:151], a[16:19], a[32:35], v[192:195]
ds_read_b64_tr_b16 a[8:9], v205 offset:49664
ds_read_b64_tr_b16 a[10:11], v205 offset:53760
v_exp_f32_e32 v165, v165
v_exp_f32_e32 v166, v166
v_cvt_pk_bf16_f32 v186, v164, v165
v_mfma_f32_16x16x32_bf16 v[148:151], a[20:23], a[40:43], v[148:151]
s_waitcnt vmcnt(22)
s_mov_b32 m0, 0x3000
s_mov_b32 s78, 0x17000
buffer_load_dwordx4 v8, s[44:47], s78 offen nt lds
ds_read_b64_tr_b16 a[12:13], v206 offset:49664
ds_read_b64_tr_b16 a[14:15], v206 offset:53760
s_waitcnt lgkmcnt(8)
v_mfma_f32_16x16x32_bf16 v[148:151], a[24:27], a[48:51], v[148:151]
ds_read_b64_tr_b16 a[16:17], v205 offset:50176
ds_read_b64_tr_b16 a[18:19], v205 offset:54272
buffer_load_dwordx4 v9, s[44:47], s78 offen offset:1024 nt lds
v_add_f32_e32 v242, v242, v162
v_mfma_f32_16x16x32_bf16 v[148:151], a[28:31], a[56:59], v[148:151]
s_waitcnt vmcnt(22)
buffer_load_dwordx4 v10, s[44:47], s78 offen offset:2048 nt lds
ds_read_b64_tr_b16 a[20:21], v206 offset:50176
ds_read_b64_tr_b16 a[22:23], v206 offset:54272
v_exp_f32_e32 v167, v167
s_waitcnt lgkmcnt(8)
v_mfma_f32_16x16x32_bf16 v[0:3], a[0:3], v[176:179], v[0:3]
ds_read_b64_tr_b16 a[24:25], v205 offset:50688
ds_read_b64_tr_b16 a[26:27], v205 offset:54784
buffer_load_dwordx4 v11, s[44:47], s78 offen offset:3072 nt lds
v_cvt_pk_bf16_f32 v187, v166, v167
v_mfma_f32_16x16x32_bf16 v[4:7], a[4:7], v[176:179], v[4:7]
s_waitcnt lgkmcnt(6)
ds_read_b64_tr_b16 a[28:29], v206 offset:50688
ds_read_b64_tr_b16 a[30:31], v206 offset:54784
v_add_f32_e32 v242, v242, v163
v_add_f32_e32 v242, v242, v164
v_add_f32_e32 v242, v242, v165
v_add_f32_e32 v242, v242, v166
v_add_f32_e32 v242, v242, v167
v_mfma_f32_16x16x32_bf16 v[16:19], a[8:11], v[176:179], v[16:19]
ds_read_b64_tr_b16 a[0:1], v205 offset:57344
ds_read_b64_tr_b16 a[2:3], v205 offset:61440
v_max3_f32 v236, v236, v131, v132
v_max3_f32 v236, v236, v133, v134
v_max3_f32 v236, v236, v135, v144
v_max3_f32 v236, v236, v145, v146
v_max3_f32 v236, v236, v147, v148
v_mfma_f32_16x16x32_bf16 v[20:23], a[12:15], v[176:179], v[20:23]
s_waitcnt vmcnt(22)
s_mov_b32 m0, 0x8000
s_mov_b32 s78, 0x10000
buffer_load_dwordx4 v12, s[48:51], s78 offen nt lds
ds_read_b64_tr_b16 a[4:5], v206 offset:57344
ds_read_b64_tr_b16 a[6:7], v206 offset:61440
v_max3_f32 v236, v236, v149, v150
s_waitcnt lgkmcnt(8)
v_mfma_f32_16x16x32_bf16 v[32:35], a[16:19], v[176:179], v[32:35]
buffer_load_dwordx4 v13, s[48:51], s78 offen offset:1024 nt lds
s_waitcnt vmcnt(22)
buffer_load_dwordx4 v14, s[48:51], s78 offen offset:2048 nt lds
ds_read_b64_tr_b16 a[8:9], v205 offset:57856
ds_read_b64_tr_b16 a[10:11], v205 offset:61952
v_mfma_f32_16x16x32_bf16 v[36:39], a[20:23], v[176:179], v[36:39]
ds_read_b64_tr_b16 a[12:13], v206 offset:57856
ds_read_b64_tr_b16 a[14:15], v206 offset:61952
buffer_load_dwordx4 v15, s[48:51], s78 offen offset:3072 nt lds
s_waitcnt vmcnt(22)
s_mov_b32 m0, 0x9000
s_mov_b32 s78, 0x11000
buffer_load_dwordx4 v12, s[48:51], s78 offen nt lds
s_waitcnt lgkmcnt(8)
v_mfma_f32_16x16x32_bf16 v[48:51], a[24:27], v[176:179], v[48:51]
ds_read_b64_tr_b16 a[16:17], v205 offset:58368
ds_read_b64_tr_b16 a[18:19], v205 offset:62464
buffer_load_dwordx4 v13, s[48:51], s78 offen offset:1024 nt lds
v_max_f32_e32 v248, v236, v151
v_cmp_lt_f32_e32 vcc, s70, v248
v_mfma_f32_16x16x32_bf16 v[52:55], a[28:31], v[176:179], v[52:55]
s_waitcnt vmcnt(22)
buffer_load_dwordx4 v14, s[48:51], s78 offen offset:2048 nt lds
ds_read_b64_tr_b16 a[20:21], v206 offset:58368
ds_read_b64_tr_b16 a[22:23], v206 offset:62464
buffer_load_dwordx4 v15, s[48:51], s78 offen offset:3072 nt lds
s_waitcnt lgkmcnt(8)
v_mfma_f32_16x16x32_bf16 v[0:3], a[0:3], v[184:187], v[0:3]
s_waitcnt vmcnt(22)
s_mov_b32 m0, 0xa000
s_mov_b32 s78, 0x12000
buffer_load_dwordx4 v12, s[48:51], s78 offen nt lds
s_cbranch_vccnz .Lk_bra3
.Lk_bba3:
ds_read_b64_tr_b16 a[24:25], v205 offset:58880
ds_read_b64_tr_b16 a[26:27], v205 offset:62976
v_mfma_f32_16x16x32_bf16 v[4:7], a[4:7], v[184:187], v[4:7]
buffer_load_dwordx4 v13, s[48:51], s78 offen offset:1024 nt lds
s_waitcnt vmcnt(22)
buffer_load_dwordx4 v14, s[48:51], s78 offen offset:2048 nt lds
s_waitcnt vmcnt(43)
s_barrier
ds_read_b64_tr_b16 a[28:29], v206 offset:58880
ds_read_b64_tr_b16 a[30:31], v206 offset:62976
s_waitcnt lgkmcnt(8)
v_mfma_f32_16x16x32_bf16 v[16:19], a[8:11], v[184:187], v[16:19]
ds_read_b64_tr_b16 a[0:1], v213
ds_read_b64_tr_b16 a[2:3], v213 offset:256
buffer_load_dwordx4 v15, s[48:51], s78 offen offset:3072 nt lds
v_exp_f32_e32 v128, v128
v_mfma_f32_16x16x32_bf16 v[20:23], a[12:15], v[184:187], v[20:23]
s_waitcnt vmcnt(22)
s_mov_b32 m0, 0xb000
s_mov_b32 s78, 0x13000
buffer_load_dwordx4 v12, s[48:51], s78 offen nt lds
ds_read_b64_tr_b16 a[4:5], v214
ds_read_b64_tr_b16 a[6:7], v214 offset:256
s_waitcnt lgkmcnt(8)
v_mfma_f32_16x16x32_bf16 v[32:35], a[16:19], v[184:187], v[32:35]
buffer_load_dwordx4 v13, s[48:51], s78 offen offset:1024 nt lds
ds_read_b64_tr_b16 a[8:9], v213 offset:512
ds_read_b64_tr_b16 a[10:11], v213 offset:768
v_add_f32_e32 v242, v242, v128
v_mfma_f32_16x16x32_bf16 v[36:39], a[20:23], v[184:187], v[36:39]
s_waitcnt vmcnt(22)
buffer_load_dwordx4 v14, s[48:51], s78 offen offset:2048 nt lds
ds_read_b64_tr_b16 a[12:13], v214 offset:512
ds_read_b64_tr_b16 a[14:15], v214 offset:768
buffer_load_dwordx4 v15, s[48:51], s78 offen offset:3072 nt lds
s_waitcnt lgkmcnt(8)
v_mfma_f32_16x16x32_bf16 v[48:51], a[24:27], v[184:187], v[48:51]
s_mov_b32 m0, 0xc000
s_add_u32 s78, s85, 0
buffer_load_dwordx4 v24, s[40:43], s78 offen lds
ds_read_b64_tr_b16 a[16:17], v213 offset:1024
ds_read_b64_tr_b16 a[18:19], v213 offset:1280
v_mfma_f32_16x16x32_bf16 v[52:55], a[28:31], v[184:187], v[52:55]
s_waitcnt lgkmcnt(6)
buffer_load_dwordx4 v25, s[40:43], s78 offen offset:1024 lds
ds_read_b64_tr_b16 a[20:21], v214 offset:1024
ds_read_b64_tr_b16 a[22:23], v214 offset:1280
buffer_load_dwordx4 v26, s[40:43], s78 offen offset:2048 lds
v_mfma_f32_16x16x32_bf16 v[64:67], a[0:3], a[176:179], v[64:67]
ds_read_b64_tr_b16 a[24:25], v213 offset:1536
ds_read_b64_tr_b16 a[26:27], v213 offset:1792
buffer_load_dwordx4 v27, s[40:43], s78 offen offset:3072 lds
v_exp_f32_e32 v129, v129
v_mfma_f32_16x16x32_bf16 v[68:71], a[4:7], a[176:179], v[68:71]
s_mov_b32 m0, 0xd000
s_add_u32 s78, s85, 0x20000
buffer_load_dwordx4 v28, s[40:43], s78 offen lds
ds_read_b64_tr_b16 a[28:29], v214 offset:1536
ds_read_b64_tr_b16 a[30:31], v214 offset:1792
s_waitcnt lgkmcnt(8)
v_mfma_f32_16x16x32_bf16 v[80:83], a[8:11], a[176:179], v[80:83]
buffer_load_dwordx4 v29, s[40:43], s78 offen offset:1024 lds
ds_read_b64_tr_b16 a[0:1], v213 offset:8192
ds_read_b64_tr_b16 a[2:3], v213 offset:8448
buffer_load_dwordx4 v30, s[40:43], s78 offen offset:2048 lds
v_mfma_f32_16x16x32_bf16 v[84:87], a[12:15], a[176:179], v[84:87]
s_waitcnt lgkmcnt(6)
buffer_load_dwordx4 v31, s[40:43], s78 offen offset:3072 lds
s_mov_b32 m0, 0xe000
s_add_u32 s78, s85, 0x40000
buffer_load_dwordx4 v24, s[40:43], s78 offen lds
ds_read_b64_tr_b16 a[4:5], v214 offset:8192
ds_read_b64_tr_b16 a[6:7], v214 offset:8448
v_mfma_f32_16x16x32_bf16 v[96:99], a[16:19], a[176:179], v[96:99]
ds_read_b64_tr_b16 a[8:9], v213 offset:8704
ds_read_b64_tr_b16 a[10:11], v213 offset:8960
buffer_load_dwordx4 v25, s[40:43], s78 offen offset:1024 lds
buffer_load_dwordx4 v26, s[40:43], s78 offen offset:2048 lds
v_mfma_f32_16x16x32_bf16 v[100:103], a[20:23], a[176:179], v[100:103]
s_waitcnt lgkmcnt(6)
ds_read_b64_tr_b16 a[12:13], v214 offset:8704
ds_read_b64_tr_b16 a[14:15], v214 offset:8960
buffer_load_dwordx4 v27, s[40:43], s78 offen offset:3072 lds
v_add_f32_e32 v242, v242, v129
v_mfma_f32_16x16x32_bf16 v[112:115], a[24:27], a[176:179], v[112:115]
s_mov_b32 m0, 0xf000
s_add_u32 s78, s85, 0x60000
buffer_load_dwordx4 v28, s[40:43], s78 offen lds
ds_read_b64_tr_b16 a[16:17], v213 offset:9216
ds_read_b64_tr_b16 a[18:19], v213 offset:9472
buffer_load_dwordx4 v29, s[40:43], s78 offen offset:1024 lds
v_mfma_f32_16x16x32_bf16 v[116:119], a[28:31], a[176:179], v[116:119]
s_waitcnt lgkmcnt(6)
ds_read_b64_tr_b16 a[20:21], v214 offset:9216
ds_read_b64_tr_b16 a[22:23], v214 offset:9472
buffer_load_dwordx4 v30, s[40:43], s78 offen offset:2048 lds
v_cvt_pk_bf16_f32 v176, v128, v129
v_mfma_f32_16x16x32_bf16 v[64:67], a[0:3], a[184:187], v[64:67]
ds_read_b64_tr_b16 a[24:25], v213 offset:9728
ds_read_b64_tr_b16 a[26:27], v213 offset:9984
buffer_load_dwordx4 v31, s[40:43], s78 offen offset:3072 lds
s_add_u32 s85, s85, 0x80000
s_and_b32 s85, s85, 0x3fffff
v_exp_f32_e32 v130, v130
s_nop 0
v_add_f32_e32 v242, v242, v130
v_mfma_f32_16x16x32_bf16 v[68:71], a[4:7], a[184:187], v[68:71]
s_waitcnt vmcnt(38)
)"
R"(
s_mov_b32 m0, 0x14000
s_mov_b32 s78, 0x18000
buffer_load_dwordx4 v8, s[44:47], s78 offen nt lds
s_waitcnt vmcnt(63)
s_barrier
ds_read_b64_tr_b16 a[28:29], v214 offset:9728
ds_read_b64_tr_b16 a[30:31], v214 offset:9984
s_waitcnt lgkmcnt(8)
v_mfma_f32_16x16x32_bf16 v[80:83], a[8:11], a[184:187], v[80:83]
ds_read_b128 a[0:3], v202 offset:16384
buffer_load_dwordx4 v9, s[44:47], s78 offen offset:1024 nt lds
v_exp_f32_e32 v131, v131
v_mfma_f32_16x16x32_bf16 v[84:87], a[12:15], a[184:187], v[84:87]
s_waitcnt vmcnt(38)
buffer_load_dwordx4 v10, s[44:47], s78 offen offset:2048 nt lds
ds_read_b128 a[4:7], v202 offset:16896
v_add_f32_e32 v242, v242, v131
v_cvt_pk_bf16_f32 v177, v130, v131
s_waitcnt lgkmcnt(6)
v_mfma_f32_16x16x32_bf16 v[96:99], a[16:19], a[184:187], v[96:99]
ds_read_b128 a[8:11], v202 offset:17408
buffer_load_dwordx4 v11, s[44:47], s78 offen offset:3072 nt lds
v_exp_f32_e32 v132, v132
v_exp_f32_e32 v133, v133
v_mfma_f32_16x16x32_bf16 v[100:103], a[20:23], a[184:187], v[100:103]
s_waitcnt lgkmcnt(3)
ds_read_b128 a[12:15], v202 offset:17920
v_add_f32_e32 v242, v242, v132
v_add_f32_e32 v242, v242, v133
v_cvt_pk_bf16_f32 v178, v132, v133
v_exp_f32_e32 v134, v134
v_mfma_f32_16x16x32_bf16 v[112:115], a[24:27], a[184:187], v[112:115]
s_waitcnt vmcnt(38)
s_mov_b32 m0, 0x15000
s_mov_b32 s78, 0x19000
buffer_load_dwordx4 v8, s[44:47], s78 offen nt lds
ds_read_b128 a[16:19], v202 offset:20480
v_mfma_f32_16x16x32_bf16 v[116:119], a[28:31], a[184:187], v[116:119]
s_waitcnt lgkmcnt(3)
ds_read_b128 a[20:23], v202 offset:20992
buffer_load_dwordx4 v9, s[44:47], s78 offen offset:1024 nt lds
v_add_f32_e32 v242, v242, v134
v_exp_f32_e32 v135, v135
v_mfma_f32_16x16x32_bf16 v[160:163], a[0:3], a[32:35], v[192:195]
s_waitcnt vmcnt(38)
buffer_load_dwordx4 v10, s[44:47], s78 offen offset:2048 nt lds
ds_read_b128 a[24:27], v202 offset:21504
v_add_f32_e32 v242, v242, v135
v_cvt_pk_bf16_f32 v179, v134, v135
v_mfma_f32_16x16x32_bf16 v[160:163], a[4:7], a[40:43], v[160:163]
s_cbranch_vccnz .Lk_brb3
.Lk_bbb3:
ds_read_b128 a[28:31], v202 offset:22016
buffer_load_dwordx4 v11, s[44:47], s78 offen offset:3072 nt lds
s_waitcnt lgkmcnt(4)
v_mfma_f32_16x16x32_bf16 v[160:163], a[8:11], a[48:51], v[160:163]
ds_read_b128 a[0:3], v202 offset:24576
v_exp_f32_e32 v144, v144
v_exp_f32_e32 v145, v145
v_exp_f32_e32 v146, v146
v_mfma_f32_16x16x32_bf16 v[160:163], a[12:15], a[56:59], v[160:163]
s_waitcnt vmcnt(38)
s_mov_b32 m0, 0x16000
s_mov_b32 s78, 0x1a000
buffer_load_dwordx4 v8, s[44:47], s78 offen nt lds
ds_read_b128 a[4:7], v202 offset:25088
s_waitcnt lgkmcnt(4)
v_mfma_f32_16x16x32_bf16 v[164:167], a[16:19], a[32:35], v[192:195]
ds_read_b128 a[8:11], v202 offset:25600
buffer_load_dwordx4 v9, s[44:47], s78 offen offset:1024 nt lds
v_cvt_pk_bf16_f32 v184, v144, v145
v_add_f32_e32 v242, v242, v144
v_mfma_f32_16x16x32_bf16 v[164:167], a[20:23], a[40:43], v[164:167]
s_waitcnt vmcnt(38)
buffer_load_dwordx4 v10, s[44:47], s78 offen offset:2048 nt lds
ds_read_b128 a[12:15], v202 offset:26112
v_exp_f32_e32 v147, v147
s_waitcnt lgkmcnt(4)
v_mfma_f32_16x16x32_bf16 v[164:167], a[24:27], a[48:51], v[164:167]
ds_read_b128 a[16:19], v202 offset:28672
buffer_load_dwordx4 v11, s[44:47], s78 offen offset:3072 nt lds
v_cvt_pk_bf16_f32 v185, v146, v147
v_exp_f32_e32 v148, v148
v_mfma_f32_16x16x32_bf16 v[164:167], a[28:31], a[56:59], v[164:167]
s_waitcnt lgkmcnt(3)
ds_read_b128 a[20:23], v202 offset:29184
v_exp_f32_e32 v149, v149
v_exp_f32_e32 v150, v150
v_cvt_pk_bf16_f32 v186, v148, v149
v_mfma_f32_16x16x32_bf16 v[128:131], a[0:3], a[32:35], v[192:195]
s_waitcnt vmcnt(38)
s_mov_b32 m0, 0x17000
s_mov_b32 s78, 0x1b000
buffer_load_dwordx4 v8, s[44:47], s78 offen nt lds
ds_read_b128 a[24:27], v202 offset:29696
v_mfma_f32_16x16x32_bf16 v[128:131], a[4:7], a[40:43], v[128:131]
buffer_load_dwordx4 v9, s[44:47], s78 offen offset:1024 nt lds
s_waitcnt vmcnt(62)
s_barrier
ds_read_b128 a[28:31], v202 offset:30208
s_waitcnt lgkmcnt(4)
v_mfma_f32_16x16x32_bf16 v[128:131], a[8:11], a[48:51], v[128:131]
s_waitcnt vmcnt(38)
buffer_load_dwordx4 v10, s[44:47], s78 offen offset:2048 nt lds
ds_read_b64_tr_b16 a[0:1], v203 offset:16384
ds_read_b64_tr_b16 a[2:3], v203 offset:20480
v_exp_f32_e32 v151, v151
v_mfma_f32_16x16x32_bf16 v[128:131], a[12:15], a[56:59], v[128:131]
s_waitcnt lgkmcnt(4)
ds_read_b64_tr_b16 a[4:5], v204 offset:16384
ds_read_b64_tr_b16 a[6:7], v204 offset:20480
buffer_load_dwordx4 v11, s[44:47], s78 offen offset:3072 nt lds
v_cvt_pk_bf16_f32 v187, v150, v151
v_mfma_f32_16x16x32_bf16 v[132:135], a[16:19], a[32:35], v[192:195]
ds_read_b64_tr_b16 a[8:9], v203 offset:16896
ds_read_b64_tr_b16 a[10:11], v203 offset:20992
v_add_f32_e32 v242, v242, v145
v_add_f32_e32 v242, v242, v146
v_add_f32_e32 v242, v242, v147
v_add_f32_e32 v242, v242, v148
v_add_f32_e32 v242, v242, v149
v_mfma_f32_16x16x32_bf16 v[132:135], a[20:23], a[40:43], v[132:135]
s_waitcnt lgkmcnt(6)
ds_read_b64_tr_b16 a[12:13], v204 offset:16896
ds_read_b64_tr_b16 a[14:15], v204 offset:20992
v_add_f32_e32 v242, v242, v150
v_add_f32_e32 v242, v242, v151
v_max3_f32 v236, v160, v161, v162
v_max3_f32 v236, v236, v163, v164
v_mfma_f32_16x16x32_bf16 v[132:135], a[24:27], a[48:51], v[132:135]
ds_read_b64_tr_b16 a[16:17], v203 offset:17408
ds_read_b64_tr_b16 a[18:19], v203 offset:21504
v_max3_f32 v236, v236, v165, v166
v_max3_f32 v236, v236, v167, v128
v_max3_f32 v236, v236, v129, v130
v_mfma_f32_16x16x32_bf16 v[132:135], a[28:31], a[56:59], v[132:135]
s_waitcnt vmcnt(38)
s_mov_b32 m0, 0x10000
s_mov_b32 s78, 0x14000
buffer_load_dwordx4 v12, s[48:51], s78 offen nt lds
ds_read_b64_tr_b16 a[20:21], v204 offset:17408
ds_read_b64_tr_b16 a[22:23], v204 offset:21504
s_waitcnt lgkmcnt(8)
v_mfma_f32_16x16x32_bf16 v[0:3], a[0:3], v[176:179], v[0:3]
ds_read_b64_tr_b16 a[24:25], v203 offset:17920
ds_read_b64_tr_b16 a[26:27], v203 offset:22016
buffer_load_dwordx4 v13, s[48:51], s78 offen offset:1024 nt lds
v_mfma_f32_16x16x32_bf16 v[4:7], a[4:7], v[176:179], v[4:7]
s_waitcnt vmcnt(38)
buffer_load_dwordx4 v14, s[48:51], s78 offen offset:2048 nt lds
ds_read_b64_tr_b16 a[28:29], v204 offset:17920
ds_read_b64_tr_b16 a[30:31], v204 offset:22016
s_waitcnt lgkmcnt(8)
v_mfma_f32_16x16x32_bf16 v[16:19], a[8:11], v[176:179], v[16:19]
ds_read_b64_tr_b16 a[0:1], v203 offset:24576
ds_read_b64_tr_b16 a[2:3], v203 offset:28672
buffer_load_dwordx4 v15, s[48:51], s78 offen offset:3072 nt lds
v_max3_f32 v236, v236, v131, v132
v_max3_f32 v236, v236, v133, v134
v_max_f32_e32 v248, v236, v135
v_cmp_lt_f32_e32 vcc, s70, v248
v_mfma_f32_16x16x32_bf16 v[20:23], a[12:15], v[176:179], v[20:23]
s_nop 0
s_cbranch_vccnz .Lk_bra4
.Lk_bba4:
ds_read_b64_tr_b16 a[4:5], v204 offset:24576
ds_read_b64_tr_b16 a[6:7], v204 offset:28672
v_exp_f32_e32 v160, v160
s_waitcnt lgkmcnt(8)
v_mfma_f32_16x16x32_bf16 v[32:35], a[16:19], v[176:179], v[32:35]
s_waitcnt vmcnt(38)
s_mov_b32 m0, 0x11000
s_mov_b32 s78, 0x15000
buffer_load_dwordx4 v12, s[48:51], s78 offen nt lds
ds_read_b64_tr_b16 a[8:9], v203 offset:25088
ds_read_b64_tr_b16 a[10:11], v203 offset:29184
v_mfma_f32_16x16x32_bf16 v[36:39], a[20:23], v[176:179], v[36:39]
s_waitcnt lgkmcnt(6)
ds_read_b64_tr_b16 a[12:13], v204 offset:25088
ds_read_b64_tr_b16 a[14:15], v204 offset:29184
buffer_load_dwordx4 v13, s[48:51], s78 offen offset:1024 nt lds
v_mfma_f32_16x16x32_bf16 v[48:51], a[24:27], v[176:179], v[48:51]
s_waitcnt vmcnt(38)
buffer_load_dwordx4 v14, s[48:51], s78 offen offset:2048 nt lds
ds_read_b64_tr_b16 a[16:17], v203 offset:25600
ds_read_b64_tr_b16 a[18:19], v203 offset:29696
v_add_f32_e32 v242, v242, v160
v_mfma_f32_16x16x32_bf16 v[52:55], a[28:31], v[176:179], v[52:55]
s_waitcnt lgkmcnt(6)
ds_read_b64_tr_b16 a[20:21], v204 offset:25600
ds_read_b64_tr_b16 a[22:23], v204 offset:29696
buffer_load_dwordx4 v15, s[48:51], s78 offen offset:3072 nt lds
v_exp_f32_e32 v161, v161
v_mfma_f32_16x16x32_bf16 v[0:3], a[0:3], v[184:187], v[0:3]
ds_read_b64_tr_b16 a[24:25], v203 offset:26112
ds_read_b64_tr_b16 a[26:27], v203 offset:30208
v_add_f32_e32 v242, v242, v161
v_cvt_pk_bf16_f32 v176, v160, v161
v_exp_f32_e32 v162, v162
s_nop 0
)"
R"(
v_add_f32_e32 v242, v242, v162
v_mfma_f32_16x16x32_bf16 v[4:7], a[4:7], v[184:187], v[4:7]
s_waitcnt vmcnt(22)
s_mov_b32 m0, 0x12000
s_mov_b32 s78, 0x16000
buffer_load_dwordx4 v12, s[48:51], s78 offen nt lds
s_waitcnt vmcnt(25)
s_barrier
ds_read_b64_tr_b16 a[28:29], v204 offset:26112
ds_read_b64_tr_b16 a[30:31], v204 offset:30208
s_waitcnt lgkmcnt(8)
v_mfma_f32_16x16x32_bf16 v[16:19], a[8:11], v[184:187], v[16:19]
ds_read_b64_tr_b16 a[0:1], v209 offset:49152
ds_read_b64_tr_b16 a[2:3], v209 offset:49408
buffer_load_dwordx4 v13, s[48:51], s78 offen offset:1024 nt lds
v_exp_f32_e32 v163, v163
v_mfma_f32_16x16x32_bf16 v[20:23], a[12:15], v[184:187], v[20:23]
s_waitcnt vmcnt(22)
buffer_load_dwordx4 v14, s[48:51], s78 offen offset:2048 nt lds
ds_read_b64_tr_b16 a[4:5], v210 offset:49152
ds_read_b64_tr_b16 a[6:7], v210 offset:49408
v_add_f32_e32 v242, v242, v163
s_waitcnt lgkmcnt(8)
v_mfma_f32_16x16x32_bf16 v[32:35], a[16:19], v[184:187], v[32:35]
ds_read_b64_tr_b16 a[8:9], v209 offset:49664
ds_read_b64_tr_b16 a[10:11], v209 offset:49920
buffer_load_dwordx4 v15, s[48:51], s78 offen offset:3072 nt lds
v_cvt_pk_bf16_f32 v177, v162, v163
v_mfma_f32_16x16x32_bf16 v[36:39], a[20:23], v[184:187], v[36:39]
s_waitcnt lgkmcnt(6)
ds_read_b64_tr_b16 a[12:13], v210 offset:49664
ds_read_b64_tr_b16 a[14:15], v210 offset:49920
v_exp_f32_e32 v164, v164
v_exp_f32_e32 v165, v165
v_add_f32_e32 v242, v242, v164
v_mfma_f32_16x16x32_bf16 v[48:51], a[24:27], v[184:187], v[48:51]
s_waitcnt vmcnt(22)
s_mov_b32 m0, 0x13000
s_mov_b32 s78, 0x17000
buffer_load_dwordx4 v12, s[48:51], s78 offen nt lds
ds_read_b64_tr_b16 a[16:17], v209 offset:50176
ds_read_b64_tr_b16 a[18:19], v209 offset:50432
v_mfma_f32_16x16x32_bf16 v[52:55], a[28:31], v[184:187], v[52:55]
s_waitcnt lgkmcnt(6)
ds_read_b64_tr_b16 a[20:21], v210 offset:50176
ds_read_b64_tr_b16 a[22:23], v210 offset:50432
buffer_load_dwordx4 v13, s[48:51], s78 offen offset:1024 nt lds
v_mfma_f32_16x16x32_bf16 v[64:67], a[0:3], a[192:195], v[64:67]
s_waitcnt vmcnt(22)
buffer_load_dwordx4 v14, s[48:51], s78 offen offset:2048 nt lds
ds_read_b64_tr_b16 a[24:25], v209 offset:50688
ds_read_b64_tr_b16 a[26:27], v209 offset:50944
v_add_f32_e32 v242, v242, v165
v_mfma_f32_16x16x32_bf16 v[68:71], a[4:7], a[192:195], v[68:71]
s_waitcnt lgkmcnt(6)
ds_read_b64_tr_b16 a[28:29], v210 offset:50688
ds_read_b64_tr_b16 a[30:31], v210 offset:50944
buffer_load_dwordx4 v15, s[48:51], s78 offen offset:3072 nt lds
v_cvt_pk_bf16_f32 v178, v164, v165
v_exp_f32_e32 v166, v166
v_mfma_f32_16x16x32_bf16 v[80:83], a[8:11], a[192:195], v[80:83]
ds_read_b64_tr_b16 a[0:1], v209 offset:57344
ds_read_b64_tr_b16 a[2:3], v209 offset:57600
v_add_f32_e32 v242, v242, v166
v_exp_f32_e32 v167, v167
s_nop 0
v_add_f32_e32 v242, v242, v167
v_mfma_f32_16x16x32_bf16 v[84:87], a[12:15], a[192:195], v[84:87]
s_cbranch_vccnz .Lk_brb4
.Lk_bbb4:
ds_read_b64_tr_b16 a[4:5], v210 offset:57344
ds_read_b64_tr_b16 a[6:7], v210 offset:57600
v_cvt_pk_bf16_f32 v179, v166, v167
v_exp_f32_e32 v128, v128
s_waitcnt lgkmcnt(8)
v_mfma_f32_16x16x32_bf16 v[96:99], a[16:19], a[192:195], v[96:99]
s_waitcnt vmcnt(22)
s_mov_b32 m0, 0x18000
s_mov_b32 s78, 0x1c000
buffer_load_dwordx4 v8, s[44:47], s78 offen nt lds
ds_read_b64_tr_b16 a[8:9], v209 offset:57856
ds_read_b64_tr_b16 a[10:11], v209 offset:58112
v_mfma_f32_16x16x32_bf16 v[100:103], a[20:23], a[192:195], v[100:103]
buffer_load_dwordx4 v9, s[44:47], s78 offen offset:1024 nt lds
s_waitcnt vmcnt(22)
buffer_load_dwordx4 v10, s[44:47], s78 offen offset:2048 nt lds
ds_read_b64_tr_b16 a[12:13], v210 offset:57856
ds_read_b64_tr_b16 a[14:15], v210 offset:58112
s_waitcnt lgkmcnt(8)
v_mfma_f32_16x16x32_bf16 v[112:115], a[24:27], a[192:195], v[112:115]
ds_read_b64_tr_b16 a[16:17], v209 offset:58368
ds_read_b64_tr_b16 a[18:19], v209 offset:58624
buffer_load_dwordx4 v11, s[44:47], s78 offen offset:3072 nt lds
s_waitcnt vmcnt(22)
s_mov_b32 m0, 0x19000
s_mov_b32 s78, 0x1d000
buffer_load_dwordx4 v8, s[44:47], s78 offen nt lds
v_mfma_f32_16x16x32_bf16 v[116:119], a[28:31], a[192:195], v[116:119]
s_waitcnt lgkmcnt(6)
ds_read_b64_tr_b16 a[20:21], v210 offset:58368
ds_read_b64_tr_b16 a[22:23], v210 offset:58624
buffer_load_dwordx4 v9, s[44:47], s78 offen offset:1024 nt lds
v_exp_f32_e32 v129, v129
v_mfma_f32_16x16x32_bf16 v[64:67], a[0:3], a[200:203], v[64:67]
s_waitcnt vmcnt(22)
buffer_load_dwordx4 v10, s[44:47], s78 offen offset:2048 nt lds
ds_read_b64_tr_b16 a[24:25], v209 offset:58880
ds_read_b64_tr_b16 a[26:27], v209 offset:59136
buffer_load_dwordx4 v11, s[44:47], s78 offen offset:3072 nt lds
v_cvt_pk_bf16_f32 v184, v128, v129
v_mfma_f32_16x16x32_bf16 v[68:71], a[4:7], a[200:203], v[68:71]
s_waitcnt vmcnt(22)
s_mov_b32 m0, 0x1a000
s_mov_b32 s78, 0x1e000
buffer_load_dwordx4 v8, s[44:47], s78 offen nt lds
s_waitcnt vmcnt(63)
s_barrier
ds_read_b64_tr_b16 a[28:29], v210 offset:58880
ds_read_b64_tr_b16 a[30:31], v210 offset:59136
s_waitcnt lgkmcnt(8)
v_mfma_f32_16x16x32_bf16 v[80:83], a[8:11], a[200:203], v[80:83]
ds_read_b128 a[0:3], v200
buffer_load_dwordx4 v9, s[44:47], s78 offen offset:1024 nt lds
s_waitcnt vmcnt(22)
buffer_load_dwordx4 v10, s[44:47], s78 offen offset:2048 nt lds
v_mfma_f32_16x16x32_bf16 v[84:87], a[12:15], a[200:203], v[84:87]
s_waitcnt lgkmcnt(5)
ds_read_b128 a[4:7], v200 offset:512
buffer_load_dwordx4 v11, s[44:47], s78 offen offset:3072 nt lds
v_exp_f32_e32 v130, v130
v_mfma_f32_16x16x32_bf16 v[96:99], a[16:19], a[200:203], v[96:99]
s_waitcnt vmcnt(22)
s_mov_b32 m0, 0x1b000
s_mov_b32 s78, 0x1f000
buffer_load_dwordx4 v8, s[44:47], s78 offen nt lds
ds_read_b128 a[8:11], v200 offset:1024
v_mfma_f32_16x16x32_bf16 v[100:103], a[20:23], a[200:203], v[100:103]
s_waitcnt lgkmcnt(3)
buffer_load_dwordx4 v9, s[44:47], s78 offen offset:1024 nt lds
ds_read_b128 a[12:15], v200 offset:1536
v_exp_f32_e32 v131, v131
v_mfma_f32_16x16x32_bf16 v[112:115], a[24:27], a[200:203], v[112:115]
s_waitcnt vmcnt(22)
buffer_load_dwordx4 v10, s[44:47], s78 offen offset:2048 nt lds
ds_read_b128 a[16:19], v200 offset:4096
v_cvt_pk_bf16_f32 v185, v130, v131
v_mfma_f32_16x16x32_bf16 v[116:119], a[28:31], a[200:203], v[116:119]
s_waitcnt lgkmcnt(3)
buffer_load_dwordx4 v11, s[44:47], s78 offen offset:3072 nt lds
ds_read_b128 a[20:23], v200 offset:4608
v_exp_f32_e32 v132, v132
v_mfma_f32_16x16x32_bf16 v[144:147], a[0:3], a[32:35], v[192:195]
s_mov_b32 m0, 0x1c000
s_add_u32 s78, s85, 0
buffer_load_dwordx4 v24, s[40:43], s78 offen lds
buffer_load_dwordx4 v25, s[40:43], s78 offen offset:1024 lds
ds_read_b128 a[24:27], v200 offset:5120
v_mfma_f32_16x16x32_bf16 v[144:147], a[4:7], a[40:43], v[144:147]
s_waitcnt lgkmcnt(3)
buffer_load_dwordx4 v26, s[40:43], s78 offen offset:2048 lds
ds_read_b128 a[28:31], v200 offset:5632
buffer_load_dwordx4 v27, s[40:43], s78 offen offset:3072 lds
v_mfma_f32_16x16x32_bf16 v[144:147], a[8:11], a[48:51], v[144:147]
s_mov_b32 m0, 0x1d000
s_add_u32 s78, s85, 0x20000
buffer_load_dwordx4 v28, s[40:43], s78 offen lds
ds_read_b128 a[0:3], v200 offset:8192
v_add_f32_e32 v242, v242, v128
v_mfma_f32_16x16x32_bf16 v[144:147], a[12:15], a[56:59], v[144:147]
s_waitcnt lgkmcnt(3)
buffer_load_dwordx4 v29, s[40:43], s78 offen offset:1024 lds
ds_read_b128 a[4:7], v200 offset:8704
buffer_load_dwordx4 v30, s[40:43], s78 offen offset:2048 lds
v_mfma_f32_16x16x32_bf16 v[148:151], a[16:19], a[32:35], v[192:195]
buffer_load_dwordx4 v31, s[40:43], s78 offen offset:3072 lds
s_mov_b32 m0, 0x1e000
s_add_u32 s78, s85, 0x40000
buffer_load_dwordx4 v24, s[40:43], s78 offen lds
ds_read_b128 a[8:11], v200 offset:9216
v_mfma_f32_16x16x32_bf16 v[148:151], a[20:23], a[40:43], v[148:151]
s_waitcnt lgkmcnt(3)
ds_read_b128 a[12:15], v200 offset:9728
buffer_load_dwordx4 v25, s[40:43], s78 offen offset:1024 lds
v_add_f32_e32 v242, v242, v129
v_mfma_f32_16x16x32_bf16 v[148:151], a[24:27], a[48:51], v[148:151]
buffer_load_dwordx4 v26, s[40:43], s78 offen offset:2048 lds
ds_read_b128 a[16:19], v200 offset:12288
buffer_load_dwordx4 v27, s[40:43], s78 offen offset:3072 lds
v_mfma_f32_16x16x32_bf16 v[148:151], a[28:31], a[56:59], v[148:151]
s_mov_b32 m0, 0x1f000
s_add_u32 s78, s85, 0x60000
buffer_load_dwordx4 v28, s[40:43], s78 offen lds
ds_read_b128 a[20:23], v200 offset:12800
buffer_load_dwordx4 v29, s[40:43], s78 offen offset:1024 lds
s_waitcnt lgkmcnt(4)
v_mfma_f32_16x16x32_bf16 v[160:163], a[0:3], a[32:35], v[192:195]
ds_read_b128 a[24:27], v200 offset:13312
buffer_load_dwordx4 v30, s[40:43], s78 offen offset:2048 lds
v_exp_f32_e32 v133, v133
v_mfma_f32_16x16x32_bf16 v[160:163], a[4:7], a[40:43], v[160:163]
)"
R"(
buffer_load_dwordx4 v31, s[40:43], s78 offen offset:3072 lds
s_add_u32 s85, s85, 0x80000
s_and_b32 s85, s85, 0x3fffff
s_waitcnt vmcnt(63)
s_barrier
ds_read_b128 a[28:31], v200 offset:13824
s_waitcnt lgkmcnt(4)
v_mfma_f32_16x16x32_bf16 v[160:163], a[8:11], a[48:51], v[160:163]
s_waitcnt vmcnt(38)
s_mov_b32 m0, 0x20000
s_mov_b32 s78, 0x18000
buffer_load_dwordx4 v12, s[48:51], s78 offen nt lds
ds_read_b64_tr_b16 a[0:1], v203 offset:32768
ds_read_b64_tr_b16 a[2:3], v203 offset:36864
v_mfma_f32_16x16x32_bf16 v[160:163], a[12:15], a[56:59], v[160:163]
s_waitcnt lgkmcnt(4)
ds_read_b64_tr_b16 a[4:5], v204 offset:32768
ds_read_b64_tr_b16 a[6:7], v204 offset:36864
buffer_load_dwordx4 v13, s[48:51], s78 offen offset:1024 nt lds
v_mfma_f32_16x16x32_bf16 v[164:167], a[16:19], a[32:35], v[192:195]
s_waitcnt vmcnt(38)
buffer_load_dwordx4 v14, s[48:51], s78 offen offset:2048 nt lds
ds_read_b64_tr_b16 a[8:9], v203 offset:33280
ds_read_b64_tr_b16 a[10:11], v203 offset:37376
v_mfma_f32_16x16x32_bf16 v[164:167], a[20:23], a[40:43], v[164:167]
s_waitcnt lgkmcnt(6)
ds_read_b64_tr_b16 a[12:13], v204 offset:33280
ds_read_b64_tr_b16 a[14:15], v204 offset:37376
buffer_load_dwordx4 v15, s[48:51], s78 offen offset:3072 nt lds
v_cvt_pk_bf16_f32 v186, v132, v133
v_add_f32_e32 v242, v242, v130
v_mfma_f32_16x16x32_bf16 v[164:167], a[24:27], a[48:51], v[164:167]
ds_read_b64_tr_b16 a[16:17], v203 offset:33792
ds_read_b64_tr_b16 a[18:19], v203 offset:37888
v_exp_f32_e32 v134, v134
v_exp_f32_e32 v135, v135
v_mfma_f32_16x16x32_bf16 v[164:167], a[28:31], a[56:59], v[164:167]
s_waitcnt vmcnt(38)
s_mov_b32 m0, 0x21000
s_mov_b32 s78, 0x19000
buffer_load_dwordx4 v12, s[48:51], s78 offen nt lds
ds_read_b64_tr_b16 a[20:21], v204 offset:33792
ds_read_b64_tr_b16 a[22:23], v204 offset:37888
s_waitcnt lgkmcnt(8)
v_mfma_f32_16x16x32_bf16 v[0:3], a[0:3], v[176:179], v[0:3]
ds_read_b64_tr_b16 a[24:25], v203 offset:34304
ds_read_b64_tr_b16 a[26:27], v203 offset:38400
buffer_load_dwordx4 v13, s[48:51], s78 offen offset:1024 nt lds
v_mfma_f32_16x16x32_bf16 v[4:7], a[4:7], v[176:179], v[4:7]
s_waitcnt vmcnt(38)
buffer_load_dwordx4 v14, s[48:51], s78 offen offset:2048 nt lds
ds_read_b64_tr_b16 a[28:29], v204 offset:34304
ds_read_b64_tr_b16 a[30:31], v204 offset:38400
v_cvt_pk_bf16_f32 v187, v134, v135
s_waitcnt lgkmcnt(8)
v_mfma_f32_16x16x32_bf16 v[16:19], a[8:11], v[176:179], v[16:19]
ds_read_b64_tr_b16 a[0:1], v203 offset:40960
ds_read_b64_tr_b16 a[2:3], v203 offset:45056
buffer_load_dwordx4 v15, s[48:51], s78 offen offset:3072 nt lds
v_add_f32_e32 v242, v242, v131
v_mfma_f32_16x16x32_bf16 v[20:23], a[12:15], v[176:179], v[20:23]
s_waitcnt lgkmcnt(6)
ds_read_b64_tr_b16 a[4:5], v204 offset:40960
ds_read_b64_tr_b16 a[6:7], v204 offset:45056
v_add_f32_e32 v242, v242, v132
v_add_f32_e32 v242, v242, v133
v_add_f32_e32 v242, v242, v134
v_add_f32_e32 v242, v242, v135
v_mfma_f32_16x16x32_bf16 v[32:35], a[16:19], v[176:179], v[32:35]
s_waitcnt vmcnt(38)
s_mov_b32 m0, 0x22000
s_mov_b32 s78, 0x1a000
buffer_load_dwordx4 v12, s[48:51], s78 offen nt lds
ds_read_b64_tr_b16 a[8:9], v203 offset:41472
ds_read_b64_tr_b16 a[10:11], v203 offset:45568
v_mfma_f32_16x16x32_bf16 v[36:39], a[20:23], v[176:179], v[36:39]
s_waitcnt lgkmcnt(6)
ds_read_b64_tr_b16 a[12:13], v204 offset:41472
ds_read_b64_tr_b16 a[14:15], v204 offset:45568
buffer_load_dwordx4 v13, s[48:51], s78 offen offset:1024 nt lds
v_max3_f32 v236, v144, v145, v146
v_mfma_f32_16x16x32_bf16 v[48:51], a[24:27], v[176:179], v[48:51]
s_waitcnt vmcnt(38)
buffer_load_dwordx4 v14, s[48:51], s78 offen offset:2048 nt lds
ds_read_b64_tr_b16 a[16:17], v203 offset:41984
ds_read_b64_tr_b16 a[18:19], v203 offset:46080
v_mfma_f32_16x16x32_bf16 v[52:55], a[28:31], v[176:179], v[52:55]
s_waitcnt lgkmcnt(6)
ds_read_b64_tr_b16 a[20:21], v204 offset:41984
ds_read_b64_tr_b16 a[22:23], v204 offset:46080
buffer_load_dwordx4 v15, s[48:51], s78 offen offset:3072 nt lds
v_max3_f32 v236, v236, v147, v148
v_max3_f32 v236, v236, v149, v150
v_mfma_f32_16x16x32_bf16 v[0:3], a[0:3], v[184:187], v[0:3]
ds_read_b64_tr_b16 a[24:25], v203 offset:42496
ds_read_b64_tr_b16 a[26:27], v203 offset:46592
v_max3_f32 v236, v236, v151, v160
v_max3_f32 v236, v236, v161, v162
v_max3_f32 v236, v236, v163, v164
v_max3_f32 v236, v236, v165, v166
v_mfma_f32_16x16x32_bf16 v[4:7], a[4:7], v[184:187], v[4:7]
s_waitcnt vmcnt(38)
s_mov_b32 m0, 0x23000
s_mov_b32 s78, 0x1b000
buffer_load_dwordx4 v12, s[48:51], s78 offen nt lds
s_waitcnt vmcnt(61)
s_barrier
ds_read_b64_tr_b16 a[28:29], v204 offset:42496
ds_read_b64_tr_b16 a[30:31], v204 offset:46592
s_waitcnt lgkmcnt(8)
v_mfma_f32_16x16x32_bf16 v[16:19], a[8:11], v[184:187], v[16:19]
ds_read_b128 a[0:3], v201 offset:16384
buffer_load_dwordx4 v13, s[48:51], s78 offen offset:1024 nt lds
v_max_f32_e32 v248, v236, v167
v_cmp_lt_f32_e32 vcc, s70, v248
v_mfma_f32_16x16x32_bf16 v[20:23], a[12:15], v[184:187], v[20:23]
s_waitcnt vmcnt(38)
buffer_load_dwordx4 v14, s[48:51], s78 offen offset:2048 nt lds
s_cbranch_vccnz .Lk_bra5
.Lk_bba5:
ds_read_b128 a[4:7], v201 offset:16896
s_waitcnt lgkmcnt(6)
v_mfma_f32_16x16x32_bf16 v[32:35], a[16:19], v[184:187], v[32:35]
ds_read_b128 a[8:11], v201 offset:17408
buffer_load_dwordx4 v15, s[48:51], s78 offen offset:3072 nt lds
v_exp_f32_e32 v144, v144
v_exp_f32_e32 v145, v145
v_mfma_f32_16x16x32_bf16 v[36:39], a[20:23], v[184:187], v[36:39]
s_waitcnt lgkmcnt(3)
ds_read_b128 a[12:15], v201 offset:17920
v_add_f32_e32 v242, v242, v144
v_add_f32_e32 v242, v242, v145
v_cvt_pk_bf16_f32 v176, v144, v145
v_exp_f32_e32 v146, v146
v_mfma_f32_16x16x32_bf16 v[48:51], a[24:27], v[184:187], v[48:51]
ds_read_b128 a[16:19], v201 offset:20480
v_add_f32_e32 v242, v242, v146
v_exp_f32_e32 v147, v147
v_exp_f32_e32 v148, v148
v_mfma_f32_16x16x32_bf16 v[52:55], a[28:31], v[184:187], v[52:55]
s_waitcnt lgkmcnt(3)
ds_read_b128 a[20:23], v201 offset:20992
v_add_f32_e32 v242, v242, v147
v_cvt_pk_bf16_f32 v177, v146, v147
v_add_f32_e32 v242, v242, v148
v_exp_f32_e32 v149, v149
v_mfma_f32_16x16x32_bf16 v[128:131], a[0:3], a[32:35], v[192:195]
s_waitcnt vmcnt(38)
s_mov_b32 m0, 0x24000
s_mov_b32 s78, 0x20000
buffer_load_dwordx4 v8, s[44:47], s78 offen nt lds
ds_read_b128 a[24:27], v201 offset:21504
v_mfma_f32_16x16x32_bf16 v[128:131], a[4:7], a[40:43], v[128:131]
s_waitcnt lgkmcnt(3)
ds_read_b128 a[28:31], v201 offset:22016
buffer_load_dwordx4 v9, s[44:47], s78 offen offset:1024 nt lds
v_add_f32_e32 v242, v242, v149
v_cvt_pk_bf16_f32 v178, v148, v149
v_exp_f32_e32 v150, v150
v_mfma_f32_16x16x32_bf16 v[128:131], a[8:11], a[48:51], v[128:131]
s_waitcnt vmcnt(38)
buffer_load_dwordx4 v10, s[44:47], s78 offen offset:2048 nt lds
ds_read_b128 a[0:3], v201 offset:24576
v_add_f32_e32 v242, v242, v150
v_mfma_f32_16x16x32_bf16 v[128:131], a[12:15], a[56:59], v[128:131]
s_cbranch_vccnz .Lk_brb5
.Lk_bbb5:
ds_read_b128 a[4:7], v201 offset:25088
buffer_load_dwordx4 v11, s[44:47], s78 offen offset:3072 nt lds
v_exp_f32_e32 v151, v151
s_waitcnt lgkmcnt(4)
v_mfma_f32_16x16x32_bf16 v[132:135], a[16:19], a[32:35], v[192:195]
ds_read_b128 a[8:11], v201 offset:25600
v_add_f32_e32 v242, v242, v151
v_cvt_pk_bf16_f32 v179, v150, v151
v_exp_f32_e32 v160, v160
v_exp_f32_e32 v161, v161
v_mfma_f32_16x16x32_bf16 v[132:135], a[20:23], a[40:43], v[132:135]
s_waitcnt vmcnt(38)
s_mov_b32 m0, 0x25000
s_mov_b32 s78, 0x21000
buffer_load_dwordx4 v8, s[44:47], s78 offen nt lds
ds_read_b128 a[12:15], v201 offset:26112
s_waitcnt lgkmcnt(4)
v_mfma_f32_16x16x32_bf16 v[132:135], a[24:27], a[48:51], v[132:135]
ds_read_b128 a[16:19], v201 offset:28672
buffer_load_dwordx4 v9, s[44:47], s78 offen offset:1024 nt lds
v_cvt_pk_bf16_f32 v184, v160, v161
v_exp_f32_e32 v162, v162
v_add_f32_e32 v242, v242, v160
v_mfma_f32_16x16x32_bf16 v[132:135], a[28:31], a[56:59], v[132:135]
s_waitcnt vmcnt(38)
buffer_load_dwordx4 v10, s[44:47], s78 offen offset:2048 nt lds
ds_read_b128 a[20:23], v201 offset:29184
v_exp_f32_e32 v163, v163
s_waitcnt lgkmcnt(4)
v_mfma_f32_16x16x32_bf16 v[144:147], a[0:3], a[32:35], v[192:195]
ds_read_b128 a[24:27], v201 offset:29696
buffer_load_dwordx4 v11, s[44:47], s78 offen offset:3072 nt lds
v_cvt_pk_bf16_f32 v185, v162, v163
)"
R"(
v_exp_f32_e32 v164, v164
v_exp_f32_e32 v165, v165
v_mfma_f32_16x16x32_bf16 v[144:147], a[4:7], a[40:43], v[144:147]
s_waitcnt vmcnt(24)
s_barrier
ds_read_b128 a[28:31], v201 offset:30208
v_cvt_pk_bf16_f32 v186, v164, v165
v_exp_f32_e32 v166, v166
s_waitcnt lgkmcnt(4)
v_mfma_f32_16x16x32_bf16 v[144:147], a[8:11], a[48:51], v[144:147]
s_waitcnt vmcnt(22)
s_mov_b32 m0, 0x26000
s_mov_b32 s78, 0x22000
buffer_load_dwordx4 v8, s[44:47], s78 offen nt lds
ds_read_b64_tr_b16 a[0:1], v211 offset:49152
ds_read_b64_tr_b16 a[2:3], v211 offset:49408
v_mfma_f32_16x16x32_bf16 v[144:147], a[12:15], a[56:59], v[144:147]
s_waitcnt lgkmcnt(4)
ds_read_b64_tr_b16 a[4:5], v212 offset:49152
ds_read_b64_tr_b16 a[6:7], v212 offset:49408
buffer_load_dwordx4 v9, s[44:47], s78 offen offset:1024 nt lds
v_exp_f32_e32 v167, v167
v_mfma_f32_16x16x32_bf16 v[148:151], a[16:19], a[32:35], v[192:195]
s_waitcnt vmcnt(22)
buffer_load_dwordx4 v10, s[44:47], s78 offen offset:2048 nt lds
ds_read_b64_tr_b16 a[8:9], v211 offset:49664
ds_read_b64_tr_b16 a[10:11], v211 offset:49920
v_cvt_pk_bf16_f32 v187, v166, v167
v_add_f32_e32 v242, v242, v161
v_mfma_f32_16x16x32_bf16 v[148:151], a[20:23], a[40:43], v[148:151]
s_waitcnt lgkmcnt(6)
ds_read_b64_tr_b16 a[12:13], v212 offset:49664
ds_read_b64_tr_b16 a[14:15], v212 offset:49920
buffer_load_dwordx4 v11, s[44:47], s78 offen offset:3072 nt lds
v_add_f32_e32 v242, v242, v162
v_add_f32_e32 v242, v242, v163
v_add_f32_e32 v242, v242, v164
v_mfma_f32_16x16x32_bf16 v[148:151], a[24:27], a[48:51], v[148:151]
ds_read_b64_tr_b16 a[16:17], v211 offset:50176
ds_read_b64_tr_b16 a[18:19], v211 offset:50432
v_add_f32_e32 v242, v242, v165
v_add_f32_e32 v242, v242, v166
v_add_f32_e32 v242, v242, v167
v_max3_f32 v236, v128, v129, v130
v_max3_f32 v236, v236, v131, v132
v_mfma_f32_16x16x32_bf16 v[148:151], a[28:31], a[56:59], v[148:151]
s_waitcnt vmcnt(22)
s_mov_b32 m0, 0x27000
s_mov_b32 s78, 0x23000
buffer_load_dwordx4 v8, s[44:47], s78 offen nt lds
ds_read_b64_tr_b16 a[20:21], v212 offset:50176
ds_read_b64_tr_b16 a[22:23], v212 offset:50432
s_waitcnt lgkmcnt(8)
v_mfma_f32_16x16x32_bf16 v[64:67], a[0:3], a[208:211], v[64:67]
ds_read_b64_tr_b16 a[24:25], v211 offset:50688
ds_read_b64_tr_b16 a[26:27], v211 offset:50944
buffer_load_dwordx4 v9, s[44:47], s78 offen offset:1024 nt lds
v_max3_f32 v236, v236, v133, v134
v_max3_f32 v236, v236, v135, v144
v_max3_f32 v236, v236, v145, v146
v_mfma_f32_16x16x32_bf16 v[68:71], a[4:7], a[208:211], v[68:71]
s_waitcnt vmcnt(22)
buffer_load_dwordx4 v10, s[44:47], s78 offen offset:2048 nt lds
ds_read_b64_tr_b16 a[28:29], v212 offset:50688
ds_read_b64_tr_b16 a[30:31], v212 offset:50944
s_waitcnt lgkmcnt(8)
v_mfma_f32_16x16x32_bf16 v[80:83], a[8:11], a[208:211], v[80:83]
ds_read_b64_tr_b16 a[0:1], v211 offset:57344
ds_read_b64_tr_b16 a[2:3], v211 offset:57600
buffer_load_dwordx4 v11, s[44:47], s78 offen offset:3072 nt lds
v_max3_f32 v236, v236, v147, v148
v_max3_f32 v236, v236, v149, v150
v_max_f32_e32 v248, v236, v151
v_cmp_lt_f32_e32 vcc, s70, v248
v_mfma_f32_16x16x32_bf16 v[84:87], a[12:15], a[208:211], v[84:87]
s_nop 0
s_cbranch_vccnz .Lk_bra6
.Lk_bba6:
ds_read_b64_tr_b16 a[4:5], v212 offset:57344
ds_read_b64_tr_b16 a[6:7], v212 offset:57600
v_exp_f32_e32 v128, v128
v_exp_f32_e32 v129, v129
s_waitcnt lgkmcnt(8)
v_mfma_f32_16x16x32_bf16 v[96:99], a[16:19], a[208:211], v[96:99]
ds_read_b64_tr_b16 a[8:9], v211 offset:57856
ds_read_b64_tr_b16 a[10:11], v211 offset:58112
v_add_f32_e32 v242, v242, v128
v_add_f32_e32 v242, v242, v129
v_exp_f32_e32 v130, v130
v_mfma_f32_16x16x32_bf16 v[100:103], a[20:23], a[208:211], v[100:103]
s_waitcnt vmcnt(22)
s_mov_b32 m0, 0x4000
s_mov_b32 s78, 0x1c000
buffer_load_dwordx4 v12, s[48:51], s78 offen nt lds
ds_read_b64_tr_b16 a[12:13], v212 offset:57856
ds_read_b64_tr_b16 a[14:15], v212 offset:58112
s_waitcnt lgkmcnt(8)
v_mfma_f32_16x16x32_bf16 v[112:115], a[24:27], a[208:211], v[112:115]
ds_read_b64_tr_b16 a[16:17], v211 offset:58368
ds_read_b64_tr_b16 a[18:19], v211 offset:58624
buffer_load_dwordx4 v13, s[48:51], s78 offen offset:1024 nt lds
v_add_f32_e32 v242, v242, v130
v_exp_f32_e32 v131, v131
v_mfma_f32_16x16x32_bf16 v[116:119], a[28:31], a[208:211], v[116:119]
s_waitcnt vmcnt(22)
buffer_load_dwordx4 v14, s[48:51], s78 offen offset:2048 nt lds
ds_read_b64_tr_b16 a[20:21], v212 offset:58368
ds_read_b64_tr_b16 a[22:23], v212 offset:58624
v_add_f32_e32 v242, v242, v131
v_exp_f32_e32 v132, v132
s_waitcnt lgkmcnt(8)
v_mfma_f32_16x16x32_bf16 v[64:67], a[0:3], a[216:219], v[64:67]
ds_read_b64_tr_b16 a[24:25], v211 offset:58880
ds_read_b64_tr_b16 a[26:27], v211 offset:59136
buffer_load_dwordx4 v15, s[48:51], s78 offen offset:3072 nt lds
v_add_f32_e32 v242, v242, v132
v_exp_f32_e32 v133, v133
v_mfma_f32_16x16x32_bf16 v[68:71], a[4:7], a[216:219], v[68:71]
s_waitcnt vmcnt(63)
s_barrier
ds_read_b64_tr_b16 a[28:29], v212 offset:58880
ds_read_b64_tr_b16 a[30:31], v212 offset:59136
v_add_f32_e32 v242, v242, v133
s_waitcnt lgkmcnt(8)
v_mfma_f32_16x16x32_bf16 v[80:83], a[8:11], a[216:219], v[80:83]
s_waitcnt vmcnt(22)
s_mov_b32 m0, 0x5000
s_mov_b32 s78, 0x1d000
buffer_load_dwordx4 v12, s[48:51], s78 offen nt lds
ds_read_b64_tr_b16 a[0:1], v205
ds_read_b64_tr_b16 a[2:3], v205 offset:4096
v_mfma_f32_16x16x32_bf16 v[84:87], a[12:15], a[216:219], v[84:87]
s_waitcnt lgkmcnt(6)
ds_read_b64_tr_b16 a[4:5], v206
ds_read_b64_tr_b16 a[6:7], v206 offset:4096
buffer_load_dwordx4 v13, s[48:51], s78 offen offset:1024 nt lds
v_exp_f32_e32 v134, v134
v_exp_f32_e32 v135, v135
v_mfma_f32_16x16x32_bf16 v[96:99], a[16:19], a[216:219], v[96:99]
s_waitcnt vmcnt(22)
buffer_load_dwordx4 v14, s[48:51], s78 offen offset:2048 nt lds
ds_read_b64_tr_b16 a[8:9], v205 offset:512
ds_read_b64_tr_b16 a[10:11], v205 offset:4608
v_add_f32_e32 v242, v242, v134
v_add_f32_e32 v242, v242, v135
v_mfma_f32_16x16x32_bf16 v[100:103], a[20:23], a[216:219], v[100:103]
s_waitcnt lgkmcnt(6)
ds_read_b64_tr_b16 a[12:13], v206 offset:512
ds_read_b64_tr_b16 a[14:15], v206 offset:4608
buffer_load_dwordx4 v15, s[48:51], s78 offen offset:3072 nt lds
v_exp_f32_e32 v144, v144
v_mul_f32_e32 v64, s69, v64
v_mfma_f32_16x16x32_bf16 v[112:115], a[24:27], a[216:219], v[112:115]
ds_read_b64_tr_b16 a[16:17], v205 offset:1024
ds_read_b64_tr_b16 a[18:19], v205 offset:5120
v_exp_f32_e32 v145, v145
v_exp_f32_e32 v146, v146
v_exp_f32_e32 v147, v147
v_mfma_f32_16x16x32_bf16 v[116:119], a[28:31], a[216:219], v[116:119]
s_waitcnt vmcnt(22)
s_mov_b32 m0, 0x6000
s_mov_b32 s78, 0x1e000
buffer_load_dwordx4 v12, s[48:51], s78 offen nt lds
ds_read_b64_tr_b16 a[20:21], v206 offset:1024
ds_read_b64_tr_b16 a[22:23], v206 offset:5120
s_waitcnt lgkmcnt(8)
v_mfma_f32_16x16x32_bf16 v[0:3], a[0:3], v[176:179], v[0:3]
ds_read_b64_tr_b16 a[24:25], v205 offset:1536
ds_read_b64_tr_b16 a[26:27], v205 offset:5632
buffer_load_dwordx4 v13, s[48:51], s78 offen offset:1024 nt lds
buffer_load_dword v234, v232, s[56:59], 0 offen offset:128 sc1
v_mfma_f32_16x16x32_bf16 v[4:7], a[4:7], v[176:179], v[4:7]
s_waitcnt vmcnt(23)
buffer_load_dwordx4 v14, s[48:51], s78 offen offset:2048 nt lds
ds_read_b64_tr_b16 a[28:29], v206 offset:1536
ds_read_b64_tr_b16 a[30:31], v206 offset:5632
v_exp_f32_e32 v148, v148
s_waitcnt lgkmcnt(8)
v_mfma_f32_16x16x32_bf16 v[16:19], a[8:11], v[176:179], v[16:19]
ds_read_b64_tr_b16 a[0:1], v205 offset:8192
ds_read_b64_tr_b16 a[2:3], v205 offset:12288
buffer_load_dwordx4 v15, s[48:51], s78 offen offset:3072 nt lds
v_exp_f32_e32 v149, v149
v_mfma_f32_16x16x32_bf16 v[20:23], a[12:15], v[176:179], v[20:23]
s_waitcnt lgkmcnt(6)
ds_read_b64_tr_b16 a[4:5], v206 offset:8192
ds_read_b64_tr_b16 a[6:7], v206 offset:12288
v_exp_f32_e32 v150, v150
v_exp_f32_e32 v151, v151
v_mul_f32_e32 v65, s69, v65
v_mul_f32_e32 v66, s69, v66
v_mfma_f32_16x16x32_bf16 v[32:35], a[16:19], v[176:179], v[32:35]
s_waitcnt vmcnt(23)
s_mov_b32 m0, 0x7000
s_mov_b32 s78, 0x1f000
buffer_load_dwordx4 v12, s[48:51], s78 offen nt lds
ds_read_b64_tr_b16 a[8:9], v205 offset:8704
ds_read_b64_tr_b16 a[10:11], v205 offset:12800
v_mfma_f32_16x16x32_bf16 v[36:39], a[20:23], v[176:179], v[36:39]
s_waitcnt lgkmcnt(6)
)"
R"(
ds_read_b64_tr_b16 a[12:13], v206 offset:8704
ds_read_b64_tr_b16 a[14:15], v206 offset:12800
buffer_load_dwordx4 v13, s[48:51], s78 offen offset:1024 nt lds
v_mul_f32_e32 v67, s69, v67
v_mul_f32_e32 v68, s69, v68
v_mul_f32_e32 v69, s69, v69
v_mfma_f32_16x16x32_bf16 v[48:51], a[24:27], v[176:179], v[48:51]
s_waitcnt vmcnt(23)
buffer_load_dwordx4 v14, s[48:51], s78 offen offset:2048 nt lds
ds_read_b64_tr_b16 a[16:17], v205 offset:9216
ds_read_b64_tr_b16 a[18:19], v205 offset:13312
v_mul_f32_e32 v70, s69, v70
v_mul_f32_e32 v71, s69, v71
v_mfma_f32_16x16x32_bf16 v[52:55], a[28:31], v[176:179], v[52:55]
s_waitcnt lgkmcnt(6)
ds_read_b64_tr_b16 a[20:21], v206 offset:9216
ds_read_b64_tr_b16 a[22:23], v206 offset:13312
buffer_load_dwordx4 v15, s[48:51], s78 offen offset:3072 nt lds
v_cvt_pk_bf16_f32 v176, v128, v129
v_cvt_pk_bf16_f32 v177, v130, v131
v_cvt_pk_bf16_f32 v178, v132, v133
v_mfma_f32_16x16x32_bf16 v[0:3], a[0:3], v[184:187], v[0:3]
ds_read_b64_tr_b16 a[24:25], v205 offset:9728
ds_read_b64_tr_b16 a[26:27], v205 offset:13824
v_cvt_pk_bf16_f32 v179, v134, v135
v_cvt_pk_bf16_f32 v64, v64, v65
v_cvt_pk_bf16_f32 v65, v66, v67
v_cvt_pk_bf16_f32 v66, v68, v69
v_cvt_pk_bf16_f32 v67, v70, v71
v_mfma_f32_16x16x32_bf16 v[4:7], a[4:7], v[184:187], v[4:7]
s_waitcnt vmcnt(63)
s_barrier
ds_read_b64_tr_b16 a[28:29], v206 offset:9728
ds_read_b64_tr_b16 a[30:31], v206 offset:13824
v_permlane16_swap_b32_e32 v64, v66
v_permlane16_swap_b32_e32 v65, v67
v_accvgpr_write_b32 a64, v64
s_waitcnt lgkmcnt(8)
v_mfma_f32_16x16x32_bf16 v[16:19], a[8:11], v[184:187], v[16:19]
ds_read_b128 a[0:3], v201 offset:32768
v_accvgpr_write_b32 a65, v65
v_accvgpr_write_b32 a66, v66
v_accvgpr_write_b32 a67, v67
v_add_f32_e32 v242, v242, v144
v_add_f32_e32 v242, v242, v145
v_add_f32_e32 v242, v242, v146
v_mfma_f32_16x16x32_bf16 v[20:23], a[12:15], v[184:187], v[20:23]
s_waitcnt vmcnt(23)
s_mov_b32 m0, 0xc000
s_mov_b32 s78, 0x24000
buffer_load_dwordx4 v8, s[44:47], s78 offen nt lds
ds_read_b128 a[4:7], v201 offset:33280
v_add_f32_e32 v242, v242, v147
s_waitcnt lgkmcnt(6)
v_mfma_f32_16x16x32_bf16 v[32:35], a[16:19], v[184:187], v[32:35]
ds_read_b128 a[8:11], v201 offset:33792
buffer_load_dwordx4 v9, s[44:47], s78 offen offset:1024 nt lds
v_mul_f32_e32 v80, s69, v80
v_mul_f32_e32 v81, s69, v81
v_mul_f32_e32 v82, s69, v82
v_mul_f32_e32 v83, s69, v83
v_mfma_f32_16x16x32_bf16 v[36:39], a[20:23], v[184:187], v[36:39]
s_waitcnt vmcnt(23)
buffer_load_dwordx4 v10, s[44:47], s78 offen offset:2048 nt lds
ds_read_b128 a[12:15], v201 offset:34304
v_mul_f32_e32 v84, s69, v84
v_mul_f32_e32 v85, s69, v85
v_mul_f32_e32 v86, s69, v86
s_waitcnt lgkmcnt(4)
v_mfma_f32_16x16x32_bf16 v[48:51], a[24:27], v[184:187], v[48:51]
ds_read_b128 a[16:19], v201 offset:36864
buffer_load_dwordx4 v11, s[44:47], s78 offen offset:3072 nt lds
v_mul_f32_e32 v87, s69, v87
v_cvt_pk_bf16_f32 v80, v80, v81
v_cvt_pk_bf16_f32 v81, v82, v83
v_cvt_pk_bf16_f32 v82, v84, v85
v_mfma_f32_16x16x32_bf16 v[52:55], a[28:31], v[184:187], v[52:55]
s_waitcnt vmcnt(23)
s_mov_b32 m0, 0xd000
s_mov_b32 s78, 0x25000
buffer_load_dwordx4 v8, s[44:47], s78 offen nt lds
ds_read_b128 a[20:23], v201 offset:37376
v_cvt_pk_bf16_f32 v184, v144, v145
s_waitcnt lgkmcnt(4)
v_mfma_f32_16x16x32_bf16 v[160:163], a[0:3], a[32:35], v[192:195]
s_cbranch_vccnz .Lk_brb6
.Lk_bbb6:
ds_read_b128 a[24:27], v201 offset:37888
buffer_load_dwordx4 v9, s[44:47], s78 offen offset:1024 nt lds
v_cvt_pk_bf16_f32 v185, v146, v147
v_mfma_f32_16x16x32_bf16 v[160:163], a[4:7], a[40:43], v[160:163]
s_waitcnt vmcnt(23)
buffer_load_dwordx4 v10, s[44:47], s78 offen offset:2048 nt lds
ds_read_b128 a[28:31], v201 offset:38400
v_cvt_pk_bf16_f32 v186, v148, v149
v_cvt_pk_bf16_f32 v187, v150, v151
v_cvt_pk_bf16_f32 v83, v86, v87
s_waitcnt lgkmcnt(4)
v_mfma_f32_16x16x32_bf16 v[160:163], a[8:11], a[48:51], v[160:163]
ds_read_b128 a[0:3], v201 offset:40960
buffer_load_dwordx4 v11, s[44:47], s78 offen offset:3072 nt lds
v_permlane16_swap_b32_e32 v80, v82
v_permlane16_swap_b32_e32 v81, v83
v_accvgpr_write_b32 a72, v80
v_accvgpr_write_b32 a73, v81
v_mfma_f32_16x16x32_bf16 v[160:163], a[12:15], a[56:59], v[160:163]
s_waitcnt vmcnt(23)
s_mov_b32 m0, 0xe000
s_mov_b32 s78, 0x26000
buffer_load_dwordx4 v8, s[44:47], s78 offen nt lds
ds_read_b128 a[4:7], v201 offset:41472
v_accvgpr_write_b32 a74, v82
s_waitcnt lgkmcnt(4)
v_mfma_f32_16x16x32_bf16 v[164:167], a[16:19], a[32:35], v[192:195]
ds_read_b128 a[8:11], v201 offset:41984
buffer_load_dwordx4 v9, s[44:47], s78 offen offset:1024 nt lds
v_accvgpr_write_b32 a75, v83
v_mul_f32_e32 v96, s69, v96
v_mul_f32_e32 v97, s69, v97
v_mul_f32_e32 v98, s69, v98
v_mfma_f32_16x16x32_bf16 v[164:167], a[20:23], a[40:43], v[164:167]
s_waitcnt vmcnt(23)
buffer_load_dwordx4 v10, s[44:47], s78 offen offset:2048 nt lds
ds_read_b128 a[12:15], v201 offset:42496
v_mul_f32_e32 v99, s69, v99
v_mul_f32_e32 v100, s69, v100
v_mul_f32_e32 v101, s69, v101
s_waitcnt lgkmcnt(4)
v_mfma_f32_16x16x32_bf16 v[164:167], a[24:27], a[48:51], v[164:167]
ds_read_b128 a[16:19], v201 offset:45056
buffer_load_dwordx4 v11, s[44:47], s78 offen offset:3072 nt lds
v_mul_f32_e32 v102, s69, v102
v_mul_f32_e32 v103, s69, v103
v_cvt_pk_bf16_f32 v96, v96, v97
v_cvt_pk_bf16_f32 v97, v98, v99
v_mfma_f32_16x16x32_bf16 v[164:167], a[28:31], a[56:59], v[164:167]
s_waitcnt vmcnt(23)
s_mov_b32 m0, 0xf000
s_mov_b32 s78, 0x27000
buffer_load_dwordx4 v8, s[44:47], s78 offen nt lds
ds_read_b128 a[20:23], v201 offset:45568
v_cvt_pk_bf16_f32 v98, v100, v101
s_waitcnt lgkmcnt(4)
v_mfma_f32_16x16x32_bf16 v[128:131], a[0:3], a[32:35], v[192:195]
ds_read_b128 a[24:27], v201 offset:46080
buffer_load_dwordx4 v9, s[44:47], s78 offen offset:1024 nt lds
v_cvt_pk_bf16_f32 v99, v102, v103
v_permlane16_swap_b32_e32 v96, v98
s_nop 0
v_permlane16_swap_b32_e32 v97, v99
v_mfma_f32_16x16x32_bf16 v[128:131], a[4:7], a[40:43], v[128:131]
s_waitcnt vmcnt(23)
buffer_load_dwordx4 v10, s[44:47], s78 offen offset:2048 nt lds
s_waitcnt vmcnt(48)
s_barrier
ds_read_b128 a[28:31], v201 offset:46592
s_waitcnt lgkmcnt(4)
v_mfma_f32_16x16x32_bf16 v[128:131], a[8:11], a[48:51], v[128:131]
ds_read_b64_tr_b16 a[0:1], v207
ds_read_b64_tr_b16 a[2:3], v207 offset:4096
buffer_load_dwordx4 v11, s[44:47], s78 offen offset:3072 nt lds
v_accvgpr_write_b32 a80, v96
v_accvgpr_write_b32 a81, v97
v_accvgpr_write_b32 a82, v98
v_mfma_f32_16x16x32_bf16 v[128:131], a[12:15], a[56:59], v[128:131]
s_mov_b32 m0, 0
s_xor_b32 s78, s80, 0
buffer_load_dwordx4 v40, s[52:55], s78 offen lds
ds_read_b64_tr_b16 a[4:5], v208
ds_read_b64_tr_b16 a[6:7], v208 offset:4096
buffer_load_dwordx4 v41, s[52:55], s78 offen offset:1024 lds
s_waitcnt lgkmcnt(6)
v_mfma_f32_16x16x32_bf16 v[132:135], a[16:19], a[32:35], v[192:195]
ds_read_b64_tr_b16 a[8:9], v207 offset:512
ds_read_b64_tr_b16 a[10:11], v207 offset:4608
buffer_load_dwordx4 v42, s[52:55], s78 offen offset:2048 lds
v_accvgpr_write_b32 a83, v99
v_mul_f32_e32 v112, s69, v112
v_mfma_f32_16x16x32_bf16 v[132:135], a[20:23], a[40:43], v[132:135]
s_waitcnt lgkmcnt(6)
ds_read_b64_tr_b16 a[12:13], v208 offset:512
ds_read_b64_tr_b16 a[14:15], v208 offset:4608
buffer_load_dwordx4 v43, s[52:55], s78 offen offset:3072 lds
s_mov_b32 m0, 0x1000
s_xor_b32 s78, s80, 0x4000
buffer_load_dwordx4 v40, s[52:55], s78 offen lds
v_mfma_f32_16x16x32_bf16 v[132:135], a[24:27], a[48:51], v[132:135]
ds_read_b64_tr_b16 a[16:17], v207 offset:1024
ds_read_b64_tr_b16 a[18:19], v207 offset:5120
buffer_load_dwordx4 v41, s[52:55], s78 offen offset:1024 lds
v_mul_f32_e32 v113, s69, v113
v_mfma_f32_16x16x32_bf16 v[132:135], a[28:31], a[56:59], v[132:135]
s_waitcnt lgkmcnt(6)
ds_read_b64_tr_b16 a[20:21], v208 offset:1024
ds_read_b64_tr_b16 a[22:23], v208 offset:5120
buffer_load_dwordx4 v42, s[52:55], s78 offen offset:2048 lds
buffer_load_dwordx4 v43, s[52:55], s78 offen offset:3072 lds
v_mul_f32_e32 v114, s69, v114
v_mfma_f32_16x16x32_bf16 v[0:3], a[0:3], v[176:179], v[0:3]
s_mov_b32 m0, 0x2000
)"
R"(
s_xor_b32 s78, s80, 0x8000
buffer_load_dwordx4 v40, s[52:55], s78 offen lds
ds_read_b64_tr_b16 a[24:25], v207 offset:1536
ds_read_b64_tr_b16 a[26:27], v207 offset:5632
v_mul_f32_e32 v115, s69, v115
v_mfma_f32_16x16x32_bf16 v[4:7], a[4:7], v[176:179], v[4:7]
s_waitcnt lgkmcnt(6)
ds_read_b64_tr_b16 a[28:29], v208 offset:1536
ds_read_b64_tr_b16 a[30:31], v208 offset:5632
buffer_load_dwordx4 v41, s[52:55], s78 offen offset:1024 lds
buffer_load_dwordx4 v42, s[52:55], s78 offen offset:2048 lds
v_mfma_f32_16x16x32_bf16 v[16:19], a[8:11], v[176:179], v[16:19]
ds_read_b64_tr_b16 a[0:1], v207 offset:8192
ds_read_b64_tr_b16 a[2:3], v207 offset:12288
buffer_load_dwordx4 v43, s[52:55], s78 offen offset:3072 lds
v_mul_f32_e32 v116, s69, v116
v_mul_f32_e32 v117, s69, v117
v_mul_f32_e32 v118, s69, v118
v_mfma_f32_16x16x32_bf16 v[20:23], a[12:15], v[176:179], v[20:23]
s_mov_b32 m0, 0x3000
s_xor_b32 s78, s80, 0xc000
buffer_load_dwordx4 v40, s[52:55], s78 offen lds
ds_read_b64_tr_b16 a[4:5], v208 offset:8192
ds_read_b64_tr_b16 a[6:7], v208 offset:12288
v_mul_f32_e32 v119, s69, v119
s_waitcnt lgkmcnt(8)
v_mfma_f32_16x16x32_bf16 v[32:35], a[16:19], v[176:179], v[32:35]
buffer_load_dwordx4 v41, s[52:55], s78 offen offset:1024 lds
ds_read_b64_tr_b16 a[8:9], v207 offset:8704
ds_read_b64_tr_b16 a[10:11], v207 offset:12800
buffer_load_dwordx4 v42, s[52:55], s78 offen offset:2048 lds
v_cvt_pk_bf16_f32 v112, v112, v113
v_mfma_f32_16x16x32_bf16 v[36:39], a[20:23], v[176:179], v[36:39]
s_waitcnt lgkmcnt(6)
ds_read_b64_tr_b16 a[12:13], v208 offset:8704
ds_read_b64_tr_b16 a[14:15], v208 offset:12800
buffer_load_dwordx4 v43, s[52:55], s78 offen offset:3072 lds
v_cvt_pk_bf16_f32 v113, v114, v115
v_cvt_pk_bf16_f32 v114, v116, v117
v_mfma_f32_16x16x32_bf16 v[48:51], a[24:27], v[176:179], v[48:51]
s_waitcnt vmcnt(39)
s_mov_b32 m0, 0x8000
s_mov_b32 s78, 0x20000
buffer_load_dwordx4 v12, s[48:51], s78 offen nt lds
ds_read_b64_tr_b16 a[16:17], v207 offset:9216
ds_read_b64_tr_b16 a[18:19], v207 offset:13312
v_mfma_f32_16x16x32_bf16 v[52:55], a[28:31], v[176:179], v[52:55]
s_waitcnt lgkmcnt(6)
ds_read_b64_tr_b16 a[20:21], v208 offset:9216
ds_read_b64_tr_b16 a[22:23], v208 offset:13312
buffer_load_dwordx4 v13, s[48:51], s78 offen offset:1024 nt lds
v_cvt_pk_bf16_f32 v115, v118, v119
v_permlane16_swap_b32_e32 v112, v114
s_nop 0
v_permlane16_swap_b32_e32 v113, v115
v_mfma_f32_16x16x32_bf16 v[0:3], a[0:3], v[184:187], v[0:3]
s_waitcnt vmcnt(38)
buffer_load_dwordx4 v14, s[48:51], s78 offen offset:2048 nt lds
ds_read_b64_tr_b16 a[24:25], v207 offset:9728
ds_read_b64_tr_b16 a[26:27], v207 offset:13824
v_accvgpr_write_b32 a88, v112
v_accvgpr_write_b32 a89, v113
v_accvgpr_write_b32 a90, v114
v_mfma_f32_16x16x32_bf16 v[4:7], a[4:7], v[184:187], v[4:7]
buffer_load_dwordx4 v15, s[48:51], s78 offen offset:3072 nt lds
s_waitcnt vmcnt(53)
s_barrier
ds_read_b64_tr_b16 a[28:29], v208 offset:9728
ds_read_b64_tr_b16 a[30:31], v208 offset:13824
s_waitcnt lgkmcnt(8)
v_mfma_f32_16x16x32_bf16 v[16:19], a[8:11], v[184:187], v[16:19]
s_waitcnt vmcnt(38)
s_mov_b32 m0, 0x9000
s_mov_b32 s78, 0x21000
buffer_load_dwordx4 v12, s[48:51], s78 offen nt lds
ds_read_b128 a[0:3], v202 offset:16384
v_accvgpr_write_b32 a91, v115
v_mfma_f32_16x16x32_bf16 v[20:23], a[12:15], v[184:187], v[20:23]
s_waitcnt lgkmcnt(5)
ds_read_b128 a[4:7], v202 offset:16896
buffer_load_dwordx4 v13, s[48:51], s78 offen offset:1024 nt lds
v_add_f32_e32 v242, v242, v148
v_add_f32_e32 v242, v242, v149
v_add_f32_e32 v242, v242, v150
v_add_f32_e32 v242, v242, v151
v_max3_f32 v236, v160, v161, v162
v_mfma_f32_16x16x32_bf16 v[32:35], a[16:19], v[184:187], v[32:35]
s_waitcnt vmcnt(38)
buffer_load_dwordx4 v14, s[48:51], s78 offen offset:2048 nt lds
ds_read_b128 a[8:11], v202 offset:17408
v_max3_f32 v236, v236, v163, v164
v_max3_f32 v236, v236, v165, v166
v_max3_f32 v236, v236, v167, v128
v_mfma_f32_16x16x32_bf16 v[36:39], a[20:23], v[184:187], v[36:39]
s_waitcnt lgkmcnt(3)
ds_read_b128 a[12:15], v202 offset:17920
buffer_load_dwordx4 v15, s[48:51], s78 offen offset:3072 nt lds
v_max3_f32 v236, v236, v129, v130
v_max3_f32 v236, v236, v131, v132
v_max3_f32 v236, v236, v133, v134
v_max_f32_e32 v248, v236, v135
v_mfma_f32_16x16x32_bf16 v[48:51], a[24:27], v[184:187], v[48:51]
s_waitcnt vmcnt(38)
s_mov_b32 m0, 0xa000
s_mov_b32 s78, 0x22000
buffer_load_dwordx4 v12, s[48:51], s78 offen nt lds
ds_read_b128 a[16:19], v202 offset:20480
v_cmp_lt_f32_e32 vcc, s70, v248
v_mfma_f32_16x16x32_bf16 v[52:55], a[28:31], v[184:187], v[52:55]
s_nop 0
s_cbranch_vccnz .Lk_bra7
.Lk_bba7:
ds_read_b128 a[20:23], v202 offset:20992
buffer_load_dwordx4 v13, s[48:51], s78 offen offset:1024 nt lds
v_exp_f32_e32 v160, v160
s_waitcnt lgkmcnt(4)
v_mfma_f32_16x16x32_bf16 v[144:147], a[0:3], a[64:67], 0
s_waitcnt vmcnt(38)
buffer_load_dwordx4 v14, s[48:51], s78 offen offset:2048 nt lds
ds_read_b128 a[24:27], v202 offset:21504
v_add_f32_e32 v242, v242, v160
v_exp_f32_e32 v161, v161
v_mfma_f32_16x16x32_bf16 v[144:147], a[4:7], a[72:75], v[144:147]
s_waitcnt lgkmcnt(3)
ds_read_b128 a[28:31], v202 offset:22016
buffer_load_dwordx4 v15, s[48:51], s78 offen offset:3072 nt lds
v_add_f32_e32 v242, v242, v161
v_cvt_pk_bf16_f32 v176, v160, v161
v_exp_f32_e32 v162, v162
s_nop 0
v_add_f32_e32 v242, v242, v162
v_mfma_f32_16x16x32_bf16 v[144:147], a[8:11], a[80:83], v[144:147]
s_waitcnt vmcnt(38)
s_mov_b32 m0, 0xb000
s_mov_b32 s78, 0x23000
buffer_load_dwordx4 v12, s[48:51], s78 offen nt lds
ds_read_b128 a[0:3], v202 offset:24576
v_mfma_f32_16x16x32_bf16 v[144:147], a[12:15], a[88:91], v[144:147]
s_waitcnt lgkmcnt(3)
ds_read_b128 a[4:7], v202 offset:25088
buffer_load_dwordx4 v13, s[48:51], s78 offen offset:1024 nt lds
v_exp_f32_e32 v163, v163
v_exp_f32_e32 v164, v164
v_add_f32_e32 v242, v242, v163
v_mfma_f32_16x16x32_bf16 v[148:151], a[16:19], a[64:67], 0
s_waitcnt vmcnt(38)
buffer_load_dwordx4 v14, s[48:51], s78 offen offset:2048 nt lds
ds_read_b128 a[8:11], v202 offset:25600
v_cvt_pk_bf16_f32 v177, v162, v163
v_add_f32_e32 v242, v242, v164
v_exp_f32_e32 v165, v165
v_mfma_f32_16x16x32_bf16 v[148:151], a[20:23], a[72:75], v[148:151]
s_waitcnt lgkmcnt(3)
ds_read_b128 a[12:15], v202 offset:26112
buffer_load_dwordx4 v15, s[48:51], s78 offen offset:3072 nt lds
v_add_f32_e32 v242, v242, v165
v_cvt_pk_bf16_f32 v178, v164, v165
v_exp_f32_e32 v166, v166
v_mfma_f32_16x16x32_bf16 v[148:151], a[24:27], a[80:83], v[148:151]
s_waitcnt vmcnt(38)
s_mov_b32 m0, 0x14000
s_mov_b32 s78, 0x28000
buffer_load_dwordx4 v8, s[44:47], s78 offen nt lds
ds_read_b128 a[16:19], v202 offset:28672
v_add_f32_e32 v242, v242, v166
v_mfma_f32_16x16x32_bf16 v[148:151], a[28:31], a[88:91], v[148:151]
s_cbranch_vccnz .Lk_brb7
.Lk_bbb7:
ds_read_b128 a[20:23], v202 offset:29184
buffer_load_dwordx4 v9, s[44:47], s78 offen offset:1024 nt lds
v_exp_f32_e32 v167, v167
s_waitcnt lgkmcnt(4)
v_mfma_f32_16x16x32_bf16 v[160:163], a[0:3], a[64:67], 0
s_waitcnt vmcnt(38)
buffer_load_dwordx4 v10, s[44:47], s78 offen offset:2048 nt lds
ds_read_b128 a[24:27], v202 offset:29696
v_add_f32_e32 v242, v242, v167
v_cvt_pk_bf16_f32 v179, v166, v167
v_max3_f32 v236, v144, v145, v146
v_mfma_f32_16x16x32_bf16 v[160:163], a[4:7], a[72:75], v[160:163]
buffer_load_dwordx4 v11, s[44:47], s78 offen offset:3072 nt lds
s_waitcnt vmcnt(52)
s_barrier
ds_read_b128 a[28:31], v202 offset:30208
v_exp_f32_e32 v128, v128
s_waitcnt lgkmcnt(4)
v_mfma_f32_16x16x32_bf16 v[160:163], a[8:11], a[80:83], v[160:163]
ds_read_b64_tr_b16 a[0:1], v203 offset:16384
ds_read_b64_tr_b16 a[2:3], v203 offset:20480
v_exp_f32_e32 v129, v129
v_exp_f32_e32 v130, v130
v_cvt_pk_bf16_f32 v184, v128, v129
v_mfma_f32_16x16x32_bf16 v[160:163], a[12:15], a[88:91], v[160:163]
s_waitcnt vmcnt(38)
s_mov_b32 m0, 0x15000
s_mov_b32 s78, 0x29000
buffer_load_dwordx4 v8, s[44:47], s78 offen nt lds
ds_read_b64_tr_b16 a[4:5], v203 offset:24576
ds_read_b64_tr_b16 a[6:7], v203 offset:28672
s_waitcnt lgkmcnt(6)
)"
R"(
v_mfma_f32_16x16x32_bf16 v[164:167], a[16:19], a[64:67], 0
ds_read_b64_tr_b16 a[8:9], v204 offset:16384
ds_read_b64_tr_b16 a[10:11], v204 offset:20480
buffer_load_dwordx4 v9, s[44:47], s78 offen offset:1024 nt lds
v_exp_f32_e32 v131, v131
v_exp_f32_e32 v132, v132
v_mfma_f32_16x16x32_bf16 v[164:167], a[20:23], a[72:75], v[164:167]
s_waitcnt vmcnt(38)
buffer_load_dwordx4 v10, s[44:47], s78 offen offset:2048 nt lds
ds_read_b64_tr_b16 a[12:13], v204 offset:24576
ds_read_b64_tr_b16 a[14:15], v204 offset:28672
v_cvt_pk_bf16_f32 v185, v130, v131
v_exp_f32_e32 v133, v133
s_waitcnt lgkmcnt(8)
v_mfma_f32_16x16x32_bf16 v[164:167], a[24:27], a[80:83], v[164:167]
ds_read_b64_tr_b16 a[16:17], v203 offset:16896
ds_read_b64_tr_b16 a[18:19], v203 offset:20992
buffer_load_dwordx4 v11, s[44:47], s78 offen offset:3072 nt lds
v_cvt_pk_bf16_f32 v186, v132, v133
v_exp_f32_e32 v134, v134
v_mfma_f32_16x16x32_bf16 v[164:167], a[28:31], a[88:91], v[164:167]
s_waitcnt lgkmcnt(6)
ds_read_b64_tr_b16 a[20:21], v203 offset:25088
ds_read_b64_tr_b16 a[22:23], v203 offset:29184
v_exp_f32_e32 v135, v135
v_add_f32_e32 v242, v242, v128
v_cvt_pk_bf16_f32 v187, v134, v135
v_add_f32_e32 v242, v242, v129
v_add_f32_e32 v242, v242, v130
v_mfma_f32_16x16x32_bf16 v[0:3], a[0:3], v[176:179], v[0:3]
s_waitcnt vmcnt(22)
s_mov_b32 m0, 0x16000
s_mov_b32 s78, 0x2a000
buffer_load_dwordx4 v8, s[44:47], s78 offen nt lds
ds_read_b64_tr_b16 a[24:25], v204 offset:16896
ds_read_b64_tr_b16 a[26:27], v204 offset:20992
v_mfma_f32_16x16x32_bf16 v[0:3], a[4:7], v[184:187], v[0:3]
s_waitcnt lgkmcnt(6)
ds_read_b64_tr_b16 a[28:29], v204 offset:25088
ds_read_b64_tr_b16 a[30:31], v204 offset:29184
buffer_load_dwordx4 v9, s[44:47], s78 offen offset:1024 nt lds
v_add_f32_e32 v242, v242, v131
v_add_f32_e32 v242, v242, v132
v_add_f32_e32 v242, v242, v133
v_add_f32_e32 v242, v242, v134
v_mfma_f32_16x16x32_bf16 v[4:7], a[8:11], v[176:179], v[4:7]
s_waitcnt vmcnt(22)
buffer_load_dwordx4 v10, s[44:47], s78 offen offset:2048 nt lds
ds_read_b64_tr_b16 a[0:1], v203 offset:17408
ds_read_b64_tr_b16 a[2:3], v203 offset:21504
v_add_f32_e32 v242, v242, v135
v_mov_b32_e32 v248, v242
v_mfma_f32_16x16x32_bf16 v[4:7], a[12:15], v[184:187], v[4:7]
s_waitcnt lgkmcnt(6)
ds_read_b64_tr_b16 a[4:5], v203 offset:25600
ds_read_b64_tr_b16 a[6:7], v203 offset:29696
buffer_load_dwordx4 v11, s[44:47], s78 offen offset:3072 nt lds
v_permlane16_swap_b32_e32 v242, v248
v_add_f32_e32 v242, v242, v248
v_mov_b32_e32 v248, v242
v_max3_f32 v236, v236, v147, v148
v_mfma_f32_16x16x32_bf16 v[16:19], a[16:19], v[176:179], v[16:19]
ds_read_b64_tr_b16 a[8:9], v204 offset:17408
ds_read_b64_tr_b16 a[10:11], v204 offset:21504
v_permlane32_swap_b32_e32 v242, v248
v_add_f32_e32 v242, v242, v248
v_rcp_f32_e32 v240, v242
s_nop 0
v_mul_f32_e32 v0, v240, v0
v_mul_f32_e32 v1, v240, v1
v_mfma_f32_16x16x32_bf16 v[16:19], a[20:23], v[184:187], v[16:19]
s_waitcnt vmcnt(22)
s_mov_b32 m0, 0x17000
s_mov_b32 s78, 0x2b000
buffer_load_dwordx4 v8, s[44:47], s78 offen nt lds
ds_read_b64_tr_b16 a[12:13], v204 offset:25600
ds_read_b64_tr_b16 a[14:15], v204 offset:29696
s_waitcnt lgkmcnt(8)
v_mfma_f32_16x16x32_bf16 v[20:23], a[24:27], v[176:179], v[20:23]
ds_read_b64_tr_b16 a[16:17], v203 offset:17920
ds_read_b64_tr_b16 a[18:19], v203 offset:22016
buffer_load_dwordx4 v9, s[44:47], s78 offen offset:1024 nt lds
v_mul_f32_e32 v2, v240, v2
v_mul_f32_e32 v3, v240, v3
v_mul_f32_e32 v4, v240, v4
v_mfma_f32_16x16x32_bf16 v[20:23], a[28:31], v[184:187], v[20:23]
s_waitcnt vmcnt(22)
buffer_load_dwordx4 v10, s[44:47], s78 offen offset:2048 nt lds
ds_read_b64_tr_b16 a[20:21], v203 offset:26112
ds_read_b64_tr_b16 a[22:23], v203 offset:30208
v_mul_f32_e32 v5, v240, v5
v_mul_f32_e32 v6, v240, v6
v_mul_f32_e32 v7, v240, v7
s_waitcnt lgkmcnt(8)
v_mfma_f32_16x16x32_bf16 v[32:35], a[0:3], v[176:179], v[32:35]
ds_read_b64_tr_b16 a[24:25], v204 offset:17920
ds_read_b64_tr_b16 a[26:27], v204 offset:22016
buffer_load_dwordx4 v11, s[44:47], s78 offen offset:3072 nt lds
v_cvt_pk_bf16_f32 v0, v0, v1
v_cvt_pk_bf16_f32 v1, v2, v3
v_cvt_pk_bf16_f32 v2, v4, v5
v_cvt_pk_bf16_f32 v3, v6, v7
v_mfma_f32_16x16x32_bf16 v[32:35], a[4:7], v[184:187], v[32:35]
s_waitcnt vmcnt(32)
s_barrier
ds_read_b64_tr_b16 a[28:29], v204 offset:26112
ds_read_b64_tr_b16 a[30:31], v204 offset:30208
v_mul_f32_e32 v16, v240, v16
v_mul_f32_e32 v17, v240, v17
v_mul_f32_e32 v18, v240, v18
s_waitcnt lgkmcnt(8)
v_mfma_f32_16x16x32_bf16 v[36:39], a[8:11], v[176:179], v[36:39]
ds_read_b64_tr_b16 a[0:1], v203 offset:512
ds_read_b64_tr_b16 a[2:3], v203 offset:4608
v_mul_f32_e32 v19, v240, v19
v_mul_f32_e32 v20, v240, v20
v_mul_f32_e32 v21, v240, v21
v_mul_f32_e32 v22, v240, v22
v_mul_f32_e32 v23, v240, v23
v_cvt_pk_bf16_f32 v16, v16, v17
v_mfma_f32_16x16x32_bf16 v[36:39], a[12:15], v[184:187], v[36:39]
s_waitcnt vmcnt(22)
s_mov_b32 m0, 0x1c000
s_mov_b32 s78, 0x24000
buffer_load_dwordx4 v12, s[48:51], s78 offen nt lds
ds_read_b64_tr_b16 a[4:5], v204 offset:512
ds_read_b64_tr_b16 a[6:7], v204 offset:4608
s_waitcnt lgkmcnt(8)
v_mfma_f32_16x16x32_bf16 v[48:51], a[16:19], v[176:179], v[48:51]
ds_read_b64_tr_b16 a[8:9], v203 offset:1024
ds_read_b64_tr_b16 a[10:11], v203 offset:5120
buffer_load_dwordx4 v13, s[48:51], s78 offen offset:1024 nt lds
v_cvt_pk_bf16_f32 v17, v18, v19
v_cvt_pk_bf16_f32 v18, v20, v21
v_cvt_pk_bf16_f32 v19, v22, v23
v_max3_f32 v236, v236, v149, v150
v_mfma_f32_16x16x32_bf16 v[48:51], a[20:23], v[184:187], v[48:51]
s_waitcnt vmcnt(22)
buffer_load_dwordx4 v14, s[48:51], s78 offen offset:2048 nt lds
ds_read_b64_tr_b16 a[12:13], v204 offset:1024
ds_read_b64_tr_b16 a[14:15], v204 offset:5120
v_max3_f32 v236, v236, v151, v160
v_max3_f32 v236, v236, v161, v162
s_waitcnt lgkmcnt(8)
v_mfma_f32_16x16x32_bf16 v[52:55], a[24:27], v[176:179], v[52:55]
ds_read_b64_tr_b16 a[16:17], v203 offset:1536
ds_read_b64_tr_b16 a[18:19], v203 offset:5632
buffer_load_dwordx4 v15, s[48:51], s78 offen offset:3072 nt lds
v_max3_f32 v236, v236, v163, v164
v_max3_f32 v236, v236, v165, v166
v_max_f32_e32 v236, v236, v167
v_mov_b32_e32 v246, v236
v_mfma_f32_16x16x32_bf16 v[52:55], a[28:31], v[184:187], v[52:55]
s_waitcnt lgkmcnt(6)
ds_read_b64_tr_b16 a[20:21], v204 offset:1536
ds_read_b64_tr_b16 a[22:23], v204 offset:5632
v_permlane16_swap_b32_e32 v236, v246
v_max_f32_e32 v236, v236, v246
v_mov_b32_e32 v246, v236
v_accvgpr_write_b32 a32, v0
s_nop 0
v_permlane32_swap_b32_e32 v236, v246
v_max_f32_e32 v236, v236, v246
v_mfma_f32_16x16x32_bf16 v[80:83], a[0:3], v[0:3], 0
s_waitcnt vmcnt(22)
s_mov_b32 m0, 0x1d000
s_mov_b32 s78, 0x25000
buffer_load_dwordx4 v12, s[48:51], s78 offen nt lds
ds_read_b64_tr_b16 a[24:25], v203
ds_read_b64_tr_b16 a[26:27], v203 offset:4096
v_sub_f32_e32 v192, 0, v236
v_mfma_f32_16x16x32_bf16 v[84:87], a[4:7], v[0:3], 0
s_waitcnt lgkmcnt(6)
ds_read_b64_tr_b16 a[28:29], v204
ds_read_b64_tr_b16 a[30:31], v204 offset:4096
buffer_load_dwordx4 v13, s[48:51], s78 offen offset:1024 nt lds
v_sub_f32_e32 v193, 0, v236
v_sub_f32_e32 v194, 0, v236
v_sub_f32_e32 v195, 0, v236
v_mfma_f32_16x16x32_bf16 v[96:99], a[8:11], v[0:3], 0
s_waitcnt vmcnt(22)
buffer_load_dwordx4 v14, s[48:51], s78 offen offset:2048 nt lds
ds_read_b64_tr_b16 a[0:1], v203 offset:8704
ds_read_b64_tr_b16 a[2:3], v203 offset:12800
v_sub_f32_e32 v144, v144, v236
v_sub_f32_e32 v145, v145, v236
v_sub_f32_e32 v146, v146, v236
v_mfma_f32_16x16x32_bf16 v[100:103], a[12:15], v[0:3], 0
s_waitcnt lgkmcnt(6)
ds_read_b64_tr_b16 a[4:5], v204 offset:8704
ds_read_b64_tr_b16 a[6:7], v204 offset:12800
buffer_load_dwordx4 v15, s[48:51], s78 offen offset:3072 nt lds
v_sub_f32_e32 v147, v147, v236
v_exp_f32_e32 v144, v144
v_sub_f32_e32 v148, v148, v236
v_mfma_f32_16x16x32_bf16 v[112:115], a[16:19], v[0:3], 0
ds_read_b64_tr_b16 a[8:9], v203 offset:9216
ds_read_b64_tr_b16 a[10:11], v203 offset:13312
v_add_f32_e32 v244, v244, v144
v_exp_f32_e32 v145, v145
)"
R"(
v_exp_f32_e32 v146, v146
v_add_f32_e32 v244, v244, v145
v_mfma_f32_16x16x32_bf16 v[116:119], a[20:23], v[0:3], 0
s_waitcnt vmcnt(22)
s_mov_b32 m0, 0x1e000
s_mov_b32 s78, 0x26000
buffer_load_dwordx4 v12, s[48:51], s78 offen nt lds
ds_read_b64_tr_b16 a[12:13], v204 offset:9216
ds_read_b64_tr_b16 a[14:15], v204 offset:13312
s_waitcnt lgkmcnt(8)
v_mfma_f32_16x16x32_bf16 v[64:67], a[24:27], v[0:3], 0
ds_read_b64_tr_b16 a[16:17], v203 offset:9728
ds_read_b64_tr_b16 a[18:19], v203 offset:13824
buffer_load_dwordx4 v13, s[48:51], s78 offen offset:1024 nt lds
v_cvt_pk_bf16_f32 v176, v144, v145
v_add_f32_e32 v244, v244, v146
v_exp_f32_e32 v147, v147
v_mfma_f32_16x16x32_bf16 v[68:71], a[28:31], v[0:3], 0
s_waitcnt vmcnt(22)
buffer_load_dwordx4 v14, s[48:51], s78 offen offset:2048 nt lds
ds_read_b64_tr_b16 a[20:21], v204 offset:9728
ds_read_b64_tr_b16 a[22:23], v204 offset:13824
v_add_f32_e32 v244, v244, v147
v_cvt_pk_bf16_f32 v177, v146, v147
v_sub_f32_e32 v149, v149, v236
s_waitcnt lgkmcnt(8)
v_mfma_f32_16x16x32_bf16 v[80:83], a[0:3], v[16:19], v[80:83]
ds_read_b64_tr_b16 a[24:25], v203 offset:8192
ds_read_b64_tr_b16 a[26:27], v203 offset:12288
buffer_load_dwordx4 v15, s[48:51], s78 offen offset:3072 nt lds
v_sub_f32_e32 v150, v150, v236
v_sub_f32_e32 v151, v151, v236
v_exp_f32_e32 v148, v148
v_mfma_f32_16x16x32_bf16 v[84:87], a[4:7], v[16:19], v[84:87]
s_waitcnt vmcnt(60)
s_barrier
ds_read_b64_tr_b16 a[28:29], v204 offset:8192
ds_read_b64_tr_b16 a[30:31], v204 offset:12288
v_add_f32_e32 v244, v244, v148
v_exp_f32_e32 v149, v149
s_waitcnt lgkmcnt(8)
v_mfma_f32_16x16x32_bf16 v[96:99], a[8:11], v[16:19], v[96:99]
s_waitcnt vmcnt(22)
s_mov_b32 m0, 0x1f000
s_mov_b32 s78, 0x27000
buffer_load_dwordx4 v12, s[48:51], s78 offen nt lds
ds_read_b128 a[0:3], v200 offset:49152
v_add_f32_e32 v244, v244, v149
v_mfma_f32_16x16x32_bf16 v[100:103], a[12:15], v[16:19], v[100:103]
s_waitcnt lgkmcnt(5)
ds_read_b128 a[4:7], v200 offset:49664
buffer_load_dwordx4 v13, s[48:51], s78 offen offset:1024 nt lds
v_cvt_pk_bf16_f32 v178, v148, v149
v_exp_f32_e32 v150, v150
v_exp_f32_e32 v151, v151
v_mfma_f32_16x16x32_bf16 v[112:115], a[16:19], v[16:19], v[112:115]
s_waitcnt vmcnt(22)
buffer_load_dwordx4 v14, s[48:51], s78 offen offset:2048 nt lds
ds_read_b128 a[8:11], v200 offset:50176
v_add_f32_e32 v244, v244, v150
v_add_f32_e32 v244, v244, v151
v_cvt_pk_bf16_f32 v179, v150, v151
v_mfma_f32_16x16x32_bf16 v[116:119], a[20:23], v[16:19], v[116:119]
s_waitcnt lgkmcnt(3)
ds_read_b128 a[12:15], v200 offset:50688
buffer_load_dwordx4 v15, s[48:51], s78 offen offset:3072 nt lds
v_accvgpr_write_b32 a33, v1
v_accvgpr_write_b32 a34, v2
v_accvgpr_write_b32 a35, v3
v_accvgpr_write_b32 a40, v16
v_accvgpr_write_b32 a41, v17
v_mfma_f32_16x16x32_bf16 v[64:67], a[24:27], v[16:19], v[64:67]
ds_read_b128 a[16:19], v200 offset:53248
v_accvgpr_write_b32 a42, v18
v_accvgpr_write_b32 a43, v19
v_mul_f32_e32 v32, v240, v32
v_mul_f32_e32 v33, v240, v33
v_mul_f32_e32 v34, v240, v34
v_mul_f32_e32 v35, v240, v35
v_mul_f32_e32 v36, v240, v36
v_mfma_f32_16x16x32_bf16 v[68:71], a[28:31], v[16:19], v[68:71]
s_waitcnt lgkmcnt(3)
ds_read_b128 a[20:23], v200 offset:53760
v_mul_f32_e32 v37, v240, v37
v_mul_f32_e32 v38, v240, v38
v_mul_f32_e32 v39, v240, v39
v_cvt_pk_bf16_f32 v32, v32, v33
v_cvt_pk_bf16_f32 v33, v34, v35
v_cvt_pk_bf16_f32 v34, v36, v37
v_cvt_pk_bf16_f32 v35, v38, v39
v_mfma_f32_16x16x32_bf16 v[128:131], a[0:3], a[64:67], v[192:195]
ds_read_b128 a[24:27], v200 offset:54272
v_accvgpr_write_b32 a48, v32
v_accvgpr_write_b32 a49, v33
v_accvgpr_write_b32 a50, v34
v_accvgpr_write_b32 a51, v35
v_mul_f32_e32 v48, v240, v48
v_mul_f32_e32 v49, v240, v49
v_mul_f32_e32 v50, v240, v50
v_mfma_f32_16x16x32_bf16 v[128:131], a[4:7], a[72:75], v[128:131]
s_waitcnt vmcnt(22)
s_mov_b32 m0, 0x10000
s_mov_b32 s78, 0x2c000
buffer_load_dwordx4 v8, s[44:47], s78 offen nt lds
ds_read_b128 a[28:31], v200 offset:54784
v_mul_f32_e32 v51, v240, v51
v_mul_f32_e32 v52, v240, v52
s_waitcnt lgkmcnt(4)
v_mfma_f32_16x16x32_bf16 v[128:131], a[8:11], a[80:83], v[128:131]
ds_read_b128 a[0:3], v200 offset:57344
buffer_load_dwordx4 v9, s[44:47], s78 offen offset:1024 nt lds
v_mul_f32_e32 v53, v240, v53
v_mul_f32_e32 v54, v240, v54
v_mul_f32_e32 v55, v240, v55
v_cvt_pk_bf16_f32 v48, v48, v49
v_mfma_f32_16x16x32_bf16 v[128:131], a[12:15], a[88:91], v[128:131]
s_waitcnt vmcnt(22)
buffer_load_dwordx4 v10, s[44:47], s78 offen offset:2048 nt lds
ds_read_b128 a[4:7], v200 offset:57856
v_cvt_pk_bf16_f32 v49, v50, v51
v_cvt_pk_bf16_f32 v50, v52, v53
v_cvt_pk_bf16_f32 v51, v54, v55
v_accvgpr_write_b32 a56, v48
s_waitcnt lgkmcnt(4)
v_mfma_f32_16x16x32_bf16 v[132:135], a[16:19], a[64:67], v[192:195]
ds_read_b128 a[8:11], v200 offset:58368
buffer_load_dwordx4 v11, s[44:47], s78 offen offset:3072 nt lds
v_accvgpr_write_b32 a57, v49
v_accvgpr_write_b32 a58, v50
v_accvgpr_write_b32 a59, v51
v_sub_f32_e32 v160, v160, v236
v_sub_f32_e32 v161, v161, v236
v_mfma_f32_16x16x32_bf16 v[132:135], a[20:23], a[72:75], v[132:135]
s_waitcnt vmcnt(22)
s_mov_b32 m0, 0x11000
s_mov_b32 s78, 0x2d000
buffer_load_dwordx4 v8, s[44:47], s78 offen nt lds
ds_read_b128 a[12:15], v200 offset:58880
v_sub_f32_e32 v162, v162, v236
s_waitcnt lgkmcnt(4)
v_mfma_f32_16x16x32_bf16 v[132:135], a[24:27], a[80:83], v[132:135]
ds_read_b128 a[16:19], v200 offset:61440
buffer_load_dwordx4 v9, s[44:47], s78 offen offset:1024 nt lds
v_sub_f32_e32 v163, v163, v236
v_sub_f32_e32 v164, v164, v236
v_sub_f32_e32 v165, v165, v236
v_sub_f32_e32 v166, v166, v236
v_sub_f32_e32 v167, v167, v236
v_mfma_f32_16x16x32_bf16 v[132:135], a[28:31], a[88:91], v[132:135]
s_waitcnt vmcnt(22)
buffer_load_dwordx4 v10, s[44:47], s78 offen offset:2048 nt lds
ds_read_b128 a[20:23], v200 offset:61952
v_exp_f32_e32 v160, v160
v_exp_f32_e32 v161, v161
s_waitcnt lgkmcnt(4)
v_mfma_f32_16x16x32_bf16 v[144:147], a[0:3], a[64:67], v[192:195]
ds_read_b128 a[24:27], v200 offset:62464
buffer_load_dwordx4 v11, s[44:47], s78 offen offset:3072 nt lds
v_cvt_pk_bf16_f32 v184, v160, v161
v_exp_f32_e32 v162, v162
v_add_f32_e32 v244, v244, v160
v_mfma_f32_16x16x32_bf16 v[144:147], a[4:7], a[72:75], v[144:147]
s_waitcnt vmcnt(22)
s_mov_b32 m0, 0x12000
s_mov_b32 s78, 0x2e000
buffer_load_dwordx4 v8, s[44:47], s78 offen nt lds
s_waitcnt vmcnt(41)
s_barrier
ds_read_b128 a[28:31], v200 offset:62976
s_waitcnt lgkmcnt(4)
v_mfma_f32_16x16x32_bf16 v[144:147], a[8:11], a[80:83], v[144:147]
ds_read_b64_tr_b16 a[0:1], v203 offset:32768
ds_read_b64_tr_b16 a[2:3], v203 offset:36864
buffer_load_dwordx4 v9, s[44:47], s78 offen offset:1024 nt lds
v_exp_f32_e32 v163, v163
v_exp_f32_e32 v164, v164
v_mfma_f32_16x16x32_bf16 v[144:147], a[12:15], a[88:91], v[144:147]
s_waitcnt vmcnt(22)
buffer_load_dwordx4 v10, s[44:47], s78 offen offset:2048 nt lds
ds_read_b64_tr_b16 a[4:5], v204 offset:32768
ds_read_b64_tr_b16 a[6:7], v204 offset:36864
v_cvt_pk_bf16_f32 v185, v162, v163
v_exp_f32_e32 v165, v165
s_waitcnt lgkmcnt(6)
v_mfma_f32_16x16x32_bf16 v[148:151], a[16:19], a[64:67], v[192:195]
ds_read_b64_tr_b16 a[8:9], v203 offset:33280
ds_read_b64_tr_b16 a[10:11], v203 offset:37376
buffer_load_dwordx4 v11, s[44:47], s78 offen offset:3072 nt lds
v_cvt_pk_bf16_f32 v186, v164, v165
v_exp_f32_e32 v166, v166
v_mfma_f32_16x16x32_bf16 v[148:151], a[20:23], a[72:75], v[148:151]
s_waitcnt vmcnt(22)
s_mov_b32 m0, 0x13000
s_mov_b32 s78, 0x2f000
buffer_load_dwordx4 v8, s[44:47], s78 offen nt lds
ds_read_b64_tr_b16 a[12:13], v204 offset:33280
ds_read_b64_tr_b16 a[14:15], v204 offset:37376
s_waitcnt lgkmcnt(8)
v_mfma_f32_16x16x32_bf16 v[148:151], a[24:27], a[80:83], v[148:151]
ds_read_b64_tr_b16 a[16:17], v203 offset:33792
)"
R"(
ds_read_b64_tr_b16 a[18:19], v203 offset:37888
buffer_load_dwordx4 v9, s[44:47], s78 offen offset:1024 nt lds
v_exp_f32_e32 v167, v167
v_add_f32_e32 v244, v244, v161
v_cvt_pk_bf16_f32 v187, v166, v167
v_mfma_f32_16x16x32_bf16 v[148:151], a[28:31], a[88:91], v[148:151]
s_waitcnt vmcnt(22)
buffer_load_dwordx4 v10, s[44:47], s78 offen offset:2048 nt lds
ds_read_b64_tr_b16 a[20:21], v204 offset:33792
ds_read_b64_tr_b16 a[22:23], v204 offset:37888
v_add_f32_e32 v244, v244, v162
v_add_f32_e32 v244, v244, v163
s_waitcnt lgkmcnt(8)
v_mfma_f32_16x16x32_bf16 v[0:3], a[0:3], v[176:179], 0
ds_read_b64_tr_b16 a[24:25], v203 offset:34304
ds_read_b64_tr_b16 a[26:27], v203 offset:38400
buffer_load_dwordx4 v11, s[44:47], s78 offen offset:3072 nt lds
v_add_f32_e32 v244, v244, v164
v_add_f32_e32 v244, v244, v165
v_add_f32_e32 v244, v244, v166
v_add_f32_e32 v244, v244, v167
v_mfma_f32_16x16x32_bf16 v[4:7], a[4:7], v[176:179], 0
s_waitcnt lgkmcnt(6)
ds_read_b64_tr_b16 a[28:29], v204 offset:34304
ds_read_b64_tr_b16 a[30:31], v204 offset:38400
v_max3_f32 v236, v128, v129, v130
v_max3_f32 v236, v236, v131, v132
v_max3_f32 v236, v236, v133, v134
v_max3_f32 v236, v236, v135, v144
v_max3_f32 v236, v236, v145, v146
v_mfma_f32_16x16x32_bf16 v[16:19], a[8:11], v[176:179], 0
s_waitcnt vmcnt(22)
s_mov_b32 m0, 0x18000
s_mov_b32 s78, 0x28000
buffer_load_dwordx4 v12, s[48:51], s78 offen nt lds
ds_read_b64_tr_b16 a[0:1], v203 offset:40960
ds_read_b64_tr_b16 a[2:3], v203 offset:45056
buffer_load_dwordx4 v13, s[48:51], s78 offen offset:1024 nt lds
v_mfma_f32_16x16x32_bf16 v[20:23], a[12:15], v[176:179], 0
s_waitcnt vmcnt(22)
buffer_load_dwordx4 v14, s[48:51], s78 offen offset:2048 nt lds
ds_read_b64_tr_b16 a[4:5], v204 offset:40960
ds_read_b64_tr_b16 a[6:7], v204 offset:45056
v_max3_f32 v236, v236, v147, v148
v_max3_f32 v236, v236, v149, v150
s_waitcnt lgkmcnt(8)
v_mfma_f32_16x16x32_bf16 v[32:35], a[16:19], v[176:179], 0
ds_read_b64_tr_b16 a[8:9], v203 offset:41472
ds_read_b64_tr_b16 a[10:11], v203 offset:45568
buffer_load_dwordx4 v15, s[48:51], s78 offen offset:3072 nt lds
s_waitcnt vmcnt(22)
s_mov_b32 m0, 0x19000
s_mov_b32 s78, 0x29000
buffer_load_dwordx4 v12, s[48:51], s78 offen nt lds
v_mfma_f32_16x16x32_bf16 v[36:39], a[20:23], v[176:179], 0
s_waitcnt lgkmcnt(6)
ds_read_b64_tr_b16 a[12:13], v204 offset:41472
ds_read_b64_tr_b16 a[14:15], v204 offset:45568
buffer_load_dwordx4 v13, s[48:51], s78 offen offset:1024 nt lds
v_max_f32_e32 v248, v236, v151
v_cmp_lt_f32_e32 vcc, s70, v248
v_mfma_f32_16x16x32_bf16 v[48:51], a[24:27], v[176:179], 0
s_waitcnt vmcnt(22)
buffer_load_dwordx4 v14, s[48:51], s78 offen offset:2048 nt lds
ds_read_b64_tr_b16 a[16:17], v203 offset:41984
ds_read_b64_tr_b16 a[18:19], v203 offset:46080
buffer_load_dwordx4 v15, s[48:51], s78 offen offset:3072 nt lds
v_mfma_f32_16x16x32_bf16 v[52:55], a[28:31], v[176:179], 0
s_waitcnt vmcnt(22)
s_mov_b32 m0, 0x1a000
s_mov_b32 s78, 0x2a000
buffer_load_dwordx4 v12, s[48:51], s78 offen nt lds
ds_read_b64_tr_b16 a[20:21], v204 offset:41984
ds_read_b64_tr_b16 a[22:23], v204 offset:46080
s_waitcnt lgkmcnt(8)
v_mfma_f32_16x16x32_bf16 v[0:3], a[0:3], v[184:187], v[0:3]
ds_read_b64_tr_b16 a[24:25], v203 offset:42496
ds_read_b64_tr_b16 a[26:27], v203 offset:46592
buffer_load_dwordx4 v13, s[48:51], s78 offen offset:1024 nt lds
s_waitcnt vmcnt(22)
buffer_load_dwordx4 v14, s[48:51], s78 offen offset:2048 nt lds
v_mfma_f32_16x16x32_bf16 v[4:7], a[4:7], v[184:187], v[4:7]
buffer_load_dwordx4 v15, s[48:51], s78 offen offset:3072 nt lds
s_waitcnt vmcnt(44)
s_barrier
ds_read_b64_tr_b16 a[28:29], v204 offset:42496
ds_read_b64_tr_b16 a[30:31], v204 offset:46592
s_waitcnt lgkmcnt(8)
v_mfma_f32_16x16x32_bf16 v[16:19], a[8:11], v[184:187], v[16:19]
s_waitcnt vmcnt(22)
s_mov_b32 m0, 0x1b000
s_mov_b32 s78, 0x2b000
buffer_load_dwordx4 v12, s[48:51], s78 offen nt lds
ds_read_b128 a[0:3], v201 offset:16384
buffer_load_dwordx4 v13, s[48:51], s78 offen offset:1024 nt lds
v_mfma_f32_16x16x32_bf16 v[20:23], a[12:15], v[184:187], v[20:23]
s_waitcnt vmcnt(22)
buffer_load_dwordx4 v14, s[48:51], s78 offen offset:2048 nt lds
s_cbranch_vccnz .Lk_bra9
.Lk_bba9:
ds_read_b128 a[4:7], v201 offset:16896
s_waitcnt lgkmcnt(6)
v_mfma_f32_16x16x32_bf16 v[32:35], a[16:19], v[184:187], v[32:35]
ds_read_b128 a[8:11], v201 offset:17408
buffer_load_dwordx4 v15, s[48:51], s78 offen offset:3072 nt lds
v_exp_f32_e32 v128, v128
v_exp_f32_e32 v129, v129
v_add_f32_e32 v244, v244, v128
v_mfma_f32_16x16x32_bf16 v[36:39], a[20:23], v[184:187], v[36:39]
s_mov_b32 m0, 0x20000
s_xor_b32 s78, s80, 0x10000
buffer_load_dwordx4 v40, s[52:55], s78 offen lds
ds_read_b128 a[12:15], v201 offset:17920
buffer_load_dwordx4 v41, s[52:55], s78 offen offset:1024 lds
s_waitcnt lgkmcnt(4)
v_mfma_f32_16x16x32_bf16 v[48:51], a[24:27], v[184:187], v[48:51]
ds_read_b128 a[16:19], v201 offset:20480
buffer_load_dwordx4 v42, s[52:55], s78 offen offset:2048 lds
v_add_f32_e32 v244, v244, v129
v_cvt_pk_bf16_f32 v176, v128, v129
v_exp_f32_e32 v130, v130
s_nop 0
v_add_f32_e32 v244, v244, v130
v_mfma_f32_16x16x32_bf16 v[52:55], a[28:31], v[184:187], v[52:55]
s_waitcnt lgkmcnt(3)
ds_read_b128 a[20:23], v201 offset:20992
buffer_load_dwordx4 v43, s[52:55], s78 offen offset:3072 lds
s_mov_b32 m0, 0x21000
s_xor_b32 s78, s80, 0x14000
buffer_load_dwordx4 v40, s[52:55], s78 offen lds
v_mfma_f32_16x16x32_bf16 v[160:163], a[0:3], a[64:67], v[192:195]
ds_read_b128 a[24:27], v201 offset:21504
buffer_load_dwordx4 v41, s[52:55], s78 offen offset:1024 lds
v_exp_f32_e32 v131, v131
v_exp_f32_e32 v132, v132
v_mfma_f32_16x16x32_bf16 v[160:163], a[4:7], a[72:75], v[160:163]
s_waitcnt lgkmcnt(3)
ds_read_b128 a[28:31], v201 offset:22016
buffer_load_dwordx4 v42, s[52:55], s78 offen offset:2048 lds
buffer_load_dwordx4 v43, s[52:55], s78 offen offset:3072 lds
v_add_f32_e32 v244, v244, v131
v_cvt_pk_bf16_f32 v177, v130, v131
v_add_f32_e32 v244, v244, v132
v_mfma_f32_16x16x32_bf16 v[160:163], a[8:11], a[80:83], v[160:163]
s_mov_b32 m0, 0x22000
s_xor_b32 s78, s80, 0x18000
buffer_load_dwordx4 v40, s[52:55], s78 offen lds
ds_read_b128 a[0:3], v201 offset:24576
v_exp_f32_e32 v133, v133
v_mfma_f32_16x16x32_bf16 v[160:163], a[12:15], a[88:91], v[160:163]
s_waitcnt lgkmcnt(3)
ds_read_b128 a[4:7], v201 offset:25088
buffer_load_dwordx4 v41, s[52:55], s78 offen offset:1024 lds
buffer_load_dwordx4 v42, s[52:55], s78 offen offset:2048 lds
v_add_f32_e32 v244, v244, v133
v_cvt_pk_bf16_f32 v178, v132, v133
v_mfma_f32_16x16x32_bf16 v[164:167], a[16:19], a[64:67], v[192:195]
ds_read_b128 a[8:11], v201 offset:25600
buffer_load_dwordx4 v43, s[52:55], s78 offen offset:3072 lds
v_exp_f32_e32 v134, v134
v_exp_f32_e32 v135, v135
v_add_f32_e32 v244, v244, v134
v_mfma_f32_16x16x32_bf16 v[164:167], a[20:23], a[72:75], v[164:167]
s_mov_b32 m0, 0x23000
s_xor_b32 s78, s80, 0x1c000
buffer_load_dwordx4 v40, s[52:55], s78 offen lds
ds_read_b128 a[12:15], v201 offset:26112
buffer_load_dwordx4 v41, s[52:55], s78 offen offset:1024 lds
s_waitcnt lgkmcnt(4)
v_mfma_f32_16x16x32_bf16 v[164:167], a[24:27], a[80:83], v[164:167]
s_cbranch_vccnz .Lk_brb9
.Lk_bbb9:
ds_read_b128 a[16:19], v201 offset:28672
buffer_load_dwordx4 v42, s[52:55], s78 offen offset:2048 lds
v_add_f32_e32 v244, v244, v135
v_cvt_pk_bf16_f32 v179, v134, v135
v_mfma_f32_16x16x32_bf16 v[164:167], a[28:31], a[88:91], v[164:167]
s_waitcnt lgkmcnt(3)
ds_read_b128 a[20:23], v201 offset:29184
buffer_load_dwordx4 v43, s[52:55], s78 offen offset:3072 lds
v_exp_f32_e32 v144, v144
v_exp_f32_e32 v145, v145
v_mfma_f32_16x16x32_bf16 v[128:131], a[0:3], a[64:67], v[192:195]
s_waitcnt vmcnt(38)
s_mov_b32 m0, 0x24000
s_mov_b32 s78, 0x30000
buffer_load_dwordx4 v8, s[44:47], s78 offen nt lds
ds_read_b128 a[24:27], v201 offset:29696
buffer_load_dwordx4 v9, s[44:47], s78 offen offset:1024 nt lds
v_mfma_f32_16x16x32_bf16 v[128:131], a[4:7], a[72:75], v[128:131]
s_waitcnt vmcnt(38)
buffer_load_dwordx4 v10, s[44:47], s78 offen offset:2048 nt lds
s_waitcnt vmcnt(51)
s_barrier
ds_read_b128 a[28:31], v201 offset:30208
s_waitcnt lgkmcnt(4)
v_mfma_f32_16x16x32_bf16 v[128:131], a[8:11], a[80:83], v[128:131]
ds_read_b64_tr_b16 a[0:1], v205 offset:49152
ds_read_b64_tr_b16 a[2:3], v205 offset:53248
buffer_load_dwordx4 v11, s[44:47], s78 offen offset:3072 nt lds
)"
R"(
s_waitcnt vmcnt(38)
s_mov_b32 m0, 0x25000
s_mov_b32 s78, 0x31000
buffer_load_dwordx4 v8, s[44:47], s78 offen nt lds
v_mfma_f32_16x16x32_bf16 v[128:131], a[12:15], a[88:91], v[128:131]
s_waitcnt lgkmcnt(4)
ds_read_b64_tr_b16 a[4:5], v206 offset:49152
ds_read_b64_tr_b16 a[6:7], v206 offset:53248
buffer_load_dwordx4 v9, s[44:47], s78 offen offset:1024 nt lds
v_cvt_pk_bf16_f32 v184, v144, v145
v_mfma_f32_16x16x32_bf16 v[132:135], a[16:19], a[64:67], v[192:195]
s_waitcnt vmcnt(38)
buffer_load_dwordx4 v10, s[44:47], s78 offen offset:2048 nt lds
ds_read_b64_tr_b16 a[8:9], v205 offset:49664
ds_read_b64_tr_b16 a[10:11], v205 offset:53760
buffer_load_dwordx4 v11, s[44:47], s78 offen offset:3072 nt lds
v_add_f32_e32 v244, v244, v144
v_mfma_f32_16x16x32_bf16 v[132:135], a[20:23], a[72:75], v[132:135]
s_waitcnt vmcnt(38)
s_mov_b32 m0, 0x26000
s_mov_b32 s78, 0x32000
buffer_load_dwordx4 v8, s[44:47], s78 offen nt lds
ds_read_b64_tr_b16 a[12:13], v206 offset:49664
ds_read_b64_tr_b16 a[14:15], v206 offset:53760
s_waitcnt lgkmcnt(8)
v_mfma_f32_16x16x32_bf16 v[132:135], a[24:27], a[80:83], v[132:135]
ds_read_b64_tr_b16 a[16:17], v205 offset:50176
ds_read_b64_tr_b16 a[18:19], v205 offset:54272
buffer_load_dwordx4 v9, s[44:47], s78 offen offset:1024 nt lds
s_waitcnt vmcnt(38)
buffer_load_dwordx4 v10, s[44:47], s78 offen offset:2048 nt lds
v_mfma_f32_16x16x32_bf16 v[132:135], a[28:31], a[88:91], v[132:135]
s_waitcnt lgkmcnt(6)
ds_read_b64_tr_b16 a[20:21], v206 offset:50176
ds_read_b64_tr_b16 a[22:23], v206 offset:54272
buffer_load_dwordx4 v11, s[44:47], s78 offen offset:3072 nt lds
v_exp_f32_e32 v146, v146
v_exp_f32_e32 v147, v147
v_mfma_f32_16x16x32_bf16 v[0:3], a[0:3], v[176:179], v[0:3]
s_waitcnt vmcnt(38)
s_mov_b32 m0, 0x27000
s_mov_b32 s78, 0x33000
buffer_load_dwordx4 v8, s[44:47], s78 offen nt lds
ds_read_b64_tr_b16 a[24:25], v205 offset:50688
ds_read_b64_tr_b16 a[26:27], v205 offset:54784
v_cvt_pk_bf16_f32 v185, v146, v147
v_mfma_f32_16x16x32_bf16 v[4:7], a[4:7], v[176:179], v[4:7]
buffer_load_dwordx4 v9, s[44:47], s78 offen offset:1024 nt lds
s_waitcnt vmcnt(38)
buffer_load_dwordx4 v10, s[44:47], s78 offen offset:2048 nt lds
ds_read_b64_tr_b16 a[28:29], v206 offset:50688
ds_read_b64_tr_b16 a[30:31], v206 offset:54784
s_waitcnt lgkmcnt(8)
v_mfma_f32_16x16x32_bf16 v[16:19], a[8:11], v[176:179], v[16:19]
ds_read_b64_tr_b16 a[0:1], v205 offset:57344
ds_read_b64_tr_b16 a[2:3], v205 offset:61440
buffer_load_dwordx4 v11, s[44:47], s78 offen offset:3072 nt lds
v_exp_f32_e32 v148, v148
v_exp_f32_e32 v149, v149
v_mfma_f32_16x16x32_bf16 v[20:23], a[12:15], v[176:179], v[20:23]
s_mov_b32 m0, 0x4000
s_xor_b32 s78, s80, 0x20000
buffer_load_dwordx4 v40, s[52:55], s78 offen lds
ds_read_b64_tr_b16 a[4:5], v206 offset:57344
ds_read_b64_tr_b16 a[6:7], v206 offset:61440
v_cvt_pk_bf16_f32 v186, v148, v149
s_waitcnt lgkmcnt(8)
v_mfma_f32_16x16x32_bf16 v[32:35], a[16:19], v[176:179], v[32:35]
buffer_load_dwordx4 v41, s[52:55], s78 offen offset:1024 lds
ds_read_b64_tr_b16 a[8:9], v205 offset:57856
ds_read_b64_tr_b16 a[10:11], v205 offset:61952
buffer_load_dwordx4 v42, s[52:55], s78 offen offset:2048 lds
v_exp_f32_e32 v150, v150
v_mfma_f32_16x16x32_bf16 v[36:39], a[20:23], v[176:179], v[36:39]
s_waitcnt lgkmcnt(6)
ds_read_b64_tr_b16 a[12:13], v206 offset:57856
ds_read_b64_tr_b16 a[14:15], v206 offset:61952
buffer_load_dwordx4 v43, s[52:55], s78 offen offset:3072 lds
s_mov_b32 m0, 0x5000
s_xor_b32 s78, s80, 0x24000
buffer_load_dwordx4 v40, s[52:55], s78 offen lds
v_mfma_f32_16x16x32_bf16 v[48:51], a[24:27], v[176:179], v[48:51]
ds_read_b64_tr_b16 a[16:17], v205 offset:58368
ds_read_b64_tr_b16 a[18:19], v205 offset:62464
buffer_load_dwordx4 v41, s[52:55], s78 offen offset:1024 lds
v_exp_f32_e32 v151, v151
v_mfma_f32_16x16x32_bf16 v[52:55], a[28:31], v[176:179], v[52:55]
s_waitcnt lgkmcnt(6)
ds_read_b64_tr_b16 a[20:21], v206 offset:58368
ds_read_b64_tr_b16 a[22:23], v206 offset:62464
buffer_load_dwordx4 v42, s[52:55], s78 offen offset:2048 lds
buffer_load_dwordx4 v43, s[52:55], s78 offen offset:3072 lds
v_cvt_pk_bf16_f32 v187, v150, v151
s_nop 1
v_mfma_f32_16x16x32_bf16 v[0:3], a[0:3], v[184:187], v[0:3]
s_mov_b32 m0, 0x6000
s_xor_b32 s78, s80, 0x28000
buffer_load_dwordx4 v40, s[52:55], s78 offen lds
ds_read_b64_tr_b16 a[24:25], v205 offset:58880
ds_read_b64_tr_b16 a[26:27], v205 offset:62976
v_add_f32_e32 v244, v244, v145
v_mfma_f32_16x16x32_bf16 v[4:7], a[4:7], v[184:187], v[4:7]
buffer_load_dwordx4 v41, s[52:55], s78 offen offset:1024 lds
buffer_load_dwordx4 v42, s[52:55], s78 offen offset:2048 lds
s_waitcnt vmcnt(27)
s_barrier
ds_read_b64_tr_b16 a[28:29], v206 offset:58880
ds_read_b64_tr_b16 a[30:31], v206 offset:62976
s_waitcnt lgkmcnt(8)
v_mfma_f32_16x16x32_bf16 v[16:19], a[8:11], v[184:187], v[16:19]
ds_read_b64_tr_b16 a[0:1], v207 offset:512
ds_read_b64_tr_b16 a[2:3], v207 offset:4608
buffer_load_dwordx4 v43, s[52:55], s78 offen offset:3072 lds
v_add_f32_e32 v244, v244, v146
v_add_f32_e32 v244, v244, v147
v_add_f32_e32 v244, v244, v148
v_add_f32_e32 v244, v244, v149
v_mfma_f32_16x16x32_bf16 v[20:23], a[12:15], v[184:187], v[20:23]
s_mov_b32 m0, 0x7000
s_xor_b32 s78, s80, 0x2c000
buffer_load_dwordx4 v40, s[52:55], s78 offen lds
ds_read_b64_tr_b16 a[4:5], v208 offset:512
ds_read_b64_tr_b16 a[6:7], v208 offset:4608
v_add_f32_e32 v244, v244, v150
s_waitcnt lgkmcnt(8)
v_mfma_f32_16x16x32_bf16 v[32:35], a[16:19], v[184:187], v[32:35]
buffer_load_dwordx4 v41, s[52:55], s78 offen offset:1024 lds
ds_read_b64_tr_b16 a[8:9], v207 offset:1024
ds_read_b64_tr_b16 a[10:11], v207 offset:5120
buffer_load_dwordx4 v42, s[52:55], s78 offen offset:2048 lds
v_add_f32_e32 v244, v244, v151
v_mfma_f32_16x16x32_bf16 v[36:39], a[20:23], v[184:187], v[36:39]
s_waitcnt lgkmcnt(6)
ds_read_b64_tr_b16 a[12:13], v208 offset:1024
ds_read_b64_tr_b16 a[14:15], v208 offset:5120
buffer_load_dwordx4 v43, s[52:55], s78 offen offset:3072 lds
v_max3_f32 v236, v160, v161, v162
v_max3_f32 v236, v236, v163, v164
v_max3_f32 v236, v236, v165, v166
v_mfma_f32_16x16x32_bf16 v[48:51], a[24:27], v[184:187], v[48:51]
s_waitcnt vmcnt(54)
s_mov_b32 m0, 0
s_mov_b32 s78, 0x2c000
buffer_load_dwordx4 v12, s[48:51], s78 offen nt lds
ds_read_b64_tr_b16 a[16:17], v207 offset:1536
ds_read_b64_tr_b16 a[18:19], v207 offset:5632
v_max3_f32 v236, v236, v167, v128
v_mfma_f32_16x16x32_bf16 v[52:55], a[28:31], v[184:187], v[52:55]
buffer_load_dwordx4 v13, s[48:51], s78 offen offset:1024 nt lds
s_waitcnt vmcnt(54)
buffer_load_dwordx4 v14, s[48:51], s78 offen offset:2048 nt lds
ds_read_b64_tr_b16 a[20:21], v208 offset:1536
ds_read_b64_tr_b16 a[22:23], v208 offset:5632
s_waitcnt lgkmcnt(8)
v_mfma_f32_16x16x32_bf16 v[80:83], a[0:3], a[48:51], v[80:83]
ds_read_b64_tr_b16 a[24:25], v207
ds_read_b64_tr_b16 a[26:27], v207 offset:4096
buffer_load_dwordx4 v15, s[48:51], s78 offen offset:3072 nt lds
s_waitcnt vmcnt(54)
s_mov_b32 m0, 0x1000
s_mov_b32 s78, 0x2d000
buffer_load_dwordx4 v12, s[48:51], s78 offen nt lds
v_mfma_f32_16x16x32_bf16 v[84:87], a[4:7], a[48:51], v[84:87]
s_waitcnt lgkmcnt(6)
ds_read_b64_tr_b16 a[28:29], v208
ds_read_b64_tr_b16 a[30:31], v208 offset:4096
buffer_load_dwordx4 v13, s[48:51], s78 offen offset:1024 nt lds
v_max3_f32 v236, v236, v129, v130
v_mfma_f32_16x16x32_bf16 v[96:99], a[8:11], a[48:51], v[96:99]
s_waitcnt vmcnt(54)
buffer_load_dwordx4 v14, s[48:51], s78 offen offset:2048 nt lds
ds_read_b64_tr_b16 a[0:1], v207 offset:8704
ds_read_b64_tr_b16 a[2:3], v207 offset:12800
buffer_load_dwordx4 v15, s[48:51], s78 offen offset:3072 nt lds
v_mfma_f32_16x16x32_bf16 v[100:103], a[12:15], a[48:51], v[100:103]
s_waitcnt vmcnt(38)
s_mov_b32 m0, 0x2000
s_mov_b32 s78, 0x2e000
buffer_load_dwordx4 v12, s[48:51], s78 offen nt lds
ds_read_b64_tr_b16 a[4:5], v208 offset:8704
ds_read_b64_tr_b16 a[6:7], v208 offset:12800
v_max3_f32 v236, v236, v131, v132
s_waitcnt lgkmcnt(8)
v_mfma_f32_16x16x32_bf16 v[112:115], a[16:19], a[48:51], v[112:115]
ds_read_b64_tr_b16 a[8:9], v207 offset:9216
ds_read_b64_tr_b16 a[10:11], v207 offset:13312
buffer_load_dwordx4 v13, s[48:51], s78 offen offset:1024 nt lds
s_waitcnt vmcnt(38)
buffer_load_dwordx4 v14, s[48:51], s78 offen offset:2048 nt lds
v_mfma_f32_16x16x32_bf16 v[116:119], a[20:23], a[48:51], v[116:119]
s_waitcnt lgkmcnt(6)
ds_read_b64_tr_b16 a[12:13], v208 offset:9216
ds_read_b64_tr_b16 a[14:15], v208 offset:13312
buffer_load_dwordx4 v15, s[48:51], s78 offen offset:3072 nt lds
v_max3_f32 v236, v236, v133, v134
v_max_f32_e32 v248, v236, v135
v_cmp_lt_f32_e32 vcc, s70, v248
v_mfma_f32_16x16x32_bf16 v[64:67], a[24:27], a[48:51], v[64:67]
s_waitcnt vmcnt(38)
s_mov_b32 m0, 0x3000
)"
R"(
s_mov_b32 s78, 0x2f000
buffer_load_dwordx4 v12, s[48:51], s78 offen nt lds
ds_read_b64_tr_b16 a[16:17], v207 offset:9728
ds_read_b64_tr_b16 a[18:19], v207 offset:13824
v_mfma_f32_16x16x32_bf16 v[68:71], a[28:31], a[48:51], v[68:71]
buffer_load_dwordx4 v13, s[48:51], s78 offen offset:1024 nt lds
s_waitcnt vmcnt(38)
buffer_load_dwordx4 v14, s[48:51], s78 offen offset:2048 nt lds
ds_read_b64_tr_b16 a[20:21], v208 offset:9728
ds_read_b64_tr_b16 a[22:23], v208 offset:13824
s_waitcnt lgkmcnt(8)
v_mfma_f32_16x16x32_bf16 v[80:83], a[0:3], a[56:59], v[80:83]
s_cbranch_vccnz .Lk_bra10
.Lk_bba10:
ds_read_b64_tr_b16 a[24:25], v207 offset:8192
ds_read_b64_tr_b16 a[26:27], v207 offset:12288
buffer_load_dwordx4 v15, s[48:51], s78 offen offset:3072 nt lds
v_mfma_f32_16x16x32_bf16 v[84:87], a[4:7], a[56:59], v[84:87]
s_waitcnt vmcnt(38)
s_mov_b32 m0, 0xc000
s_mov_b32 s78, 0x34000
buffer_load_dwordx4 v8, s[44:47], s78 offen nt lds
s_waitcnt vmcnt(63)
s_barrier
ds_read_b64_tr_b16 a[28:29], v208 offset:8192
ds_read_b64_tr_b16 a[30:31], v208 offset:12288
s_waitcnt lgkmcnt(8)
v_mfma_f32_16x16x32_bf16 v[96:99], a[8:11], a[56:59], v[96:99]
ds_read_b128 a[0:3], v201
buffer_load_dwordx4 v9, s[44:47], s78 offen offset:1024 nt lds
v_exp_f32_e32 v160, v160
v_exp_f32_e32 v161, v161
v_add_f32_e32 v244, v244, v160
v_mfma_f32_16x16x32_bf16 v[100:103], a[12:15], a[56:59], v[100:103]
s_waitcnt vmcnt(38)
buffer_load_dwordx4 v10, s[44:47], s78 offen offset:2048 nt lds
ds_read_b128 a[4:7], v201 offset:512
v_add_f32_e32 v244, v244, v161
v_cvt_pk_bf16_f32 v176, v160, v161
s_waitcnt lgkmcnt(6)
v_mfma_f32_16x16x32_bf16 v[112:115], a[16:19], a[56:59], v[112:115]
ds_read_b128 a[8:11], v201 offset:1024
buffer_load_dwordx4 v11, s[44:47], s78 offen offset:3072 nt lds
v_exp_f32_e32 v162, v162
v_exp_f32_e32 v163, v163
v_add_f32_e32 v244, v244, v162
v_mfma_f32_16x16x32_bf16 v[116:119], a[20:23], a[56:59], v[116:119]
s_waitcnt vmcnt(38)
s_mov_b32 m0, 0xd000
s_mov_b32 s78, 0x35000
buffer_load_dwordx4 v8, s[44:47], s78 offen nt lds
ds_read_b128 a[12:15], v201 offset:1536
v_add_f32_e32 v244, v244, v163
v_cvt_pk_bf16_f32 v177, v162, v163
s_waitcnt lgkmcnt(4)
v_mfma_f32_16x16x32_bf16 v[64:67], a[24:27], a[56:59], v[64:67]
ds_read_b128 a[16:19], v201 offset:4096
buffer_load_dwordx4 v9, s[44:47], s78 offen offset:1024 nt lds
v_exp_f32_e32 v164, v164
v_exp_f32_e32 v165, v165
v_mfma_f32_16x16x32_bf16 v[68:71], a[28:31], a[56:59], v[68:71]
s_waitcnt vmcnt(38)
buffer_load_dwordx4 v10, s[44:47], s78 offen offset:2048 nt lds
ds_read_b128 a[20:23], v201 offset:4608
v_add_f32_e32 v244, v244, v164
v_add_f32_e32 v244, v244, v165
v_cvt_pk_bf16_f32 v178, v164, v165
s_waitcnt lgkmcnt(4)
v_mfma_f32_16x16x32_bf16 v[144:147], a[0:3], a[64:67], v[192:195]
ds_read_b128 a[24:27], v201 offset:5120
buffer_load_dwordx4 v11, s[44:47], s78 offen offset:3072 nt lds
v_exp_f32_e32 v166, v166
v_exp_f32_e32 v167, v167
v_mfma_f32_16x16x32_bf16 v[144:147], a[4:7], a[72:75], v[144:147]
s_waitcnt vmcnt(22)
s_mov_b32 m0, 0xe000
s_mov_b32 s78, 0x36000
buffer_load_dwordx4 v8, s[44:47], s78 offen nt lds
ds_read_b128 a[28:31], v201 offset:5632
v_add_f32_e32 v244, v244, v166
v_add_f32_e32 v244, v244, v167
s_waitcnt lgkmcnt(4)
v_mfma_f32_16x16x32_bf16 v[144:147], a[8:11], a[80:83], v[144:147]
s_cbranch_vccnz .Lk_brb10
.Lk_bbb10:
ds_read_b128 a[0:3], v201 offset:8192
buffer_load_dwordx4 v9, s[44:47], s78 offen offset:1024 nt lds
v_cvt_pk_bf16_f32 v179, v166, v167
v_mfma_f32_16x16x32_bf16 v[144:147], a[12:15], a[88:91], v[144:147]
s_waitcnt vmcnt(22)
buffer_load_dwordx4 v10, s[44:47], s78 offen offset:2048 nt lds
ds_read_b128 a[4:7], v201 offset:8704
v_exp_f32_e32 v128, v128
v_exp_f32_e32 v129, v129
s_waitcnt lgkmcnt(4)
v_mfma_f32_16x16x32_bf16 v[148:151], a[16:19], a[64:67], v[192:195]
ds_read_b128 a[8:11], v201 offset:9216
buffer_load_dwordx4 v11, s[44:47], s78 offen offset:3072 nt lds
v_cvt_pk_bf16_f32 v184, v128, v129
v_exp_f32_e32 v130, v130
v_add_f32_e32 v244, v244, v128
v_mfma_f32_16x16x32_bf16 v[148:151], a[20:23], a[72:75], v[148:151]
s_waitcnt vmcnt(22)
s_mov_b32 m0, 0xf000
s_mov_b32 s78, 0x37000
buffer_load_dwordx4 v8, s[44:47], s78 offen nt lds
ds_read_b128 a[12:15], v201 offset:9728
v_add_f32_e32 v244, v244, v129
s_waitcnt lgkmcnt(4)
v_mfma_f32_16x16x32_bf16 v[148:151], a[24:27], a[80:83], v[148:151]
ds_read_b128 a[16:19], v201 offset:12288
buffer_load_dwordx4 v9, s[44:47], s78 offen offset:1024 nt lds
v_exp_f32_e32 v131, v131
v_exp_f32_e32 v132, v132
v_mfma_f32_16x16x32_bf16 v[148:151], a[28:31], a[88:91], v[148:151]
s_waitcnt vmcnt(22)
buffer_load_dwordx4 v10, s[44:47], s78 offen offset:2048 nt lds
ds_read_b128 a[20:23], v201 offset:12800
v_cvt_pk_bf16_f32 v185, v130, v131
v_exp_f32_e32 v133, v133
v_add_f32_e32 v244, v244, v130
s_waitcnt lgkmcnt(4)
v_mfma_f32_16x16x32_bf16 v[160:163], a[0:3], a[64:67], v[192:195]
ds_read_b128 a[24:27], v201 offset:13312
buffer_load_dwordx4 v11, s[44:47], s78 offen offset:3072 nt lds
v_cvt_pk_bf16_f32 v186, v132, v133
v_exp_f32_e32 v134, v134
v_add_f32_e32 v244, v244, v131
v_mfma_f32_16x16x32_bf16 v[160:163], a[4:7], a[72:75], v[160:163]
s_waitcnt vmcnt(22)
s_mov_b32 m0, 0x8000
s_mov_b32 s78, 0x30000
buffer_load_dwordx4 v12, s[48:51], s78 offen nt lds
buffer_load_dwordx4 v13, s[48:51], s78 offen offset:1024 nt lds
s_waitcnt vmcnt(63)
s_barrier
ds_read_b128 a[28:31], v201 offset:13824
s_waitcnt lgkmcnt(4)
v_mfma_f32_16x16x32_bf16 v[160:163], a[8:11], a[80:83], v[160:163]
s_waitcnt vmcnt(22)
buffer_load_dwordx4 v14, s[48:51], s78 offen offset:2048 nt lds
ds_read_b64_tr_b16 a[0:1], v205 offset:32768
ds_read_b64_tr_b16 a[2:3], v205 offset:36864
v_exp_f32_e32 v135, v135
v_mfma_f32_16x16x32_bf16 v[160:163], a[12:15], a[88:91], v[160:163]
ds_read_b64_tr_b16 a[4:5], v206 offset:32768
ds_read_b64_tr_b16 a[6:7], v206 offset:36864
buffer_load_dwordx4 v15, s[48:51], s78 offen offset:3072 nt lds
s_waitcnt vmcnt(22)
s_mov_b32 m0, 0x9000
s_mov_b32 s78, 0x31000
buffer_load_dwordx4 v12, s[48:51], s78 offen nt lds
s_waitcnt lgkmcnt(6)
v_mfma_f32_16x16x32_bf16 v[164:167], a[16:19], a[64:67], v[192:195]
ds_read_b64_tr_b16 a[8:9], v205 offset:33280
ds_read_b64_tr_b16 a[10:11], v205 offset:37376
buffer_load_dwordx4 v13, s[48:51], s78 offen offset:1024 nt lds
v_cvt_pk_bf16_f32 v187, v134, v135
v_mfma_f32_16x16x32_bf16 v[164:167], a[20:23], a[72:75], v[164:167]
s_waitcnt vmcnt(22)
buffer_load_dwordx4 v14, s[48:51], s78 offen offset:2048 nt lds
ds_read_b64_tr_b16 a[12:13], v206 offset:33280
ds_read_b64_tr_b16 a[14:15], v206 offset:37376
buffer_load_dwordx4 v15, s[48:51], s78 offen offset:3072 nt lds
s_waitcnt lgkmcnt(8)
v_mfma_f32_16x16x32_bf16 v[164:167], a[24:27], a[80:83], v[164:167]
s_waitcnt vmcnt(22)
s_mov_b32 m0, 0xa000
s_mov_b32 s78, 0x32000
buffer_load_dwordx4 v12, s[48:51], s78 offen nt lds
ds_read_b64_tr_b16 a[16:17], v205 offset:33792
ds_read_b64_tr_b16 a[18:19], v205 offset:37888
v_mfma_f32_16x16x32_bf16 v[164:167], a[28:31], a[88:91], v[164:167]
ds_read_b64_tr_b16 a[20:21], v206 offset:33792
ds_read_b64_tr_b16 a[22:23], v206 offset:37888
buffer_load_dwordx4 v13, s[48:51], s78 offen offset:1024 nt lds
s_waitcnt vmcnt(22)
buffer_load_dwordx4 v14, s[48:51], s78 offen offset:2048 nt lds
s_waitcnt lgkmcnt(8)
v_mfma_f32_16x16x32_bf16 v[0:3], a[0:3], v[176:179], v[0:3]
ds_read_b64_tr_b16 a[24:25], v205 offset:34304
ds_read_b64_tr_b16 a[26:27], v205 offset:38400
buffer_load_dwordx4 v15, s[48:51], s78 offen offset:3072 nt lds
v_add_f32_e32 v244, v244, v132
v_add_f32_e32 v244, v244, v133
v_add_f32_e32 v244, v244, v134
v_mfma_f32_16x16x32_bf16 v[4:7], a[4:7], v[176:179], v[4:7]
s_waitcnt vmcnt(22)
s_mov_b32 m0, 0xb000
s_mov_b32 s78, 0x33000
buffer_load_dwordx4 v12, s[48:51], s78 offen nt lds
ds_read_b64_tr_b16 a[28:29], v206 offset:34304
ds_read_b64_tr_b16 a[30:31], v206 offset:38400
v_add_f32_e32 v244, v244, v135
s_waitcnt lgkmcnt(8)
v_mfma_f32_16x16x32_bf16 v[16:19], a[8:11], v[176:179], v[16:19]
buffer_load_dwordx4 v13, s[48:51], s78 offen offset:1024 nt lds
s_waitcnt vmcnt(22)
buffer_load_dwordx4 v14, s[48:51], s78 offen offset:2048 nt lds
ds_read_b64_tr_b16 a[0:1], v205 offset:40960
)"
R"(
ds_read_b64_tr_b16 a[2:3], v205 offset:45056
v_mfma_f32_16x16x32_bf16 v[20:23], a[12:15], v[176:179], v[20:23]
s_waitcnt lgkmcnt(6)
ds_read_b64_tr_b16 a[4:5], v206 offset:40960
ds_read_b64_tr_b16 a[6:7], v206 offset:45056
buffer_load_dwordx4 v15, s[48:51], s78 offen offset:3072 nt lds
v_max3_f32 v236, v144, v145, v146
v_max3_f32 v236, v236, v147, v148
v_max3_f32 v236, v236, v149, v150
v_mfma_f32_16x16x32_bf16 v[32:35], a[16:19], v[176:179], v[32:35]
s_mov_b32 m0, 0x14000
s_xor_b32 s78, s80, 0x30000
buffer_load_dwordx4 v40, s[52:55], s78 offen lds
ds_read_b64_tr_b16 a[8:9], v205 offset:41472
ds_read_b64_tr_b16 a[10:11], v205 offset:45568
buffer_load_dwordx4 v41, s[52:55], s78 offen offset:1024 lds
v_mfma_f32_16x16x32_bf16 v[36:39], a[20:23], v[176:179], v[36:39]
s_waitcnt lgkmcnt(6)
ds_read_b64_tr_b16 a[12:13], v206 offset:41472
ds_read_b64_tr_b16 a[14:15], v206 offset:45568
buffer_load_dwordx4 v42, s[52:55], s78 offen offset:2048 lds
v_max3_f32 v236, v236, v151, v160
v_max3_f32 v236, v236, v161, v162
v_mfma_f32_16x16x32_bf16 v[48:51], a[24:27], v[176:179], v[48:51]
ds_read_b64_tr_b16 a[16:17], v205 offset:41984
ds_read_b64_tr_b16 a[18:19], v205 offset:46080
buffer_load_dwordx4 v43, s[52:55], s78 offen offset:3072 lds
s_mov_b32 m0, 0x15000
s_xor_b32 s78, s80, 0x34000
buffer_load_dwordx4 v40, s[52:55], s78 offen lds
v_mfma_f32_16x16x32_bf16 v[52:55], a[28:31], v[176:179], v[52:55]
s_waitcnt lgkmcnt(6)
ds_read_b64_tr_b16 a[20:21], v206 offset:41984
ds_read_b64_tr_b16 a[22:23], v206 offset:46080
buffer_load_dwordx4 v41, s[52:55], s78 offen offset:1024 lds
v_max3_f32 v236, v236, v163, v164
v_max3_f32 v236, v236, v165, v166
v_mfma_f32_16x16x32_bf16 v[0:3], a[0:3], v[184:187], v[0:3]
ds_read_b64_tr_b16 a[24:25], v205 offset:42496
ds_read_b64_tr_b16 a[26:27], v205 offset:46592
buffer_load_dwordx4 v42, s[52:55], s78 offen offset:2048 lds
buffer_load_dwordx4 v43, s[52:55], s78 offen offset:3072 lds
v_max_f32_e32 v248, v236, v167
v_cmp_lt_f32_e32 vcc, s70, v248
v_mfma_f32_16x16x32_bf16 v[4:7], a[4:7], v[184:187], v[4:7]
s_mov_b32 m0, 0x16000
s_xor_b32 s78, s80, 0x38000
buffer_load_dwordx4 v40, s[52:55], s78 offen lds
s_waitcnt vmcnt(63)
s_barrier
ds_read_b64_tr_b16 a[28:29], v206 offset:42496
ds_read_b64_tr_b16 a[30:31], v206 offset:46592
s_waitcnt lgkmcnt(8)
v_mfma_f32_16x16x32_bf16 v[16:19], a[8:11], v[184:187], v[16:19]
ds_read_b128 a[0:3], v202 offset:16384
buffer_load_dwordx4 v41, s[52:55], s78 offen offset:1024 lds
buffer_load_dwordx4 v42, s[52:55], s78 offen offset:2048 lds
v_mfma_f32_16x16x32_bf16 v[20:23], a[12:15], v[184:187], v[20:23]
s_cbranch_vccnz .Lk_bra11
.Lk_bba11:
ds_read_b128 a[4:7], v202 offset:16896
buffer_load_dwordx4 v43, s[52:55], s78 offen offset:3072 lds
v_exp_f32_e32 v144, v144
s_nop 0
v_add_f32_e32 v244, v244, v144
s_waitcnt lgkmcnt(6)
v_mfma_f32_16x16x32_bf16 v[32:35], a[16:19], v[184:187], v[32:35]
s_mov_b32 m0, 0x17000
s_xor_b32 s78, s80, 0x3c000
buffer_load_dwordx4 v40, s[52:55], s78 offen lds
ds_read_b128 a[8:11], v202 offset:17408
buffer_load_dwordx4 v41, s[52:55], s78 offen offset:1024 lds
v_mfma_f32_16x16x32_bf16 v[36:39], a[20:23], v[184:187], v[36:39]
s_waitcnt lgkmcnt(3)
ds_read_b128 a[12:15], v202 offset:17920
buffer_load_dwordx4 v42, s[52:55], s78 offen offset:2048 lds
v_exp_f32_e32 v145, v145
v_exp_f32_e32 v146, v146
v_mfma_f32_16x16x32_bf16 v[48:51], a[24:27], v[184:187], v[48:51]
ds_read_b128 a[16:19], v202 offset:20480
buffer_load_dwordx4 v43, s[52:55], s78 offen offset:3072 lds
v_add_f32_e32 v244, v244, v145
v_cvt_pk_bf16_f32 v176, v144, v145
v_add_f32_e32 v244, v244, v146
v_exp_f32_e32 v147, v147
v_mfma_f32_16x16x32_bf16 v[52:55], a[28:31], v[184:187], v[52:55]
s_waitcnt vmcnt(38)
s_mov_b32 m0, 0x1c000
s_mov_b32 s78, 0x38000
buffer_load_dwordx4 v8, s[44:47], s78 offen nt lds
ds_read_b128 a[20:23], v202 offset:20992
s_waitcnt lgkmcnt(4)
v_mfma_f32_16x16x32_bf16 v[128:131], a[0:3], a[64:67], v[192:195]
buffer_load_dwordx4 v9, s[44:47], s78 offen offset:1024 nt lds
s_waitcnt vmcnt(38)
buffer_load_dwordx4 v10, s[44:47], s78 offen offset:2048 nt lds
ds_read_b128 a[24:27], v202 offset:21504
v_add_f32_e32 v244, v244, v147
v_mfma_f32_16x16x32_bf16 v[128:131], a[4:7], a[72:75], v[128:131]
s_waitcnt lgkmcnt(3)
ds_read_b128 a[28:31], v202 offset:22016
buffer_load_dwordx4 v11, s[44:47], s78 offen offset:3072 nt lds
v_cvt_pk_bf16_f32 v177, v146, v147
v_exp_f32_e32 v148, v148
v_exp_f32_e32 v149, v149
v_mfma_f32_16x16x32_bf16 v[128:131], a[8:11], a[80:83], v[128:131]
s_waitcnt vmcnt(38)
s_mov_b32 m0, 0x1d000
s_mov_b32 s78, 0x39000
buffer_load_dwordx4 v8, s[44:47], s78 offen nt lds
ds_read_b128 a[0:3], v202 offset:24576
v_add_f32_e32 v244, v244, v148
v_mfma_f32_16x16x32_bf16 v[128:131], a[12:15], a[88:91], v[128:131]
ds_read_b128 a[4:7], v202 offset:25088
buffer_load_dwordx4 v9, s[44:47], s78 offen offset:1024 nt lds
s_waitcnt vmcnt(38)
buffer_load_dwordx4 v10, s[44:47], s78 offen offset:2048 nt lds
v_add_f32_e32 v244, v244, v149
s_waitcnt lgkmcnt(4)
v_mfma_f32_16x16x32_bf16 v[132:135], a[16:19], a[64:67], v[192:195]
ds_read_b128 a[8:11], v202 offset:25600
buffer_load_dwordx4 v11, s[44:47], s78 offen offset:3072 nt lds
v_cvt_pk_bf16_f32 v178, v148, v149
v_exp_f32_e32 v150, v150
v_exp_f32_e32 v151, v151
v_mfma_f32_16x16x32_bf16 v[132:135], a[20:23], a[72:75], v[132:135]
s_waitcnt vmcnt(38)
s_mov_b32 m0, 0x1e000
s_mov_b32 s78, 0x3a000
buffer_load_dwordx4 v8, s[44:47], s78 offen nt lds
ds_read_b128 a[12:15], v202 offset:26112
s_waitcnt lgkmcnt(4)
v_mfma_f32_16x16x32_bf16 v[132:135], a[24:27], a[80:83], v[132:135]
s_cbranch_vccnz .Lk_brb11
.Lk_bbb11:
ds_read_b128 a[16:19], v202 offset:28672
buffer_load_dwordx4 v9, s[44:47], s78 offen offset:1024 nt lds
v_add_f32_e32 v244, v244, v150
v_add_f32_e32 v244, v244, v151
v_cvt_pk_bf16_f32 v179, v150, v151
v_mfma_f32_16x16x32_bf16 v[132:135], a[28:31], a[88:91], v[132:135]
s_waitcnt vmcnt(38)
buffer_load_dwordx4 v10, s[44:47], s78 offen offset:2048 nt lds
ds_read_b128 a[20:23], v202 offset:29184
buffer_load_dwordx4 v11, s[44:47], s78 offen offset:3072 nt lds
s_waitcnt lgkmcnt(4)
v_mfma_f32_16x16x32_bf16 v[144:147], a[0:3], a[64:67], v[192:195]
s_waitcnt vmcnt(38)
s_mov_b32 m0, 0x1f000
s_mov_b32 s78, 0x3b000
buffer_load_dwordx4 v8, s[44:47], s78 offen nt lds
ds_read_b128 a[24:27], v202 offset:29696
v_max3_f32 v236, v128, v129, v130
v_mfma_f32_16x16x32_bf16 v[144:147], a[4:7], a[72:75], v[144:147]
buffer_load_dwordx4 v9, s[44:47], s78 offen offset:1024 nt lds
s_waitcnt vmcnt(62)
s_barrier
ds_read_b128 a[28:31], v202 offset:30208
v_exp_f32_e32 v160, v160
s_waitcnt lgkmcnt(4)
v_mfma_f32_16x16x32_bf16 v[144:147], a[8:11], a[80:83], v[144:147]
s_waitcnt vmcnt(38)
buffer_load_dwordx4 v10, s[44:47], s78 offen offset:2048 nt lds
ds_read_b64_tr_b16 a[0:1], v203
ds_read_b64_tr_b16 a[2:3], v203 offset:4096
v_exp_f32_e32 v161, v161
v_mfma_f32_16x16x32_bf16 v[144:147], a[12:15], a[88:91], v[144:147]
s_waitcnt lgkmcnt(4)
ds_read_b64_tr_b16 a[4:5], v204
ds_read_b64_tr_b16 a[6:7], v204 offset:4096
buffer_load_dwordx4 v11, s[44:47], s78 offen offset:3072 nt lds
v_cvt_pk_bf16_f32 v184, v160, v161
v_exp_f32_e32 v162, v162
v_mfma_f32_16x16x32_bf16 v[148:151], a[16:19], a[64:67], v[192:195]
s_mov_b32 m0, 0x20000
s_xor_b32 s78, s34, 0
buffer_load_dwordx4 v40, s[52:55], s78 offen lds
ds_read_b64_tr_b16 a[8:9], v203 offset:512
ds_read_b64_tr_b16 a[10:11], v203 offset:4608
v_add_f32_e32 v244, v244, v160
v_mfma_f32_16x16x32_bf16 v[148:151], a[20:23], a[72:75], v[148:151]
s_waitcnt lgkmcnt(6)
buffer_load_dwordx4 v41, s[52:55], s78 offen offset:1024 lds
ds_read_b64_tr_b16 a[12:13], v204 offset:512
ds_read_b64_tr_b16 a[14:15], v204 offset:4608
buffer_load_dwordx4 v42, s[52:55], s78 offen offset:2048 lds
v_add_f32_e32 v244, v244, v161
v_mfma_f32_16x16x32_bf16 v[148:151], a[24:27], a[80:83], v[148:151]
ds_read_b64_tr_b16 a[16:17], v203 offset:1024
ds_read_b64_tr_b16 a[18:19], v203 offset:5120
buffer_load_dwordx4 v43, s[52:55], s78 offen offset:3072 lds
s_mov_b32 m0, 0x21000
s_xor_b32 s78, s34, 0x4000
buffer_load_dwordx4 v40, s[52:55], s78 offen lds
v_mfma_f32_16x16x32_bf16 v[148:151], a[28:31], a[88:91], v[148:151]
s_waitcnt lgkmcnt(6)
ds_read_b64_tr_b16 a[20:21], v204 offset:1024
ds_read_b64_tr_b16 a[22:23], v204 offset:5120
buffer_load_dwordx4 v41, s[52:55], s78 offen offset:1024 lds
v_exp_f32_e32 v163, v163
)"
R"(
v_mfma_f32_16x16x32_bf16 v[0:3], a[0:3], v[176:179], v[0:3]
ds_read_b64_tr_b16 a[24:25], v203 offset:1536
ds_read_b64_tr_b16 a[26:27], v203 offset:5632
buffer_load_dwordx4 v42, s[52:55], s78 offen offset:2048 lds
buffer_load_dwordx4 v43, s[52:55], s78 offen offset:3072 lds
v_cvt_pk_bf16_f32 v185, v162, v163
v_mfma_f32_16x16x32_bf16 v[4:7], a[4:7], v[176:179], v[4:7]
s_mov_b32 m0, 0x22000
s_xor_b32 s78, s34, 0x8000
buffer_load_dwordx4 v40, s[52:55], s78 offen lds
ds_read_b64_tr_b16 a[28:29], v204 offset:1536
ds_read_b64_tr_b16 a[30:31], v204 offset:5632
v_add_f32_e32 v244, v244, v162
s_waitcnt lgkmcnt(8)
v_mfma_f32_16x16x32_bf16 v[16:19], a[8:11], v[176:179], v[16:19]
ds_read_b64_tr_b16 a[0:1], v203 offset:8192
ds_read_b64_tr_b16 a[2:3], v203 offset:12288
buffer_load_dwordx4 v41, s[52:55], s78 offen offset:1024 lds
buffer_load_dwordx4 v42, s[52:55], s78 offen offset:2048 lds
v_add_f32_e32 v244, v244, v163
v_mfma_f32_16x16x32_bf16 v[20:23], a[12:15], v[176:179], v[20:23]
s_waitcnt lgkmcnt(6)
ds_read_b64_tr_b16 a[4:5], v204 offset:8192
ds_read_b64_tr_b16 a[6:7], v204 offset:12288
buffer_load_dwordx4 v43, s[52:55], s78 offen offset:3072 lds
v_exp_f32_e32 v164, v164
v_exp_f32_e32 v165, v165
v_mfma_f32_16x16x32_bf16 v[32:35], a[16:19], v[176:179], v[32:35]
s_mov_b32 m0, 0x23000
s_xor_b32 s78, s34, 0xc000
buffer_load_dwordx4 v40, s[52:55], s78 offen lds
ds_read_b64_tr_b16 a[8:9], v203 offset:8704
ds_read_b64_tr_b16 a[10:11], v203 offset:12800
v_mfma_f32_16x16x32_bf16 v[36:39], a[20:23], v[176:179], v[36:39]
s_waitcnt lgkmcnt(6)
buffer_load_dwordx4 v41, s[52:55], s78 offen offset:1024 lds
ds_read_b64_tr_b16 a[12:13], v204 offset:8704
ds_read_b64_tr_b16 a[14:15], v204 offset:12800
buffer_load_dwordx4 v42, s[52:55], s78 offen offset:2048 lds
v_cvt_pk_bf16_f32 v186, v164, v165
v_mfma_f32_16x16x32_bf16 v[48:51], a[24:27], v[176:179], v[48:51]
ds_read_b64_tr_b16 a[16:17], v203 offset:9216
ds_read_b64_tr_b16 a[18:19], v203 offset:13312
buffer_load_dwordx4 v43, s[52:55], s78 offen offset:3072 lds
v_exp_f32_e32 v166, v166
v_exp_f32_e32 v167, v167
v_mfma_f32_16x16x32_bf16 v[52:55], a[28:31], v[176:179], v[52:55]
s_waitcnt vmcnt(54)
s_mov_b32 m0, 0x10000
s_mov_b32 s78, 0x34000
buffer_load_dwordx4 v12, s[48:51], s78 offen nt lds
ds_read_b64_tr_b16 a[20:21], v204 offset:9216
ds_read_b64_tr_b16 a[22:23], v204 offset:13312
v_cvt_pk_bf16_f32 v187, v166, v167
s_waitcnt lgkmcnt(8)
s_nop 0
v_mfma_f32_16x16x32_bf16 v[0:3], a[0:3], v[184:187], v[0:3]
ds_read_b64_tr_b16 a[24:25], v203 offset:9728
ds_read_b64_tr_b16 a[26:27], v203 offset:13824
buffer_load_dwordx4 v13, s[48:51], s78 offen offset:1024 nt lds
v_add_f32_e32 v244, v244, v164
v_add_f32_e32 v244, v244, v165
v_mfma_f32_16x16x32_bf16 v[4:7], a[4:7], v[184:187], v[4:7]
s_waitcnt vmcnt(54)
buffer_load_dwordx4 v14, s[48:51], s78 offen offset:2048 nt lds
s_waitcnt vmcnt(63)
s_barrier
ds_read_b64_tr_b16 a[28:29], v204 offset:9728
ds_read_b64_tr_b16 a[30:31], v204 offset:13824
s_waitcnt lgkmcnt(8)
v_mfma_f32_16x16x32_bf16 v[16:19], a[8:11], v[184:187], v[16:19]
ds_read_b128 a[0:3], v200 offset:49152
buffer_load_dwordx4 v15, s[48:51], s78 offen offset:3072 nt lds
v_add_f32_e32 v244, v244, v166
v_add_f32_e32 v244, v244, v167
v_max3_f32 v236, v236, v131, v132
v_max3_f32 v236, v236, v133, v134
v_mfma_f32_16x16x32_bf16 v[20:23], a[12:15], v[184:187], v[20:23]
s_waitcnt vmcnt(54)
s_mov_b32 m0, 0x11000
s_mov_b32 s78, 0x35000
buffer_load_dwordx4 v12, s[48:51], s78 offen nt lds
ds_read_b128 a[4:7], v200 offset:49664
v_max3_f32 v236, v236, v135, v144
v_max3_f32 v236, v236, v145, v146
s_waitcnt lgkmcnt(6)
v_mfma_f32_16x16x32_bf16 v[32:35], a[16:19], v[184:187], v[32:35]
ds_read_b128 a[8:11], v200 offset:50176
buffer_load_dwordx4 v13, s[48:51], s78 offen offset:1024 nt lds
v_max3_f32 v236, v236, v147, v148
v_max3_f32 v236, v236, v149, v150
v_max_f32_e32 v248, v236, v151
v_cmp_lt_f32_e32 vcc, s70, v248
v_mfma_f32_16x16x32_bf16 v[36:39], a[20:23], v[184:187], v[36:39]
s_nop 0
s_cbranch_vccnz .Lk_bra12
.Lk_bba12:
ds_read_b128 a[12:15], v200 offset:50688
v_exp_f32_e32 v128, v128
v_exp_f32_e32 v129, v129
s_waitcnt lgkmcnt(4)
v_mfma_f32_16x16x32_bf16 v[48:51], a[24:27], v[184:187], v[48:51]
s_waitcnt vmcnt(54)
buffer_load_dwordx4 v14, s[48:51], s78 offen offset:2048 nt lds
ds_read_b128 a[16:19], v200 offset:53248
v_add_f32_e32 v244, v244, v128
v_add_f32_e32 v244, v244, v129
v_cvt_pk_bf16_f32 v176, v128, v129
v_mfma_f32_16x16x32_bf16 v[52:55], a[28:31], v[184:187], v[52:55]
s_waitcnt lgkmcnt(3)
ds_read_b128 a[20:23], v200 offset:53760
buffer_load_dwordx4 v15, s[48:51], s78 offen offset:3072 nt lds
v_exp_f32_e32 v130, v130
v_exp_f32_e32 v131, v131
v_mfma_f32_16x16x32_bf16 v[160:163], a[0:3], a[64:67], v[192:195]
s_waitcnt vmcnt(38)
s_mov_b32 m0, 0x12000
s_mov_b32 s78, 0x36000
buffer_load_dwordx4 v12, s[48:51], s78 offen nt lds
ds_read_b128 a[24:27], v200 offset:54272
v_add_f32_e32 v244, v244, v130
v_add_f32_e32 v244, v244, v131
v_mfma_f32_16x16x32_bf16 v[160:163], a[4:7], a[72:75], v[160:163]
s_waitcnt lgkmcnt(3)
ds_read_b128 a[28:31], v200 offset:54784
buffer_load_dwordx4 v13, s[48:51], s78 offen offset:1024 nt lds
v_cvt_pk_bf16_f32 v177, v130, v131
v_exp_f32_e32 v132, v132
v_exp_f32_e32 v133, v133
v_mfma_f32_16x16x32_bf16 v[160:163], a[8:11], a[80:83], v[160:163]
s_waitcnt vmcnt(38)
buffer_load_dwordx4 v14, s[48:51], s78 offen offset:2048 nt lds
ds_read_b128 a[0:3], v200 offset:57344
v_add_f32_e32 v244, v244, v132
v_add_f32_e32 v244, v244, v133
v_mfma_f32_16x16x32_bf16 v[160:163], a[12:15], a[88:91], v[160:163]
s_mov_b32 exec_lo, 0
s_mov_b32 exec_hi, 0xff0000
buffer_load_dword v251, v252, s[52:55], 0 offen
s_mov_b64 exec, -1
ds_read_b128 a[4:7], v200 offset:57856
v_cvt_pk_bf16_f32 v178, v132, v133
s_waitcnt lgkmcnt(4)
v_mfma_f32_16x16x32_bf16 v[164:167], a[16:19], a[64:67], v[192:195]
ds_read_b128 a[8:11], v200 offset:58368
buffer_load_dwordx4 v15, s[48:51], s78 offen offset:3072 nt lds
v_exp_f32_e32 v134, v134
v_exp_f32_e32 v135, v135
v_add_f32_e32 v244, v244, v134
v_mfma_f32_16x16x32_bf16 v[164:167], a[20:23], a[72:75], v[164:167]
s_waitcnt vmcnt(39)
s_mov_b32 m0, 0x13000
s_mov_b32 s78, 0x37000
buffer_load_dwordx4 v12, s[48:51], s78 offen nt lds
ds_read_b128 a[12:15], v200 offset:58880
v_add_f32_e32 v244, v244, v135
s_waitcnt lgkmcnt(4)
v_mfma_f32_16x16x32_bf16 v[164:167], a[24:27], a[80:83], v[164:167]
s_cbranch_vccnz .Lk_brb12
.Lk_bbb12:
ds_read_b128 a[16:19], v200 offset:61440
buffer_load_dwordx4 v13, s[48:51], s78 offen offset:1024 nt lds
v_cvt_pk_bf16_f32 v179, v134, v135
v_max3_f32 v236, v160, v161, v162
v_mfma_f32_16x16x32_bf16 v[164:167], a[28:31], a[88:91], v[164:167]
s_waitcnt vmcnt(39)
buffer_load_dwordx4 v14, s[48:51], s78 offen offset:2048 nt lds
ds_read_b128 a[20:23], v200 offset:61952
v_exp_f32_e32 v144, v144
s_nop 0
v_add_f32_e32 v244, v244, v144
s_waitcnt lgkmcnt(4)
v_mfma_f32_16x16x32_bf16 v[128:131], a[0:3], a[64:67], v[192:195]
ds_read_b128 a[24:27], v200 offset:62464
buffer_load_dwordx4 v15, s[48:51], s78 offen offset:3072 nt lds
v_exp_f32_e32 v145, v145
v_exp_f32_e32 v146, v146
v_mfma_f32_16x16x32_bf16 v[128:131], a[4:7], a[72:75], v[128:131]
s_waitcnt vmcnt(63)
s_barrier
ds_read_b128 a[28:31], v200 offset:62976
v_cvt_pk_bf16_f32 v184, v144, v145
v_exp_f32_e32 v147, v147
v_add_f32_e32 v244, v244, v145
s_waitcnt lgkmcnt(4)
v_mfma_f32_16x16x32_bf16 v[128:131], a[8:11], a[80:83], v[128:131]
s_waitcnt vmcnt(39)
s_mov_b32 m0, 0x18000
s_mov_b32 s78, 0x3c000
buffer_load_dwordx4 v8, s[44:47], s78 offen nt lds
ds_read_b64_tr_b16 a[0:1], v203 offset:32768
ds_read_b64_tr_b16 a[2:3], v203 offset:36864
v_mfma_f32_16x16x32_bf16 v[128:131], a[12:15], a[88:91], v[128:131]
buffer_load_dwordx4 v9, s[44:47], s78 offen offset:1024 nt lds
s_waitcnt vmcnt(39)
buffer_load_dwordx4 v10, s[44:47], s78 offen offset:2048 nt lds
ds_read_b64_tr_b16 a[4:5], v204 offset:32768
ds_read_b64_tr_b16 a[6:7], v204 offset:36864
s_waitcnt lgkmcnt(6)
v_mfma_f32_16x16x32_bf16 v[132:135], a[16:19], a[64:67], v[192:195]
)"
R"(
ds_read_b64_tr_b16 a[8:9], v203 offset:33280
ds_read_b64_tr_b16 a[10:11], v203 offset:37376
buffer_load_dwordx4 v11, s[44:47], s78 offen offset:3072 nt lds
s_waitcnt vmcnt(39)
s_mov_b32 m0, 0x19000
s_mov_b32 s78, 0x3d000
buffer_load_dwordx4 v8, s[44:47], s78 offen nt lds
v_mfma_f32_16x16x32_bf16 v[132:135], a[20:23], a[72:75], v[132:135]
s_waitcnt lgkmcnt(6)
ds_read_b64_tr_b16 a[12:13], v204 offset:33280
ds_read_b64_tr_b16 a[14:15], v204 offset:37376
buffer_load_dwordx4 v9, s[44:47], s78 offen offset:1024 nt lds
v_cvt_pk_bf16_f32 v185, v146, v147
v_mfma_f32_16x16x32_bf16 v[132:135], a[24:27], a[80:83], v[132:135]
s_waitcnt vmcnt(39)
buffer_load_dwordx4 v10, s[44:47], s78 offen offset:2048 nt lds
ds_read_b64_tr_b16 a[16:17], v203 offset:33792
ds_read_b64_tr_b16 a[18:19], v203 offset:37888
buffer_load_dwordx4 v11, s[44:47], s78 offen offset:3072 nt lds
v_mfma_f32_16x16x32_bf16 v[132:135], a[28:31], a[88:91], v[132:135]
s_waitcnt vmcnt(23)
s_mov_b32 m0, 0x1a000
s_mov_b32 s78, 0x3e000
buffer_load_dwordx4 v8, s[44:47], s78 offen nt lds
ds_read_b64_tr_b16 a[20:21], v204 offset:33792
ds_read_b64_tr_b16 a[22:23], v204 offset:37888
s_waitcnt lgkmcnt(8)
v_mfma_f32_16x16x32_bf16 v[0:3], a[0:3], v[176:179], v[0:3]
ds_read_b64_tr_b16 a[24:25], v203 offset:34304
ds_read_b64_tr_b16 a[26:27], v203 offset:38400
buffer_load_dwordx4 v9, s[44:47], s78 offen offset:1024 nt lds
s_waitcnt vmcnt(23)
buffer_load_dwordx4 v10, s[44:47], s78 offen offset:2048 nt lds
v_add_f32_e32 v244, v244, v146
v_mfma_f32_16x16x32_bf16 v[4:7], a[4:7], v[176:179], v[4:7]
s_waitcnt lgkmcnt(6)
ds_read_b64_tr_b16 a[28:29], v204 offset:34304
ds_read_b64_tr_b16 a[30:31], v204 offset:38400
buffer_load_dwordx4 v11, s[44:47], s78 offen offset:3072 nt lds
v_exp_f32_e32 v148, v148
v_exp_f32_e32 v149, v149
v_mfma_f32_16x16x32_bf16 v[16:19], a[8:11], v[176:179], v[16:19]
s_waitcnt vmcnt(23)
s_mov_b32 m0, 0x1b000
s_mov_b32 s78, 0x3f000
buffer_load_dwordx4 v8, s[44:47], s78 offen nt lds
ds_read_b64_tr_b16 a[0:1], v203 offset:40960
ds_read_b64_tr_b16 a[2:3], v203 offset:45056
v_mfma_f32_16x16x32_bf16 v[20:23], a[12:15], v[176:179], v[20:23]
buffer_load_dwordx4 v9, s[44:47], s78 offen offset:1024 nt lds
s_waitcnt vmcnt(23)
buffer_load_dwordx4 v10, s[44:47], s78 offen offset:2048 nt lds
ds_read_b64_tr_b16 a[4:5], v204 offset:40960
ds_read_b64_tr_b16 a[6:7], v204 offset:45056
v_cvt_pk_bf16_f32 v186, v148, v149
s_waitcnt lgkmcnt(8)
v_mfma_f32_16x16x32_bf16 v[32:35], a[16:19], v[176:179], v[32:35]
ds_read_b64_tr_b16 a[8:9], v203 offset:41472
ds_read_b64_tr_b16 a[10:11], v203 offset:45568
buffer_load_dwordx4 v11, s[44:47], s78 offen offset:3072 nt lds
v_exp_f32_e32 v150, v150
v_exp_f32_e32 v151, v151
v_mfma_f32_16x16x32_bf16 v[36:39], a[20:23], v[176:179], v[36:39]
s_mov_b32 m0, 0x24000
s_xor_b32 s78, s34, 0x10000
buffer_load_dwordx4 v40, s[52:55], s78 offen lds
ds_read_b64_tr_b16 a[12:13], v204 offset:41472
ds_read_b64_tr_b16 a[14:15], v204 offset:45568
buffer_load_dwordx4 v41, s[52:55], s78 offen offset:1024 lds
s_waitcnt lgkmcnt(8)
v_mfma_f32_16x16x32_bf16 v[48:51], a[24:27], v[176:179], v[48:51]
ds_read_b64_tr_b16 a[16:17], v203 offset:41984
ds_read_b64_tr_b16 a[18:19], v203 offset:46080
buffer_load_dwordx4 v42, s[52:55], s78 offen offset:2048 lds
v_cvt_pk_bf16_f32 v187, v150, v151
v_add_f32_e32 v244, v244, v147
v_add_f32_e32 v244, v244, v148
v_mfma_f32_16x16x32_bf16 v[52:55], a[28:31], v[176:179], v[52:55]
s_waitcnt lgkmcnt(6)
ds_read_b64_tr_b16 a[20:21], v204 offset:41984
ds_read_b64_tr_b16 a[22:23], v204 offset:46080
buffer_load_dwordx4 v43, s[52:55], s78 offen offset:3072 lds
v_add_f32_e32 v244, v244, v149
v_add_f32_e32 v244, v244, v150
v_add_f32_e32 v244, v244, v151
v_max3_f32 v236, v236, v163, v164
v_mfma_f32_16x16x32_bf16 v[0:3], a[0:3], v[184:187], v[0:3]
s_mov_b32 m0, 0x25000
s_xor_b32 s78, s34, 0x14000
buffer_load_dwordx4 v40, s[52:55], s78 offen lds
ds_read_b64_tr_b16 a[24:25], v203 offset:42496
ds_read_b64_tr_b16 a[26:27], v203 offset:46592
v_max3_f32 v236, v236, v165, v166
v_max3_f32 v236, v236, v167, v128
v_mfma_f32_16x16x32_bf16 v[4:7], a[4:7], v[184:187], v[4:7]
buffer_load_dwordx4 v41, s[52:55], s78 offen offset:1024 lds
buffer_load_dwordx4 v42, s[52:55], s78 offen offset:2048 lds
s_waitcnt vmcnt(56)
s_barrier
ds_read_b64_tr_b16 a[28:29], v204 offset:42496
ds_read_b64_tr_b16 a[30:31], v204 offset:46592
s_waitcnt lgkmcnt(8)
v_mfma_f32_16x16x32_bf16 v[16:19], a[8:11], v[184:187], v[16:19]
ds_read_b128 a[0:3], v201 offset:49152
buffer_load_dwordx4 v43, s[52:55], s78 offen offset:3072 lds
s_add_u32 s71, s81, 0
buffer_store_dwordx4 a[32:35], v229, s[56:59], s71 offen sc1
v_max3_f32 v236, v236, v129, v130
v_mfma_f32_16x16x32_bf16 v[20:23], a[12:15], v[184:187], v[20:23]
s_mov_b32 m0, 0x26000
s_xor_b32 s78, s34, 0x18000
buffer_load_dwordx4 v40, s[52:55], s78 offen lds
ds_read_b128 a[4:7], v201 offset:49664
v_max3_f32 v236, v236, v131, v132
v_max3_f32 v236, v236, v133, v134
v_max_f32_e32 v248, v236, v135
s_waitcnt lgkmcnt(6)
v_mfma_f32_16x16x32_bf16 v[32:35], a[16:19], v[184:187], v[32:35]
ds_read_b128 a[8:11], v201 offset:50176
buffer_load_dwordx4 v41, s[52:55], s78 offen offset:1024 lds
buffer_store_dwordx4 a[40:43], v229, s[56:59], s71 offen offset:2048 sc1
v_cmp_lt_f32_e32 vcc, s70, v248
v_mfma_f32_16x16x32_bf16 v[36:39], a[20:23], v[184:187], v[36:39]
s_nop 0
s_cbranch_vccnz .Lk_bra13
.Lk_bba13:
ds_read_b128 a[12:15], v201 offset:50688
buffer_load_dwordx4 v42, s[52:55], s78 offen offset:2048 lds
buffer_load_dwordx4 v43, s[52:55], s78 offen offset:3072 lds
s_waitcnt lgkmcnt(4)
v_mfma_f32_16x16x32_bf16 v[48:51], a[24:27], v[184:187], v[48:51]
s_mov_b32 m0, 0x27000
s_xor_b32 s78, s34, 0x1c000
buffer_load_dwordx4 v40, s[52:55], s78 offen lds
s_add_u32 s71, s81, 0x1000
buffer_store_dwordx4 a[48:51], v229, s[56:59], s71 offen sc1
ds_read_b128 a[16:19], v201 offset:53248
v_mfma_f32_16x16x32_bf16 v[52:55], a[28:31], v[184:187], v[52:55]
s_waitcnt lgkmcnt(3)
ds_read_b128 a[20:23], v201 offset:53760
buffer_load_dwordx4 v41, s[52:55], s78 offen offset:1024 lds
v_exp_f32_e32 v160, v160
v_exp_f32_e32 v161, v161
v_add_f32_e32 v244, v244, v160
v_mfma_f32_16x16x32_bf16 v[144:147], a[0:3], a[64:67], v[192:195]
ds_read_b128 a[24:27], v201 offset:54272
buffer_load_dwordx4 v42, s[52:55], s78 offen offset:2048 lds
buffer_store_dwordx4 a[56:59], v229, s[56:59], s71 offen offset:2048 sc1
v_add_f32_e32 v244, v244, v161
v_cvt_pk_bf16_f32 v176, v160, v161
v_mfma_f32_16x16x32_bf16 v[144:147], a[4:7], a[72:75], v[144:147]
s_waitcnt lgkmcnt(3)
ds_read_b128 a[28:31], v201 offset:54784
buffer_load_dwordx4 v43, s[52:55], s78 offen offset:3072 lds
v_exp_f32_e32 v162, v162
v_exp_f32_e32 v163, v163
v_add_f32_e32 v244, v244, v162
v_add_f32_e32 v244, v244, v163
v_mfma_f32_16x16x32_bf16 v[144:147], a[8:11], a[80:83], v[144:147]
s_waitcnt vmcnt(63)
s_mov_b32 m0, 0
s_mov_b32 s78, 0x38000
buffer_load_dwordx4 v12, s[48:51], s78 offen nt lds
ds_read_b128 a[0:3], v201 offset:57344
v_cvt_pk_bf16_f32 v177, v162, v163
v_mfma_f32_16x16x32_bf16 v[144:147], a[12:15], a[88:91], v[144:147]
s_waitcnt lgkmcnt(3)
ds_read_b128 a[4:7], v201 offset:57856
buffer_load_dwordx4 v13, s[48:51], s78 offen offset:1024 nt lds
v_exp_f32_e32 v164, v164
v_exp_f32_e32 v165, v165
v_add_f32_e32 v244, v244, v164
v_add_f32_e32 v244, v244, v165
v_mfma_f32_16x16x32_bf16 v[148:151], a[16:19], a[64:67], v[192:195]
s_waitcnt vmcnt(63)
buffer_load_dwordx4 v14, s[48:51], s78 offen offset:2048 nt lds
ds_read_b128 a[8:11], v201 offset:58368
v_cvt_pk_bf16_f32 v178, v164, v165
v_exp_f32_e32 v166, v166
v_exp_f32_e32 v167, v167
v_mfma_f32_16x16x32_bf16 v[148:151], a[20:23], a[72:75], v[148:151]
s_waitcnt lgkmcnt(3)
ds_read_b128 a[12:15], v201 offset:58880
buffer_load_dwordx4 v15, s[48:51], s78 offen offset:3072 nt lds
v_add_f32_e32 v244, v244, v166
v_add_f32_e32 v244, v244, v167
v_cvt_pk_bf16_f32 v179, v166, v167
v_exp_f32_e32 v128, v128
v_mfma_f32_16x16x32_bf16 v[148:151], a[24:27], a[80:83], v[148:151]
s_cbranch_vccnz .Lk_brb13
.Lk_bbb13:
ds_read_b128 a[16:19], v201 offset:61440
v_exp_f32_e32 v129, v129
v_exp_f32_e32 v130, v130
v_mfma_f32_16x16x32_bf16 v[148:151], a[28:31], a[88:91], v[148:151]
s_waitcnt vmcnt(63)
s_mov_b32 m0, 0x1000
s_mov_b32 s78, 0x39000
buffer_load_dwordx4 v12, s[48:51], s78 offen nt lds
ds_read_b128 a[20:23], v201 offset:61952
)"
R"(
v_cvt_pk_bf16_f32 v184, v128, v129
v_add_f32_e32 v244, v244, v128
s_waitcnt lgkmcnt(4)
v_mfma_f32_16x16x32_bf16 v[160:163], a[0:3], a[64:67], v[192:195]
ds_read_b128 a[24:27], v201 offset:62464
buffer_load_dwordx4 v13, s[48:51], s78 offen offset:1024 nt lds
v_exp_f32_e32 v131, v131
v_exp_f32_e32 v132, v132
v_cvt_pk_bf16_f32 v185, v130, v131
v_mfma_f32_16x16x32_bf16 v[160:163], a[4:7], a[72:75], v[160:163]
s_waitcnt vmcnt(63)
buffer_load_dwordx4 v14, s[48:51], s78 offen offset:2048 nt lds
s_waitcnt vmcnt(43)
s_barrier
ds_read_b128 a[28:31], v201 offset:62976
v_exp_f32_e32 v133, v133
s_waitcnt lgkmcnt(4)
v_mfma_f32_16x16x32_bf16 v[160:163], a[8:11], a[80:83], v[160:163]
ds_read_b64_tr_b16 a[0:1], v205
ds_read_b64_tr_b16 a[2:3], v205 offset:4096
buffer_load_dwordx4 v15, s[48:51], s78 offen offset:3072 nt lds
v_cvt_pk_bf16_f32 v186, v132, v133
v_exp_f32_e32 v134, v134
v_mfma_f32_16x16x32_bf16 v[160:163], a[12:15], a[88:91], v[160:163]
s_waitcnt lgkmcnt(4)
ds_read_b64_tr_b16 a[4:5], v206
ds_read_b64_tr_b16 a[6:7], v206 offset:4096
v_exp_f32_e32 v135, v135
v_add_f32_e32 v244, v244, v129
v_cvt_pk_bf16_f32 v187, v134, v135
v_add_f32_e32 v244, v244, v130
v_add_f32_e32 v244, v244, v131
v_add_f32_e32 v244, v244, v132
v_mfma_f32_16x16x32_bf16 v[164:167], a[16:19], a[64:67], v[192:195]
s_waitcnt vmcnt(59)
s_mov_b32 m0, 0x2000
s_mov_b32 s78, 0x3a000
buffer_load_dwordx4 v12, s[48:51], s78 offen nt lds
ds_read_b64_tr_b16 a[8:9], v205 offset:512
ds_read_b64_tr_b16 a[10:11], v205 offset:4608
v_add_f32_e32 v244, v244, v133
v_mfma_f32_16x16x32_bf16 v[164:167], a[20:23], a[72:75], v[164:167]
s_waitcnt lgkmcnt(6)
ds_read_b64_tr_b16 a[12:13], v206 offset:512
ds_read_b64_tr_b16 a[14:15], v206 offset:4608
buffer_load_dwordx4 v13, s[48:51], s78 offen offset:1024 nt lds
v_add_f32_e32 v244, v244, v134
v_add_f32_e32 v244, v244, v135
v_max3_f32 v236, v144, v145, v146
v_max3_f32 v236, v236, v147, v148
v_mfma_f32_16x16x32_bf16 v[164:167], a[24:27], a[80:83], v[164:167]
s_waitcnt vmcnt(59)
buffer_load_dwordx4 v14, s[48:51], s78 offen offset:2048 nt lds
ds_read_b64_tr_b16 a[16:17], v205 offset:1024
ds_read_b64_tr_b16 a[18:19], v205 offset:5120
v_max3_f32 v236, v236, v149, v150
v_max3_f32 v236, v236, v151, v160
v_max3_f32 v236, v236, v161, v162
v_mfma_f32_16x16x32_bf16 v[164:167], a[28:31], a[88:91], v[164:167]
s_waitcnt lgkmcnt(6)
ds_read_b64_tr_b16 a[20:21], v206 offset:1024
ds_read_b64_tr_b16 a[22:23], v206 offset:5120
buffer_load_dwordx4 v15, s[48:51], s78 offen offset:3072 nt lds
v_mfma_f32_16x16x32_bf16 v[0:3], a[0:3], v[176:179], v[0:3]
ds_read_b64_tr_b16 a[24:25], v205 offset:1536
ds_read_b64_tr_b16 a[26:27], v205 offset:5632
v_mfma_f32_16x16x32_bf16 v[4:7], a[4:7], v[176:179], v[4:7]
s_waitcnt vmcnt(59)
s_mov_b32 m0, 0x3000
s_mov_b32 s78, 0x3b000
buffer_load_dwordx4 v12, s[48:51], s78 offen nt lds
ds_read_b64_tr_b16 a[28:29], v206 offset:1536
ds_read_b64_tr_b16 a[30:31], v206 offset:5632
s_waitcnt lgkmcnt(8)
v_mfma_f32_16x16x32_bf16 v[16:19], a[8:11], v[176:179], v[16:19]
ds_read_b64_tr_b16 a[0:1], v205 offset:8192
ds_read_b64_tr_b16 a[2:3], v205 offset:12288
buffer_load_dwordx4 v13, s[48:51], s78 offen offset:1024 nt lds
v_max3_f32 v236, v236, v163, v164
v_max3_f32 v236, v236, v165, v166
v_max_f32_e32 v248, v236, v167
v_cmp_lt_f32_e32 vcc, s70, v248
s_nop 1
s_cbranch_vccnz .Lk_bra14
.Lk_bba14:
v_mfma_f32_16x16x32_bf16 v[20:23], a[12:15], v[176:179], v[20:23]
s_waitcnt vmcnt(59)
buffer_load_dwordx4 v14, s[48:51], s78 offen offset:2048 nt lds
ds_read_b64_tr_b16 a[4:5], v206 offset:8192
ds_read_b64_tr_b16 a[6:7], v206 offset:12288
v_exp_f32_e32 v144, v144
s_nop 0
v_add_f32_e32 v244, v244, v144
s_waitcnt lgkmcnt(8)
v_mfma_f32_16x16x32_bf16 v[32:35], a[16:19], v[176:179], v[32:35]
ds_read_b64_tr_b16 a[8:9], v205 offset:8704
ds_read_b64_tr_b16 a[10:11], v205 offset:12800
buffer_load_dwordx4 v15, s[48:51], s78 offen offset:3072 nt lds
v_exp_f32_e32 v145, v145
v_exp_f32_e32 v146, v146
v_mfma_f32_16x16x32_bf16 v[36:39], a[20:23], v[176:179], v[36:39]
s_waitcnt lgkmcnt(6)
ds_read_b64_tr_b16 a[12:13], v206 offset:8704
ds_read_b64_tr_b16 a[14:15], v206 offset:12800
v_add_f32_e32 v244, v244, v145
v_add_f32_e32 v244, v244, v146
v_exp_f32_e32 v147, v147
v_exp_f32_e32 v148, v148
v_add_f32_e32 v244, v244, v147
v_mfma_f32_16x16x32_bf16 v[48:51], a[24:27], v[176:179], v[48:51]
ds_read_b64_tr_b16 a[16:17], v205 offset:9216
ds_read_b64_tr_b16 a[18:19], v205 offset:13312
v_add_f32_e32 v244, v244, v148
v_exp_f32_e32 v149, v149
v_exp_f32_e32 v150, v150
v_add_f32_e32 v244, v244, v149
v_add_f32_e32 v244, v244, v150
v_mfma_f32_16x16x32_bf16 v[52:55], a[28:31], v[176:179], v[52:55]
s_waitcnt lgkmcnt(6)
ds_read_b64_tr_b16 a[20:21], v206 offset:9216
ds_read_b64_tr_b16 a[22:23], v206 offset:13312
v_cvt_pk_bf16_f32 v176, v144, v145
v_cvt_pk_bf16_f32 v177, v146, v147
v_cvt_pk_bf16_f32 v178, v148, v149
v_exp_f32_e32 v151, v151
v_exp_f32_e32 v160, v160
v_mfma_f32_16x16x32_bf16 v[0:3], a[0:3], v[184:187], v[0:3]
s_waitcnt vmcnt(17)
s_mov_b64 exec, 14
buffer_store_dword v234, v232, s[56:59], 0 offen sc1
s_mov_b64 exec, -1
ds_read_b64_tr_b16 a[24:25], v205 offset:9728
ds_read_b64_tr_b16 a[26:27], v205 offset:13824
v_add_f32_e32 v244, v244, v151
v_mfma_f32_16x16x32_bf16 v[4:7], a[4:7], v[184:187], v[4:7]
s_waitcnt vmcnt(37)
s_barrier
ds_read_b64_tr_b16 a[28:29], v206 offset:9728
ds_read_b64_tr_b16 a[30:31], v206 offset:13824
v_cvt_pk_bf16_f32 v179, v150, v151
v_exp_f32_e32 v161, v161
v_add_f32_e32 v244, v244, v160
s_waitcnt lgkmcnt(8)
v_mfma_f32_16x16x32_bf16 v[16:19], a[8:11], v[184:187], v[16:19]
s_waitcnt vmcnt(60)
s_mov_b32 m0, 0xc000
s_mov_b32 s78, 0x3c000
buffer_load_dwordx4 v12, s[48:51], s78 offen nt lds
ds_read_b128 a[0:3], v201 offset:32768
buffer_load_dwordx4 v13, s[48:51], s78 offen offset:1024 nt lds
v_mfma_f32_16x16x32_bf16 v[20:23], a[12:15], v[184:187], v[20:23]
s_waitcnt vmcnt(59)
buffer_load_dwordx4 v14, s[48:51], s78 offen offset:2048 nt lds
ds_read_b128 a[4:7], v201 offset:33280
v_exp_f32_e32 v162, v162
v_exp_f32_e32 v163, v163
s_waitcnt lgkmcnt(6)
v_mfma_f32_16x16x32_bf16 v[32:35], a[16:19], v[184:187], v[32:35]
ds_read_b128 a[8:11], v201 offset:33792
buffer_load_dwordx4 v15, s[48:51], s78 offen offset:3072 nt lds
s_waitcnt vmcnt(59)
s_mov_b32 m0, 0xd000
s_mov_b32 s78, 0x3d000
buffer_load_dwordx4 v12, s[48:51], s78 offen nt lds
v_mfma_f32_16x16x32_bf16 v[36:39], a[20:23], v[184:187], v[36:39]
s_waitcnt lgkmcnt(3)
ds_read_b128 a[12:15], v201 offset:34304
buffer_load_dwordx4 v13, s[48:51], s78 offen offset:1024 nt lds
v_exp_f32_e32 v164, v164
v_exp_f32_e32 v165, v165
v_exp_f32_e32 v166, v166
v_mfma_f32_16x16x32_bf16 v[48:51], a[24:27], v[184:187], v[48:51]
s_waitcnt vmcnt(59)
buffer_load_dwordx4 v14, s[48:51], s78 offen offset:2048 nt lds
ds_read_b128 a[16:19], v201 offset:36864
buffer_load_dwordx4 v15, s[48:51], s78 offen offset:3072 nt lds
v_exp_f32_e32 v167, v167
v_mfma_f32_16x16x32_bf16 v[52:55], a[28:31], v[184:187], v[52:55]
s_waitcnt vmcnt(59)
s_mov_b32 m0, 0xe000
s_mov_b32 s78, 0x3e000
buffer_load_dwordx4 v12, s[48:51], s78 offen nt lds
ds_read_b128 a[20:23], v201 offset:37376
v_cvt_pk_bf16_f32 v184, v160, v161
s_waitcnt lgkmcnt(4)
v_mfma_f32_16x16x32_bf16 v[128:131], a[0:3], a[64:67], v[192:195]
s_cbranch_vccnz .Lk_brb14
.Lk_bbb14:
ds_read_b128 a[24:27], v201 offset:37888
buffer_load_dwordx4 v13, s[48:51], s78 offen offset:1024 nt lds
s_waitcnt vmcnt(59)
buffer_load_dwordx4 v14, s[48:51], s78 offen offset:2048 nt lds
v_mfma_f32_16x16x32_bf16 v[128:131], a[4:7], a[72:75], v[128:131]
s_waitcnt lgkmcnt(3)
ds_read_b128 a[28:31], v201 offset:38400
buffer_load_dwordx4 v15, s[48:51], s78 offen offset:3072 nt lds
v_cvt_pk_bf16_f32 v185, v162, v163
v_cvt_pk_bf16_f32 v186, v164, v165
v_cvt_pk_bf16_f32 v187, v166, v167
v_add_f32_e32 v244, v244, v161
)"
R"(
v_add_f32_e32 v244, v244, v162
v_mfma_f32_16x16x32_bf16 v[128:131], a[8:11], a[80:83], v[128:131]
s_waitcnt vmcnt(59)
s_mov_b32 m0, 0xf000
s_mov_b32 s78, 0x3f000
buffer_load_dwordx4 v12, s[48:51], s78 offen nt lds
ds_read_b128 a[0:3], v201 offset:40960
buffer_load_dwordx4 v13, s[48:51], s78 offen offset:1024 nt lds
v_mfma_f32_16x16x32_bf16 v[128:131], a[12:15], a[88:91], v[128:131]
s_waitcnt vmcnt(59)
buffer_load_dwordx4 v14, s[48:51], s78 offen offset:2048 nt lds
ds_read_b128 a[4:7], v201 offset:41472
v_add_f32_e32 v244, v244, v163
v_add_f32_e32 v244, v244, v164
v_add_f32_e32 v244, v244, v165
v_add_f32_e32 v244, v244, v166
s_waitcnt lgkmcnt(4)
v_mfma_f32_16x16x32_bf16 v[132:135], a[16:19], a[64:67], v[192:195]
ds_read_b128 a[8:11], v201 offset:41984
buffer_load_dwordx4 v15, s[48:51], s78 offen offset:3072 nt lds
v_add_f32_e32 v244, v244, v167
v_mfma_f32_16x16x32_bf16 v[132:135], a[20:23], a[72:75], v[132:135]
s_mov_b32 m0, 0x8000
s_xor_b32 s78, s35, 0
buffer_load_dwordx4 v40, s[52:55], s78 offen lds
ds_read_b128 a[12:15], v201 offset:42496
buffer_load_dwordx4 v41, s[52:55], s78 offen offset:1024 lds
s_waitcnt lgkmcnt(4)
v_mfma_f32_16x16x32_bf16 v[132:135], a[24:27], a[80:83], v[132:135]
ds_read_b128 a[16:19], v201 offset:45056
buffer_load_dwordx4 v42, s[52:55], s78 offen offset:2048 lds
v_max3_f32 v236, v128, v129, v130
v_mfma_f32_16x16x32_bf16 v[132:135], a[28:31], a[88:91], v[132:135]
s_waitcnt lgkmcnt(3)
ds_read_b128 a[20:23], v201 offset:45568
buffer_load_dwordx4 v43, s[52:55], s78 offen offset:3072 lds
s_mov_b32 m0, 0x9000
s_xor_b32 s78, s35, 0x4000
buffer_load_dwordx4 v40, s[52:55], s78 offen lds
v_mfma_f32_16x16x32_bf16 v[144:147], a[0:3], a[64:67], v[192:195]
ds_read_b128 a[24:27], v201 offset:46080
buffer_load_dwordx4 v41, s[52:55], s78 offen offset:1024 lds
v_mfma_f32_16x16x32_bf16 v[144:147], a[4:7], a[72:75], v[144:147]
buffer_load_dwordx4 v42, s[52:55], s78 offen offset:2048 lds
buffer_load_dwordx4 v43, s[52:55], s78 offen offset:3072 lds
s_waitcnt vmcnt(25)
s_barrier
ds_read_b128 a[28:31], v201 offset:46592
s_waitcnt lgkmcnt(4)
v_mfma_f32_16x16x32_bf16 v[144:147], a[8:11], a[80:83], v[144:147]
s_mov_b32 m0, 0xa000
s_xor_b32 s78, s35, 0x8000
buffer_load_dwordx4 v40, s[52:55], s78 offen lds
ds_read_b64_tr_b16 a[0:1], v203
ds_read_b64_tr_b16 a[2:3], v203 offset:4096
v_max3_f32 v236, v236, v131, v132
v_max3_f32 v236, v236, v133, v134
v_mfma_f32_16x16x32_bf16 v[144:147], a[12:15], a[88:91], v[144:147]
s_waitcnt lgkmcnt(4)
ds_read_b64_tr_b16 a[4:5], v204
ds_read_b64_tr_b16 a[6:7], v204 offset:4096
buffer_load_dwordx4 v41, s[52:55], s78 offen offset:1024 lds
buffer_load_dwordx4 v42, s[52:55], s78 offen offset:2048 lds
v_mfma_f32_16x16x32_bf16 v[148:151], a[16:19], a[64:67], v[192:195]
ds_read_b64_tr_b16 a[8:9], v203 offset:512
ds_read_b64_tr_b16 a[10:11], v203 offset:4608
buffer_load_dwordx4 v43, s[52:55], s78 offen offset:3072 lds
v_mfma_f32_16x16x32_bf16 v[148:151], a[20:23], a[72:75], v[148:151]
s_mov_b32 m0, 0xb000
s_xor_b32 s78, s35, 0xc000
buffer_load_dwordx4 v40, s[52:55], s78 offen lds
ds_read_b64_tr_b16 a[12:13], v204 offset:512
ds_read_b64_tr_b16 a[14:15], v204 offset:4608
buffer_load_dwordx4 v41, s[52:55], s78 offen offset:1024 lds
s_waitcnt lgkmcnt(8)
v_mfma_f32_16x16x32_bf16 v[148:151], a[24:27], a[80:83], v[148:151]
ds_read_b64_tr_b16 a[16:17], v203 offset:1024
ds_read_b64_tr_b16 a[18:19], v203 offset:5120
buffer_load_dwordx4 v42, s[52:55], s78 offen offset:2048 lds
v_max3_f32 v236, v236, v135, v144
v_max3_f32 v236, v236, v145, v146
v_mfma_f32_16x16x32_bf16 v[148:151], a[28:31], a[88:91], v[148:151]
s_waitcnt lgkmcnt(6)
buffer_load_dwordx4 v43, s[52:55], s78 offen offset:3072 lds
s_barrier
ds_read_b64_tr_b16 a[20:21], v204 offset:1024
ds_read_b64_tr_b16 a[22:23], v204 offset:5120
v_mfma_f32_16x16x32_bf16 v[0:3], a[0:3], v[176:179], v[0:3]
s_mov_b32 m0, 0x1c000
s_xor_b32 s78, s35, 0x10000
buffer_load_dwordx4 v40, s[52:55], s78 offen lds
ds_read_b64_tr_b16 a[24:25], v203 offset:1536
ds_read_b64_tr_b16 a[26:27], v203 offset:5632
buffer_load_dwordx4 v41, s[52:55], s78 offen offset:1024 lds
v_mfma_f32_16x16x32_bf16 v[4:7], a[4:7], v[176:179], v[4:7]
s_waitcnt lgkmcnt(6)
ds_read_b64_tr_b16 a[28:29], v204 offset:1536
ds_read_b64_tr_b16 a[30:31], v204 offset:5632
buffer_load_dwordx4 v42, s[52:55], s78 offen offset:2048 lds
v_mfma_f32_16x16x32_bf16 v[16:19], a[8:11], v[176:179], v[16:19]
ds_read_b64_tr_b16 a[0:1], v203 offset:8192
ds_read_b64_tr_b16 a[2:3], v203 offset:12288
buffer_load_dwordx4 v43, s[52:55], s78 offen offset:3072 lds
s_mov_b32 m0, 0x1d000
s_xor_b32 s78, s35, 0x14000
buffer_load_dwordx4 v40, s[52:55], s78 offen lds
v_max3_f32 v236, v236, v147, v148
v_max3_f32 v236, v236, v149, v150
v_max_f32_e32 v248, v236, v151
v_mfma_f32_16x16x32_bf16 v[20:23], a[12:15], v[176:179], v[20:23]
ds_read_b64_tr_b16 a[4:5], v204 offset:8192
ds_read_b64_tr_b16 a[6:7], v204 offset:12288
buffer_load_dwordx4 v41, s[52:55], s78 offen offset:1024 lds
v_cmp_lt_f32_e32 vcc, s70, v248
s_nop 1
s_cbranch_vccnz .Lk_bra15
.Lk_bba15:
v_exp_f32_e32 v128, v128
s_waitcnt lgkmcnt(8)
v_mfma_f32_16x16x32_bf16 v[32:35], a[16:19], v[176:179], v[32:35]
ds_read_b64_tr_b16 a[8:9], v203 offset:8704
ds_read_b64_tr_b16 a[10:11], v203 offset:12800
buffer_load_dwordx4 v42, s[52:55], s78 offen offset:2048 lds
buffer_load_dwordx4 v43, s[52:55], s78 offen offset:3072 lds
v_exp_f32_e32 v129, v129
v_mfma_f32_16x16x32_bf16 v[36:39], a[20:23], v[176:179], v[36:39]
s_mov_b32 m0, 0x1e000
s_xor_b32 s78, s35, 0x18000
buffer_load_dwordx4 v40, s[52:55], s78 offen lds
ds_read_b64_tr_b16 a[12:13], v204 offset:8704
ds_read_b64_tr_b16 a[14:15], v204 offset:12800
v_exp_f32_e32 v130, v130
v_add_f32_e32 v244, v244, v128
s_waitcnt lgkmcnt(8)
v_mfma_f32_16x16x32_bf16 v[48:51], a[24:27], v[176:179], v[48:51]
ds_read_b64_tr_b16 a[16:17], v203 offset:9216
ds_read_b64_tr_b16 a[18:19], v203 offset:13312
buffer_load_dwordx4 v41, s[52:55], s78 offen offset:1024 lds
buffer_load_dwordx4 v42, s[52:55], s78 offen offset:2048 lds
v_exp_f32_e32 v131, v131
v_add_f32_e32 v244, v244, v129
v_mfma_f32_16x16x32_bf16 v[52:55], a[28:31], v[176:179], v[52:55]
s_waitcnt lgkmcnt(6)
ds_read_b64_tr_b16 a[20:21], v204 offset:9216
ds_read_b64_tr_b16 a[22:23], v204 offset:13312
buffer_load_dwordx4 v43, s[52:55], s78 offen offset:3072 lds
v_cvt_pk_bf16_f32 v176, v128, v129
v_cvt_pk_bf16_f32 v177, v130, v131
v_exp_f32_e32 v132, v132
v_exp_f32_e32 v133, v133
v_mfma_f32_16x16x32_bf16 v[0:3], a[0:3], v[184:187], v[0:3]
s_mov_b32 m0, 0x1f000
s_xor_b32 s78, s35, 0x1c000
buffer_load_dwordx4 v40, s[52:55], s78 offen lds
ds_read_b64_tr_b16 a[24:25], v203 offset:9728
ds_read_b64_tr_b16 a[26:27], v203 offset:13824
buffer_load_dwordx4 v41, s[52:55], s78 offen offset:1024 lds
v_mfma_f32_16x16x32_bf16 v[4:7], a[4:7], v[184:187], v[4:7]
s_waitcnt lgkmcnt(6)
ds_read_b64_tr_b16 a[28:29], v204 offset:9728
ds_read_b64_tr_b16 a[30:31], v204 offset:13824
buffer_load_dwordx4 v42, s[52:55], s78 offen offset:2048 lds
v_cvt_pk_bf16_f32 v178, v132, v133
v_exp_f32_e32 v134, v134
v_exp_f32_e32 v135, v135
v_mfma_f32_16x16x32_bf16 v[16:19], a[8:11], v[184:187], v[16:19]
buffer_load_dwordx4 v43, s[52:55], s78 offen offset:3072 lds
v_cvt_pk_bf16_f32 v179, v134, v135
v_exp_f32_e32 v144, v144
v_exp_f32_e32 v145, v145
v_exp_f32_e32 v146, v146
v_add_f32_e32 v244, v244, v130
v_mfma_f32_16x16x32_bf16 v[20:23], a[12:15], v[184:187], v[20:23]
s_mov_b32 m0, 0x10000
s_xor_b32 s78, s73, 0
buffer_load_dwordx4 v40, s[52:55], s78 offen lds
buffer_load_dwordx4 v41, s[52:55], s78 offen offset:1024 lds
v_exp_f32_e32 v147, v147
v_exp_f32_e32 v148, v148
s_waitcnt lgkmcnt(4)
v_mfma_f32_16x16x32_bf16 v[32:35], a[16:19], v[184:187], v[32:35]
buffer_load_dwordx4 v42, s[52:55], s78 offen offset:2048 lds
v_exp_f32_e32 v149, v149
v_exp_f32_e32 v150, v150
v_exp_f32_e32 v151, v151
v_mfma_f32_16x16x32_bf16 v[36:39], a[20:23], v[184:187], v[36:39]
s_waitcnt lgkmcnt(0)
buffer_load_dwordx4 v43, s[52:55], s78 offen offset:3072 lds
s_mov_b32 m0, 0x11000
s_xor_b32 s78, s73, 0x4000
buffer_load_dwordx4 v40, s[52:55], s78 offen lds
buffer_load_dword v235, v231, s[56:59], 0 offen sc1
v_add_f32_e32 v244, v244, v131
v_add_f32_e32 v244, v244, v132
v_add_f32_e32 v244, v244, v133
v_mfma_f32_16x16x32_bf16 v[48:51], a[24:27], v[184:187], v[48:51]
buffer_load_dwordx4 v41, s[52:55], s78 offen offset:1024 lds
v_add_f32_e32 v244, v244, v134
v_add_f32_e32 v244, v244, v135
v_add_f32_e32 v244, v244, v144
)"
R"(
v_add_f32_e32 v244, v244, v145
v_add_f32_e32 v244, v244, v146
v_add_f32_e32 v244, v244, v147
v_add_f32_e32 v244, v244, v148
v_add_f32_e32 v244, v244, v149
v_add_f32_e32 v244, v244, v150
v_add_f32_e32 v244, v244, v151
v_mfma_f32_16x16x32_bf16 v[52:55], a[28:31], v[184:187], v[52:55]
s_cbranch_vccnz .Lk_brb15
.Lk_bbb15:
buffer_load_dwordx4 v42, s[52:55], s78 offen offset:2048 lds
buffer_load_dwordx4 v43, s[52:55], s78 offen offset:3072 lds
s_waitcnt vmcnt(41)
s_barrier
ds_read_b64_tr_b16 a[0:1], v203 offset:49152
ds_read_b64_tr_b16 a[2:3], v203 offset:53248
ds_read_b64_tr_b16 a[4:5], v203 offset:57344
ds_read_b64_tr_b16 a[6:7], v203 offset:61440
ds_read_b64_tr_b16 a[8:9], v204 offset:49152
ds_read_b64_tr_b16 a[10:11], v204 offset:53248
ds_read_b64_tr_b16 a[12:13], v204 offset:57344
ds_read_b64_tr_b16 a[14:15], v204 offset:61440
ds_read_b64_tr_b16 a[16:17], v203 offset:49664
ds_read_b64_tr_b16 a[18:19], v203 offset:53760
ds_read_b64_tr_b16 a[20:21], v203 offset:57856
ds_read_b64_tr_b16 a[22:23], v203 offset:61952
s_waitcnt lgkmcnt(8)
v_mfma_f32_16x16x32_bf16 v[0:3], a[0:3], v[176:179], v[0:3]
s_mov_b32 m0, 0x12000
s_xor_b32 s78, s73, 0x8000
buffer_load_dwordx4 v40, s[52:55], s78 offen lds
ds_read_b64_tr_b16 a[24:25], v204 offset:49664
ds_read_b64_tr_b16 a[26:27], v204 offset:53760
v_cvt_pk_bf16_f32 v184, v144, v145
v_cvt_pk_bf16_f32 v185, v146, v147
v_cvt_pk_bf16_f32 v186, v148, v149
v_cvt_pk_bf16_f32 v187, v150, v151
s_nop 1
v_mfma_f32_16x16x32_bf16 v[0:3], a[4:7], v[184:187], v[0:3]
s_waitcnt lgkmcnt(6)
ds_read_b64_tr_b16 a[28:29], v204 offset:57856
ds_read_b64_tr_b16 a[30:31], v204 offset:61952
buffer_load_dwordx4 v41, s[52:55], s78 offen offset:1024 lds
buffer_load_dwordx4 v42, s[52:55], s78 offen offset:2048 lds
v_mfma_f32_16x16x32_bf16 v[4:7], a[8:11], v[176:179], v[4:7]
ds_read_b64_tr_b16 a[0:1], v203 offset:50176
ds_read_b64_tr_b16 a[2:3], v203 offset:54272
buffer_load_dwordx4 v43, s[52:55], s78 offen offset:3072 lds
v_mov_b32_e32 v248, v244
s_nop 1
v_permlane16_swap_b32_e32 v244, v248
v_add_f32_e32 v244, v244, v248
v_mov_b32_e32 v248, v244
v_mfma_f32_16x16x32_bf16 v[4:7], a[12:15], v[184:187], v[4:7]
s_mov_b32 m0, 0x13000
s_xor_b32 s78, s73, 0xc000
buffer_load_dwordx4 v40, s[52:55], s78 offen lds
ds_read_b64_tr_b16 a[4:5], v203 offset:58368
ds_read_b64_tr_b16 a[6:7], v203 offset:62464
buffer_load_dwordx4 v41, s[52:55], s78 offen offset:1024 lds
s_waitcnt lgkmcnt(8)
v_mfma_f32_16x16x32_bf16 v[16:19], a[16:19], v[176:179], v[16:19]
ds_read_b64_tr_b16 a[8:9], v204 offset:50176
ds_read_b64_tr_b16 a[10:11], v204 offset:54272
buffer_load_dwordx4 v42, s[52:55], s78 offen offset:2048 lds
v_permlane32_swap_b32_e32 v244, v248
v_add_f32_e32 v244, v244, v248
v_rcp_f32_e32 v240, v244
v_mfma_f32_16x16x32_bf16 v[16:19], a[20:23], v[184:187], v[16:19]
s_waitcnt lgkmcnt(6)
ds_read_b64_tr_b16 a[12:13], v204 offset:58368
ds_read_b64_tr_b16 a[14:15], v204 offset:62464
buffer_load_dwordx4 v43, s[52:55], s78 offen offset:3072 lds
v_mul_f32_e32 v0, v240, v0
v_mul_f32_e32 v1, v240, v1
v_mul_f32_e32 v2, v240, v2
v_mul_f32_e32 v3, v240, v3
v_mfma_f32_16x16x32_bf16 v[20:23], a[24:27], v[176:179], v[20:23]
s_mov_b32 m0, 0x18000
s_xor_b32 s78, s73, 0x10000
buffer_load_dwordx4 v40, s[52:55], s78 offen lds
ds_read_b64_tr_b16 a[16:17], v203 offset:50688
ds_read_b64_tr_b16 a[18:19], v203 offset:54784
v_mul_f32_e32 v4, v240, v4
v_mul_f32_e32 v5, v240, v5
v_mfma_f32_16x16x32_bf16 v[20:23], a[28:31], v[184:187], v[20:23]
s_waitcnt lgkmcnt(6)
ds_read_b64_tr_b16 a[20:21], v203 offset:58880
ds_read_b64_tr_b16 a[22:23], v203 offset:62976
buffer_load_dwordx4 v41, s[52:55], s78 offen offset:1024 lds
v_mul_f32_e32 v6, v240, v6
v_mul_f32_e32 v7, v240, v7
v_cvt_pk_bf16_f32 v0, v0, v1
v_cvt_pk_bf16_f32 v1, v2, v3
v_cvt_pk_bf16_f32 v2, v4, v5
v_mfma_f32_16x16x32_bf16 v[32:35], a[0:3], v[176:179], v[32:35]
ds_read_b64_tr_b16 a[24:25], v204 offset:50688
ds_read_b64_tr_b16 a[26:27], v204 offset:54784
buffer_load_dwordx4 v42, s[52:55], s78 offen offset:2048 lds
v_cvt_pk_bf16_f32 v3, v6, v7
v_mul_f32_e32 v16, v240, v16
v_mul_f32_e32 v17, v240, v17
v_mul_f32_e32 v18, v240, v18
v_mfma_f32_16x16x32_bf16 v[32:35], a[4:7], v[184:187], v[32:35]
buffer_load_dwordx4 v43, s[52:55], s78 offen offset:3072 lds
s_waitcnt vmcnt(63)
s_barrier
ds_read_b64_tr_b16 a[28:29], v204 offset:58880
ds_read_b64_tr_b16 a[30:31], v204 offset:62976
v_mul_f32_e32 v19, v240, v19
v_cvt_pk_bf16_f32 v16, v16, v17
v_cvt_pk_bf16_f32 v17, v18, v19
s_waitcnt lgkmcnt(8)
v_mfma_f32_16x16x32_bf16 v[36:39], a[8:11], v[176:179], v[36:39]
ds_read_b64_tr_b16 a[0:1], v203 offset:16896
ds_read_b64_tr_b16 a[2:3], v203 offset:20992
s_waitcnt vmcnt(15)
v_cmp_eq_u32_e64 s[74:75], v235, v233
s_nop 1
s_and_b32 s79, s74, 14
s_cmp_eq_u32 s79, 14
s_cbranch_scc0 .Lk_bslow1
.Lk_bok1:
buffer_load_dword v234, v232, s[56:59], 0 offen offset:128 sc1
v_mfma_f32_16x16x32_bf16 v[36:39], a[12:15], v[184:187], v[36:39]
s_xor_b32 s86, s96, 0
s_add_u32 s87, s86, 0x1000
buffer_load_dwordx4 a[96:99], v229, s[56:59], s86 offen sc1
ds_read_b64_tr_b16 a[4:5], v204 offset:16896
ds_read_b64_tr_b16 a[6:7], v204 offset:20992
v_mul_f32_e32 v20, v240, v20
v_mul_f32_e32 v21, v240, v21
s_waitcnt lgkmcnt(8)
v_mfma_f32_16x16x32_bf16 v[48:51], a[16:19], v[176:179], v[48:51]
s_mov_b32 m0, 0x19000
s_xor_b32 s78, s73, 0x14000
buffer_load_dwordx4 v40, s[52:55], s78 offen lds
buffer_load_dwordx4 a[104:107], v229, s[56:59], s86 offen offset:2048 sc1
ds_read_b64_tr_b16 a[8:9], v203 offset:17408
ds_read_b64_tr_b16 a[10:11], v203 offset:21504
v_mfma_f32_16x16x32_bf16 v[48:51], a[20:23], v[184:187], v[48:51]
s_waitcnt lgkmcnt(6)
buffer_load_dwordx4 v41, s[52:55], s78 offen offset:1024 lds
buffer_load_dwordx4 a[112:115], v229, s[56:59], s87 offen sc1
ds_read_b64_tr_b16 a[12:13], v204 offset:17408
ds_read_b64_tr_b16 a[14:15], v204 offset:21504
v_mul_f32_e32 v22, v240, v22
v_mfma_f32_16x16x32_bf16 v[52:55], a[24:27], v[176:179], v[52:55]
buffer_load_dwordx4 v42, s[52:55], s78 offen offset:2048 lds
buffer_load_dwordx4 a[120:123], v229, s[56:59], s87 offen offset:2048 sc1
ds_read_b64_tr_b16 a[16:17], v203 offset:17920
ds_read_b64_tr_b16 a[18:19], v203 offset:22016
v_mul_f32_e32 v23, v240, v23
v_mfma_f32_16x16x32_bf16 v[52:55], a[28:31], v[184:187], v[52:55]
s_waitcnt lgkmcnt(6)
buffer_load_dwordx4 v43, s[52:55], s78 offen offset:3072 lds
ds_read_b64_tr_b16 a[20:21], v204 offset:17920
ds_read_b64_tr_b16 a[22:23], v204 offset:22016
v_cvt_pk_bf16_f32 v18, v20, v21
v_cvt_pk_bf16_f32 v19, v22, v23
v_mul_f32_e32 v32, v240, v32
v_mul_f32_e32 v33, v240, v33
v_mfma_f32_16x16x32_bf16 v[80:83], a[0:3], v[0:3], v[80:83]
s_mov_b32 m0, 0x1a000
s_xor_b32 s78, s73, 0x18000
buffer_load_dwordx4 v40, s[52:55], s78 offen lds
ds_read_b64_tr_b16 a[24:25], v203 offset:16384
ds_read_b64_tr_b16 a[26:27], v203 offset:20480
v_mul_f32_e32 v34, v240, v34
v_mfma_f32_16x16x32_bf16 v[84:87], a[4:7], v[0:3], v[84:87]
s_add_u32 s84, s81, 0x800000
buffer_store_dwordx4 v[0:3], v229, s[56:59], s84 offen sc1
ds_read_b64_tr_b16 a[28:29], v204 offset:16384
ds_read_b64_tr_b16 a[30:31], v204 offset:20480
buffer_load_dwordx4 v41, s[52:55], s78 offen offset:1024 lds
s_waitcnt lgkmcnt(8)
v_mfma_f32_16x16x32_bf16 v[96:99], a[8:11], v[0:3], v[96:99]
ds_read_b64_tr_b16 a[0:1], v203 offset:25088
ds_read_b64_tr_b16 a[2:3], v203 offset:29184
buffer_load_dwordx4 v42, s[52:55], s78 offen offset:2048 lds
buffer_store_dwordx4 v[16:19], v229, s[56:59], s84 offen offset:2048 sc1
v_mul_f32_e32 v35, v240, v35
v_mul_f32_e32 v36, v240, v36
v_mfma_f32_16x16x32_bf16 v[100:103], a[12:15], v[0:3], v[100:103]
s_waitcnt lgkmcnt(6)
ds_read_b64_tr_b16 a[4:5], v204 offset:25088
ds_read_b64_tr_b16 a[6:7], v204 offset:29184
buffer_load_dwordx4 v43, s[52:55], s78 offen offset:3072 lds
v_mul_f32_e32 v37, v240, v37
v_mul_f32_e32 v38, v240, v38
v_mul_f32_e32 v39, v240, v39
v_mfma_f32_16x16x32_bf16 v[112:115], a[16:19], v[0:3], v[112:115]
ds_read_b64_tr_b16 a[8:9], v203 offset:25600
ds_read_b64_tr_b16 a[10:11], v203 offset:29696
v_cvt_pk_bf16_f32 v32, v32, v33
v_cvt_pk_bf16_f32 v33, v34, v35
v_cvt_pk_bf16_f32 v34, v36, v37
v_cvt_pk_bf16_f32 v35, v38, v39
s_add_u32 s84, s81, 0x801000
buffer_store_dwordx4 v[32:35], v229, s[56:59], s84 offen sc1
)"
R"(
v_mfma_f32_16x16x32_bf16 v[116:119], a[20:23], v[0:3], v[116:119]
s_mov_b32 m0, 0x1b000
s_xor_b32 s78, s73, 0x1c000
buffer_load_dwordx4 v40, s[52:55], s78 offen lds
ds_read_b64_tr_b16 a[12:13], v204 offset:25600
ds_read_b64_tr_b16 a[14:15], v204 offset:29696
v_mul_f32_e32 v48, v240, v48
s_waitcnt lgkmcnt(8)
v_mfma_f32_16x16x32_bf16 v[64:67], a[24:27], v[0:3], v[64:67]
ds_read_b64_tr_b16 a[16:17], v203 offset:26112
ds_read_b64_tr_b16 a[18:19], v203 offset:30208
buffer_load_dwordx4 v41, s[52:55], s78 offen offset:1024 lds
v_mul_f32_e32 v49, v240, v49
v_mul_f32_e32 v50, v240, v50
v_mul_f32_e32 v51, v240, v51
v_mfma_f32_16x16x32_bf16 v[68:71], a[28:31], v[0:3], v[68:71]
s_waitcnt lgkmcnt(6)
ds_read_b64_tr_b16 a[20:21], v204 offset:26112
ds_read_b64_tr_b16 a[22:23], v204 offset:30208
v_mul_f32_e32 v52, v240, v52
v_mul_f32_e32 v53, v240, v53
v_mul_f32_e32 v54, v240, v54
v_mul_f32_e32 v55, v240, v55
v_cvt_pk_bf16_f32 v48, v48, v49
v_cvt_pk_bf16_f32 v49, v50, v51
v_cvt_pk_bf16_f32 v50, v52, v53
v_mfma_f32_16x16x32_bf16 v[80:83], a[0:3], v[16:19], v[80:83]
ds_read_b64_tr_b16 a[24:25], v203 offset:24576
ds_read_b64_tr_b16 a[26:27], v203 offset:28672
v_cvt_pk_bf16_f32 v51, v54, v55
buffer_store_dwordx4 v[48:51], v229, s[56:59], s84 offen offset:2048 sc1
buffer_load_dwordx4 v42, s[52:55], s78 offen offset:2048 lds
v_mfma_f32_16x16x32_bf16 v[84:87], a[4:7], v[16:19], v[84:87]
buffer_load_dwordx4 v43, s[52:55], s78 offen offset:3072 lds
s_waitcnt vmcnt(63)
s_barrier
ds_read_b64_tr_b16 a[28:29], v204 offset:24576
ds_read_b64_tr_b16 a[30:31], v204 offset:28672
s_waitcnt lgkmcnt(8)
v_mfma_f32_16x16x32_bf16 v[96:99], a[8:11], v[16:19], v[96:99]
ds_read_b64_tr_b16 a[0:1], v205 offset:16896
ds_read_b64_tr_b16 a[2:3], v205 offset:20992
v_mfma_f32_16x16x32_bf16 v[100:103], a[12:15], v[16:19], v[100:103]
s_mov_b32 exec_lo, 0xff000000
s_mov_b32 exec_hi, 0xff00ff00
buffer_load_dword v251, v252, s[52:55], 0 offen
s_mov_b64 exec, -1
ds_read_b64_tr_b16 a[4:5], v206 offset:16896
ds_read_b64_tr_b16 a[6:7], v206 offset:20992
s_waitcnt lgkmcnt(8)
v_mfma_f32_16x16x32_bf16 v[112:115], a[16:19], v[16:19], v[112:115]
s_mov_b32 m0, 0
s_xor_b32 s78, s34, 0x20000
buffer_load_dwordx4 v40, s[52:55], s78 offen lds
ds_read_b64_tr_b16 a[8:9], v205 offset:17408
ds_read_b64_tr_b16 a[10:11], v205 offset:21504
v_mfma_f32_16x16x32_bf16 v[116:119], a[20:23], v[16:19], v[116:119]
s_waitcnt lgkmcnt(6)
ds_read_b64_tr_b16 a[12:13], v206 offset:17408
ds_read_b64_tr_b16 a[14:15], v206 offset:21504
buffer_load_dwordx4 v41, s[52:55], s78 offen offset:1024 lds
v_mfma_f32_16x16x32_bf16 v[64:67], a[24:27], v[16:19], v[64:67]
ds_read_b64_tr_b16 a[16:17], v205 offset:17920
ds_read_b64_tr_b16 a[18:19], v205 offset:22016
buffer_load_dwordx4 v42, s[52:55], s78 offen offset:2048 lds
v_mfma_f32_16x16x32_bf16 v[68:71], a[28:31], v[16:19], v[68:71]
s_waitcnt lgkmcnt(6)
ds_read_b64_tr_b16 a[20:21], v206 offset:17920
ds_read_b64_tr_b16 a[22:23], v206 offset:22016
buffer_load_dwordx4 v43, s[52:55], s78 offen offset:3072 lds
v_mfma_f32_16x16x32_bf16 v[80:83], a[0:3], v[32:35], v[80:83]
ds_read_b64_tr_b16 a[24:25], v205 offset:16384
ds_read_b64_tr_b16 a[26:27], v205 offset:20480
v_mfma_f32_16x16x32_bf16 v[84:87], a[4:7], v[32:35], v[84:87]
s_mov_b32 m0, 0x1000
s_xor_b32 s78, s34, 0x24000
buffer_load_dwordx4 v40, s[52:55], s78 offen lds
ds_read_b64_tr_b16 a[28:29], v206 offset:16384
ds_read_b64_tr_b16 a[30:31], v206 offset:20480
s_waitcnt lgkmcnt(8)
v_mfma_f32_16x16x32_bf16 v[96:99], a[8:11], v[32:35], v[96:99]
ds_read_b64_tr_b16 a[0:1], v205 offset:25088
ds_read_b64_tr_b16 a[2:3], v205 offset:29184
buffer_load_dwordx4 v41, s[52:55], s78 offen offset:1024 lds
v_mfma_f32_16x16x32_bf16 v[100:103], a[12:15], v[32:35], v[100:103]
s_waitcnt lgkmcnt(6)
ds_read_b64_tr_b16 a[4:5], v206 offset:25088
ds_read_b64_tr_b16 a[6:7], v206 offset:29184
buffer_load_dwordx4 v42, s[52:55], s78 offen offset:2048 lds
v_mfma_f32_16x16x32_bf16 v[112:115], a[16:19], v[32:35], v[112:115]
ds_read_b64_tr_b16 a[8:9], v205 offset:25600
ds_read_b64_tr_b16 a[10:11], v205 offset:29696
buffer_load_dwordx4 v43, s[52:55], s78 offen offset:3072 lds
v_mfma_f32_16x16x32_bf16 v[116:119], a[20:23], v[32:35], v[116:119]
s_waitcnt lgkmcnt(6)
ds_read_b64_tr_b16 a[12:13], v206 offset:25600
ds_read_b64_tr_b16 a[14:15], v206 offset:29696
v_mfma_f32_16x16x32_bf16 v[64:67], a[24:27], v[32:35], v[64:67]
s_mov_b32 m0, 0x2000
s_xor_b32 s78, s34, 0x28000
buffer_load_dwordx4 v40, s[52:55], s78 offen lds
ds_read_b64_tr_b16 a[16:17], v205 offset:26112
ds_read_b64_tr_b16 a[18:19], v205 offset:30208
v_mfma_f32_16x16x32_bf16 v[68:71], a[28:31], v[32:35], v[68:71]
s_waitcnt lgkmcnt(6)
ds_read_b64_tr_b16 a[20:21], v206 offset:26112
ds_read_b64_tr_b16 a[22:23], v206 offset:30208
buffer_load_dwordx4 v41, s[52:55], s78 offen offset:1024 lds
v_mfma_f32_16x16x32_bf16 v[80:83], a[0:3], v[48:51], v[80:83]
ds_read_b64_tr_b16 a[24:25], v205 offset:24576
ds_read_b64_tr_b16 a[26:27], v205 offset:28672
buffer_load_dwordx4 v42, s[52:55], s78 offen offset:2048 lds
v_mfma_f32_16x16x32_bf16 v[84:87], a[4:7], v[48:51], v[84:87]
buffer_load_dwordx4 v43, s[52:55], s78 offen offset:3072 lds
s_waitcnt vmcnt(63)
s_barrier
ds_read_b64_tr_b16 a[28:29], v206 offset:24576
ds_read_b64_tr_b16 a[30:31], v206 offset:28672
s_waitcnt lgkmcnt(8)
v_mfma_f32_16x16x32_bf16 v[96:99], a[8:11], v[48:51], v[96:99]
ds_read_b64_tr_b16 a[0:1], v207 offset:512
ds_read_b64_tr_b16 a[2:3], v207 offset:4608
v_mfma_f32_16x16x32_bf16 v[100:103], a[12:15], v[48:51], v[100:103]
s_mov_b32 m0, 0x3000
s_xor_b32 s78, s34, 0x2c000
buffer_load_dwordx4 v40, s[52:55], s78 offen lds
ds_read_b64_tr_b16 a[4:5], v208 offset:512
ds_read_b64_tr_b16 a[6:7], v208 offset:4608
s_waitcnt lgkmcnt(8)
v_mfma_f32_16x16x32_bf16 v[112:115], a[16:19], v[48:51], v[112:115]
ds_read_b64_tr_b16 a[8:9], v207 offset:1024
ds_read_b64_tr_b16 a[10:11], v207 offset:5120
buffer_load_dwordx4 v41, s[52:55], s78 offen offset:1024 lds
v_mfma_f32_16x16x32_bf16 v[116:119], a[20:23], v[48:51], v[116:119]
s_waitcnt lgkmcnt(6)
ds_read_b64_tr_b16 a[12:13], v208 offset:1024
ds_read_b64_tr_b16 a[14:15], v208 offset:5120
buffer_load_dwordx4 v42, s[52:55], s78 offen offset:2048 lds
v_mfma_f32_16x16x32_bf16 v[64:67], a[24:27], v[48:51], v[64:67]
ds_read_b64_tr_b16 a[16:17], v207 offset:1536
ds_read_b64_tr_b16 a[18:19], v207 offset:5632
buffer_load_dwordx4 v43, s[52:55], s78 offen offset:3072 lds
v_mfma_f32_16x16x32_bf16 v[68:71], a[28:31], v[48:51], v[68:71]
s_waitcnt vmcnt(36)
ds_read_b64_tr_b16 a[20:21], v208 offset:1536
ds_read_b64_tr_b16 a[22:23], v208 offset:5632
s_waitcnt lgkmcnt(8)
v_mfma_f32_16x16x32_bf16 v[80:83], a[0:3], a[96:99], v[80:83]
ds_read_b64_tr_b16 a[24:25], v207
ds_read_b64_tr_b16 a[26:27], v207 offset:4096
v_mfma_f32_16x16x32_bf16 v[84:87], a[4:7], a[96:99], v[84:87]
s_waitcnt lgkmcnt(6)
ds_read_b64_tr_b16 a[28:29], v208
ds_read_b64_tr_b16 a[30:31], v208 offset:4096
v_mfma_f32_16x16x32_bf16 v[96:99], a[8:11], a[96:99], v[96:99]
ds_read_b64_tr_b16 a[0:1], v207 offset:8704
ds_read_b64_tr_b16 a[2:3], v207 offset:12800
v_mfma_f32_16x16x32_bf16 v[100:103], a[12:15], a[96:99], v[100:103]
s_waitcnt lgkmcnt(6)
ds_read_b64_tr_b16 a[4:5], v208 offset:8704
ds_read_b64_tr_b16 a[6:7], v208 offset:12800
v_mfma_f32_16x16x32_bf16 v[112:115], a[16:19], a[96:99], v[112:115]
ds_read_b64_tr_b16 a[8:9], v207 offset:9216
ds_read_b64_tr_b16 a[10:11], v207 offset:13312
v_mfma_f32_16x16x32_bf16 v[116:119], a[20:23], a[96:99], v[116:119]
s_waitcnt lgkmcnt(6)
ds_read_b64_tr_b16 a[12:13], v208 offset:9216
ds_read_b64_tr_b16 a[14:15], v208 offset:13312
v_mfma_f32_16x16x32_bf16 v[64:67], a[24:27], a[96:99], v[64:67]
ds_read_b64_tr_b16 a[16:17], v207 offset:9728
ds_read_b64_tr_b16 a[18:19], v207 offset:13824
v_mfma_f32_16x16x32_bf16 v[68:71], a[28:31], a[96:99], v[68:71]
s_waitcnt vmcnt(34)
ds_read_b64_tr_b16 a[20:21], v208 offset:9728
ds_read_b64_tr_b16 a[22:23], v208 offset:13824
s_waitcnt lgkmcnt(8)
v_mfma_f32_16x16x32_bf16 v[80:83], a[0:3], a[104:107], v[80:83]
ds_read_b64_tr_b16 a[24:25], v207 offset:8192
ds_read_b64_tr_b16 a[26:27], v207 offset:12288
v_mfma_f32_16x16x32_bf16 v[84:87], a[4:7], a[104:107], v[84:87]
s_waitcnt vmcnt(63)
s_barrier
ds_read_b64_tr_b16 a[28:29], v208 offset:8192
ds_read_b64_tr_b16 a[30:31], v208 offset:12288
s_waitcnt lgkmcnt(8)
v_mfma_f32_16x16x32_bf16 v[96:99], a[8:11], a[104:107], v[96:99]
s_waitcnt vmcnt(19)
s_mov_b64 exec, 0xee
buffer_store_dword v234, v232, s[56:59], 0 offen sc1
s_mov_b64 exec, -1
ds_read_b64_tr_b16 a[0:1], v207 offset:16896
ds_read_b64_tr_b16 a[2:3], v207 offset:20992
v_mfma_f32_16x16x32_bf16 v[100:103], a[12:15], a[104:107], v[100:103]
s_xor_b32 s86, s97, 0
s_add_u32 s87, s86, 0x1000
buffer_load_dwordx4 a[128:131], v229, s[56:59], s86 offen sc1
ds_read_b64_tr_b16 a[4:5], v208 offset:16896
ds_read_b64_tr_b16 a[6:7], v208 offset:20992
s_waitcnt lgkmcnt(8)
v_mfma_f32_16x16x32_bf16 v[112:115], a[16:19], a[104:107], v[112:115]
)"
R"(
ds_read_b64_tr_b16 a[8:9], v207 offset:17408
ds_read_b64_tr_b16 a[10:11], v207 offset:21504
v_mfma_f32_16x16x32_bf16 v[116:119], a[20:23], a[104:107], v[116:119]
s_waitcnt lgkmcnt(6)
ds_read_b64_tr_b16 a[12:13], v208 offset:17408
ds_read_b64_tr_b16 a[14:15], v208 offset:21504
buffer_load_dwordx4 a[136:139], v229, s[56:59], s86 offen offset:2048 sc1
v_mfma_f32_16x16x32_bf16 v[64:67], a[24:27], a[104:107], v[64:67]
ds_read_b64_tr_b16 a[16:17], v207 offset:17920
ds_read_b64_tr_b16 a[18:19], v207 offset:22016
v_mfma_f32_16x16x32_bf16 v[68:71], a[28:31], a[104:107], v[68:71]
s_waitcnt vmcnt(35)
ds_read_b64_tr_b16 a[20:21], v208 offset:17920
ds_read_b64_tr_b16 a[22:23], v208 offset:22016
s_waitcnt lgkmcnt(8)
v_mfma_f32_16x16x32_bf16 v[80:83], a[0:3], a[112:115], v[80:83]
ds_read_b64_tr_b16 a[24:25], v207 offset:16384
ds_read_b64_tr_b16 a[26:27], v207 offset:20480
v_mfma_f32_16x16x32_bf16 v[84:87], a[4:7], a[112:115], v[84:87]
s_waitcnt lgkmcnt(6)
ds_read_b64_tr_b16 a[28:29], v208 offset:16384
ds_read_b64_tr_b16 a[30:31], v208 offset:20480
buffer_load_dwordx4 a[144:147], v229, s[56:59], s87 offen sc1
v_mfma_f32_16x16x32_bf16 v[96:99], a[8:11], a[112:115], v[96:99]
ds_read_b64_tr_b16 a[0:1], v207 offset:25088
ds_read_b64_tr_b16 a[2:3], v207 offset:29184
v_mfma_f32_16x16x32_bf16 v[100:103], a[12:15], a[112:115], v[100:103]
s_waitcnt lgkmcnt(6)
ds_read_b64_tr_b16 a[4:5], v208 offset:25088
ds_read_b64_tr_b16 a[6:7], v208 offset:29184
v_mfma_f32_16x16x32_bf16 v[112:115], a[16:19], a[112:115], v[112:115]
ds_read_b64_tr_b16 a[8:9], v207 offset:25600
ds_read_b64_tr_b16 a[10:11], v207 offset:29696
buffer_load_dwordx4 a[152:155], v229, s[56:59], s87 offen offset:2048 sc1
v_mfma_f32_16x16x32_bf16 v[116:119], a[20:23], a[112:115], v[116:119]
s_waitcnt lgkmcnt(6)
ds_read_b64_tr_b16 a[12:13], v208 offset:25600
ds_read_b64_tr_b16 a[14:15], v208 offset:29696
v_mfma_f32_16x16x32_bf16 v[64:67], a[24:27], a[112:115], v[64:67]
ds_read_b64_tr_b16 a[16:17], v207 offset:26112
ds_read_b64_tr_b16 a[18:19], v207 offset:30208
v_mfma_f32_16x16x32_bf16 v[68:71], a[28:31], a[112:115], v[68:71]
s_waitcnt vmcnt(35)
ds_read_b64_tr_b16 a[20:21], v208 offset:26112
ds_read_b64_tr_b16 a[22:23], v208 offset:30208
s_waitcnt lgkmcnt(8)
v_mfma_f32_16x16x32_bf16 v[80:83], a[0:3], a[120:123], v[80:83]
ds_read_b64_tr_b16 a[24:25], v207 offset:24576
ds_read_b64_tr_b16 a[26:27], v207 offset:28672
v_mfma_f32_16x16x32_bf16 v[84:87], a[4:7], a[120:123], v[84:87]
s_waitcnt vmcnt(63)
s_barrier
ds_read_b64_tr_b16 a[28:29], v208 offset:24576
ds_read_b64_tr_b16 a[30:31], v208 offset:28672
s_waitcnt lgkmcnt(8)
v_mfma_f32_16x16x32_bf16 v[96:99], a[8:11], a[120:123], v[96:99]
ds_read_b64_tr_b16 a[0:1], v203 offset:33280
ds_read_b64_tr_b16 a[2:3], v203 offset:37376
v_mfma_f32_16x16x32_bf16 v[100:103], a[12:15], a[120:123], v[100:103]
s_waitcnt lgkmcnt(6)
ds_read_b64_tr_b16 a[4:5], v204 offset:33280
ds_read_b64_tr_b16 a[6:7], v204 offset:37376
v_mfma_f32_16x16x32_bf16 v[112:115], a[16:19], a[120:123], v[112:115]
s_xor_b32 s86, s98, 0
s_add_u32 s87, s86, 0x1000
buffer_load_dwordx4 a[160:163], v229, s[56:59], s86 offen sc1
ds_read_b64_tr_b16 a[8:9], v203 offset:33792
ds_read_b64_tr_b16 a[10:11], v203 offset:37888
v_mfma_f32_16x16x32_bf16 v[116:119], a[20:23], a[120:123], v[116:119]
s_mov_b32 m0, 0xc000
s_xor_b32 s78, s34, 0x30000
buffer_load_dwordx4 v40, s[52:55], s78 offen lds
ds_read_b64_tr_b16 a[12:13], v204 offset:33792
ds_read_b64_tr_b16 a[14:15], v204 offset:37888
buffer_load_dwordx4 v41, s[52:55], s78 offen offset:1024 lds
s_waitcnt lgkmcnt(8)
v_mfma_f32_16x16x32_bf16 v[64:67], a[24:27], a[120:123], v[64:67]
ds_read_b64_tr_b16 a[16:17], v203 offset:34304
ds_read_b64_tr_b16 a[18:19], v203 offset:38400
buffer_load_dwordx4 v42, s[52:55], s78 offen offset:2048 lds
v_mfma_f32_16x16x32_bf16 v[68:71], a[28:31], a[120:123], v[68:71]
buffer_load_dwordx4 v43, s[52:55], s78 offen offset:3072 lds
s_waitcnt vmcnt(8)
ds_read_b64_tr_b16 a[20:21], v204 offset:34304
ds_read_b64_tr_b16 a[22:23], v204 offset:38400
s_waitcnt lgkmcnt(8)
v_mfma_f32_16x16x32_bf16 v[80:83], a[0:3], a[128:131], v[80:83]
s_mov_b32 m0, 0xd000
s_xor_b32 s78, s34, 0x34000
buffer_load_dwordx4 v40, s[52:55], s78 offen lds
ds_read_b64_tr_b16 a[24:25], v203 offset:32768
ds_read_b64_tr_b16 a[26:27], v203 offset:36864
buffer_load_dwordx4 v41, s[52:55], s78 offen offset:1024 lds
v_mfma_f32_16x16x32_bf16 v[84:87], a[4:7], a[128:131], v[84:87]
s_waitcnt lgkmcnt(6)
ds_read_b64_tr_b16 a[28:29], v204 offset:32768
ds_read_b64_tr_b16 a[30:31], v204 offset:36864
buffer_load_dwordx4 v42, s[52:55], s78 offen offset:2048 lds
v_mfma_f32_16x16x32_bf16 v[96:99], a[8:11], a[128:131], v[96:99]
ds_read_b64_tr_b16 a[0:1], v203 offset:41472
ds_read_b64_tr_b16 a[2:3], v203 offset:45568
buffer_load_dwordx4 v43, s[52:55], s78 offen offset:3072 lds
buffer_load_dwordx4 a[168:171], v229, s[56:59], s86 offen offset:2048 sc1
v_mfma_f32_16x16x32_bf16 v[100:103], a[12:15], a[128:131], v[100:103]
s_mov_b32 m0, 0xe000
s_xor_b32 s78, s34, 0x38000
buffer_load_dwordx4 v40, s[52:55], s78 offen lds
ds_read_b64_tr_b16 a[4:5], v204 offset:41472
ds_read_b64_tr_b16 a[6:7], v204 offset:45568
buffer_load_dwordx4 v41, s[52:55], s78 offen offset:1024 lds
s_waitcnt lgkmcnt(8)
v_mfma_f32_16x16x32_bf16 v[112:115], a[16:19], a[128:131], v[112:115]
ds_read_b64_tr_b16 a[8:9], v203 offset:41984
ds_read_b64_tr_b16 a[10:11], v203 offset:46080
buffer_load_dwordx4 v42, s[52:55], s78 offen offset:2048 lds
v_mfma_f32_16x16x32_bf16 v[116:119], a[20:23], a[128:131], v[116:119]
s_waitcnt lgkmcnt(6)
ds_read_b64_tr_b16 a[12:13], v204 offset:41984
ds_read_b64_tr_b16 a[14:15], v204 offset:46080
buffer_load_dwordx4 v43, s[52:55], s78 offen offset:3072 lds
v_mfma_f32_16x16x32_bf16 v[64:67], a[24:27], a[128:131], v[64:67]
s_mov_b32 m0, 0xf000
s_xor_b32 s78, s34, 0x3c000
buffer_load_dwordx4 v40, s[52:55], s78 offen lds
ds_read_b64_tr_b16 a[16:17], v203 offset:42496
ds_read_b64_tr_b16 a[18:19], v203 offset:46592
buffer_load_dwordx4 v41, s[52:55], s78 offen offset:1024 lds
v_mfma_f32_16x16x32_bf16 v[68:71], a[28:31], a[128:131], v[68:71]
buffer_load_dwordx4 v42, s[52:55], s78 offen offset:2048 lds
s_waitcnt vmcnt(19)
ds_read_b64_tr_b16 a[20:21], v204 offset:42496
ds_read_b64_tr_b16 a[22:23], v204 offset:46592
s_waitcnt lgkmcnt(8)
v_mfma_f32_16x16x32_bf16 v[80:83], a[0:3], a[136:139], v[80:83]
ds_read_b64_tr_b16 a[24:25], v203 offset:40960
ds_read_b64_tr_b16 a[26:27], v203 offset:45056
buffer_load_dwordx4 v43, s[52:55], s78 offen offset:3072 lds
buffer_load_dwordx4 a[176:179], v229, s[56:59], s87 offen sc1
v_mfma_f32_16x16x32_bf16 v[84:87], a[4:7], a[136:139], v[84:87]
s_mov_b32 m0, 0x4000
s_xor_b32 s78, s35, 0x20000
buffer_load_dwordx4 v40, s[52:55], s78 offen lds
buffer_load_dwordx4 v41, s[52:55], s78 offen offset:1024 lds
s_waitcnt vmcnt(63)
s_barrier
ds_read_b64_tr_b16 a[28:29], v204 offset:40960
ds_read_b64_tr_b16 a[30:31], v204 offset:45056
s_waitcnt lgkmcnt(8)
v_mfma_f32_16x16x32_bf16 v[96:99], a[8:11], a[136:139], v[96:99]
ds_read_b64_tr_b16 a[0:1], v205 offset:49664
ds_read_b64_tr_b16 a[2:3], v205 offset:53760
buffer_load_dwordx4 v42, s[52:55], s78 offen offset:2048 lds
v_mfma_f32_16x16x32_bf16 v[100:103], a[12:15], a[136:139], v[100:103]
s_waitcnt lgkmcnt(6)
ds_read_b64_tr_b16 a[4:5], v206 offset:49664
ds_read_b64_tr_b16 a[6:7], v206 offset:53760
buffer_load_dwordx4 v43, s[52:55], s78 offen offset:3072 lds
v_mfma_f32_16x16x32_bf16 v[112:115], a[16:19], a[136:139], v[112:115]
s_mov_b32 m0, 0x5000
s_xor_b32 s78, s35, 0x24000
buffer_load_dwordx4 v40, s[52:55], s78 offen lds
ds_read_b64_tr_b16 a[8:9], v205 offset:50176
ds_read_b64_tr_b16 a[10:11], v205 offset:54272
buffer_load_dwordx4 v41, s[52:55], s78 offen offset:1024 lds
v_mfma_f32_16x16x32_bf16 v[116:119], a[20:23], a[136:139], v[116:119]
s_waitcnt lgkmcnt(6)
ds_read_b64_tr_b16 a[12:13], v206 offset:50176
ds_read_b64_tr_b16 a[14:15], v206 offset:54272
buffer_load_dwordx4 v42, s[52:55], s78 offen offset:2048 lds
v_mfma_f32_16x16x32_bf16 v[64:67], a[24:27], a[136:139], v[64:67]
ds_read_b64_tr_b16 a[16:17], v205 offset:50688
ds_read_b64_tr_b16 a[18:19], v205 offset:54784
buffer_load_dwordx4 v43, s[52:55], s78 offen offset:3072 lds
buffer_load_dwordx4 a[184:187], v229, s[56:59], s87 offen offset:2048 sc1
v_mfma_f32_16x16x32_bf16 v[68:71], a[28:31], a[136:139], v[68:71]
s_mov_b32 m0, 0x6000
s_xor_b32 s78, s35, 0x28000
buffer_load_dwordx4 v40, s[52:55], s78 offen lds
buffer_load_dwordx4 v41, s[52:55], s78 offen offset:1024 lds
s_waitcnt vmcnt(31)
ds_read_b64_tr_b16 a[20:21], v206 offset:50688
ds_read_b64_tr_b16 a[22:23], v206 offset:54784
s_waitcnt lgkmcnt(8)
v_mfma_f32_16x16x32_bf16 v[80:83], a[0:3], a[144:147], v[80:83]
ds_read_b64_tr_b16 a[24:25], v205 offset:49152
ds_read_b64_tr_b16 a[26:27], v205 offset:53248
buffer_load_dwordx4 v42, s[52:55], s78 offen offset:2048 lds
v_mfma_f32_16x16x32_bf16 v[84:87], a[4:7], a[144:147], v[84:87]
s_waitcnt lgkmcnt(6)
ds_read_b64_tr_b16 a[28:29], v206 offset:49152
ds_read_b64_tr_b16 a[30:31], v206 offset:53248
buffer_load_dwordx4 v43, s[52:55], s78 offen offset:3072 lds
v_mfma_f32_16x16x32_bf16 v[96:99], a[8:11], a[144:147], v[96:99]
s_mov_b32 m0, 0x7000
s_xor_b32 s78, s35, 0x2c000
buffer_load_dwordx4 v40, s[52:55], s78 offen lds
ds_read_b64_tr_b16 a[0:1], v205 offset:57856
ds_read_b64_tr_b16 a[2:3], v205 offset:61952
buffer_load_dwordx4 v41, s[52:55], s78 offen offset:1024 lds
v_mfma_f32_16x16x32_bf16 v[100:103], a[12:15], a[144:147], v[100:103]
)"
R"(
s_waitcnt lgkmcnt(6)
ds_read_b64_tr_b16 a[4:5], v206 offset:57856
ds_read_b64_tr_b16 a[6:7], v206 offset:61952
buffer_load_dwordx4 v42, s[52:55], s78 offen offset:2048 lds
v_mfma_f32_16x16x32_bf16 v[112:115], a[16:19], a[144:147], v[112:115]
ds_read_b64_tr_b16 a[8:9], v205 offset:58368
ds_read_b64_tr_b16 a[10:11], v205 offset:62464
buffer_load_dwordx4 v43, s[52:55], s78 offen offset:3072 lds
v_mfma_f32_16x16x32_bf16 v[116:119], a[20:23], a[144:147], v[116:119]
s_mov_b32 m0, 0x14000
s_xor_b32 s78, s35, 0x30000
buffer_load_dwordx4 v40, s[52:55], s78 offen lds
ds_read_b64_tr_b16 a[12:13], v206 offset:58368
ds_read_b64_tr_b16 a[14:15], v206 offset:62464
buffer_load_dwordx4 v41, s[52:55], s78 offen offset:1024 lds
s_waitcnt lgkmcnt(8)
v_mfma_f32_16x16x32_bf16 v[64:67], a[24:27], a[144:147], v[64:67]
ds_read_b64_tr_b16 a[16:17], v205 offset:58880
ds_read_b64_tr_b16 a[18:19], v205 offset:62976
buffer_load_dwordx4 v42, s[52:55], s78 offen offset:2048 lds
buffer_load_dword v235, v231, s[56:59], 0 offen sc1
v_mfma_f32_16x16x32_bf16 v[68:71], a[28:31], a[144:147], v[68:71]
buffer_load_dwordx4 v43, s[52:55], s78 offen offset:3072 lds
s_waitcnt vmcnt(41)
ds_read_b64_tr_b16 a[20:21], v206 offset:58880
ds_read_b64_tr_b16 a[22:23], v206 offset:62976
s_waitcnt lgkmcnt(8)
v_mfma_f32_16x16x32_bf16 v[80:83], a[0:3], a[152:155], v[80:83]
s_mov_b32 m0, 0x15000
s_xor_b32 s78, s35, 0x34000
buffer_load_dwordx4 v40, s[52:55], s78 offen lds
ds_read_b64_tr_b16 a[24:25], v205 offset:57344
ds_read_b64_tr_b16 a[26:27], v205 offset:61440
buffer_load_dwordx4 v41, s[52:55], s78 offen offset:1024 lds
v_mfma_f32_16x16x32_bf16 v[84:87], a[4:7], a[152:155], v[84:87]
buffer_load_dwordx4 v42, s[52:55], s78 offen offset:2048 lds
s_waitcnt vmcnt(63)
s_barrier
ds_read_b64_tr_b16 a[28:29], v206 offset:57344
ds_read_b64_tr_b16 a[30:31], v206 offset:61440
s_waitcnt lgkmcnt(8)
v_mfma_f32_16x16x32_bf16 v[96:99], a[8:11], a[152:155], v[96:99]
ds_read_b64_tr_b16 a[0:1], v205 offset:512
ds_read_b64_tr_b16 a[2:3], v205 offset:4608
buffer_load_dwordx4 v43, s[52:55], s78 offen offset:3072 lds
v_mfma_f32_16x16x32_bf16 v[100:103], a[12:15], a[152:155], v[100:103]
s_mov_b32 m0, 0x16000
s_xor_b32 s78, s35, 0x38000
buffer_load_dwordx4 v40, s[52:55], s78 offen lds
ds_read_b64_tr_b16 a[4:5], v206 offset:512
ds_read_b64_tr_b16 a[6:7], v206 offset:4608
buffer_load_dwordx4 v41, s[52:55], s78 offen offset:1024 lds
s_waitcnt lgkmcnt(8)
v_mfma_f32_16x16x32_bf16 v[112:115], a[16:19], a[152:155], v[112:115]
ds_read_b64_tr_b16 a[8:9], v205 offset:1024
ds_read_b64_tr_b16 a[10:11], v205 offset:5120
buffer_load_dwordx4 v42, s[52:55], s78 offen offset:2048 lds
v_mfma_f32_16x16x32_bf16 v[116:119], a[20:23], a[152:155], v[116:119]
s_waitcnt lgkmcnt(6)
ds_read_b64_tr_b16 a[12:13], v206 offset:1024
ds_read_b64_tr_b16 a[14:15], v206 offset:5120
buffer_load_dwordx4 v43, s[52:55], s78 offen offset:3072 lds
v_mfma_f32_16x16x32_bf16 v[64:67], a[24:27], a[152:155], v[64:67]
s_mov_b32 m0, 0x17000
s_xor_b32 s78, s35, 0x3c000
buffer_load_dwordx4 v40, s[52:55], s78 offen lds
ds_read_b64_tr_b16 a[16:17], v205 offset:1536
ds_read_b64_tr_b16 a[18:19], v205 offset:5632
buffer_load_dwordx4 v41, s[52:55], s78 offen offset:1024 lds
v_mfma_f32_16x16x32_bf16 v[68:71], a[28:31], a[152:155], v[68:71]
buffer_load_dwordx4 v42, s[52:55], s78 offen offset:2048 lds
s_waitcnt vmcnt(51)
ds_read_b64_tr_b16 a[20:21], v206 offset:1536
ds_read_b64_tr_b16 a[22:23], v206 offset:5632
s_waitcnt lgkmcnt(8)
v_mfma_f32_16x16x32_bf16 v[80:83], a[0:3], a[160:163], v[80:83]
buffer_load_dwordx4 v43, s[52:55], s78 offen offset:3072 lds
ds_read_b64_tr_b16 a[24:25], v205 offset:8704
ds_read_b64_tr_b16 a[26:27], v205 offset:12800
s_waitcnt vmcnt(13)
v_cmp_eq_u32_e64 s[74:75], v235, v233
s_nop 1
s_and_b32 s79, s74, 0xe0
s_cmp_eq_u32 s79, 0xe0
s_cbranch_scc0 .Lk_bslow2
.Lk_bok2:
v_mfma_f32_16x16x32_bf16 v[84:87], a[4:7], a[160:163], v[84:87]
s_xor_b32 s86, s96, 0x800000
s_add_u32 s87, s86, 0x1000
buffer_load_dwordx4 a[192:195], v229, s[56:59], s86 offen sc1
s_mov_b32 m0, 0x20000
s_xor_b32 s78, s73, 0x20000
buffer_load_dwordx4 v40, s[52:55], s78 offen lds
ds_read_b64_tr_b16 a[28:29], v206 offset:8704
ds_read_b64_tr_b16 a[30:31], v206 offset:12800
s_waitcnt lgkmcnt(8)
v_mfma_f32_16x16x32_bf16 v[96:99], a[8:11], a[160:163], v[96:99]
buffer_load_dwordx4 v41, s[52:55], s78 offen offset:1024 lds
ds_read_b64_tr_b16 a[0:1], v205 offset:9216
ds_read_b64_tr_b16 a[2:3], v205 offset:13312
buffer_load_dwordx4 v42, s[52:55], s78 offen offset:2048 lds
v_mfma_f32_16x16x32_bf16 v[100:103], a[12:15], a[160:163], v[100:103]
s_waitcnt lgkmcnt(6)
ds_read_b64_tr_b16 a[4:5], v206 offset:9216
ds_read_b64_tr_b16 a[6:7], v206 offset:13312
buffer_load_dwordx4 v43, s[52:55], s78 offen offset:3072 lds
s_mov_b32 m0, 0x21000
s_xor_b32 s78, s73, 0x24000
buffer_load_dwordx4 v40, s[52:55], s78 offen lds
buffer_load_dwordx4 a[200:203], v229, s[56:59], s86 offen offset:2048 sc1
v_mfma_f32_16x16x32_bf16 v[112:115], a[16:19], a[160:163], v[112:115]
ds_read_b64_tr_b16 a[8:9], v205 offset:9728
ds_read_b64_tr_b16 a[10:11], v205 offset:13824
buffer_load_dwordx4 v41, s[52:55], s78 offen offset:1024 lds
v_mfma_f32_16x16x32_bf16 v[116:119], a[20:23], a[160:163], v[116:119]
buffer_load_dwordx4 v42, s[52:55], s78 offen offset:2048 lds
buffer_load_dwordx4 v43, s[52:55], s78 offen offset:3072 lds
s_waitcnt vmcnt(63)
s_barrier
s_waitcnt vmcnt(53)
ds_read_b64_tr_b16 a[12:13], v206 offset:9728
ds_read_b64_tr_b16 a[14:15], v206 offset:13824
s_waitcnt lgkmcnt(8)
v_mfma_f32_16x16x32_bf16 v[80:83], a[24:27], a[168:171], v[80:83]
s_mov_b32 m0, 0x22000
s_xor_b32 s78, s73, 0x28000
buffer_load_dwordx4 v40, s[52:55], s78 offen lds
ds_read_b64_tr_b16 a[16:17], v205 offset:33280
ds_read_b64_tr_b16 a[18:19], v205 offset:37376
v_mfma_f32_16x16x32_bf16 v[84:87], a[28:31], a[168:171], v[84:87]
s_waitcnt lgkmcnt(6)
ds_read_b64_tr_b16 a[20:21], v206 offset:33280
ds_read_b64_tr_b16 a[22:23], v206 offset:37376
buffer_load_dwordx4 v41, s[52:55], s78 offen offset:1024 lds
buffer_load_dwordx4 v42, s[52:55], s78 offen offset:2048 lds
buffer_load_dwordx4 a[208:211], v229, s[56:59], s87 offen sc1
v_mfma_f32_16x16x32_bf16 v[96:99], a[0:3], a[168:171], v[96:99]
ds_read_b64_tr_b16 a[24:25], v205 offset:33792
ds_read_b64_tr_b16 a[26:27], v205 offset:37888
buffer_load_dwordx4 v43, s[52:55], s78 offen offset:3072 lds
v_mfma_f32_16x16x32_bf16 v[100:103], a[4:7], a[168:171], v[100:103]
s_mov_b32 m0, 0x23000
s_xor_b32 s78, s73, 0x2c000
buffer_load_dwordx4 v40, s[52:55], s78 offen lds
ds_read_b64_tr_b16 a[28:29], v206 offset:33792
ds_read_b64_tr_b16 a[30:31], v206 offset:37888
buffer_load_dwordx4 v41, s[52:55], s78 offen offset:1024 lds
s_waitcnt lgkmcnt(8)
v_mfma_f32_16x16x32_bf16 v[112:115], a[8:11], a[168:171], v[112:115]
ds_read_b64_tr_b16 a[0:1], v205 offset:34304
ds_read_b64_tr_b16 a[2:3], v205 offset:38400
buffer_load_dwordx4 v42, s[52:55], s78 offen offset:2048 lds
v_mfma_f32_16x16x32_bf16 v[116:119], a[12:15], a[168:171], v[116:119]
buffer_load_dwordx4 v43, s[52:55], s78 offen offset:3072 lds
buffer_load_dwordx4 a[216:219], v229, s[56:59], s87 offen offset:2048 sc1
s_waitcnt vmcnt(54)
ds_read_b64_tr_b16 a[4:5], v206 offset:34304
ds_read_b64_tr_b16 a[6:7], v206 offset:38400
s_waitcnt lgkmcnt(8)
v_mfma_f32_16x16x32_bf16 v[80:83], a[16:19], a[176:179], v[80:83]
s_mov_b32 m0, 0x24000
s_xor_b32 s78, s73, 0x30000
buffer_load_dwordx4 v40, s[52:55], s78 offen lds
ds_read_b64_tr_b16 a[8:9], v205 offset:41472
ds_read_b64_tr_b16 a[10:11], v205 offset:45568
v_mfma_f32_16x16x32_bf16 v[84:87], a[20:23], a[176:179], v[84:87]
s_waitcnt lgkmcnt(6)
ds_read_b64_tr_b16 a[12:13], v206 offset:41472
ds_read_b64_tr_b16 a[14:15], v206 offset:45568
buffer_load_dwordx4 v41, s[52:55], s78 offen offset:1024 lds
v_mfma_f32_16x16x32_bf16 v[96:99], a[24:27], a[176:179], v[96:99]
ds_read_b64_tr_b16 a[16:17], v205 offset:41984
ds_read_b64_tr_b16 a[18:19], v205 offset:46080
buffer_load_dwordx4 v42, s[52:55], s78 offen offset:2048 lds
s_xor_b32 s86, s97, 0x800000
s_add_u32 s87, s86, 0x1000
buffer_load_dwordx4 a[224:227], v229, s[56:59], s86 offen sc1
v_mfma_f32_16x16x32_bf16 v[100:103], a[28:31], a[176:179], v[100:103]
s_waitcnt lgkmcnt(6)
ds_read_b64_tr_b16 a[20:21], v206 offset:41984
ds_read_b64_tr_b16 a[22:23], v206 offset:46080
buffer_load_dwordx4 v43, s[52:55], s78 offen offset:3072 lds
v_mfma_f32_16x16x32_bf16 v[112:115], a[0:3], a[176:179], v[112:115]
s_mov_b32 m0, 0x25000
s_xor_b32 s78, s73, 0x34000
buffer_load_dwordx4 v40, s[52:55], s78 offen lds
ds_read_b64_tr_b16 a[24:25], v205 offset:42496
ds_read_b64_tr_b16 a[26:27], v205 offset:46592
v_mfma_f32_16x16x32_bf16 v[116:119], a[4:7], a[176:179], v[116:119]
buffer_load_dwordx4 v41, s[52:55], s78 offen offset:1024 lds
s_waitcnt vmcnt(63)
s_barrier
s_waitcnt vmcnt(52)
ds_read_b64_tr_b16 a[28:29], v206 offset:42496
ds_read_b64_tr_b16 a[30:31], v206 offset:46592
s_waitcnt lgkmcnt(8)
v_mfma_f32_16x16x32_bf16 v[80:83], a[8:11], a[184:187], v[80:83]
ds_read_b64_tr_b16 a[0:1], v203 offset:512
ds_read_b64_tr_b16 a[2:3], v203 offset:4608
buffer_load_dwordx4 v42, s[52:55], s78 offen offset:2048 lds
)"
R"(
buffer_load_dwordx4 a[232:235], v229, s[56:59], s86 offen offset:2048 sc1
v_mfma_f32_16x16x32_bf16 v[84:87], a[12:15], a[184:187], v[84:87]
s_waitcnt lgkmcnt(6)
ds_read_b64_tr_b16 a[4:5], v204 offset:512
ds_read_b64_tr_b16 a[6:7], v204 offset:4608
buffer_load_dwordx4 v43, s[52:55], s78 offen offset:3072 lds
v_mfma_f32_16x16x32_bf16 v[96:99], a[16:19], a[184:187], v[96:99]
s_mov_b32 m0, 0x26000
s_xor_b32 s78, s73, 0x38000
buffer_load_dwordx4 v40, s[52:55], s78 offen lds
ds_read_b64_tr_b16 a[8:9], v203 offset:1024
ds_read_b64_tr_b16 a[10:11], v203 offset:5120
v_mfma_f32_16x16x32_bf16 v[100:103], a[20:23], a[184:187], v[100:103]
s_waitcnt lgkmcnt(6)
ds_read_b64_tr_b16 a[12:13], v204 offset:1024
ds_read_b64_tr_b16 a[14:15], v204 offset:5120
buffer_load_dwordx4 v41, s[52:55], s78 offen offset:1024 lds
v_mfma_f32_16x16x32_bf16 v[112:115], a[24:27], a[184:187], v[112:115]
ds_read_b64_tr_b16 a[16:17], v203 offset:1536
ds_read_b64_tr_b16 a[18:19], v203 offset:5632
buffer_load_dwordx4 v42, s[52:55], s78 offen offset:2048 lds
buffer_load_dwordx4 a[240:243], v229, s[56:59], s87 offen sc1
v_mfma_f32_16x16x32_bf16 v[116:119], a[28:31], a[184:187], v[116:119]
buffer_load_dwordx4 v43, s[52:55], s78 offen offset:3072 lds
s_waitcnt vmcnt(34)
ds_read_b64_tr_b16 a[20:21], v204 offset:1536
ds_read_b64_tr_b16 a[22:23], v204 offset:5632
s_waitcnt lgkmcnt(8)
v_mfma_f32_16x16x32_bf16 v[80:83], a[0:3], a[192:195], v[80:83]
s_mov_b32 m0, 0x27000
s_xor_b32 s78, s73, 0x3c000
buffer_load_dwordx4 v40, s[52:55], s78 offen lds
ds_read_b64_tr_b16 a[24:25], v203 offset:8704
ds_read_b64_tr_b16 a[26:27], v203 offset:12800
v_mfma_f32_16x16x32_bf16 v[84:87], a[4:7], a[192:195], v[84:87]
s_waitcnt lgkmcnt(6)
ds_read_b64_tr_b16 a[28:29], v204 offset:8704
ds_read_b64_tr_b16 a[30:31], v204 offset:12800
buffer_load_dwordx4 v41, s[52:55], s78 offen offset:1024 lds
buffer_load_dwordx4 a[248:251], v229, s[56:59], s87 offen offset:2048 sc1
v_mfma_f32_16x16x32_bf16 v[96:99], a[8:11], a[192:195], v[96:99]
ds_read_b64_tr_b16 a[0:1], v203 offset:9216
ds_read_b64_tr_b16 a[2:3], v203 offset:13312
buffer_load_dwordx4 v42, s[52:55], s78 offen offset:2048 lds
v_mfma_f32_16x16x32_bf16 v[100:103], a[12:15], a[192:195], v[100:103]
s_waitcnt lgkmcnt(6)
ds_read_b64_tr_b16 a[4:5], v204 offset:9216
ds_read_b64_tr_b16 a[6:7], v204 offset:13312
buffer_load_dwordx4 v43, s[52:55], s78 offen offset:3072 lds
v_mfma_f32_16x16x32_bf16 v[112:115], a[16:19], a[192:195], v[112:115]
ds_read_b64_tr_b16 a[8:9], v203 offset:9728
ds_read_b64_tr_b16 a[10:11], v203 offset:13824
v_mfma_f32_16x16x32_bf16 v[116:119], a[20:23], a[192:195], v[116:119]
s_xor_b32 s86, s98, 0x800000
s_add_u32 s87, s86, 0x1000
buffer_load_dwordx4 a[64:67], v229, s[56:59], s86 offen sc1
s_waitcnt vmcnt(63)
s_barrier
s_waitcnt vmcnt(34)
ds_read_b64_tr_b16 a[12:13], v204 offset:9728
ds_read_b64_tr_b16 a[14:15], v204 offset:13824
s_waitcnt lgkmcnt(8)
v_mfma_f32_16x16x32_bf16 v[80:83], a[24:27], a[200:203], v[80:83]
ds_read_b64_tr_b16 a[16:17], v203 offset:49664
ds_read_b64_tr_b16 a[18:19], v203 offset:53760
v_mfma_f32_16x16x32_bf16 v[84:87], a[28:31], a[200:203], v[84:87]
s_waitcnt lgkmcnt(6)
ds_read_b64_tr_b16 a[20:21], v204 offset:49664
ds_read_b64_tr_b16 a[22:23], v204 offset:53760
v_mfma_f32_16x16x32_bf16 v[96:99], a[0:3], a[200:203], v[96:99]
ds_read_b64_tr_b16 a[24:25], v203 offset:50176
ds_read_b64_tr_b16 a[26:27], v203 offset:54272
v_mfma_f32_16x16x32_bf16 v[100:103], a[4:7], a[200:203], v[100:103]
s_waitcnt lgkmcnt(6)
ds_read_b64_tr_b16 a[28:29], v204 offset:50176
ds_read_b64_tr_b16 a[30:31], v204 offset:54272
buffer_load_dwordx4 a[72:75], v229, s[56:59], s86 offen offset:2048 sc1
v_mfma_f32_16x16x32_bf16 v[112:115], a[8:11], a[200:203], v[112:115]
ds_read_b64_tr_b16 a[0:1], v203 offset:50688
ds_read_b64_tr_b16 a[2:3], v203 offset:54784
v_mfma_f32_16x16x32_bf16 v[116:119], a[12:15], a[200:203], v[116:119]
s_waitcnt vmcnt(28)
ds_read_b64_tr_b16 a[4:5], v204 offset:50688
ds_read_b64_tr_b16 a[6:7], v204 offset:54784
s_waitcnt lgkmcnt(8)
v_mfma_f32_16x16x32_bf16 v[80:83], a[16:19], a[208:211], v[80:83]
ds_read_b64_tr_b16 a[8:9], v203 offset:57856
ds_read_b64_tr_b16 a[10:11], v203 offset:61952
buffer_load_dwordx4 a[80:83], v229, s[56:59], s87 offen sc1
v_mfma_f32_16x16x32_bf16 v[84:87], a[20:23], a[208:211], v[84:87]
s_waitcnt lgkmcnt(6)
ds_read_b64_tr_b16 a[12:13], v204 offset:57856
ds_read_b64_tr_b16 a[14:15], v204 offset:61952
v_mfma_f32_16x16x32_bf16 v[96:99], a[24:27], a[208:211], v[96:99]
ds_read_b64_tr_b16 a[16:17], v203 offset:58368
ds_read_b64_tr_b16 a[18:19], v203 offset:62464
v_mfma_f32_16x16x32_bf16 v[100:103], a[28:31], a[208:211], v[100:103]
s_waitcnt lgkmcnt(6)
ds_read_b64_tr_b16 a[20:21], v204 offset:58368
ds_read_b64_tr_b16 a[22:23], v204 offset:62464
v_mfma_f32_16x16x32_bf16 v[112:115], a[0:3], a[208:211], v[112:115]
ds_read_b64_tr_b16 a[24:25], v203 offset:58880
ds_read_b64_tr_b16 a[26:27], v203 offset:62976
buffer_load_dwordx4 a[88:91], v229, s[56:59], s87 offen offset:2048 sc1
v_mfma_f32_16x16x32_bf16 v[116:119], a[4:7], a[208:211], v[116:119]
s_waitcnt vmcnt(61)
s_barrier
s_waitcnt vmcnt(24)
ds_read_b64_tr_b16 a[28:29], v204 offset:58880
ds_read_b64_tr_b16 a[30:31], v204 offset:62976
s_waitcnt lgkmcnt(8)
v_mfma_f32_16x16x32_bf16 v[80:83], a[8:11], a[216:219], v[80:83]
ds_read_b64_tr_b16 a[0:1], v203 offset:16896
ds_read_b64_tr_b16 a[2:3], v203 offset:20992
v_mfma_f32_16x16x32_bf16 v[84:87], a[12:15], a[216:219], v[84:87]
s_waitcnt lgkmcnt(6)
ds_read_b64_tr_b16 a[4:5], v204 offset:16896
ds_read_b64_tr_b16 a[6:7], v204 offset:20992
v_mfma_f32_16x16x32_bf16 v[96:99], a[16:19], a[216:219], v[96:99]
ds_read_b64_tr_b16 a[8:9], v203 offset:17408
ds_read_b64_tr_b16 a[10:11], v203 offset:21504
v_mfma_f32_16x16x32_bf16 v[100:103], a[20:23], a[216:219], v[100:103]
s_waitcnt lgkmcnt(6)
ds_read_b64_tr_b16 a[12:13], v204 offset:17408
ds_read_b64_tr_b16 a[14:15], v204 offset:21504
v_mfma_f32_16x16x32_bf16 v[112:115], a[24:27], a[216:219], v[112:115]
ds_read_b64_tr_b16 a[16:17], v203 offset:17920
ds_read_b64_tr_b16 a[18:19], v203 offset:22016
v_mfma_f32_16x16x32_bf16 v[116:119], a[28:31], a[216:219], v[116:119]
s_waitcnt vmcnt(20)
ds_read_b64_tr_b16 a[20:21], v204 offset:17920
ds_read_b64_tr_b16 a[22:23], v204 offset:22016
s_waitcnt lgkmcnt(8)
v_mfma_f32_16x16x32_bf16 v[80:83], a[0:3], a[224:227], v[80:83]
ds_read_b64_tr_b16 a[24:25], v203 offset:25088
ds_read_b64_tr_b16 a[26:27], v203 offset:29184
v_mfma_f32_16x16x32_bf16 v[84:87], a[4:7], a[224:227], v[84:87]
s_waitcnt lgkmcnt(6)
ds_read_b64_tr_b16 a[28:29], v204 offset:25088
ds_read_b64_tr_b16 a[30:31], v204 offset:29184
v_mfma_f32_16x16x32_bf16 v[96:99], a[8:11], a[224:227], v[96:99]
ds_read_b64_tr_b16 a[0:1], v203 offset:25600
ds_read_b64_tr_b16 a[2:3], v203 offset:29696
v_mfma_f32_16x16x32_bf16 v[100:103], a[12:15], a[224:227], v[100:103]
s_waitcnt lgkmcnt(6)
ds_read_b64_tr_b16 a[4:5], v204 offset:25600
ds_read_b64_tr_b16 a[6:7], v204 offset:29696
v_mfma_f32_16x16x32_bf16 v[112:115], a[16:19], a[224:227], v[112:115]
ds_read_b64_tr_b16 a[8:9], v203 offset:26112
ds_read_b64_tr_b16 a[10:11], v203 offset:30208
v_mfma_f32_16x16x32_bf16 v[116:119], a[20:23], a[224:227], v[116:119]
s_waitcnt vmcnt(44)
s_barrier
s_waitcnt vmcnt(15)
ds_read_b64_tr_b16 a[12:13], v204 offset:26112
ds_read_b64_tr_b16 a[14:15], v204 offset:30208
s_waitcnt lgkmcnt(8)
v_mfma_f32_16x16x32_bf16 v[80:83], a[24:27], a[232:235], v[80:83]
ds_read_b64_tr_b16 a[16:17], v205 offset:16896
ds_read_b64_tr_b16 a[18:19], v205 offset:20992
v_mfma_f32_16x16x32_bf16 v[84:87], a[28:31], a[232:235], v[84:87]
s_waitcnt lgkmcnt(6)
ds_read_b64_tr_b16 a[20:21], v206 offset:16896
ds_read_b64_tr_b16 a[22:23], v206 offset:20992
v_mfma_f32_16x16x32_bf16 v[96:99], a[0:3], a[232:235], v[96:99]
ds_read_b64_tr_b16 a[24:25], v205 offset:17408
ds_read_b64_tr_b16 a[26:27], v205 offset:21504
v_mfma_f32_16x16x32_bf16 v[100:103], a[4:7], a[232:235], v[100:103]
s_waitcnt lgkmcnt(6)
ds_read_b64_tr_b16 a[28:29], v206 offset:17408
ds_read_b64_tr_b16 a[30:31], v206 offset:21504
v_mfma_f32_16x16x32_bf16 v[112:115], a[8:11], a[232:235], v[112:115]
ds_read_b64_tr_b16 a[0:1], v205 offset:17920
ds_read_b64_tr_b16 a[2:3], v205 offset:22016
v_mfma_f32_16x16x32_bf16 v[116:119], a[12:15], a[232:235], v[116:119]
s_waitcnt vmcnt(10)
ds_read_b64_tr_b16 a[4:5], v206 offset:17920
ds_read_b64_tr_b16 a[6:7], v206 offset:22016
s_waitcnt lgkmcnt(8)
v_mfma_f32_16x16x32_bf16 v[80:83], a[16:19], a[240:243], v[80:83]
ds_read_b64_tr_b16 a[8:9], v205 offset:25088
ds_read_b64_tr_b16 a[10:11], v205 offset:29184
v_mfma_f32_16x16x32_bf16 v[84:87], a[20:23], a[240:243], v[84:87]
s_waitcnt lgkmcnt(6)
ds_read_b64_tr_b16 a[12:13], v206 offset:25088
ds_read_b64_tr_b16 a[14:15], v206 offset:29184
v_mfma_f32_16x16x32_bf16 v[96:99], a[24:27], a[240:243], v[96:99]
ds_read_b64_tr_b16 a[16:17], v205 offset:25600
ds_read_b64_tr_b16 a[18:19], v205 offset:29696
v_mfma_f32_16x16x32_bf16 v[100:103], a[28:31], a[240:243], v[100:103]
s_waitcnt lgkmcnt(6)
ds_read_b64_tr_b16 a[20:21], v206 offset:25600
ds_read_b64_tr_b16 a[22:23], v206 offset:29696
v_mfma_f32_16x16x32_bf16 v[112:115], a[0:3], a[240:243], v[112:115]
ds_read_b64_tr_b16 a[24:25], v205 offset:26112
ds_read_b64_tr_b16 a[26:27], v205 offset:30208
v_mfma_f32_16x16x32_bf16 v[116:119], a[4:7], a[240:243], v[116:119]
s_waitcnt vmcnt(25)
s_barrier
s_waitcnt vmcnt(6)
)"
R"(
ds_read_b64_tr_b16 a[28:29], v206 offset:26112
ds_read_b64_tr_b16 a[30:31], v206 offset:30208
s_waitcnt lgkmcnt(8)
v_mfma_f32_16x16x32_bf16 v[80:83], a[8:11], a[248:251], v[80:83]
ds_read_b64_tr_b16 a[0:1], v207 offset:512
ds_read_b64_tr_b16 a[2:3], v207 offset:4608
v_mfma_f32_16x16x32_bf16 v[84:87], a[12:15], a[248:251], v[84:87]
s_waitcnt lgkmcnt(6)
ds_read_b64_tr_b16 a[4:5], v208 offset:512
ds_read_b64_tr_b16 a[6:7], v208 offset:4608
v_mfma_f32_16x16x32_bf16 v[96:99], a[16:19], a[248:251], v[96:99]
ds_read_b64_tr_b16 a[8:9], v207 offset:8704
ds_read_b64_tr_b16 a[10:11], v207 offset:12800
v_mfma_f32_16x16x32_bf16 v[100:103], a[20:23], a[248:251], v[100:103]
s_waitcnt vmcnt(4)
s_barrier
ds_read_b64_tr_b16 a[12:13], v208 offset:8704
ds_read_b64_tr_b16 a[14:15], v208 offset:12800
s_waitcnt lgkmcnt(8)
v_mfma_f32_16x16x32_bf16 v[112:115], a[24:27], a[248:251], v[112:115]
ds_read_b64_tr_b16 a[16:17], v207 offset:16896
ds_read_b64_tr_b16 a[18:19], v207 offset:20992
v_mfma_f32_16x16x32_bf16 v[116:119], a[28:31], a[248:251], v[116:119]
s_waitcnt vmcnt(3)
ds_read_b64_tr_b16 a[20:21], v208 offset:16896
ds_read_b64_tr_b16 a[22:23], v208 offset:20992
s_waitcnt lgkmcnt(8)
v_mfma_f32_16x16x32_bf16 v[80:83], a[0:3], a[64:67], v[80:83]
ds_read_b64_tr_b16 a[24:25], v207 offset:25088
ds_read_b64_tr_b16 a[26:27], v207 offset:29184
v_mfma_f32_16x16x32_bf16 v[84:87], a[4:7], a[64:67], v[84:87]
s_waitcnt vmcnt(2)
ds_read_b64_tr_b16 a[28:29], v208 offset:25088
ds_read_b64_tr_b16 a[30:31], v208 offset:29184
s_waitcnt lgkmcnt(8)
v_mfma_f32_16x16x32_bf16 v[80:83], a[8:11], a[72:75], v[80:83]
ds_read_b64_tr_b16 a[0:1], v207 offset:1024
ds_read_b64_tr_b16 a[2:3], v207 offset:5120
v_mfma_f32_16x16x32_bf16 v[84:87], a[12:15], a[72:75], v[84:87]
s_waitcnt vmcnt(1)
ds_read_b64_tr_b16 a[4:5], v208 offset:1024
ds_read_b64_tr_b16 a[6:7], v208 offset:5120
s_waitcnt lgkmcnt(8)
v_mfma_f32_16x16x32_bf16 v[80:83], a[16:19], a[80:83], v[80:83]
ds_read_b64_tr_b16 a[8:9], v207 offset:9216
ds_read_b64_tr_b16 a[10:11], v207 offset:13312
v_mfma_f32_16x16x32_bf16 v[84:87], a[20:23], a[80:83], v[84:87]
s_waitcnt vmcnt(0)
ds_read_b64_tr_b16 a[12:13], v208 offset:9216
ds_read_b64_tr_b16 a[14:15], v208 offset:13312
s_waitcnt lgkmcnt(8)
v_mfma_f32_16x16x32_bf16 v[80:83], a[24:27], a[88:91], v[80:83]
ds_read_b64_tr_b16 a[16:17], v207 offset:17408
ds_read_b64_tr_b16 a[18:19], v207 offset:21504
v_mfma_f32_16x16x32_bf16 v[84:87], a[28:31], a[88:91], v[84:87]
s_waitcnt lgkmcnt(6)
ds_read_b64_tr_b16 a[20:21], v208 offset:17408
ds_read_b64_tr_b16 a[22:23], v208 offset:21504
v_mfma_f32_16x16x32_bf16 v[96:99], a[0:3], a[64:67], v[96:99]
ds_read_b64_tr_b16 a[24:25], v207 offset:25600
ds_read_b64_tr_b16 a[26:27], v207 offset:29696
v_mfma_f32_16x16x32_bf16 v[100:103], a[4:7], a[64:67], v[100:103]
s_waitcnt lgkmcnt(6)
ds_read_b64_tr_b16 a[28:29], v208 offset:25600
ds_read_b64_tr_b16 a[30:31], v208 offset:29696
v_cvt_pk_bf16_f32 v136, v80, v81
v_cvt_pk_bf16_f32 v137, v82, v83
v_mfma_f32_16x16x32_bf16 v[96:99], a[8:11], a[72:75], v[96:99]
ds_read_b64_tr_b16 a[0:1], v207 offset:1536
ds_read_b64_tr_b16 a[2:3], v207 offset:5632
v_cvt_pk_bf16_f32 v138, v84, v85
v_cvt_pk_bf16_f32 v139, v86, v87
v_mfma_f32_16x16x32_bf16 v[100:103], a[12:15], a[72:75], v[100:103]
s_add_u32 s84, s82, 0x200000
buffer_store_dwordx4 v[136:139], v229, s[56:59], s84 offen sc1
ds_read_b64_tr_b16 a[4:5], v208 offset:1536
ds_read_b64_tr_b16 a[6:7], v208 offset:5632
s_waitcnt lgkmcnt(8)
v_mfma_f32_16x16x32_bf16 v[96:99], a[16:19], a[80:83], v[96:99]
ds_read_b64_tr_b16 a[8:9], v207 offset:9728
ds_read_b64_tr_b16 a[10:11], v207 offset:13824
v_mfma_f32_16x16x32_bf16 v[100:103], a[20:23], a[80:83], v[100:103]
s_waitcnt lgkmcnt(6)
ds_read_b64_tr_b16 a[12:13], v208 offset:9728
ds_read_b64_tr_b16 a[14:15], v208 offset:13824
v_mfma_f32_16x16x32_bf16 v[96:99], a[24:27], a[88:91], v[96:99]
ds_read_b64_tr_b16 a[16:17], v207 offset:17920
ds_read_b64_tr_b16 a[18:19], v207 offset:22016
v_mfma_f32_16x16x32_bf16 v[100:103], a[28:31], a[88:91], v[100:103]
s_waitcnt lgkmcnt(6)
ds_read_b64_tr_b16 a[20:21], v208 offset:17920
ds_read_b64_tr_b16 a[22:23], v208 offset:22016
v_mfma_f32_16x16x32_bf16 v[112:115], a[0:3], a[64:67], v[112:115]
ds_read_b64_tr_b16 a[24:25], v207 offset:26112
ds_read_b64_tr_b16 a[26:27], v207 offset:30208
v_mfma_f32_16x16x32_bf16 v[116:119], a[4:7], a[64:67], v[116:119]
s_waitcnt lgkmcnt(6)
ds_read_b64_tr_b16 a[28:29], v208 offset:26112
ds_read_b64_tr_b16 a[30:31], v208 offset:30208
v_cvt_pk_bf16_f32 v144, v96, v97
v_cvt_pk_bf16_f32 v145, v98, v99
v_mfma_f32_16x16x32_bf16 v[112:115], a[8:11], a[72:75], v[112:115]
ds_read_b64_tr_b16 a[0:1], v205
ds_read_b64_tr_b16 a[2:3], v205 offset:4096
v_cvt_pk_bf16_f32 v146, v100, v101
v_cvt_pk_bf16_f32 v147, v102, v103
v_mfma_f32_16x16x32_bf16 v[116:119], a[12:15], a[72:75], v[116:119]
s_add_u32 s84, s82, 0x400000
buffer_store_dwordx4 v[144:147], v229, s[56:59], s84 offen sc1
ds_read_b64_tr_b16 a[4:5], v206
ds_read_b64_tr_b16 a[6:7], v206 offset:4096
s_waitcnt lgkmcnt(8)
v_mfma_f32_16x16x32_bf16 v[112:115], a[16:19], a[80:83], v[112:115]
ds_read_b64_tr_b16 a[8:9], v205 offset:8192
ds_read_b64_tr_b16 a[10:11], v205 offset:12288
v_mfma_f32_16x16x32_bf16 v[116:119], a[20:23], a[80:83], v[116:119]
s_waitcnt lgkmcnt(6)
ds_read_b64_tr_b16 a[12:13], v206 offset:8192
ds_read_b64_tr_b16 a[14:15], v206 offset:12288
v_mfma_f32_16x16x32_bf16 v[112:115], a[24:27], a[88:91], v[112:115]
ds_read_b64_tr_b16 a[16:17], v205 offset:32768
ds_read_b64_tr_b16 a[18:19], v205 offset:36864
v_mfma_f32_16x16x32_bf16 v[116:119], a[28:31], a[88:91], v[116:119]
s_waitcnt lgkmcnt(6)
ds_read_b64_tr_b16 a[20:21], v206 offset:32768
ds_read_b64_tr_b16 a[22:23], v206 offset:36864
v_mfma_f32_16x16x32_bf16 v[64:67], a[0:3], a[160:163], v[64:67]
ds_read_b64_tr_b16 a[24:25], v205 offset:40960
ds_read_b64_tr_b16 a[26:27], v205 offset:45056
v_mfma_f32_16x16x32_bf16 v[68:71], a[4:7], a[160:163], v[68:71]
s_waitcnt lgkmcnt(6)
ds_read_b64_tr_b16 a[28:29], v206 offset:40960
ds_read_b64_tr_b16 a[30:31], v206 offset:45056
v_cvt_pk_bf16_f32 v152, v112, v113
v_cvt_pk_bf16_f32 v153, v114, v115
v_mfma_f32_16x16x32_bf16 v[64:67], a[8:11], a[168:171], v[64:67]
ds_read_b64_tr_b16 a[0:1], v203
ds_read_b64_tr_b16 a[2:3], v203 offset:4096
v_cvt_pk_bf16_f32 v154, v116, v117
v_cvt_pk_bf16_f32 v155, v118, v119
v_mfma_f32_16x16x32_bf16 v[68:71], a[12:15], a[168:171], v[68:71]
s_add_u32 s84, s82, 0x600000
buffer_store_dwordx4 v[152:155], v229, s[56:59], s84 offen sc1
ds_read_b64_tr_b16 a[4:5], v204
ds_read_b64_tr_b16 a[6:7], v204 offset:4096
s_waitcnt lgkmcnt(8)
v_mfma_f32_16x16x32_bf16 v[64:67], a[16:19], a[176:179], v[64:67]
ds_read_b64_tr_b16 a[8:9], v203 offset:8192
ds_read_b64_tr_b16 a[10:11], v203 offset:12288
v_mfma_f32_16x16x32_bf16 v[68:71], a[20:23], a[176:179], v[68:71]
s_waitcnt lgkmcnt(6)
ds_read_b64_tr_b16 a[12:13], v204 offset:8192
ds_read_b64_tr_b16 a[14:15], v204 offset:12288
v_mfma_f32_16x16x32_bf16 v[64:67], a[24:27], a[184:187], v[64:67]
ds_read_b64_tr_b16 a[16:17], v203 offset:49152
ds_read_b64_tr_b16 a[18:19], v203 offset:53248
v_mfma_f32_16x16x32_bf16 v[68:71], a[28:31], a[184:187], v[68:71]
s_waitcnt lgkmcnt(6)
ds_read_b64_tr_b16 a[20:21], v204 offset:49152
ds_read_b64_tr_b16 a[22:23], v204 offset:53248
v_mfma_f32_16x16x32_bf16 v[64:67], a[0:3], a[192:195], v[64:67]
ds_read_b64_tr_b16 a[24:25], v203 offset:57344
ds_read_b64_tr_b16 a[26:27], v203 offset:61440
v_mfma_f32_16x16x32_bf16 v[68:71], a[4:7], a[192:195], v[68:71]
s_waitcnt lgkmcnt(6)
ds_read_b64_tr_b16 a[28:29], v204 offset:57344
ds_read_b64_tr_b16 a[30:31], v204 offset:61440
v_mfma_f32_16x16x32_bf16 v[64:67], a[8:11], a[200:203], v[64:67]
ds_read_b64_tr_b16 a[0:1], v203 offset:16384
ds_read_b64_tr_b16 a[2:3], v203 offset:20480
v_mfma_f32_16x16x32_bf16 v[68:71], a[12:15], a[200:203], v[68:71]
s_waitcnt lgkmcnt(6)
ds_read_b64_tr_b16 a[4:5], v204 offset:16384
ds_read_b64_tr_b16 a[6:7], v204 offset:20480
v_mfma_f32_16x16x32_bf16 v[64:67], a[16:19], a[208:211], v[64:67]
ds_read_b64_tr_b16 a[8:9], v203 offset:24576
ds_read_b64_tr_b16 a[10:11], v203 offset:28672
buffer_load_dword v235, v231, s[56:59], 0 offen sc1
v_mfma_f32_16x16x32_bf16 v[68:71], a[20:23], a[208:211], v[68:71]
s_waitcnt lgkmcnt(6)
ds_read_b64_tr_b16 a[12:13], v204 offset:24576
ds_read_b64_tr_b16 a[14:15], v204 offset:28672
v_mfma_f32_16x16x32_bf16 v[64:67], a[24:27], a[216:219], v[64:67]
ds_read_b64_tr_b16 a[16:17], v205 offset:16384
ds_read_b64_tr_b16 a[18:19], v205 offset:20480
v_mfma_f32_16x16x32_bf16 v[68:71], a[28:31], a[216:219], v[68:71]
s_waitcnt lgkmcnt(6)
ds_read_b64_tr_b16 a[20:21], v206 offset:16384
ds_read_b64_tr_b16 a[22:23], v206 offset:20480
v_mfma_f32_16x16x32_bf16 v[64:67], a[0:3], a[224:227], v[64:67]
ds_read_b64_tr_b16 a[24:25], v205 offset:24576
ds_read_b64_tr_b16 a[26:27], v205 offset:28672
v_mfma_f32_16x16x32_bf16 v[68:71], a[4:7], a[224:227], v[68:71]
s_waitcnt lgkmcnt(6)
ds_read_b64_tr_b16 a[28:29], v206 offset:24576
ds_read_b64_tr_b16 a[30:31], v206 offset:28672
v_mfma_f32_16x16x32_bf16 v[64:67], a[8:11], a[232:235], v[64:67]
ds_read_b64_tr_b16 a[0:1], v207
ds_read_b64_tr_b16 a[2:3], v207 offset:4096
v_mfma_f32_16x16x32_bf16 v[68:71], a[12:15], a[232:235], v[68:71]
)"
R"(
s_waitcnt lgkmcnt(6)
ds_read_b64_tr_b16 a[4:5], v208
ds_read_b64_tr_b16 a[6:7], v208 offset:4096
v_mfma_f32_16x16x32_bf16 v[64:67], a[16:19], a[240:243], v[64:67]
s_waitcnt vmcnt(1)
s_mov_b64 exec, 0xe00
buffer_store_dword v234, v232, s[56:59], 0 offen sc1
s_mov_b64 exec, -1
ds_read_b64_tr_b16 a[8:9], v207 offset:8192
ds_read_b64_tr_b16 a[10:11], v207 offset:12288
v_mfma_f32_16x16x32_bf16 v[68:71], a[20:23], a[240:243], v[68:71]
s_waitcnt lgkmcnt(6)
ds_read_b64_tr_b16 a[12:13], v208 offset:8192
ds_read_b64_tr_b16 a[14:15], v208 offset:12288
v_mfma_f32_16x16x32_bf16 v[64:67], a[24:27], a[248:251], v[64:67]
ds_read_b64_tr_b16 a[16:17], v207 offset:16384
ds_read_b64_tr_b16 a[18:19], v207 offset:20480
v_mfma_f32_16x16x32_bf16 v[68:71], a[28:31], a[248:251], v[68:71]
s_waitcnt lgkmcnt(6)
ds_read_b64_tr_b16 a[20:21], v208 offset:16384
ds_read_b64_tr_b16 a[22:23], v208 offset:20480
v_mfma_f32_16x16x32_bf16 v[64:67], a[0:3], a[64:67], v[64:67]
ds_read_b64_tr_b16 a[24:25], v207 offset:24576
ds_read_b64_tr_b16 a[26:27], v207 offset:28672
v_mfma_f32_16x16x32_bf16 v[68:71], a[4:7], a[64:67], v[68:71]
s_waitcnt lgkmcnt(6)
ds_read_b64_tr_b16 a[28:29], v208 offset:24576
ds_read_b64_tr_b16 a[30:31], v208 offset:28672
v_mfma_f32_16x16x32_bf16 v[64:67], a[8:11], a[72:75], v[64:67]
v_mfma_f32_16x16x32_bf16 v[68:71], a[12:15], a[72:75], v[68:71]
s_waitcnt lgkmcnt(4)
v_mfma_f32_16x16x32_bf16 v[64:67], a[16:19], a[80:83], v[64:67]
v_mfma_f32_16x16x32_bf16 v[68:71], a[20:23], a[80:83], v[68:71]
s_waitcnt lgkmcnt(0)
v_mfma_f32_16x16x32_bf16 v[64:67], a[24:27], a[88:91], v[64:67]
v_mfma_f32_16x16x32_bf16 v[68:71], a[28:31], a[88:91], v[68:71]
s_waitcnt vmcnt(1)
v_cmp_eq_u32_e64 s[74:75], v235, v233
s_nop 1
s_and_b32 s79, s74, 0xe00
s_cmp_eq_u32 s79, 0xe00
s_cbranch_scc0 .Lk_bslow3
.Lk_bok3:
s_xor_b32 s78, s82, 0x202000
buffer_load_dwordx4 v[136:139], v229, s[56:59], s78 offen sc1
s_xor_b32 s78, s82, 0x404000
buffer_load_dwordx4 v[144:147], v229, s[56:59], s78 offen sc1
s_xor_b32 s78, s82, 0x606000
buffer_load_dwordx4 v[152:155], v229, s[56:59], s78 offen sc1
buffer_load_dword v234, v232, s[56:59], 0 offen offset:128 sc1
s_mov_b32 s79, 0xffff0000
s_mov_b32 s84, 0x8000
s_waitcnt vmcnt(3)
v_lshlrev_b32_e32 v246, 16, v136
v_and_b32_e32 v247, s79, v136
v_pk_add_f32 v[64:65], v[64:65], v[246:247]
v_lshlrev_b32_e32 v246, 16, v137
v_and_b32_e32 v247, s79, v137
v_pk_add_f32 v[66:67], v[66:67], v[246:247]
v_lshlrev_b32_e32 v246, 16, v138
v_and_b32_e32 v247, s79, v138
v_pk_add_f32 v[68:69], v[68:69], v[246:247]
v_lshlrev_b32_e32 v246, 16, v139
v_and_b32_e32 v247, s79, v139
v_pk_add_f32 v[70:71], v[70:71], v[246:247]
s_waitcnt vmcnt(2)
v_lshlrev_b32_e32 v246, 16, v144
v_and_b32_e32 v247, s79, v144
v_pk_add_f32 v[64:65], v[64:65], v[246:247]
v_lshlrev_b32_e32 v246, 16, v145
v_and_b32_e32 v247, s79, v145
v_pk_add_f32 v[66:67], v[66:67], v[246:247]
v_lshlrev_b32_e32 v246, 16, v146
v_and_b32_e32 v247, s79, v146
v_pk_add_f32 v[68:69], v[68:69], v[246:247]
v_lshlrev_b32_e32 v246, 16, v147
v_and_b32_e32 v247, s79, v147
v_pk_add_f32 v[70:71], v[70:71], v[246:247]
s_waitcnt vmcnt(1)
v_lshlrev_b32_e32 v246, 16, v152
v_and_b32_e32 v247, s79, v152
v_pk_add_f32 v[64:65], v[64:65], v[246:247]
v_lshlrev_b32_e32 v246, 16, v153
v_and_b32_e32 v247, s79, v153
v_pk_add_f32 v[66:67], v[66:67], v[246:247]
v_lshlrev_b32_e32 v246, 16, v154
v_and_b32_e32 v247, s79, v154
v_pk_add_f32 v[68:69], v[68:69], v[246:247]
v_lshlrev_b32_e32 v246, 16, v155
v_and_b32_e32 v247, s79, v155
v_pk_add_f32 v[70:71], v[70:71], v[246:247]
v_cvt_pk_bf16_f32 v0, v64, v65
v_cvt_pk_bf16_f32 v2, v68, v69
v_cvt_pk_bf16_f32 v1, v66, v67
v_cvt_pk_bf16_f32 v3, v70, v71
v_permlane16_swap_b32_e32 v0, v2
s_nop 0
v_permlane16_swap_b32_e32 v1, v3
buffer_store_dwordx4 v[0:3], v230, s[60:63], 0 offen sc1
s_waitcnt vmcnt(1)
s_mov_b64 exec, 0xe00
buffer_store_dword v234, v232, s[56:59], 0 offen sc1
s_mov_b64 exec, -1
s_endpgm
.Lk_bslow1:
s_mov_b32 s83, 0
.Lk_bspin1:
s_add_u32 s83, s83, 1
s_cmp_gt_u32 s83, 0x30d40
s_cbranch_scc1 .Lk_bok1
s_and_b32 s79, s83, 63
s_cbranch_scc1 .Lk_bnore1
buffer_load_dword v234, v232, s[56:59], 0 offen offset:128 sc1
s_waitcnt vmcnt(0)
s_mov_b64 exec, 14
buffer_store_dword v234, v232, s[56:59], 0 offen sc1
s_mov_b64 exec, -1
.Lk_bnore1:
buffer_load_dword v235, v231, s[56:59], 0 offen sc1
s_waitcnt vmcnt(0)
v_cmp_eq_u32_e64 s[74:75], v235, v233
s_nop 1
s_and_b32 s79, s74, 14
s_cmp_eq_u32 s79, 14
s_cbranch_scc0 .Lk_bspin1
s_branch .Lk_bok1
.Lk_bslow2:
s_mov_b32 s83, 0
.Lk_bspin2:
s_add_u32 s83, s83, 1
s_cmp_gt_u32 s83, 0x30d40
s_cbranch_scc1 .Lk_bok2
s_and_b32 s79, s83, 63
s_cbranch_scc1 .Lk_bnore2
buffer_load_dword v234, v232, s[56:59], 0 offen offset:128 sc1
s_waitcnt vmcnt(0)
s_mov_b64 exec, 0xee
buffer_store_dword v234, v232, s[56:59], 0 offen sc1
s_mov_b64 exec, -1
.Lk_bnore2:
buffer_load_dword v235, v231, s[56:59], 0 offen sc1
s_waitcnt vmcnt(0)
v_cmp_eq_u32_e64 s[74:75], v235, v233
s_nop 1
s_and_b32 s79, s74, 0xe0
s_cmp_eq_u32 s79, 0xe0
s_cbranch_scc0 .Lk_bspin2
s_branch .Lk_bok2
.Lk_bslow3:
s_mov_b32 s83, 0
.Lk_bspin3:
s_add_u32 s83, s83, 1
s_cmp_gt_u32 s83, 0x30d40
s_cbranch_scc1 .Lk_bok3
s_and_b32 s79, s83, 63
s_cbranch_scc1 .Lk_bnore3
buffer_load_dword v234, v232, s[56:59], 0 offen offset:128 sc1
s_waitcnt vmcnt(0)
s_mov_b64 exec, 0xeee
buffer_store_dword v234, v232, s[56:59], 0 offen sc1
s_mov_b64 exec, -1
.Lk_bnore3:
buffer_load_dword v235, v231, s[56:59], 0 offen sc1
s_waitcnt vmcnt(0)
v_cmp_eq_u32_e64 s[74:75], v235, v233
s_nop 1
s_and_b32 s79, s74, 0xe00
s_cmp_eq_u32 s79, 0xe00
s_cbranch_scc0 .Lk_bspin3
s_branch .Lk_bok3
.Lk_end:
.Lk_ara1:
v_max_f32_e32 v236, v236, v135
v_max_f32_e32 v237, v237, v143
v_mov_b32_e32 v246, v236
v_mov_b32_e32 v247, v237
s_nop 0
v_permlane16_swap_b32_e32 v236, v246
v_permlane16_swap_b32_e32 v237, v247
v_max_f32_e32 v236, v236, v246
v_max_f32_e32 v237, v237, v247
v_mov_b32_e32 v246, v236
v_mov_b32_e32 v247, v237
s_nop 0
v_permlane32_swap_b32_e32 v236, v246
v_permlane32_swap_b32_e32 v237, v247
v_max_f32_e32 v236, v236, v246
v_max_f32_e32 v237, v237, v247
v_max_f32_e32 v236, 0, v236
v_max_f32_e32 v237, 0, v237
v_exp_f32_e64 v238, -v236
v_exp_f32_e64 v239, -v237
v_sub_f32_e32 v192, v192, v236
v_sub_f32_e32 v196, v196, v237
v_sub_f32_e32 v193, v193, v236
v_sub_f32_e32 v197, v197, v237
v_sub_f32_e32 v194, v194, v236
v_sub_f32_e32 v198, v198, v237
v_sub_f32_e32 v195, v195, v236
v_sub_f32_e32 v199, v199, v237
)"
R"(
v_sub_f32_e32 v160, v160, v236
v_sub_f32_e32 v168, v168, v237
v_sub_f32_e32 v161, v161, v236
v_sub_f32_e32 v169, v169, v237
v_sub_f32_e32 v162, v162, v236
v_sub_f32_e32 v170, v170, v237
v_sub_f32_e32 v163, v163, v236
v_sub_f32_e32 v171, v171, v237
v_sub_f32_e32 v164, v164, v236
v_sub_f32_e32 v172, v172, v237
v_sub_f32_e32 v165, v165, v236
v_sub_f32_e32 v173, v173, v237
v_sub_f32_e32 v166, v166, v236
v_sub_f32_e32 v174, v174, v237
v_sub_f32_e32 v167, v167, v236
v_sub_f32_e32 v175, v175, v237
v_sub_f32_e32 v128, v128, v236
v_sub_f32_e32 v136, v136, v237
v_sub_f32_e32 v129, v129, v236
v_sub_f32_e32 v137, v137, v237
v_sub_f32_e32 v130, v130, v236
v_sub_f32_e32 v138, v138, v237
v_sub_f32_e32 v131, v131, v236
v_sub_f32_e32 v139, v139, v237
v_sub_f32_e32 v132, v132, v236
v_sub_f32_e32 v140, v140, v237
v_sub_f32_e32 v133, v133, v236
v_sub_f32_e32 v141, v141, v237
v_sub_f32_e32 v134, v134, v236
v_sub_f32_e32 v142, v142, v237
v_sub_f32_e32 v135, v135, v236
v_sub_f32_e32 v143, v143, v237
v_mul_f32_e32 v242, v238, v242
v_mul_f32_e32 v243, v239, v243
s_nop 1
s_branch .Lk_aba1
.Lk_arb1:
s_nop 7
v_mul_f32_e32 v0, v238, v0
v_mul_f32_e32 v1, v238, v1
v_mul_f32_e32 v2, v238, v2
v_mul_f32_e32 v3, v238, v3
v_mul_f32_e32 v4, v238, v4
v_mul_f32_e32 v5, v238, v5
v_mul_f32_e32 v6, v238, v6
v_mul_f32_e32 v7, v238, v7
v_mul_f32_e32 v8, v239, v8
v_mul_f32_e32 v9, v239, v9
v_mul_f32_e32 v10, v239, v10
v_mul_f32_e32 v11, v239, v11
v_mul_f32_e32 v12, v239, v12
v_mul_f32_e32 v13, v239, v13
v_mul_f32_e32 v14, v239, v14
v_mul_f32_e32 v15, v239, v15
v_mul_f32_e32 v16, v238, v16
v_mul_f32_e32 v17, v238, v17
v_mul_f32_e32 v18, v238, v18
v_mul_f32_e32 v19, v238, v19
v_mul_f32_e32 v20, v238, v20
v_mul_f32_e32 v21, v238, v21
v_mul_f32_e32 v22, v238, v22
v_mul_f32_e32 v23, v238, v23
v_mul_f32_e32 v24, v239, v24
v_mul_f32_e32 v25, v239, v25
v_mul_f32_e32 v26, v239, v26
v_mul_f32_e32 v27, v239, v27
v_mul_f32_e32 v28, v239, v28
v_mul_f32_e32 v29, v239, v29
v_mul_f32_e32 v30, v239, v30
v_mul_f32_e32 v31, v239, v31
v_mul_f32_e32 v32, v238, v32
v_mul_f32_e32 v33, v238, v33
v_mul_f32_e32 v34, v238, v34
v_mul_f32_e32 v35, v238, v35
v_mul_f32_e32 v36, v238, v36
v_mul_f32_e32 v37, v238, v37
v_mul_f32_e32 v38, v238, v38
v_mul_f32_e32 v39, v238, v39
v_mul_f32_e32 v40, v239, v40
v_mul_f32_e32 v41, v239, v41
v_mul_f32_e32 v42, v239, v42
v_mul_f32_e32 v43, v239, v43
v_mul_f32_e32 v44, v239, v44
v_mul_f32_e32 v45, v239, v45
v_mul_f32_e32 v46, v239, v46
v_mul_f32_e32 v47, v239, v47
v_mul_f32_e32 v48, v238, v48
v_mul_f32_e32 v49, v238, v49
v_mul_f32_e32 v50, v238, v50
v_mul_f32_e32 v51, v238, v51
v_mul_f32_e32 v52, v238, v52
v_mul_f32_e32 v53, v238, v53
v_mul_f32_e32 v54, v238, v54
v_mul_f32_e32 v55, v238, v55
v_mul_f32_e32 v56, v239, v56
v_mul_f32_e32 v57, v239, v57
v_mul_f32_e32 v58, v239, v58
v_mul_f32_e32 v59, v239, v59
v_mul_f32_e32 v60, v239, v60
v_mul_f32_e32 v61, v239, v61
v_mul_f32_e32 v62, v239, v62
v_mul_f32_e32 v63, v239, v63
s_nop 1
s_branch .Lk_abb1
.Lk_ara2:
v_max_f32_e32 v236, v236, v167
v_max_f32_e32 v237, v237, v175
v_mov_b32_e32 v246, v236
v_mov_b32_e32 v247, v237
s_nop 0
v_permlane16_swap_b32_e32 v236, v246
v_permlane16_swap_b32_e32 v237, v247
v_max_f32_e32 v236, v236, v246
v_max_f32_e32 v237, v237, v247
v_mov_b32_e32 v246, v236
v_mov_b32_e32 v247, v237
s_nop 0
v_permlane32_swap_b32_e32 v236, v246
v_permlane32_swap_b32_e32 v237, v247
v_max_f32_e32 v236, v236, v246
v_max_f32_e32 v237, v237, v247
v_max_f32_e32 v236, 0, v236
v_max_f32_e32 v237, 0, v237
v_exp_f32_e64 v238, -v236
v_exp_f32_e64 v239, -v237
v_sub_f32_e32 v192, v192, v236
v_sub_f32_e32 v196, v196, v237
v_sub_f32_e32 v193, v193, v236
v_sub_f32_e32 v197, v197, v237
v_sub_f32_e32 v194, v194, v236
v_sub_f32_e32 v198, v198, v237
v_sub_f32_e32 v195, v195, v236
v_sub_f32_e32 v199, v199, v237
v_sub_f32_e32 v144, v144, v236
v_sub_f32_e32 v152, v152, v237
v_sub_f32_e32 v145, v145, v236
v_sub_f32_e32 v153, v153, v237
v_sub_f32_e32 v146, v146, v236
v_sub_f32_e32 v154, v154, v237
v_sub_f32_e32 v147, v147, v236
v_sub_f32_e32 v155, v155, v237
v_sub_f32_e32 v148, v148, v236
v_sub_f32_e32 v156, v156, v237
v_sub_f32_e32 v149, v149, v236
v_sub_f32_e32 v157, v157, v237
v_sub_f32_e32 v150, v150, v236
v_sub_f32_e32 v158, v158, v237
v_sub_f32_e32 v151, v151, v236
v_sub_f32_e32 v159, v159, v237
v_sub_f32_e32 v160, v160, v236
v_sub_f32_e32 v168, v168, v237
v_sub_f32_e32 v161, v161, v236
v_sub_f32_e32 v169, v169, v237
v_sub_f32_e32 v162, v162, v236
v_sub_f32_e32 v170, v170, v237
v_sub_f32_e32 v163, v163, v236
v_sub_f32_e32 v171, v171, v237
v_sub_f32_e32 v164, v164, v236
v_sub_f32_e32 v172, v172, v237
v_sub_f32_e32 v165, v165, v236
v_sub_f32_e32 v173, v173, v237
v_sub_f32_e32 v166, v166, v236
v_sub_f32_e32 v174, v174, v237
v_sub_f32_e32 v167, v167, v236
v_sub_f32_e32 v175, v175, v237
v_mul_f32_e32 v242, v238, v242
v_mul_f32_e32 v243, v239, v243
s_nop 1
s_branch .Lk_aba2
.Lk_arb2:
s_nop 7
v_mul_f32_e32 v0, v238, v0
v_mul_f32_e32 v1, v238, v1
v_mul_f32_e32 v2, v238, v2
v_mul_f32_e32 v3, v238, v3
v_mul_f32_e32 v4, v238, v4
v_mul_f32_e32 v5, v238, v5
v_mul_f32_e32 v6, v238, v6
v_mul_f32_e32 v7, v238, v7
v_mul_f32_e32 v8, v239, v8
v_mul_f32_e32 v9, v239, v9
v_mul_f32_e32 v10, v239, v10
v_mul_f32_e32 v11, v239, v11
v_mul_f32_e32 v12, v239, v12
v_mul_f32_e32 v13, v239, v13
v_mul_f32_e32 v14, v239, v14
v_mul_f32_e32 v15, v239, v15
v_mul_f32_e32 v16, v238, v16
v_mul_f32_e32 v17, v238, v17
v_mul_f32_e32 v18, v238, v18
v_mul_f32_e32 v19, v238, v19
v_mul_f32_e32 v20, v238, v20
v_mul_f32_e32 v21, v238, v21
v_mul_f32_e32 v22, v238, v22
v_mul_f32_e32 v23, v238, v23
v_mul_f32_e32 v24, v239, v24
v_mul_f32_e32 v25, v239, v25
v_mul_f32_e32 v26, v239, v26
v_mul_f32_e32 v27, v239, v27
v_mul_f32_e32 v28, v239, v28
)"
R"(
v_mul_f32_e32 v29, v239, v29
v_mul_f32_e32 v30, v239, v30
v_mul_f32_e32 v31, v239, v31
v_mul_f32_e32 v32, v238, v32
v_mul_f32_e32 v33, v238, v33
v_mul_f32_e32 v34, v238, v34
v_mul_f32_e32 v35, v238, v35
v_mul_f32_e32 v36, v238, v36
v_mul_f32_e32 v37, v238, v37
v_mul_f32_e32 v38, v238, v38
v_mul_f32_e32 v39, v238, v39
v_mul_f32_e32 v40, v239, v40
v_mul_f32_e32 v41, v239, v41
v_mul_f32_e32 v42, v239, v42
v_mul_f32_e32 v43, v239, v43
v_mul_f32_e32 v44, v239, v44
v_mul_f32_e32 v45, v239, v45
v_mul_f32_e32 v46, v239, v46
v_mul_f32_e32 v47, v239, v47
v_mul_f32_e32 v48, v238, v48
v_mul_f32_e32 v49, v238, v49
v_mul_f32_e32 v50, v238, v50
v_mul_f32_e32 v51, v238, v51
v_mul_f32_e32 v52, v238, v52
v_mul_f32_e32 v53, v238, v53
v_mul_f32_e32 v54, v238, v54
v_mul_f32_e32 v55, v238, v55
v_mul_f32_e32 v56, v239, v56
v_mul_f32_e32 v57, v239, v57
v_mul_f32_e32 v58, v239, v58
v_mul_f32_e32 v59, v239, v59
v_mul_f32_e32 v60, v239, v60
v_mul_f32_e32 v61, v239, v61
v_mul_f32_e32 v62, v239, v62
v_mul_f32_e32 v63, v239, v63
s_nop 1
s_branch .Lk_abb2
.Lk_ara3:
v_max_f32_e32 v236, v236, v151
v_max_f32_e32 v237, v237, v159
v_mov_b32_e32 v246, v236
v_mov_b32_e32 v247, v237
s_nop 0
v_permlane16_swap_b32_e32 v236, v246
v_permlane16_swap_b32_e32 v237, v247
v_max_f32_e32 v236, v236, v246
v_max_f32_e32 v237, v237, v247
v_mov_b32_e32 v246, v236
v_mov_b32_e32 v247, v237
s_nop 0
v_permlane32_swap_b32_e32 v236, v246
v_permlane32_swap_b32_e32 v237, v247
v_max_f32_e32 v236, v236, v246
v_max_f32_e32 v237, v237, v247
v_max_f32_e32 v236, 0, v236
v_max_f32_e32 v237, 0, v237
v_exp_f32_e64 v238, -v236
v_exp_f32_e64 v239, -v237
v_sub_f32_e32 v192, v192, v236
v_sub_f32_e32 v196, v196, v237
v_sub_f32_e32 v193, v193, v236
v_sub_f32_e32 v197, v197, v237
v_sub_f32_e32 v194, v194, v236
v_sub_f32_e32 v198, v198, v237
v_sub_f32_e32 v195, v195, v236
v_sub_f32_e32 v199, v199, v237
v_sub_f32_e32 v128, v128, v236
v_sub_f32_e32 v136, v136, v237
v_sub_f32_e32 v129, v129, v236
v_sub_f32_e32 v137, v137, v237
v_sub_f32_e32 v130, v130, v236
v_sub_f32_e32 v138, v138, v237
v_sub_f32_e32 v131, v131, v236
v_sub_f32_e32 v139, v139, v237
v_sub_f32_e32 v132, v132, v236
v_sub_f32_e32 v140, v140, v237
v_sub_f32_e32 v133, v133, v236
v_sub_f32_e32 v141, v141, v237
v_sub_f32_e32 v134, v134, v236
v_sub_f32_e32 v142, v142, v237
v_sub_f32_e32 v135, v135, v236
v_sub_f32_e32 v143, v143, v237
v_sub_f32_e32 v144, v144, v236
v_sub_f32_e32 v152, v152, v237
v_sub_f32_e32 v145, v145, v236
v_sub_f32_e32 v153, v153, v237
v_sub_f32_e32 v146, v146, v236
v_sub_f32_e32 v154, v154, v237
v_sub_f32_e32 v147, v147, v236
v_sub_f32_e32 v155, v155, v237
v_sub_f32_e32 v148, v148, v236
v_sub_f32_e32 v156, v156, v237
v_sub_f32_e32 v149, v149, v236
v_sub_f32_e32 v157, v157, v237
v_sub_f32_e32 v150, v150, v236
v_sub_f32_e32 v158, v158, v237
v_sub_f32_e32 v151, v151, v236
v_sub_f32_e32 v159, v159, v237
v_mul_f32_e32 v242, v238, v242
v_mul_f32_e32 v243, v239, v243
s_nop 1
s_branch .Lk_aba3
.Lk_arb3:
s_nop 7
v_mul_f32_e32 v0, v238, v0
v_mul_f32_e32 v1, v238, v1
v_mul_f32_e32 v2, v238, v2
v_mul_f32_e32 v3, v238, v3
v_mul_f32_e32 v4, v238, v4
v_mul_f32_e32 v5, v238, v5
v_mul_f32_e32 v6, v238, v6
v_mul_f32_e32 v7, v238, v7
v_mul_f32_e32 v8, v239, v8
v_mul_f32_e32 v9, v239, v9
v_mul_f32_e32 v10, v239, v10
v_mul_f32_e32 v11, v239, v11
v_mul_f32_e32 v12, v239, v12
v_mul_f32_e32 v13, v239, v13
v_mul_f32_e32 v14, v239, v14
v_mul_f32_e32 v15, v239, v15
v_mul_f32_e32 v16, v238, v16
v_mul_f32_e32 v17, v238, v17
v_mul_f32_e32 v18, v238, v18
v_mul_f32_e32 v19, v238, v19
v_mul_f32_e32 v20, v238, v20
v_mul_f32_e32 v21, v238, v21
v_mul_f32_e32 v22, v238, v22
v_mul_f32_e32 v23, v238, v23
v_mul_f32_e32 v24, v239, v24
v_mul_f32_e32 v25, v239, v25
v_mul_f32_e32 v26, v239, v26
v_mul_f32_e32 v27, v239, v27
v_mul_f32_e32 v28, v239, v28
v_mul_f32_e32 v29, v239, v29
v_mul_f32_e32 v30, v239, v30
v_mul_f32_e32 v31, v239, v31
v_mul_f32_e32 v32, v238, v32
v_mul_f32_e32 v33, v238, v33
v_mul_f32_e32 v34, v238, v34
v_mul_f32_e32 v35, v238, v35
v_mul_f32_e32 v36, v238, v36
v_mul_f32_e32 v37, v238, v37
v_mul_f32_e32 v38, v238, v38
v_mul_f32_e32 v39, v238, v39
v_mul_f32_e32 v40, v239, v40
v_mul_f32_e32 v41, v239, v41
v_mul_f32_e32 v42, v239, v42
v_mul_f32_e32 v43, v239, v43
v_mul_f32_e32 v44, v239, v44
v_mul_f32_e32 v45, v239, v45
v_mul_f32_e32 v46, v239, v46
v_mul_f32_e32 v47, v239, v47
v_mul_f32_e32 v48, v238, v48
v_mul_f32_e32 v49, v238, v49
v_mul_f32_e32 v50, v238, v50
v_mul_f32_e32 v51, v238, v51
v_mul_f32_e32 v52, v238, v52
v_mul_f32_e32 v53, v238, v53
v_mul_f32_e32 v54, v238, v54
v_mul_f32_e32 v55, v238, v55
v_mul_f32_e32 v56, v239, v56
v_mul_f32_e32 v57, v239, v57
v_mul_f32_e32 v58, v239, v58
v_mul_f32_e32 v59, v239, v59
v_mul_f32_e32 v60, v239, v60
v_mul_f32_e32 v61, v239, v61
v_mul_f32_e32 v62, v239, v62
v_mul_f32_e32 v63, v239, v63
s_nop 1
s_branch .Lk_abb3
.Lk_ara4:
v_max_f32_e32 v236, v236, v135
v_max_f32_e32 v237, v237, v143
v_mov_b32_e32 v246, v236
v_mov_b32_e32 v247, v237
s_nop 0
v_permlane16_swap_b32_e32 v236, v246
v_permlane16_swap_b32_e32 v237, v247
v_max_f32_e32 v236, v236, v246
v_max_f32_e32 v237, v237, v247
v_mov_b32_e32 v246, v236
v_mov_b32_e32 v247, v237
s_nop 0
v_permlane32_swap_b32_e32 v236, v246
v_permlane32_swap_b32_e32 v237, v247
v_max_f32_e32 v236, v236, v246
v_max_f32_e32 v237, v237, v247
v_max_f32_e32 v236, 0, v236
v_max_f32_e32 v237, 0, v237
v_exp_f32_e64 v238, -v236
v_exp_f32_e64 v239, -v237
v_sub_f32_e32 v192, v192, v236
v_sub_f32_e32 v196, v196, v237
v_sub_f32_e32 v193, v193, v236
v_sub_f32_e32 v197, v197, v237
v_sub_f32_e32 v194, v194, v236
v_sub_f32_e32 v198, v198, v237
v_sub_f32_e32 v195, v195, v236
v_sub_f32_e32 v199, v199, v237
v_sub_f32_e32 v160, v160, v236
)"
R"(
v_sub_f32_e32 v168, v168, v237
v_sub_f32_e32 v161, v161, v236
v_sub_f32_e32 v169, v169, v237
v_sub_f32_e32 v162, v162, v236
v_sub_f32_e32 v170, v170, v237
v_sub_f32_e32 v163, v163, v236
v_sub_f32_e32 v171, v171, v237
v_sub_f32_e32 v164, v164, v236
v_sub_f32_e32 v172, v172, v237
v_sub_f32_e32 v165, v165, v236
v_sub_f32_e32 v173, v173, v237
v_sub_f32_e32 v166, v166, v236
v_sub_f32_e32 v174, v174, v237
v_sub_f32_e32 v167, v167, v236
v_sub_f32_e32 v175, v175, v237
v_sub_f32_e32 v128, v128, v236
v_sub_f32_e32 v136, v136, v237
v_sub_f32_e32 v129, v129, v236
v_sub_f32_e32 v137, v137, v237
v_sub_f32_e32 v130, v130, v236
v_sub_f32_e32 v138, v138, v237
v_sub_f32_e32 v131, v131, v236
v_sub_f32_e32 v139, v139, v237
v_sub_f32_e32 v132, v132, v236
v_sub_f32_e32 v140, v140, v237
v_sub_f32_e32 v133, v133, v236
v_sub_f32_e32 v141, v141, v237
v_sub_f32_e32 v134, v134, v236
v_sub_f32_e32 v142, v142, v237
v_sub_f32_e32 v135, v135, v236
v_sub_f32_e32 v143, v143, v237
v_mul_f32_e32 v242, v238, v242
v_mul_f32_e32 v243, v239, v243
s_nop 1
s_branch .Lk_aba4
.Lk_arb4:
s_nop 7
v_mul_f32_e32 v0, v238, v0
v_mul_f32_e32 v1, v238, v1
v_mul_f32_e32 v2, v238, v2
v_mul_f32_e32 v3, v238, v3
v_mul_f32_e32 v4, v238, v4
v_mul_f32_e32 v5, v238, v5
v_mul_f32_e32 v6, v238, v6
v_mul_f32_e32 v7, v238, v7
v_mul_f32_e32 v8, v239, v8
v_mul_f32_e32 v9, v239, v9
v_mul_f32_e32 v10, v239, v10
v_mul_f32_e32 v11, v239, v11
v_mul_f32_e32 v12, v239, v12
v_mul_f32_e32 v13, v239, v13
v_mul_f32_e32 v14, v239, v14
v_mul_f32_e32 v15, v239, v15
v_mul_f32_e32 v16, v238, v16
v_mul_f32_e32 v17, v238, v17
v_mul_f32_e32 v18, v238, v18
v_mul_f32_e32 v19, v238, v19
v_mul_f32_e32 v20, v238, v20
v_mul_f32_e32 v21, v238, v21
v_mul_f32_e32 v22, v238, v22
v_mul_f32_e32 v23, v238, v23
v_mul_f32_e32 v24, v239, v24
v_mul_f32_e32 v25, v239, v25
v_mul_f32_e32 v26, v239, v26
v_mul_f32_e32 v27, v239, v27
v_mul_f32_e32 v28, v239, v28
v_mul_f32_e32 v29, v239, v29
v_mul_f32_e32 v30, v239, v30
v_mul_f32_e32 v31, v239, v31
v_mul_f32_e32 v32, v238, v32
v_mul_f32_e32 v33, v238, v33
v_mul_f32_e32 v34, v238, v34
v_mul_f32_e32 v35, v238, v35
v_mul_f32_e32 v36, v238, v36
v_mul_f32_e32 v37, v238, v37
v_mul_f32_e32 v38, v238, v38
v_mul_f32_e32 v39, v238, v39
v_mul_f32_e32 v40, v239, v40
v_mul_f32_e32 v41, v239, v41
v_mul_f32_e32 v42, v239, v42
v_mul_f32_e32 v43, v239, v43
v_mul_f32_e32 v44, v239, v44
v_mul_f32_e32 v45, v239, v45
v_mul_f32_e32 v46, v239, v46
v_mul_f32_e32 v47, v239, v47
v_mul_f32_e32 v48, v238, v48
v_mul_f32_e32 v49, v238, v49
v_mul_f32_e32 v50, v238, v50
v_mul_f32_e32 v51, v238, v51
v_mul_f32_e32 v52, v238, v52
v_mul_f32_e32 v53, v238, v53
v_mul_f32_e32 v54, v238, v54
v_mul_f32_e32 v55, v238, v55
v_mul_f32_e32 v56, v239, v56
v_mul_f32_e32 v57, v239, v57
v_mul_f32_e32 v58, v239, v58
v_mul_f32_e32 v59, v239, v59
v_mul_f32_e32 v60, v239, v60
v_mul_f32_e32 v61, v239, v61
v_mul_f32_e32 v62, v239, v62
v_mul_f32_e32 v63, v239, v63
s_nop 1
s_branch .Lk_abb4
.Lk_ara5:
v_max_f32_e32 v236, v236, v167
v_max_f32_e32 v237, v237, v175
v_mov_b32_e32 v246, v236
v_mov_b32_e32 v247, v237
s_nop 0
v_permlane16_swap_b32_e32 v236, v246
v_permlane16_swap_b32_e32 v237, v247
v_max_f32_e32 v236, v236, v246
v_max_f32_e32 v237, v237, v247
v_mov_b32_e32 v246, v236
v_mov_b32_e32 v247, v237
s_nop 0
v_permlane32_swap_b32_e32 v236, v246
v_permlane32_swap_b32_e32 v237, v247
v_max_f32_e32 v236, v236, v246
v_max_f32_e32 v237, v237, v247
v_max_f32_e32 v236, 0, v236
v_max_f32_e32 v237, 0, v237
v_exp_f32_e64 v238, -v236
v_exp_f32_e64 v239, -v237
v_sub_f32_e32 v192, v192, v236
v_sub_f32_e32 v196, v196, v237
v_sub_f32_e32 v193, v193, v236
v_sub_f32_e32 v197, v197, v237
v_sub_f32_e32 v194, v194, v236
v_sub_f32_e32 v198, v198, v237
v_sub_f32_e32 v195, v195, v236
v_sub_f32_e32 v199, v199, v237
v_sub_f32_e32 v144, v144, v236
v_sub_f32_e32 v152, v152, v237
v_sub_f32_e32 v145, v145, v236
v_sub_f32_e32 v153, v153, v237
v_sub_f32_e32 v146, v146, v236
v_sub_f32_e32 v154, v154, v237
v_sub_f32_e32 v147, v147, v236
v_sub_f32_e32 v155, v155, v237
v_sub_f32_e32 v148, v148, v236
v_sub_f32_e32 v156, v156, v237
v_sub_f32_e32 v149, v149, v236
v_sub_f32_e32 v157, v157, v237
v_sub_f32_e32 v150, v150, v236
v_sub_f32_e32 v158, v158, v237
v_sub_f32_e32 v151, v151, v236
v_sub_f32_e32 v159, v159, v237
v_sub_f32_e32 v160, v160, v236
v_sub_f32_e32 v168, v168, v237
v_sub_f32_e32 v161, v161, v236
v_sub_f32_e32 v169, v169, v237
v_sub_f32_e32 v162, v162, v236
v_sub_f32_e32 v170, v170, v237
v_sub_f32_e32 v163, v163, v236
v_sub_f32_e32 v171, v171, v237
v_sub_f32_e32 v164, v164, v236
v_sub_f32_e32 v172, v172, v237
v_sub_f32_e32 v165, v165, v236
v_sub_f32_e32 v173, v173, v237
v_sub_f32_e32 v166, v166, v236
v_sub_f32_e32 v174, v174, v237
v_sub_f32_e32 v167, v167, v236
v_sub_f32_e32 v175, v175, v237
v_mul_f32_e32 v242, v238, v242
v_mul_f32_e32 v243, v239, v243
s_nop 1
s_branch .Lk_aba5
.Lk_arb5:
s_nop 7
v_mul_f32_e32 v0, v238, v0
v_mul_f32_e32 v1, v238, v1
v_mul_f32_e32 v2, v238, v2
v_mul_f32_e32 v3, v238, v3
v_mul_f32_e32 v4, v238, v4
v_mul_f32_e32 v5, v238, v5
v_mul_f32_e32 v6, v238, v6
v_mul_f32_e32 v7, v238, v7
v_mul_f32_e32 v8, v239, v8
v_mul_f32_e32 v9, v239, v9
v_mul_f32_e32 v10, v239, v10
v_mul_f32_e32 v11, v239, v11
v_mul_f32_e32 v12, v239, v12
v_mul_f32_e32 v13, v239, v13
v_mul_f32_e32 v14, v239, v14
v_mul_f32_e32 v15, v239, v15
v_mul_f32_e32 v16, v238, v16
v_mul_f32_e32 v17, v238, v17
v_mul_f32_e32 v18, v238, v18
v_mul_f32_e32 v19, v238, v19
v_mul_f32_e32 v20, v238, v20
v_mul_f32_e32 v21, v238, v21
v_mul_f32_e32 v22, v238, v22
v_mul_f32_e32 v23, v238, v23
v_mul_f32_e32 v24, v239, v24
v_mul_f32_e32 v25, v239, v25
v_mul_f32_e32 v26, v239, v26
v_mul_f32_e32 v27, v239, v27
v_mul_f32_e32 v28, v239, v28
v_mul_f32_e32 v29, v239, v29
)"
R"(
v_mul_f32_e32 v30, v239, v30
v_mul_f32_e32 v31, v239, v31
v_mul_f32_e32 v32, v238, v32
v_mul_f32_e32 v33, v238, v33
v_mul_f32_e32 v34, v238, v34
v_mul_f32_e32 v35, v238, v35
v_mul_f32_e32 v36, v238, v36
v_mul_f32_e32 v37, v238, v37
v_mul_f32_e32 v38, v238, v38
v_mul_f32_e32 v39, v238, v39
v_mul_f32_e32 v40, v239, v40
v_mul_f32_e32 v41, v239, v41
v_mul_f32_e32 v42, v239, v42
v_mul_f32_e32 v43, v239, v43
v_mul_f32_e32 v44, v239, v44
v_mul_f32_e32 v45, v239, v45
v_mul_f32_e32 v46, v239, v46
v_mul_f32_e32 v47, v239, v47
v_mul_f32_e32 v48, v238, v48
v_mul_f32_e32 v49, v238, v49
v_mul_f32_e32 v50, v238, v50
v_mul_f32_e32 v51, v238, v51
v_mul_f32_e32 v52, v238, v52
v_mul_f32_e32 v53, v238, v53
v_mul_f32_e32 v54, v238, v54
v_mul_f32_e32 v55, v238, v55
v_mul_f32_e32 v56, v239, v56
v_mul_f32_e32 v57, v239, v57
v_mul_f32_e32 v58, v239, v58
v_mul_f32_e32 v59, v239, v59
v_mul_f32_e32 v60, v239, v60
v_mul_f32_e32 v61, v239, v61
v_mul_f32_e32 v62, v239, v62
v_mul_f32_e32 v63, v239, v63
s_nop 1
s_branch .Lk_abb5
.Lk_ara6:
v_max_f32_e32 v236, v236, v151
v_max_f32_e32 v237, v237, v159
v_mov_b32_e32 v246, v236
v_mov_b32_e32 v247, v237
s_nop 0
v_permlane16_swap_b32_e32 v236, v246
v_permlane16_swap_b32_e32 v237, v247
v_max_f32_e32 v236, v236, v246
v_max_f32_e32 v237, v237, v247
v_mov_b32_e32 v246, v236
v_mov_b32_e32 v247, v237
s_nop 0
v_permlane32_swap_b32_e32 v236, v246
v_permlane32_swap_b32_e32 v237, v247
v_max_f32_e32 v236, v236, v246
v_max_f32_e32 v237, v237, v247
v_max_f32_e32 v236, 0, v236
v_max_f32_e32 v237, 0, v237
v_exp_f32_e64 v238, -v236
v_exp_f32_e64 v239, -v237
v_sub_f32_e32 v192, v192, v236
v_sub_f32_e32 v196, v196, v237
v_sub_f32_e32 v193, v193, v236
v_sub_f32_e32 v197, v197, v237
v_sub_f32_e32 v194, v194, v236
v_sub_f32_e32 v198, v198, v237
v_sub_f32_e32 v195, v195, v236
v_sub_f32_e32 v199, v199, v237
v_sub_f32_e32 v128, v128, v236
v_sub_f32_e32 v136, v136, v237
v_sub_f32_e32 v129, v129, v236
v_sub_f32_e32 v137, v137, v237
v_sub_f32_e32 v130, v130, v236
v_sub_f32_e32 v138, v138, v237
v_sub_f32_e32 v131, v131, v236
v_sub_f32_e32 v139, v139, v237
v_sub_f32_e32 v132, v132, v236
v_sub_f32_e32 v140, v140, v237
v_sub_f32_e32 v133, v133, v236
v_sub_f32_e32 v141, v141, v237
v_sub_f32_e32 v134, v134, v236
v_sub_f32_e32 v142, v142, v237
v_sub_f32_e32 v135, v135, v236
v_sub_f32_e32 v143, v143, v237
v_sub_f32_e32 v144, v144, v236
v_sub_f32_e32 v152, v152, v237
v_sub_f32_e32 v145, v145, v236
v_sub_f32_e32 v153, v153, v237
v_sub_f32_e32 v146, v146, v236
v_sub_f32_e32 v154, v154, v237
v_sub_f32_e32 v147, v147, v236
v_sub_f32_e32 v155, v155, v237
v_sub_f32_e32 v148, v148, v236
v_sub_f32_e32 v156, v156, v237
v_sub_f32_e32 v149, v149, v236
v_sub_f32_e32 v157, v157, v237
v_sub_f32_e32 v150, v150, v236
v_sub_f32_e32 v158, v158, v237
v_sub_f32_e32 v151, v151, v236
v_sub_f32_e32 v159, v159, v237
v_mul_f32_e32 v242, v238, v242
v_mul_f32_e32 v243, v239, v243
s_nop 1
s_branch .Lk_aba6
.Lk_arb6:
s_nop 7
v_mul_f32_e32 v0, v238, v0
v_mul_f32_e32 v1, v238, v1
v_mul_f32_e32 v2, v238, v2
v_mul_f32_e32 v3, v238, v3
v_mul_f32_e32 v4, v238, v4
v_mul_f32_e32 v5, v238, v5
v_mul_f32_e32 v6, v238, v6
v_mul_f32_e32 v7, v238, v7
v_mul_f32_e32 v8, v239, v8
v_mul_f32_e32 v9, v239, v9
v_mul_f32_e32 v10, v239, v10
v_mul_f32_e32 v11, v239, v11
v_mul_f32_e32 v12, v239, v12
v_mul_f32_e32 v13, v239, v13
v_mul_f32_e32 v14, v239, v14
v_mul_f32_e32 v15, v239, v15
v_mul_f32_e32 v16, v238, v16
v_mul_f32_e32 v17, v238, v17
v_mul_f32_e32 v18, v238, v18
v_mul_f32_e32 v19, v238, v19
v_mul_f32_e32 v20, v238, v20
v_mul_f32_e32 v21, v238, v21
v_mul_f32_e32 v22, v238, v22
v_mul_f32_e32 v23, v238, v23
v_mul_f32_e32 v24, v239, v24
v_mul_f32_e32 v25, v239, v25
v_mul_f32_e32 v26, v239, v26
v_mul_f32_e32 v27, v239, v27
v_mul_f32_e32 v28, v239, v28
v_mul_f32_e32 v29, v239, v29
v_mul_f32_e32 v30, v239, v30
v_mul_f32_e32 v31, v239, v31
v_mul_f32_e32 v32, v238, v32
v_mul_f32_e32 v33, v238, v33
v_mul_f32_e32 v34, v238, v34
v_mul_f32_e32 v35, v238, v35
v_mul_f32_e32 v36, v238, v36
v_mul_f32_e32 v37, v238, v37
v_mul_f32_e32 v38, v238, v38
v_mul_f32_e32 v39, v238, v39
v_mul_f32_e32 v40, v239, v40
v_mul_f32_e32 v41, v239, v41
v_mul_f32_e32 v42, v239, v42
v_mul_f32_e32 v43, v239, v43
v_mul_f32_e32 v44, v239, v44
v_mul_f32_e32 v45, v239, v45
v_mul_f32_e32 v46, v239, v46
v_mul_f32_e32 v47, v239, v47
v_mul_f32_e32 v48, v238, v48
v_mul_f32_e32 v49, v238, v49
v_mul_f32_e32 v50, v238, v50
v_mul_f32_e32 v51, v238, v51
v_mul_f32_e32 v52, v238, v52
v_mul_f32_e32 v53, v238, v53
v_mul_f32_e32 v54, v238, v54
v_mul_f32_e32 v55, v238, v55
v_mul_f32_e32 v56, v239, v56
v_mul_f32_e32 v57, v239, v57
v_mul_f32_e32 v58, v239, v58
v_mul_f32_e32 v59, v239, v59
v_mul_f32_e32 v60, v239, v60
v_mul_f32_e32 v61, v239, v61
v_mul_f32_e32 v62, v239, v62
v_mul_f32_e32 v63, v239, v63
s_nop 1
s_branch .Lk_abb6
.Lk_ara7:
v_max_f32_e32 v236, v236, v135
v_max_f32_e32 v237, v237, v143
v_mov_b32_e32 v246, v236
v_mov_b32_e32 v247, v237
s_nop 0
v_permlane16_swap_b32_e32 v236, v246
v_permlane16_swap_b32_e32 v237, v247
v_max_f32_e32 v236, v236, v246
v_max_f32_e32 v237, v237, v247
v_mov_b32_e32 v246, v236
v_mov_b32_e32 v247, v237
s_nop 0
v_permlane32_swap_b32_e32 v236, v246
v_permlane32_swap_b32_e32 v237, v247
v_max_f32_e32 v236, v236, v246
v_max_f32_e32 v237, v237, v247
v_max_f32_e32 v236, 0, v236
v_max_f32_e32 v237, 0, v237
v_exp_f32_e64 v238, -v236
v_exp_f32_e64 v239, -v237
v_sub_f32_e32 v192, v192, v236
v_sub_f32_e32 v196, v196, v237
v_sub_f32_e32 v193, v193, v236
v_sub_f32_e32 v197, v197, v237
v_sub_f32_e32 v194, v194, v236
v_sub_f32_e32 v198, v198, v237
v_sub_f32_e32 v195, v195, v236
v_sub_f32_e32 v199, v199, v237
v_sub_f32_e32 v160, v160, v236
v_sub_f32_e32 v168, v168, v237
)"
R"(
v_sub_f32_e32 v161, v161, v236
v_sub_f32_e32 v169, v169, v237
v_sub_f32_e32 v162, v162, v236
v_sub_f32_e32 v170, v170, v237
v_sub_f32_e32 v163, v163, v236
v_sub_f32_e32 v171, v171, v237
v_sub_f32_e32 v164, v164, v236
v_sub_f32_e32 v172, v172, v237
v_sub_f32_e32 v165, v165, v236
v_sub_f32_e32 v173, v173, v237
v_sub_f32_e32 v166, v166, v236
v_sub_f32_e32 v174, v174, v237
v_sub_f32_e32 v167, v167, v236
v_sub_f32_e32 v175, v175, v237
v_sub_f32_e32 v128, v128, v236
v_sub_f32_e32 v136, v136, v237
v_sub_f32_e32 v129, v129, v236
v_sub_f32_e32 v137, v137, v237
v_sub_f32_e32 v130, v130, v236
v_sub_f32_e32 v138, v138, v237
v_sub_f32_e32 v131, v131, v236
v_sub_f32_e32 v139, v139, v237
v_sub_f32_e32 v132, v132, v236
v_sub_f32_e32 v140, v140, v237
v_sub_f32_e32 v133, v133, v236
v_sub_f32_e32 v141, v141, v237
v_sub_f32_e32 v134, v134, v236
v_sub_f32_e32 v142, v142, v237
v_sub_f32_e32 v135, v135, v236
v_sub_f32_e32 v143, v143, v237
v_mul_f32_e32 v242, v238, v242
v_mul_f32_e32 v243, v239, v243
s_nop 1
s_branch .Lk_aba7
.Lk_arb7:
s_nop 7
v_mul_f32_e32 v0, v238, v0
v_mul_f32_e32 v1, v238, v1
v_mul_f32_e32 v2, v238, v2
v_mul_f32_e32 v3, v238, v3
v_mul_f32_e32 v4, v238, v4
v_mul_f32_e32 v5, v238, v5
v_mul_f32_e32 v6, v238, v6
v_mul_f32_e32 v7, v238, v7
v_mul_f32_e32 v8, v239, v8
v_mul_f32_e32 v9, v239, v9
v_mul_f32_e32 v10, v239, v10
v_mul_f32_e32 v11, v239, v11
v_mul_f32_e32 v12, v239, v12
v_mul_f32_e32 v13, v239, v13
v_mul_f32_e32 v14, v239, v14
v_mul_f32_e32 v15, v239, v15
v_mul_f32_e32 v16, v238, v16
v_mul_f32_e32 v17, v238, v17
v_mul_f32_e32 v18, v238, v18
v_mul_f32_e32 v19, v238, v19
v_mul_f32_e32 v20, v238, v20
v_mul_f32_e32 v21, v238, v21
v_mul_f32_e32 v22, v238, v22
v_mul_f32_e32 v23, v238, v23
v_mul_f32_e32 v24, v239, v24
v_mul_f32_e32 v25, v239, v25
v_mul_f32_e32 v26, v239, v26
v_mul_f32_e32 v27, v239, v27
v_mul_f32_e32 v28, v239, v28
v_mul_f32_e32 v29, v239, v29
v_mul_f32_e32 v30, v239, v30
v_mul_f32_e32 v31, v239, v31
v_mul_f32_e32 v32, v238, v32
v_mul_f32_e32 v33, v238, v33
v_mul_f32_e32 v34, v238, v34
v_mul_f32_e32 v35, v238, v35
v_mul_f32_e32 v36, v238, v36
v_mul_f32_e32 v37, v238, v37
v_mul_f32_e32 v38, v238, v38
v_mul_f32_e32 v39, v238, v39
v_mul_f32_e32 v40, v239, v40
v_mul_f32_e32 v41, v239, v41
v_mul_f32_e32 v42, v239, v42
v_mul_f32_e32 v43, v239, v43
v_mul_f32_e32 v44, v239, v44
v_mul_f32_e32 v45, v239, v45
v_mul_f32_e32 v46, v239, v46
v_mul_f32_e32 v47, v239, v47
v_mul_f32_e32 v48, v238, v48
v_mul_f32_e32 v49, v238, v49
v_mul_f32_e32 v50, v238, v50
v_mul_f32_e32 v51, v238, v51
v_mul_f32_e32 v52, v238, v52
v_mul_f32_e32 v53, v238, v53
v_mul_f32_e32 v54, v238, v54
v_mul_f32_e32 v55, v238, v55
v_mul_f32_e32 v56, v239, v56
v_mul_f32_e32 v57, v239, v57
v_mul_f32_e32 v58, v239, v58
v_mul_f32_e32 v59, v239, v59
v_mul_f32_e32 v60, v239, v60
v_mul_f32_e32 v61, v239, v61
v_mul_f32_e32 v62, v239, v62
v_mul_f32_e32 v63, v239, v63
s_nop 1
s_branch .Lk_abb7
.Lk_ara9:
v_max_f32_e32 v236, v236, v151
v_max_f32_e32 v237, v237, v159
v_mov_b32_e32 v246, v236
v_mov_b32_e32 v247, v237
s_nop 0
v_permlane16_swap_b32_e32 v236, v246
v_permlane16_swap_b32_e32 v237, v247
v_max_f32_e32 v236, v236, v246
v_max_f32_e32 v237, v237, v247
v_mov_b32_e32 v246, v236
v_mov_b32_e32 v247, v237
s_nop 0
v_permlane32_swap_b32_e32 v236, v246
v_permlane32_swap_b32_e32 v237, v247
v_max_f32_e32 v236, v236, v246
v_max_f32_e32 v237, v237, v247
v_max_f32_e32 v236, 0, v236
v_max_f32_e32 v237, 0, v237
v_exp_f32_e64 v238, -v236
v_exp_f32_e64 v239, -v237
v_sub_f32_e32 v192, v192, v236
v_sub_f32_e32 v196, v196, v237
v_sub_f32_e32 v193, v193, v236
v_sub_f32_e32 v197, v197, v237
v_sub_f32_e32 v194, v194, v236
v_sub_f32_e32 v198, v198, v237
v_sub_f32_e32 v195, v195, v236
v_sub_f32_e32 v199, v199, v237
v_sub_f32_e32 v128, v128, v236
v_sub_f32_e32 v136, v136, v237
v_sub_f32_e32 v129, v129, v236
v_sub_f32_e32 v137, v137, v237
v_sub_f32_e32 v130, v130, v236
v_sub_f32_e32 v138, v138, v237
v_sub_f32_e32 v131, v131, v236
v_sub_f32_e32 v139, v139, v237
v_sub_f32_e32 v132, v132, v236
v_sub_f32_e32 v140, v140, v237
v_sub_f32_e32 v133, v133, v236
v_sub_f32_e32 v141, v141, v237
v_sub_f32_e32 v134, v134, v236
v_sub_f32_e32 v142, v142, v237
v_sub_f32_e32 v135, v135, v236
v_sub_f32_e32 v143, v143, v237
v_sub_f32_e32 v144, v144, v236
v_sub_f32_e32 v152, v152, v237
v_sub_f32_e32 v145, v145, v236
v_sub_f32_e32 v153, v153, v237
v_sub_f32_e32 v146, v146, v236
v_sub_f32_e32 v154, v154, v237
v_sub_f32_e32 v147, v147, v236
v_sub_f32_e32 v155, v155, v237
v_sub_f32_e32 v148, v148, v236
v_sub_f32_e32 v156, v156, v237
v_sub_f32_e32 v149, v149, v236
v_sub_f32_e32 v157, v157, v237
v_sub_f32_e32 v150, v150, v236
v_sub_f32_e32 v158, v158, v237
v_sub_f32_e32 v151, v151, v236
v_sub_f32_e32 v159, v159, v237
v_mul_f32_e32 v244, v238, v244
v_mul_f32_e32 v245, v239, v245
s_nop 1
s_branch .Lk_aba9
.Lk_arb9:
s_nop 7
v_mul_f32_e32 v0, v238, v0
v_mul_f32_e32 v1, v238, v1
v_mul_f32_e32 v2, v238, v2
v_mul_f32_e32 v3, v238, v3
v_mul_f32_e32 v4, v238, v4
v_mul_f32_e32 v5, v238, v5
v_mul_f32_e32 v6, v238, v6
v_mul_f32_e32 v7, v238, v7
v_mul_f32_e32 v8, v239, v8
v_mul_f32_e32 v9, v239, v9
v_mul_f32_e32 v10, v239, v10
v_mul_f32_e32 v11, v239, v11
v_mul_f32_e32 v12, v239, v12
v_mul_f32_e32 v13, v239, v13
v_mul_f32_e32 v14, v239, v14
v_mul_f32_e32 v15, v239, v15
v_mul_f32_e32 v16, v238, v16
v_mul_f32_e32 v17, v238, v17
v_mul_f32_e32 v18, v238, v18
v_mul_f32_e32 v19, v238, v19
v_mul_f32_e32 v20, v238, v20
v_mul_f32_e32 v21, v238, v21
v_mul_f32_e32 v22, v238, v22
v_mul_f32_e32 v23, v238, v23
v_mul_f32_e32 v24, v239, v24
v_mul_f32_e32 v25, v239, v25
v_mul_f32_e32 v26, v239, v26
v_mul_f32_e32 v27, v239, v27
v_mul_f32_e32 v28, v239, v28
v_mul_f32_e32 v29, v239, v29
v_mul_f32_e32 v30, v239, v30
)"
R"(
v_mul_f32_e32 v31, v239, v31
v_mul_f32_e32 v32, v238, v32
v_mul_f32_e32 v33, v238, v33
v_mul_f32_e32 v34, v238, v34
v_mul_f32_e32 v35, v238, v35
v_mul_f32_e32 v36, v238, v36
v_mul_f32_e32 v37, v238, v37
v_mul_f32_e32 v38, v238, v38
v_mul_f32_e32 v39, v238, v39
v_mul_f32_e32 v40, v239, v40
v_mul_f32_e32 v41, v239, v41
v_mul_f32_e32 v42, v239, v42
v_mul_f32_e32 v43, v239, v43
v_mul_f32_e32 v44, v239, v44
v_mul_f32_e32 v45, v239, v45
v_mul_f32_e32 v46, v239, v46
v_mul_f32_e32 v47, v239, v47
v_mul_f32_e32 v48, v238, v48
v_mul_f32_e32 v49, v238, v49
v_mul_f32_e32 v50, v238, v50
v_mul_f32_e32 v51, v238, v51
v_mul_f32_e32 v52, v238, v52
v_mul_f32_e32 v53, v238, v53
v_mul_f32_e32 v54, v238, v54
v_mul_f32_e32 v55, v238, v55
v_mul_f32_e32 v56, v239, v56
v_mul_f32_e32 v57, v239, v57
v_mul_f32_e32 v58, v239, v58
v_mul_f32_e32 v59, v239, v59
v_mul_f32_e32 v60, v239, v60
v_mul_f32_e32 v61, v239, v61
v_mul_f32_e32 v62, v239, v62
v_mul_f32_e32 v63, v239, v63
s_nop 1
s_branch .Lk_abb9
.Lk_ara10:
v_max_f32_e32 v236, v236, v135
v_max_f32_e32 v237, v237, v143
v_mov_b32_e32 v246, v236
v_mov_b32_e32 v247, v237
s_nop 0
v_permlane16_swap_b32_e32 v236, v246
v_permlane16_swap_b32_e32 v237, v247
v_max_f32_e32 v236, v236, v246
v_max_f32_e32 v237, v237, v247
v_mov_b32_e32 v246, v236
v_mov_b32_e32 v247, v237
s_nop 0
v_permlane32_swap_b32_e32 v236, v246
v_permlane32_swap_b32_e32 v237, v247
v_max_f32_e32 v236, v236, v246
v_max_f32_e32 v237, v237, v247
v_max_f32_e32 v236, 0, v236
v_max_f32_e32 v237, 0, v237
v_exp_f32_e64 v238, -v236
v_exp_f32_e64 v239, -v237
v_sub_f32_e32 v192, v192, v236
v_sub_f32_e32 v196, v196, v237
v_sub_f32_e32 v193, v193, v236
v_sub_f32_e32 v197, v197, v237
v_sub_f32_e32 v194, v194, v236
v_sub_f32_e32 v198, v198, v237
v_sub_f32_e32 v195, v195, v236
v_sub_f32_e32 v199, v199, v237
v_sub_f32_e32 v160, v160, v236
v_sub_f32_e32 v168, v168, v237
v_sub_f32_e32 v161, v161, v236
v_sub_f32_e32 v169, v169, v237
v_sub_f32_e32 v162, v162, v236
v_sub_f32_e32 v170, v170, v237
v_sub_f32_e32 v163, v163, v236
v_sub_f32_e32 v171, v171, v237
v_sub_f32_e32 v164, v164, v236
v_sub_f32_e32 v172, v172, v237
v_sub_f32_e32 v165, v165, v236
v_sub_f32_e32 v173, v173, v237
v_sub_f32_e32 v166, v166, v236
v_sub_f32_e32 v174, v174, v237
v_sub_f32_e32 v167, v167, v236
v_sub_f32_e32 v175, v175, v237
v_sub_f32_e32 v128, v128, v236
v_sub_f32_e32 v136, v136, v237
v_sub_f32_e32 v129, v129, v236
v_sub_f32_e32 v137, v137, v237
v_sub_f32_e32 v130, v130, v236
v_sub_f32_e32 v138, v138, v237
v_sub_f32_e32 v131, v131, v236
v_sub_f32_e32 v139, v139, v237
v_sub_f32_e32 v132, v132, v236
v_sub_f32_e32 v140, v140, v237
v_sub_f32_e32 v133, v133, v236
v_sub_f32_e32 v141, v141, v237
v_sub_f32_e32 v134, v134, v236
v_sub_f32_e32 v142, v142, v237
v_sub_f32_e32 v135, v135, v236
v_sub_f32_e32 v143, v143, v237
v_mul_f32_e32 v244, v238, v244
v_mul_f32_e32 v245, v239, v245
s_nop 1
s_branch .Lk_aba10
.Lk_arb10:
s_nop 7
v_mul_f32_e32 v0, v238, v0
v_mul_f32_e32 v1, v238, v1
v_mul_f32_e32 v2, v238, v2
v_mul_f32_e32 v3, v238, v3
v_mul_f32_e32 v4, v238, v4
v_mul_f32_e32 v5, v238, v5
v_mul_f32_e32 v6, v238, v6
v_mul_f32_e32 v7, v238, v7
v_mul_f32_e32 v8, v239, v8
v_mul_f32_e32 v9, v239, v9
v_mul_f32_e32 v10, v239, v10
v_mul_f32_e32 v11, v239, v11
v_mul_f32_e32 v12, v239, v12
v_mul_f32_e32 v13, v239, v13
v_mul_f32_e32 v14, v239, v14
v_mul_f32_e32 v15, v239, v15
v_mul_f32_e32 v16, v238, v16
v_mul_f32_e32 v17, v238, v17
v_mul_f32_e32 v18, v238, v18
v_mul_f32_e32 v19, v238, v19
v_mul_f32_e32 v20, v238, v20
v_mul_f32_e32 v21, v238, v21
v_mul_f32_e32 v22, v238, v22
v_mul_f32_e32 v23, v238, v23
v_mul_f32_e32 v24, v239, v24
v_mul_f32_e32 v25, v239, v25
v_mul_f32_e32 v26, v239, v26
v_mul_f32_e32 v27, v239, v27
v_mul_f32_e32 v28, v239, v28
v_mul_f32_e32 v29, v239, v29
v_mul_f32_e32 v30, v239, v30
v_mul_f32_e32 v31, v239, v31
v_mul_f32_e32 v32, v238, v32
v_mul_f32_e32 v33, v238, v33
v_mul_f32_e32 v34, v238, v34
v_mul_f32_e32 v35, v238, v35
v_mul_f32_e32 v36, v238, v36
v_mul_f32_e32 v37, v238, v37
v_mul_f32_e32 v38, v238, v38
v_mul_f32_e32 v39, v238, v39
v_mul_f32_e32 v40, v239, v40
v_mul_f32_e32 v41, v239, v41
v_mul_f32_e32 v42, v239, v42
v_mul_f32_e32 v43, v239, v43
v_mul_f32_e32 v44, v239, v44
v_mul_f32_e32 v45, v239, v45
v_mul_f32_e32 v46, v239, v46
v_mul_f32_e32 v47, v239, v47
v_mul_f32_e32 v48, v238, v48
v_mul_f32_e32 v49, v238, v49
v_mul_f32_e32 v50, v238, v50
v_mul_f32_e32 v51, v238, v51
v_mul_f32_e32 v52, v238, v52
v_mul_f32_e32 v53, v238, v53
v_mul_f32_e32 v54, v238, v54
v_mul_f32_e32 v55, v238, v55
v_mul_f32_e32 v56, v239, v56
v_mul_f32_e32 v57, v239, v57
v_mul_f32_e32 v58, v239, v58
v_mul_f32_e32 v59, v239, v59
v_mul_f32_e32 v60, v239, v60
v_mul_f32_e32 v61, v239, v61
v_mul_f32_e32 v62, v239, v62
v_mul_f32_e32 v63, v239, v63
s_nop 1
s_branch .Lk_abb10
.Lk_ara11:
v_max_f32_e32 v236, v236, v167
v_max_f32_e32 v237, v237, v175
v_mov_b32_e32 v246, v236
v_mov_b32_e32 v247, v237
s_nop 0
v_permlane16_swap_b32_e32 v236, v246
v_permlane16_swap_b32_e32 v237, v247
v_max_f32_e32 v236, v236, v246
v_max_f32_e32 v237, v237, v247
v_mov_b32_e32 v246, v236
v_mov_b32_e32 v247, v237
s_nop 0
v_permlane32_swap_b32_e32 v236, v246
v_permlane32_swap_b32_e32 v237, v247
v_max_f32_e32 v236, v236, v246
v_max_f32_e32 v237, v237, v247
v_max_f32_e32 v236, 0, v236
v_max_f32_e32 v237, 0, v237
v_exp_f32_e64 v238, -v236
v_exp_f32_e64 v239, -v237
v_sub_f32_e32 v192, v192, v236
v_sub_f32_e32 v196, v196, v237
v_sub_f32_e32 v193, v193, v236
v_sub_f32_e32 v197, v197, v237
v_sub_f32_e32 v194, v194, v236
v_sub_f32_e32 v198, v198, v237
v_sub_f32_e32 v195, v195, v236
v_sub_f32_e32 v199, v199, v237
v_sub_f32_e32 v144, v144, v236
v_sub_f32_e32 v152, v152, v237
v_sub_f32_e32 v145, v145, v236
)"
R"(
v_sub_f32_e32 v153, v153, v237
v_sub_f32_e32 v146, v146, v236
v_sub_f32_e32 v154, v154, v237
v_sub_f32_e32 v147, v147, v236
v_sub_f32_e32 v155, v155, v237
v_sub_f32_e32 v148, v148, v236
v_sub_f32_e32 v156, v156, v237
v_sub_f32_e32 v149, v149, v236
v_sub_f32_e32 v157, v157, v237
v_sub_f32_e32 v150, v150, v236
v_sub_f32_e32 v158, v158, v237
v_sub_f32_e32 v151, v151, v236
v_sub_f32_e32 v159, v159, v237
v_sub_f32_e32 v160, v160, v236
v_sub_f32_e32 v168, v168, v237
v_sub_f32_e32 v161, v161, v236
v_sub_f32_e32 v169, v169, v237
v_sub_f32_e32 v162, v162, v236
v_sub_f32_e32 v170, v170, v237
v_sub_f32_e32 v163, v163, v236
v_sub_f32_e32 v171, v171, v237
v_sub_f32_e32 v164, v164, v236
v_sub_f32_e32 v172, v172, v237
v_sub_f32_e32 v165, v165, v236
v_sub_f32_e32 v173, v173, v237
v_sub_f32_e32 v166, v166, v236
v_sub_f32_e32 v174, v174, v237
v_sub_f32_e32 v167, v167, v236
v_sub_f32_e32 v175, v175, v237
v_mul_f32_e32 v244, v238, v244
v_mul_f32_e32 v245, v239, v245
s_nop 1
s_branch .Lk_aba11
.Lk_arb11:
s_nop 7
v_mul_f32_e32 v0, v238, v0
v_mul_f32_e32 v1, v238, v1
v_mul_f32_e32 v2, v238, v2
v_mul_f32_e32 v3, v238, v3
v_mul_f32_e32 v4, v238, v4
v_mul_f32_e32 v5, v238, v5
v_mul_f32_e32 v6, v238, v6
v_mul_f32_e32 v7, v238, v7
v_mul_f32_e32 v8, v239, v8
v_mul_f32_e32 v9, v239, v9
v_mul_f32_e32 v10, v239, v10
v_mul_f32_e32 v11, v239, v11
v_mul_f32_e32 v12, v239, v12
v_mul_f32_e32 v13, v239, v13
v_mul_f32_e32 v14, v239, v14
v_mul_f32_e32 v15, v239, v15
v_mul_f32_e32 v16, v238, v16
v_mul_f32_e32 v17, v238, v17
v_mul_f32_e32 v18, v238, v18
v_mul_f32_e32 v19, v238, v19
v_mul_f32_e32 v20, v238, v20
v_mul_f32_e32 v21, v238, v21
v_mul_f32_e32 v22, v238, v22
v_mul_f32_e32 v23, v238, v23
v_mul_f32_e32 v24, v239, v24
v_mul_f32_e32 v25, v239, v25
v_mul_f32_e32 v26, v239, v26
v_mul_f32_e32 v27, v239, v27
v_mul_f32_e32 v28, v239, v28
v_mul_f32_e32 v29, v239, v29
v_mul_f32_e32 v30, v239, v30
v_mul_f32_e32 v31, v239, v31
v_mul_f32_e32 v32, v238, v32
v_mul_f32_e32 v33, v238, v33
v_mul_f32_e32 v34, v238, v34
v_mul_f32_e32 v35, v238, v35
v_mul_f32_e32 v36, v238, v36
v_mul_f32_e32 v37, v238, v37
v_mul_f32_e32 v38, v238, v38
v_mul_f32_e32 v39, v238, v39
v_mul_f32_e32 v40, v239, v40
v_mul_f32_e32 v41, v239, v41
v_mul_f32_e32 v42, v239, v42
v_mul_f32_e32 v43, v239, v43
v_mul_f32_e32 v44, v239, v44
v_mul_f32_e32 v45, v239, v45
v_mul_f32_e32 v46, v239, v46
v_mul_f32_e32 v47, v239, v47
v_mul_f32_e32 v48, v238, v48
v_mul_f32_e32 v49, v238, v49
v_mul_f32_e32 v50, v238, v50
v_mul_f32_e32 v51, v238, v51
v_mul_f32_e32 v52, v238, v52
v_mul_f32_e32 v53, v238, v53
v_mul_f32_e32 v54, v238, v54
v_mul_f32_e32 v55, v238, v55
v_mul_f32_e32 v56, v239, v56
v_mul_f32_e32 v57, v239, v57
v_mul_f32_e32 v58, v239, v58
v_mul_f32_e32 v59, v239, v59
v_mul_f32_e32 v60, v239, v60
v_mul_f32_e32 v61, v239, v61
v_mul_f32_e32 v62, v239, v62
v_mul_f32_e32 v63, v239, v63
s_nop 1
s_branch .Lk_abb11
.Lk_ara12:
v_max_f32_e32 v236, v236, v151
v_max_f32_e32 v237, v237, v159
v_mov_b32_e32 v246, v236
v_mov_b32_e32 v247, v237
s_nop 0
v_permlane16_swap_b32_e32 v236, v246
v_permlane16_swap_b32_e32 v237, v247
v_max_f32_e32 v236, v236, v246
v_max_f32_e32 v237, v237, v247
v_mov_b32_e32 v246, v236
v_mov_b32_e32 v247, v237
s_nop 0
v_permlane32_swap_b32_e32 v236, v246
v_permlane32_swap_b32_e32 v237, v247
v_max_f32_e32 v236, v236, v246
v_max_f32_e32 v237, v237, v247
v_max_f32_e32 v236, 0, v236
v_max_f32_e32 v237, 0, v237
v_exp_f32_e64 v238, -v236
v_exp_f32_e64 v239, -v237
v_sub_f32_e32 v192, v192, v236
v_sub_f32_e32 v196, v196, v237
v_sub_f32_e32 v193, v193, v236
v_sub_f32_e32 v197, v197, v237
v_sub_f32_e32 v194, v194, v236
v_sub_f32_e32 v198, v198, v237
v_sub_f32_e32 v195, v195, v236
v_sub_f32_e32 v199, v199, v237
v_sub_f32_e32 v128, v128, v236
v_sub_f32_e32 v136, v136, v237
v_sub_f32_e32 v129, v129, v236
v_sub_f32_e32 v137, v137, v237
v_sub_f32_e32 v130, v130, v236
v_sub_f32_e32 v138, v138, v237
v_sub_f32_e32 v131, v131, v236
v_sub_f32_e32 v139, v139, v237
v_sub_f32_e32 v132, v132, v236
v_sub_f32_e32 v140, v140, v237
v_sub_f32_e32 v133, v133, v236
v_sub_f32_e32 v141, v141, v237
v_sub_f32_e32 v134, v134, v236
v_sub_f32_e32 v142, v142, v237
v_sub_f32_e32 v135, v135, v236
v_sub_f32_e32 v143, v143, v237
v_sub_f32_e32 v144, v144, v236
v_sub_f32_e32 v152, v152, v237
v_sub_f32_e32 v145, v145, v236
v_sub_f32_e32 v153, v153, v237
v_sub_f32_e32 v146, v146, v236
v_sub_f32_e32 v154, v154, v237
v_sub_f32_e32 v147, v147, v236
v_sub_f32_e32 v155, v155, v237
v_sub_f32_e32 v148, v148, v236
v_sub_f32_e32 v156, v156, v237
v_sub_f32_e32 v149, v149, v236
v_sub_f32_e32 v157, v157, v237
v_sub_f32_e32 v150, v150, v236
v_sub_f32_e32 v158, v158, v237
v_sub_f32_e32 v151, v151, v236
v_sub_f32_e32 v159, v159, v237
v_mul_f32_e32 v244, v238, v244
v_mul_f32_e32 v245, v239, v245
s_nop 1
s_branch .Lk_aba12
.Lk_arb12:
s_nop 7
v_mul_f32_e32 v0, v238, v0
v_mul_f32_e32 v1, v238, v1
v_mul_f32_e32 v2, v238, v2
v_mul_f32_e32 v3, v238, v3
v_mul_f32_e32 v4, v238, v4
v_mul_f32_e32 v5, v238, v5
v_mul_f32_e32 v6, v238, v6
v_mul_f32_e32 v7, v238, v7
v_mul_f32_e32 v8, v239, v8
v_mul_f32_e32 v9, v239, v9
v_mul_f32_e32 v10, v239, v10
v_mul_f32_e32 v11, v239, v11
v_mul_f32_e32 v12, v239, v12
v_mul_f32_e32 v13, v239, v13
v_mul_f32_e32 v14, v239, v14
v_mul_f32_e32 v15, v239, v15
v_mul_f32_e32 v16, v238, v16
v_mul_f32_e32 v17, v238, v17
v_mul_f32_e32 v18, v238, v18
v_mul_f32_e32 v19, v238, v19
v_mul_f32_e32 v20, v238, v20
v_mul_f32_e32 v21, v238, v21
v_mul_f32_e32 v22, v238, v22
v_mul_f32_e32 v23, v238, v23
v_mul_f32_e32 v24, v239, v24
v_mul_f32_e32 v25, v239, v25
v_mul_f32_e32 v26, v239, v26
v_mul_f32_e32 v27, v239, v27
v_mul_f32_e32 v28, v239, v28
v_mul_f32_e32 v29, v239, v29
v_mul_f32_e32 v30, v239, v30
v_mul_f32_e32 v31, v239, v31
)"
R"(
v_mul_f32_e32 v32, v238, v32
v_mul_f32_e32 v33, v238, v33
v_mul_f32_e32 v34, v238, v34
v_mul_f32_e32 v35, v238, v35
v_mul_f32_e32 v36, v238, v36
v_mul_f32_e32 v37, v238, v37
v_mul_f32_e32 v38, v238, v38
v_mul_f32_e32 v39, v238, v39
v_mul_f32_e32 v40, v239, v40
v_mul_f32_e32 v41, v239, v41
v_mul_f32_e32 v42, v239, v42
v_mul_f32_e32 v43, v239, v43
v_mul_f32_e32 v44, v239, v44
v_mul_f32_e32 v45, v239, v45
v_mul_f32_e32 v46, v239, v46
v_mul_f32_e32 v47, v239, v47
v_mul_f32_e32 v48, v238, v48
v_mul_f32_e32 v49, v238, v49
v_mul_f32_e32 v50, v238, v50
v_mul_f32_e32 v51, v238, v51
v_mul_f32_e32 v52, v238, v52
v_mul_f32_e32 v53, v238, v53
v_mul_f32_e32 v54, v238, v54
v_mul_f32_e32 v55, v238, v55
v_mul_f32_e32 v56, v239, v56
v_mul_f32_e32 v57, v239, v57
v_mul_f32_e32 v58, v239, v58
v_mul_f32_e32 v59, v239, v59
v_mul_f32_e32 v60, v239, v60
v_mul_f32_e32 v61, v239, v61
v_mul_f32_e32 v62, v239, v62
v_mul_f32_e32 v63, v239, v63
s_nop 1
s_branch .Lk_abb12
.Lk_ara13:
v_max_f32_e32 v236, v236, v135
v_max_f32_e32 v237, v237, v143
v_mov_b32_e32 v246, v236
v_mov_b32_e32 v247, v237
s_nop 0
v_permlane16_swap_b32_e32 v236, v246
v_permlane16_swap_b32_e32 v237, v247
v_max_f32_e32 v236, v236, v246
v_max_f32_e32 v237, v237, v247
v_mov_b32_e32 v246, v236
v_mov_b32_e32 v247, v237
s_nop 0
v_permlane32_swap_b32_e32 v236, v246
v_permlane32_swap_b32_e32 v237, v247
v_max_f32_e32 v236, v236, v246
v_max_f32_e32 v237, v237, v247
v_max_f32_e32 v236, 0, v236
v_max_f32_e32 v237, 0, v237
v_exp_f32_e64 v238, -v236
v_exp_f32_e64 v239, -v237
v_sub_f32_e32 v192, v192, v236
v_sub_f32_e32 v196, v196, v237
v_sub_f32_e32 v193, v193, v236
v_sub_f32_e32 v197, v197, v237
v_sub_f32_e32 v194, v194, v236
v_sub_f32_e32 v198, v198, v237
v_sub_f32_e32 v195, v195, v236
v_sub_f32_e32 v199, v199, v237
v_sub_f32_e32 v160, v160, v236
v_sub_f32_e32 v168, v168, v237
v_sub_f32_e32 v161, v161, v236
v_sub_f32_e32 v169, v169, v237
v_sub_f32_e32 v162, v162, v236
v_sub_f32_e32 v170, v170, v237
v_sub_f32_e32 v163, v163, v236
v_sub_f32_e32 v171, v171, v237
v_sub_f32_e32 v164, v164, v236
v_sub_f32_e32 v172, v172, v237
v_sub_f32_e32 v165, v165, v236
v_sub_f32_e32 v173, v173, v237
v_sub_f32_e32 v166, v166, v236
v_sub_f32_e32 v174, v174, v237
v_sub_f32_e32 v167, v167, v236
v_sub_f32_e32 v175, v175, v237
v_sub_f32_e32 v128, v128, v236
v_sub_f32_e32 v136, v136, v237
v_sub_f32_e32 v129, v129, v236
v_sub_f32_e32 v137, v137, v237
v_sub_f32_e32 v130, v130, v236
v_sub_f32_e32 v138, v138, v237
v_sub_f32_e32 v131, v131, v236
v_sub_f32_e32 v139, v139, v237
v_sub_f32_e32 v132, v132, v236
v_sub_f32_e32 v140, v140, v237
v_sub_f32_e32 v133, v133, v236
v_sub_f32_e32 v141, v141, v237
v_sub_f32_e32 v134, v134, v236
v_sub_f32_e32 v142, v142, v237
v_sub_f32_e32 v135, v135, v236
v_sub_f32_e32 v143, v143, v237
v_mul_f32_e32 v244, v238, v244
v_mul_f32_e32 v245, v239, v245
s_nop 1
s_branch .Lk_aba13
.Lk_arb13:
s_nop 7
v_mul_f32_e32 v0, v238, v0
v_mul_f32_e32 v1, v238, v1
v_mul_f32_e32 v2, v238, v2
v_mul_f32_e32 v3, v238, v3
v_mul_f32_e32 v4, v238, v4
v_mul_f32_e32 v5, v238, v5
v_mul_f32_e32 v6, v238, v6
v_mul_f32_e32 v7, v238, v7
v_mul_f32_e32 v8, v239, v8
v_mul_f32_e32 v9, v239, v9
v_mul_f32_e32 v10, v239, v10
v_mul_f32_e32 v11, v239, v11
v_mul_f32_e32 v12, v239, v12
v_mul_f32_e32 v13, v239, v13
v_mul_f32_e32 v14, v239, v14
v_mul_f32_e32 v15, v239, v15
v_mul_f32_e32 v16, v238, v16
v_mul_f32_e32 v17, v238, v17
v_mul_f32_e32 v18, v238, v18
v_mul_f32_e32 v19, v238, v19
v_mul_f32_e32 v20, v238, v20
v_mul_f32_e32 v21, v238, v21
v_mul_f32_e32 v22, v238, v22
v_mul_f32_e32 v23, v238, v23
v_mul_f32_e32 v24, v239, v24
v_mul_f32_e32 v25, v239, v25
v_mul_f32_e32 v26, v239, v26
v_mul_f32_e32 v27, v239, v27
v_mul_f32_e32 v28, v239, v28
v_mul_f32_e32 v29, v239, v29
v_mul_f32_e32 v30, v239, v30
v_mul_f32_e32 v31, v239, v31
v_mul_f32_e32 v32, v238, v32
v_mul_f32_e32 v33, v238, v33
v_mul_f32_e32 v34, v238, v34
v_mul_f32_e32 v35, v238, v35
v_mul_f32_e32 v36, v238, v36
v_mul_f32_e32 v37, v238, v37
v_mul_f32_e32 v38, v238, v38
v_mul_f32_e32 v39, v238, v39
v_mul_f32_e32 v40, v239, v40
v_mul_f32_e32 v41, v239, v41
v_mul_f32_e32 v42, v239, v42
v_mul_f32_e32 v43, v239, v43
v_mul_f32_e32 v44, v239, v44
v_mul_f32_e32 v45, v239, v45
v_mul_f32_e32 v46, v239, v46
v_mul_f32_e32 v47, v239, v47
v_mul_f32_e32 v48, v238, v48
v_mul_f32_e32 v49, v238, v49
v_mul_f32_e32 v50, v238, v50
v_mul_f32_e32 v51, v238, v51
v_mul_f32_e32 v52, v238, v52
v_mul_f32_e32 v53, v238, v53
v_mul_f32_e32 v54, v238, v54
v_mul_f32_e32 v55, v238, v55
v_mul_f32_e32 v56, v239, v56
v_mul_f32_e32 v57, v239, v57
v_mul_f32_e32 v58, v239, v58
v_mul_f32_e32 v59, v239, v59
v_mul_f32_e32 v60, v239, v60
v_mul_f32_e32 v61, v239, v61
v_mul_f32_e32 v62, v239, v62
v_mul_f32_e32 v63, v239, v63
s_nop 1
s_branch .Lk_abb13
.Lk_ara14:
v_max_f32_e32 v236, v236, v167
v_max_f32_e32 v237, v237, v175
v_mov_b32_e32 v246, v236
v_mov_b32_e32 v247, v237
s_nop 0
v_permlane16_swap_b32_e32 v236, v246
v_permlane16_swap_b32_e32 v237, v247
v_max_f32_e32 v236, v236, v246
v_max_f32_e32 v237, v237, v247
v_mov_b32_e32 v246, v236
v_mov_b32_e32 v247, v237
s_nop 0
v_permlane32_swap_b32_e32 v236, v246
v_permlane32_swap_b32_e32 v237, v247
v_max_f32_e32 v236, v236, v246
v_max_f32_e32 v237, v237, v247
v_max_f32_e32 v236, 0, v236
v_max_f32_e32 v237, 0, v237
v_exp_f32_e64 v238, -v236
v_exp_f32_e64 v239, -v237
v_sub_f32_e32 v192, v192, v236
v_sub_f32_e32 v196, v196, v237
v_sub_f32_e32 v193, v193, v236
v_sub_f32_e32 v197, v197, v237
v_sub_f32_e32 v194, v194, v236
v_sub_f32_e32 v198, v198, v237
v_sub_f32_e32 v195, v195, v236
v_sub_f32_e32 v199, v199, v237
v_sub_f32_e32 v144, v144, v236
v_sub_f32_e32 v152, v152, v237
v_sub_f32_e32 v145, v145, v236
v_sub_f32_e32 v153, v153, v237
)"
R"(
v_sub_f32_e32 v146, v146, v236
v_sub_f32_e32 v154, v154, v237
v_sub_f32_e32 v147, v147, v236
v_sub_f32_e32 v155, v155, v237
v_sub_f32_e32 v148, v148, v236
v_sub_f32_e32 v156, v156, v237
v_sub_f32_e32 v149, v149, v236
v_sub_f32_e32 v157, v157, v237
v_sub_f32_e32 v150, v150, v236
v_sub_f32_e32 v158, v158, v237
v_sub_f32_e32 v151, v151, v236
v_sub_f32_e32 v159, v159, v237
v_sub_f32_e32 v160, v160, v236
v_sub_f32_e32 v168, v168, v237
v_sub_f32_e32 v161, v161, v236
v_sub_f32_e32 v169, v169, v237
v_sub_f32_e32 v162, v162, v236
v_sub_f32_e32 v170, v170, v237
v_sub_f32_e32 v163, v163, v236
v_sub_f32_e32 v171, v171, v237
v_sub_f32_e32 v164, v164, v236
v_sub_f32_e32 v172, v172, v237
v_sub_f32_e32 v165, v165, v236
v_sub_f32_e32 v173, v173, v237
v_sub_f32_e32 v166, v166, v236
v_sub_f32_e32 v174, v174, v237
v_sub_f32_e32 v167, v167, v236
v_sub_f32_e32 v175, v175, v237
v_mul_f32_e32 v244, v238, v244
v_mul_f32_e32 v245, v239, v245
s_nop 1
s_branch .Lk_aba14
.Lk_arb14:
s_nop 7
v_mul_f32_e32 v0, v238, v0
v_mul_f32_e32 v1, v238, v1
v_mul_f32_e32 v2, v238, v2
v_mul_f32_e32 v3, v238, v3
v_mul_f32_e32 v4, v238, v4
v_mul_f32_e32 v5, v238, v5
v_mul_f32_e32 v6, v238, v6
v_mul_f32_e32 v7, v238, v7
v_mul_f32_e32 v8, v239, v8
v_mul_f32_e32 v9, v239, v9
v_mul_f32_e32 v10, v239, v10
v_mul_f32_e32 v11, v239, v11
v_mul_f32_e32 v12, v239, v12
v_mul_f32_e32 v13, v239, v13
v_mul_f32_e32 v14, v239, v14
v_mul_f32_e32 v15, v239, v15
v_mul_f32_e32 v16, v238, v16
v_mul_f32_e32 v17, v238, v17
v_mul_f32_e32 v18, v238, v18
v_mul_f32_e32 v19, v238, v19
v_mul_f32_e32 v20, v238, v20
v_mul_f32_e32 v21, v238, v21
v_mul_f32_e32 v22, v238, v22
v_mul_f32_e32 v23, v238, v23
v_mul_f32_e32 v24, v239, v24
v_mul_f32_e32 v25, v239, v25
v_mul_f32_e32 v26, v239, v26
v_mul_f32_e32 v27, v239, v27
v_mul_f32_e32 v28, v239, v28
v_mul_f32_e32 v29, v239, v29
v_mul_f32_e32 v30, v239, v30
v_mul_f32_e32 v31, v239, v31
v_mul_f32_e32 v32, v238, v32
v_mul_f32_e32 v33, v238, v33
v_mul_f32_e32 v34, v238, v34
v_mul_f32_e32 v35, v238, v35
v_mul_f32_e32 v36, v238, v36
v_mul_f32_e32 v37, v238, v37
v_mul_f32_e32 v38, v238, v38
v_mul_f32_e32 v39, v238, v39
v_mul_f32_e32 v40, v239, v40
v_mul_f32_e32 v41, v239, v41
v_mul_f32_e32 v42, v239, v42
v_mul_f32_e32 v43, v239, v43
v_mul_f32_e32 v44, v239, v44
v_mul_f32_e32 v45, v239, v45
v_mul_f32_e32 v46, v239, v46
v_mul_f32_e32 v47, v239, v47
v_mul_f32_e32 v48, v238, v48
v_mul_f32_e32 v49, v238, v49
v_mul_f32_e32 v50, v238, v50
v_mul_f32_e32 v51, v238, v51
v_mul_f32_e32 v52, v238, v52
v_mul_f32_e32 v53, v238, v53
v_mul_f32_e32 v54, v238, v54
v_mul_f32_e32 v55, v238, v55
v_mul_f32_e32 v56, v239, v56
v_mul_f32_e32 v57, v239, v57
v_mul_f32_e32 v58, v239, v58
v_mul_f32_e32 v59, v239, v59
v_mul_f32_e32 v60, v239, v60
v_mul_f32_e32 v61, v239, v61
v_mul_f32_e32 v62, v239, v62
v_mul_f32_e32 v63, v239, v63
s_nop 1
s_branch .Lk_abb14
.Lk_ara15:
v_max_f32_e32 v236, v236, v151
v_max_f32_e32 v237, v237, v159
v_mov_b32_e32 v246, v236
v_mov_b32_e32 v247, v237
s_nop 0
v_permlane16_swap_b32_e32 v236, v246
v_permlane16_swap_b32_e32 v237, v247
v_max_f32_e32 v236, v236, v246
v_max_f32_e32 v237, v237, v247
v_mov_b32_e32 v246, v236
v_mov_b32_e32 v247, v237
s_nop 0
v_permlane32_swap_b32_e32 v236, v246
v_permlane32_swap_b32_e32 v237, v247
v_max_f32_e32 v236, v236, v246
v_max_f32_e32 v237, v237, v247
v_max_f32_e32 v236, 0, v236
v_max_f32_e32 v237, 0, v237
v_exp_f32_e64 v238, -v236
v_exp_f32_e64 v239, -v237
v_sub_f32_e32 v192, v192, v236
v_sub_f32_e32 v196, v196, v237
v_sub_f32_e32 v193, v193, v236
v_sub_f32_e32 v197, v197, v237
v_sub_f32_e32 v194, v194, v236
v_sub_f32_e32 v198, v198, v237
v_sub_f32_e32 v195, v195, v236
v_sub_f32_e32 v199, v199, v237
v_sub_f32_e32 v128, v128, v236
v_sub_f32_e32 v136, v136, v237
v_sub_f32_e32 v129, v129, v236
v_sub_f32_e32 v137, v137, v237
v_sub_f32_e32 v130, v130, v236
v_sub_f32_e32 v138, v138, v237
v_sub_f32_e32 v131, v131, v236
v_sub_f32_e32 v139, v139, v237
v_sub_f32_e32 v132, v132, v236
v_sub_f32_e32 v140, v140, v237
v_sub_f32_e32 v133, v133, v236
v_sub_f32_e32 v141, v141, v237
v_sub_f32_e32 v134, v134, v236
v_sub_f32_e32 v142, v142, v237
v_sub_f32_e32 v135, v135, v236
v_sub_f32_e32 v143, v143, v237
v_sub_f32_e32 v144, v144, v236
v_sub_f32_e32 v152, v152, v237
v_sub_f32_e32 v145, v145, v236
v_sub_f32_e32 v153, v153, v237
v_sub_f32_e32 v146, v146, v236
v_sub_f32_e32 v154, v154, v237
v_sub_f32_e32 v147, v147, v236
v_sub_f32_e32 v155, v155, v237
v_sub_f32_e32 v148, v148, v236
v_sub_f32_e32 v156, v156, v237
v_sub_f32_e32 v149, v149, v236
v_sub_f32_e32 v157, v157, v237
v_sub_f32_e32 v150, v150, v236
v_sub_f32_e32 v158, v158, v237
v_sub_f32_e32 v151, v151, v236
v_sub_f32_e32 v159, v159, v237
v_mul_f32_e32 v244, v238, v244
v_mul_f32_e32 v245, v239, v245
s_nop 1
s_branch .Lk_aba15
.Lk_arb15:
s_nop 7
v_mul_f32_e32 v0, v238, v0
v_mul_f32_e32 v1, v238, v1
v_mul_f32_e32 v2, v238, v2
v_mul_f32_e32 v3, v238, v3
v_mul_f32_e32 v4, v238, v4
v_mul_f32_e32 v5, v238, v5
v_mul_f32_e32 v6, v238, v6
v_mul_f32_e32 v7, v238, v7
v_mul_f32_e32 v8, v239, v8
v_mul_f32_e32 v9, v239, v9
v_mul_f32_e32 v10, v239, v10
v_mul_f32_e32 v11, v239, v11
v_mul_f32_e32 v12, v239, v12
v_mul_f32_e32 v13, v239, v13
v_mul_f32_e32 v14, v239, v14
v_mul_f32_e32 v15, v239, v15
v_mul_f32_e32 v16, v238, v16
v_mul_f32_e32 v17, v238, v17
v_mul_f32_e32 v18, v238, v18
v_mul_f32_e32 v19, v238, v19
v_mul_f32_e32 v20, v238, v20
v_mul_f32_e32 v21, v238, v21
v_mul_f32_e32 v22, v238, v22
v_mul_f32_e32 v23, v238, v23
v_mul_f32_e32 v24, v239, v24
v_mul_f32_e32 v25, v239, v25
v_mul_f32_e32 v26, v239, v26
v_mul_f32_e32 v27, v239, v27
v_mul_f32_e32 v28, v239, v28
v_mul_f32_e32 v29, v239, v29
v_mul_f32_e32 v30, v239, v30
v_mul_f32_e32 v31, v239, v31
v_mul_f32_e32 v32, v238, v32
)"
R"(
v_mul_f32_e32 v33, v238, v33
v_mul_f32_e32 v34, v238, v34
v_mul_f32_e32 v35, v238, v35
v_mul_f32_e32 v36, v238, v36
v_mul_f32_e32 v37, v238, v37
v_mul_f32_e32 v38, v238, v38
v_mul_f32_e32 v39, v238, v39
v_mul_f32_e32 v40, v239, v40
v_mul_f32_e32 v41, v239, v41
v_mul_f32_e32 v42, v239, v42
v_mul_f32_e32 v43, v239, v43
v_mul_f32_e32 v44, v239, v44
v_mul_f32_e32 v45, v239, v45
v_mul_f32_e32 v46, v239, v46
v_mul_f32_e32 v47, v239, v47
v_mul_f32_e32 v48, v238, v48
v_mul_f32_e32 v49, v238, v49
v_mul_f32_e32 v50, v238, v50
v_mul_f32_e32 v51, v238, v51
v_mul_f32_e32 v52, v238, v52
v_mul_f32_e32 v53, v238, v53
v_mul_f32_e32 v54, v238, v54
v_mul_f32_e32 v55, v238, v55
v_mul_f32_e32 v56, v239, v56
v_mul_f32_e32 v57, v239, v57
v_mul_f32_e32 v58, v239, v58
v_mul_f32_e32 v59, v239, v59
v_mul_f32_e32 v60, v239, v60
v_mul_f32_e32 v61, v239, v61
v_mul_f32_e32 v62, v239, v62
v_mul_f32_e32 v63, v239, v63
s_nop 1
s_branch .Lk_abb15
.Lk_bra1:
v_max_f32_e32 v236, v236, v135
v_mov_b32_e32 v246, v236
s_nop 1
v_permlane16_swap_b32_e32 v236, v246
v_max_f32_e32 v236, v236, v246
v_mov_b32_e32 v246, v236
s_nop 1
v_permlane32_swap_b32_e32 v236, v246
v_max_f32_e32 v236, v236, v246
v_max_f32_e32 v236, 0, v236
v_exp_f32_e64 v238, -v236
v_sub_f32_e32 v192, v192, v236
v_sub_f32_e32 v193, v193, v236
v_sub_f32_e32 v194, v194, v236
v_sub_f32_e32 v195, v195, v236
v_sub_f32_e32 v160, v160, v236
v_sub_f32_e32 v161, v161, v236
v_sub_f32_e32 v162, v162, v236
v_sub_f32_e32 v163, v163, v236
v_sub_f32_e32 v164, v164, v236
v_sub_f32_e32 v165, v165, v236
v_sub_f32_e32 v166, v166, v236
v_sub_f32_e32 v167, v167, v236
v_sub_f32_e32 v128, v128, v236
v_sub_f32_e32 v129, v129, v236
v_sub_f32_e32 v130, v130, v236
v_sub_f32_e32 v131, v131, v236
v_sub_f32_e32 v132, v132, v236
v_sub_f32_e32 v133, v133, v236
v_sub_f32_e32 v134, v134, v236
v_sub_f32_e32 v135, v135, v236
v_mul_f32_e32 v242, v238, v242
s_nop 1
s_branch .Lk_bba1
.Lk_brb1:
s_nop 7
v_mul_f32_e32 v0, v238, v0
v_mul_f32_e32 v1, v238, v1
v_mul_f32_e32 v2, v238, v2
v_mul_f32_e32 v3, v238, v3
v_mul_f32_e32 v4, v238, v4
v_mul_f32_e32 v5, v238, v5
v_mul_f32_e32 v6, v238, v6
v_mul_f32_e32 v7, v238, v7
v_mul_f32_e32 v16, v238, v16
v_mul_f32_e32 v17, v238, v17
v_mul_f32_e32 v18, v238, v18
v_mul_f32_e32 v19, v238, v19
v_mul_f32_e32 v20, v238, v20
v_mul_f32_e32 v21, v238, v21
v_mul_f32_e32 v22, v238, v22
v_mul_f32_e32 v23, v238, v23
v_mul_f32_e32 v32, v238, v32
v_mul_f32_e32 v33, v238, v33
v_mul_f32_e32 v34, v238, v34
v_mul_f32_e32 v35, v238, v35
v_mul_f32_e32 v36, v238, v36
v_mul_f32_e32 v37, v238, v37
v_mul_f32_e32 v38, v238, v38
v_mul_f32_e32 v39, v238, v39
v_mul_f32_e32 v48, v238, v48
v_mul_f32_e32 v49, v238, v49
v_mul_f32_e32 v50, v238, v50
v_mul_f32_e32 v51, v238, v51
v_mul_f32_e32 v52, v238, v52
v_mul_f32_e32 v53, v238, v53
v_mul_f32_e32 v54, v238, v54
v_mul_f32_e32 v55, v238, v55
s_nop 1
s_branch .Lk_bbb1
.Lk_bra2:
v_max_f32_e32 v236, v236, v167
v_mov_b32_e32 v246, v236
s_nop 1
v_permlane16_swap_b32_e32 v236, v246
v_max_f32_e32 v236, v236, v246
v_mov_b32_e32 v246, v236
s_nop 1
v_permlane32_swap_b32_e32 v236, v246
v_max_f32_e32 v236, v236, v246
v_max_f32_e32 v236, 0, v236
v_exp_f32_e64 v238, -v236
v_sub_f32_e32 v192, v192, v236
v_sub_f32_e32 v193, v193, v236
v_sub_f32_e32 v194, v194, v236
v_sub_f32_e32 v195, v195, v236
v_sub_f32_e32 v144, v144, v236
v_sub_f32_e32 v145, v145, v236
v_sub_f32_e32 v146, v146, v236
v_sub_f32_e32 v147, v147, v236
v_sub_f32_e32 v148, v148, v236
v_sub_f32_e32 v149, v149, v236
v_sub_f32_e32 v150, v150, v236
v_sub_f32_e32 v151, v151, v236
v_sub_f32_e32 v160, v160, v236
v_sub_f32_e32 v161, v161, v236
v_sub_f32_e32 v162, v162, v236
v_sub_f32_e32 v163, v163, v236
v_sub_f32_e32 v164, v164, v236
v_sub_f32_e32 v165, v165, v236
v_sub_f32_e32 v166, v166, v236
v_sub_f32_e32 v167, v167, v236
v_mul_f32_e32 v242, v238, v242
s_nop 1
s_branch .Lk_bba2
.Lk_brb2:
s_nop 7
v_mul_f32_e32 v0, v238, v0
v_mul_f32_e32 v1, v238, v1
v_mul_f32_e32 v2, v238, v2
v_mul_f32_e32 v3, v238, v3
v_mul_f32_e32 v4, v238, v4
v_mul_f32_e32 v5, v238, v5
v_mul_f32_e32 v6, v238, v6
v_mul_f32_e32 v7, v238, v7
v_mul_f32_e32 v16, v238, v16
v_mul_f32_e32 v17, v238, v17
v_mul_f32_e32 v18, v238, v18
v_mul_f32_e32 v19, v238, v19
v_mul_f32_e32 v20, v238, v20
v_mul_f32_e32 v21, v238, v21
v_mul_f32_e32 v22, v238, v22
v_mul_f32_e32 v23, v238, v23
v_mul_f32_e32 v32, v238, v32
v_mul_f32_e32 v33, v238, v33
v_mul_f32_e32 v34, v238, v34
v_mul_f32_e32 v35, v238, v35
v_mul_f32_e32 v36, v238, v36
v_mul_f32_e32 v37, v238, v37
v_mul_f32_e32 v38, v238, v38
v_mul_f32_e32 v39, v238, v39
v_mul_f32_e32 v48, v238, v48
v_mul_f32_e32 v49, v238, v49
v_mul_f32_e32 v50, v238, v50
v_mul_f32_e32 v51, v238, v51
v_mul_f32_e32 v52, v238, v52
v_mul_f32_e32 v53, v238, v53
v_mul_f32_e32 v54, v238, v54
v_mul_f32_e32 v55, v238, v55
s_nop 1
s_branch .Lk_bbb2
.Lk_bra3:
v_max_f32_e32 v236, v236, v151
v_mov_b32_e32 v246, v236
s_nop 1
v_permlane16_swap_b32_e32 v236, v246
v_max_f32_e32 v236, v236, v246
v_mov_b32_e32 v246, v236
s_nop 1
v_permlane32_swap_b32_e32 v236, v246
v_max_f32_e32 v236, v236, v246
v_max_f32_e32 v236, 0, v236
v_exp_f32_e64 v238, -v236
v_sub_f32_e32 v192, v192, v236
v_sub_f32_e32 v193, v193, v236
v_sub_f32_e32 v194, v194, v236
v_sub_f32_e32 v195, v195, v236
v_sub_f32_e32 v128, v128, v236
v_sub_f32_e32 v129, v129, v236
v_sub_f32_e32 v130, v130, v236
v_sub_f32_e32 v131, v131, v236
v_sub_f32_e32 v132, v132, v236
v_sub_f32_e32 v133, v133, v236
v_sub_f32_e32 v134, v134, v236
v_sub_f32_e32 v135, v135, v236
v_sub_f32_e32 v144, v144, v236
)"
R"(
v_sub_f32_e32 v145, v145, v236
v_sub_f32_e32 v146, v146, v236
v_sub_f32_e32 v147, v147, v236
v_sub_f32_e32 v148, v148, v236
v_sub_f32_e32 v149, v149, v236
v_sub_f32_e32 v150, v150, v236
v_sub_f32_e32 v151, v151, v236
v_mul_f32_e32 v242, v238, v242
s_nop 1
s_branch .Lk_bba3
.Lk_brb3:
s_nop 7
v_mul_f32_e32 v0, v238, v0
v_mul_f32_e32 v1, v238, v1
v_mul_f32_e32 v2, v238, v2
v_mul_f32_e32 v3, v238, v3
v_mul_f32_e32 v4, v238, v4
v_mul_f32_e32 v5, v238, v5
v_mul_f32_e32 v6, v238, v6
v_mul_f32_e32 v7, v238, v7
v_mul_f32_e32 v16, v238, v16
v_mul_f32_e32 v17, v238, v17
v_mul_f32_e32 v18, v238, v18
v_mul_f32_e32 v19, v238, v19
v_mul_f32_e32 v20, v238, v20
v_mul_f32_e32 v21, v238, v21
v_mul_f32_e32 v22, v238, v22
v_mul_f32_e32 v23, v238, v23
v_mul_f32_e32 v32, v238, v32
v_mul_f32_e32 v33, v238, v33
v_mul_f32_e32 v34, v238, v34
v_mul_f32_e32 v35, v238, v35
v_mul_f32_e32 v36, v238, v36
v_mul_f32_e32 v37, v238, v37
v_mul_f32_e32 v38, v238, v38
v_mul_f32_e32 v39, v238, v39
v_mul_f32_e32 v48, v238, v48
v_mul_f32_e32 v49, v238, v49
v_mul_f32_e32 v50, v238, v50
v_mul_f32_e32 v51, v238, v51
v_mul_f32_e32 v52, v238, v52
v_mul_f32_e32 v53, v238, v53
v_mul_f32_e32 v54, v238, v54
v_mul_f32_e32 v55, v238, v55
s_nop 1
s_branch .Lk_bbb3
.Lk_bra4:
v_max_f32_e32 v236, v236, v135
v_mov_b32_e32 v246, v236
s_nop 1
v_permlane16_swap_b32_e32 v236, v246
v_max_f32_e32 v236, v236, v246
v_mov_b32_e32 v246, v236
s_nop 1
v_permlane32_swap_b32_e32 v236, v246
v_max_f32_e32 v236, v236, v246
v_max_f32_e32 v236, 0, v236
v_exp_f32_e64 v238, -v236
v_sub_f32_e32 v192, v192, v236
v_sub_f32_e32 v193, v193, v236
v_sub_f32_e32 v194, v194, v236
v_sub_f32_e32 v195, v195, v236
v_sub_f32_e32 v160, v160, v236
v_sub_f32_e32 v161, v161, v236
v_sub_f32_e32 v162, v162, v236
v_sub_f32_e32 v163, v163, v236
v_sub_f32_e32 v164, v164, v236
v_sub_f32_e32 v165, v165, v236
v_sub_f32_e32 v166, v166, v236
v_sub_f32_e32 v167, v167, v236
v_sub_f32_e32 v128, v128, v236
v_sub_f32_e32 v129, v129, v236
v_sub_f32_e32 v130, v130, v236
v_sub_f32_e32 v131, v131, v236
v_sub_f32_e32 v132, v132, v236
v_sub_f32_e32 v133, v133, v236
v_sub_f32_e32 v134, v134, v236
v_sub_f32_e32 v135, v135, v236
v_mul_f32_e32 v242, v238, v242
s_nop 1
s_branch .Lk_bba4
.Lk_brb4:
s_nop 7
v_mul_f32_e32 v0, v238, v0
v_mul_f32_e32 v1, v238, v1
v_mul_f32_e32 v2, v238, v2
v_mul_f32_e32 v3, v238, v3
v_mul_f32_e32 v4, v238, v4
v_mul_f32_e32 v5, v238, v5
v_mul_f32_e32 v6, v238, v6
v_mul_f32_e32 v7, v238, v7
v_mul_f32_e32 v16, v238, v16
v_mul_f32_e32 v17, v238, v17
v_mul_f32_e32 v18, v238, v18
v_mul_f32_e32 v19, v238, v19
v_mul_f32_e32 v20, v238, v20
v_mul_f32_e32 v21, v238, v21
v_mul_f32_e32 v22, v238, v22
v_mul_f32_e32 v23, v238, v23
v_mul_f32_e32 v32, v238, v32
v_mul_f32_e32 v33, v238, v33
v_mul_f32_e32 v34, v238, v34
v_mul_f32_e32 v35, v238, v35
v_mul_f32_e32 v36, v238, v36
v_mul_f32_e32 v37, v238, v37
v_mul_f32_e32 v38, v238, v38
v_mul_f32_e32 v39, v238, v39
v_mul_f32_e32 v48, v238, v48
v_mul_f32_e32 v49, v238, v49
v_mul_f32_e32 v50, v238, v50
v_mul_f32_e32 v51, v238, v51
v_mul_f32_e32 v52, v238, v52
v_mul_f32_e32 v53, v238, v53
v_mul_f32_e32 v54, v238, v54
v_mul_f32_e32 v55, v238, v55
s_nop 1
s_branch .Lk_bbb4
.Lk_bra5:
v_max_f32_e32 v236, v236, v167
v_mov_b32_e32 v246, v236
s_nop 1
v_permlane16_swap_b32_e32 v236, v246
v_max_f32_e32 v236, v236, v246
v_mov_b32_e32 v246, v236
s_nop 1
v_permlane32_swap_b32_e32 v236, v246
v_max_f32_e32 v236, v236, v246
v_max_f32_e32 v236, 0, v236
v_exp_f32_e64 v238, -v236
v_sub_f32_e32 v192, v192, v236
v_sub_f32_e32 v193, v193, v236
v_sub_f32_e32 v194, v194, v236
v_sub_f32_e32 v195, v195, v236
v_sub_f32_e32 v144, v144, v236
v_sub_f32_e32 v145, v145, v236
v_sub_f32_e32 v146, v146, v236
v_sub_f32_e32 v147, v147, v236
v_sub_f32_e32 v148, v148, v236
v_sub_f32_e32 v149, v149, v236
v_sub_f32_e32 v150, v150, v236
v_sub_f32_e32 v151, v151, v236
v_sub_f32_e32 v160, v160, v236
v_sub_f32_e32 v161, v161, v236
v_sub_f32_e32 v162, v162, v236
v_sub_f32_e32 v163, v163, v236
v_sub_f32_e32 v164, v164, v236
v_sub_f32_e32 v165, v165, v236
v_sub_f32_e32 v166, v166, v236
v_sub_f32_e32 v167, v167, v236
v_mul_f32_e32 v242, v238, v242
s_nop 1
s_branch .Lk_bba5
.Lk_brb5:
s_nop 7
v_mul_f32_e32 v0, v238, v0
v_mul_f32_e32 v1, v238, v1
v_mul_f32_e32 v2, v238, v2
v_mul_f32_e32 v3, v238, v3
v_mul_f32_e32 v4, v238, v4
v_mul_f32_e32 v5, v238, v5
v_mul_f32_e32 v6, v238, v6
v_mul_f32_e32 v7, v238, v7
v_mul_f32_e32 v16, v238, v16
v_mul_f32_e32 v17, v238, v17
v_mul_f32_e32 v18, v238, v18
v_mul_f32_e32 v19, v238, v19
v_mul_f32_e32 v20, v238, v20
v_mul_f32_e32 v21, v238, v21
v_mul_f32_e32 v22, v238, v22
v_mul_f32_e32 v23, v238, v23
v_mul_f32_e32 v32, v238, v32
v_mul_f32_e32 v33, v238, v33
v_mul_f32_e32 v34, v238, v34
v_mul_f32_e32 v35, v238, v35
v_mul_f32_e32 v36, v238, v36
v_mul_f32_e32 v37, v238, v37
v_mul_f32_e32 v38, v238, v38
v_mul_f32_e32 v39, v238, v39
v_mul_f32_e32 v48, v238, v48
v_mul_f32_e32 v49, v238, v49
v_mul_f32_e32 v50, v238, v50
v_mul_f32_e32 v51, v238, v51
v_mul_f32_e32 v52, v238, v52
v_mul_f32_e32 v53, v238, v53
v_mul_f32_e32 v54, v238, v54
v_mul_f32_e32 v55, v238, v55
s_nop 1
s_branch .Lk_bbb5
.Lk_bra6:
v_max_f32_e32 v236, v236, v151
v_mov_b32_e32 v246, v236
s_nop 1
v_permlane16_swap_b32_e32 v236, v246
v_max_f32_e32 v236, v236, v246
v_mov_b32_e32 v246, v236
s_nop 1
v_permlane32_swap_b32_e32 v236, v246
v_max_f32_e32 v236, v236, v246
v_max_f32_e32 v236, 0, v236
v_exp_f32_e64 v238, -v236
)"
R"(
v_sub_f32_e32 v192, v192, v236
v_sub_f32_e32 v193, v193, v236
v_sub_f32_e32 v194, v194, v236
v_sub_f32_e32 v195, v195, v236
v_sub_f32_e32 v128, v128, v236
v_sub_f32_e32 v129, v129, v236
v_sub_f32_e32 v130, v130, v236
v_sub_f32_e32 v131, v131, v236
v_sub_f32_e32 v132, v132, v236
v_sub_f32_e32 v133, v133, v236
v_sub_f32_e32 v134, v134, v236
v_sub_f32_e32 v135, v135, v236
v_sub_f32_e32 v144, v144, v236
v_sub_f32_e32 v145, v145, v236
v_sub_f32_e32 v146, v146, v236
v_sub_f32_e32 v147, v147, v236
v_sub_f32_e32 v148, v148, v236
v_sub_f32_e32 v149, v149, v236
v_sub_f32_e32 v150, v150, v236
v_sub_f32_e32 v151, v151, v236
v_mul_f32_e32 v242, v238, v242
s_nop 1
s_branch .Lk_bba6
.Lk_brb6:
s_nop 7
v_mul_f32_e32 v0, v238, v0
v_mul_f32_e32 v1, v238, v1
v_mul_f32_e32 v2, v238, v2
v_mul_f32_e32 v3, v238, v3
v_mul_f32_e32 v4, v238, v4
v_mul_f32_e32 v5, v238, v5
v_mul_f32_e32 v6, v238, v6
v_mul_f32_e32 v7, v238, v7
v_mul_f32_e32 v16, v238, v16
v_mul_f32_e32 v17, v238, v17
v_mul_f32_e32 v18, v238, v18
v_mul_f32_e32 v19, v238, v19
v_mul_f32_e32 v20, v238, v20
v_mul_f32_e32 v21, v238, v21
v_mul_f32_e32 v22, v238, v22
v_mul_f32_e32 v23, v238, v23
v_mul_f32_e32 v32, v238, v32
v_mul_f32_e32 v33, v238, v33
v_mul_f32_e32 v34, v238, v34
v_mul_f32_e32 v35, v238, v35
v_mul_f32_e32 v36, v238, v36
v_mul_f32_e32 v37, v238, v37
v_mul_f32_e32 v38, v238, v38
v_mul_f32_e32 v39, v238, v39
v_mul_f32_e32 v48, v238, v48
v_mul_f32_e32 v49, v238, v49
v_mul_f32_e32 v50, v238, v50
v_mul_f32_e32 v51, v238, v51
v_mul_f32_e32 v52, v238, v52
v_mul_f32_e32 v53, v238, v53
v_mul_f32_e32 v54, v238, v54
v_mul_f32_e32 v55, v238, v55
s_nop 1
s_branch .Lk_bbb6
.Lk_bra7:
v_max_f32_e32 v236, v236, v135
v_mov_b32_e32 v246, v236
s_nop 1
v_permlane16_swap_b32_e32 v236, v246
v_max_f32_e32 v236, v236, v246
v_mov_b32_e32 v246, v236
s_nop 1
v_permlane32_swap_b32_e32 v236, v246
v_max_f32_e32 v236, v236, v246
v_max_f32_e32 v236, 0, v236
v_exp_f32_e64 v238, -v236
v_sub_f32_e32 v192, v192, v236
v_sub_f32_e32 v193, v193, v236
v_sub_f32_e32 v194, v194, v236
v_sub_f32_e32 v195, v195, v236
v_sub_f32_e32 v160, v160, v236
v_sub_f32_e32 v161, v161, v236
v_sub_f32_e32 v162, v162, v236
v_sub_f32_e32 v163, v163, v236
v_sub_f32_e32 v164, v164, v236
v_sub_f32_e32 v165, v165, v236
v_sub_f32_e32 v166, v166, v236
v_sub_f32_e32 v167, v167, v236
v_sub_f32_e32 v128, v128, v236
v_sub_f32_e32 v129, v129, v236
v_sub_f32_e32 v130, v130, v236
v_sub_f32_e32 v131, v131, v236
v_sub_f32_e32 v132, v132, v236
v_sub_f32_e32 v133, v133, v236
v_sub_f32_e32 v134, v134, v236
v_sub_f32_e32 v135, v135, v236
v_mul_f32_e32 v242, v238, v242
s_nop 1
s_branch .Lk_bba7
.Lk_brb7:
s_nop 7
v_mul_f32_e32 v0, v238, v0
v_mul_f32_e32 v1, v238, v1
v_mul_f32_e32 v2, v238, v2
v_mul_f32_e32 v3, v238, v3
v_mul_f32_e32 v4, v238, v4
v_mul_f32_e32 v5, v238, v5
v_mul_f32_e32 v6, v238, v6
v_mul_f32_e32 v7, v238, v7
v_mul_f32_e32 v16, v238, v16
v_mul_f32_e32 v17, v238, v17
v_mul_f32_e32 v18, v238, v18
v_mul_f32_e32 v19, v238, v19
v_mul_f32_e32 v20, v238, v20
v_mul_f32_e32 v21, v238, v21
v_mul_f32_e32 v22, v238, v22
v_mul_f32_e32 v23, v238, v23
v_mul_f32_e32 v32, v238, v32
v_mul_f32_e32 v33, v238, v33
v_mul_f32_e32 v34, v238, v34
v_mul_f32_e32 v35, v238, v35
v_mul_f32_e32 v36, v238, v36
v_mul_f32_e32 v37, v238, v37
v_mul_f32_e32 v38, v238, v38
v_mul_f32_e32 v39, v238, v39
v_mul_f32_e32 v48, v238, v48
v_mul_f32_e32 v49, v238, v49
v_mul_f32_e32 v50, v238, v50
v_mul_f32_e32 v51, v238, v51
v_mul_f32_e32 v52, v238, v52
v_mul_f32_e32 v53, v238, v53
v_mul_f32_e32 v54, v238, v54
v_mul_f32_e32 v55, v238, v55
s_nop 1
s_branch .Lk_bbb7
.Lk_bra9:
v_max_f32_e32 v236, v236, v151
v_mov_b32_e32 v246, v236
s_nop 1
v_permlane16_swap_b32_e32 v236, v246
v_max_f32_e32 v236, v236, v246
v_mov_b32_e32 v246, v236
s_nop 1
v_permlane32_swap_b32_e32 v236, v246
v_max_f32_e32 v236, v236, v246
v_max_f32_e32 v236, 0, v236
v_exp_f32_e64 v238, -v236
v_sub_f32_e32 v192, v192, v236
v_sub_f32_e32 v193, v193, v236
v_sub_f32_e32 v194, v194, v236
v_sub_f32_e32 v195, v195, v236
v_sub_f32_e32 v128, v128, v236
v_sub_f32_e32 v129, v129, v236
v_sub_f32_e32 v130, v130, v236
v_sub_f32_e32 v131, v131, v236
v_sub_f32_e32 v132, v132, v236
v_sub_f32_e32 v133, v133, v236
v_sub_f32_e32 v134, v134, v236
v_sub_f32_e32 v135, v135, v236
v_sub_f32_e32 v144, v144, v236
v_sub_f32_e32 v145, v145, v236
v_sub_f32_e32 v146, v146, v236
v_sub_f32_e32 v147, v147, v236
v_sub_f32_e32 v148, v148, v236
v_sub_f32_e32 v149, v149, v236
v_sub_f32_e32 v150, v150, v236
v_sub_f32_e32 v151, v151, v236
v_mul_f32_e32 v244, v238, v244
s_nop 1
s_branch .Lk_bba9
.Lk_brb9:
s_nop 7
v_mul_f32_e32 v0, v238, v0
v_mul_f32_e32 v1, v238, v1
v_mul_f32_e32 v2, v238, v2
v_mul_f32_e32 v3, v238, v3
v_mul_f32_e32 v4, v238, v4
v_mul_f32_e32 v5, v238, v5
v_mul_f32_e32 v6, v238, v6
v_mul_f32_e32 v7, v238, v7
v_mul_f32_e32 v16, v238, v16
v_mul_f32_e32 v17, v238, v17
v_mul_f32_e32 v18, v238, v18
v_mul_f32_e32 v19, v238, v19
v_mul_f32_e32 v20, v238, v20
v_mul_f32_e32 v21, v238, v21
v_mul_f32_e32 v22, v238, v22
v_mul_f32_e32 v23, v238, v23
v_mul_f32_e32 v32, v238, v32
v_mul_f32_e32 v33, v238, v33
v_mul_f32_e32 v34, v238, v34
v_mul_f32_e32 v35, v238, v35
v_mul_f32_e32 v36, v238, v36
v_mul_f32_e32 v37, v238, v37
v_mul_f32_e32 v38, v238, v38
v_mul_f32_e32 v39, v238, v39
v_mul_f32_e32 v48, v238, v48
v_mul_f32_e32 v49, v238, v49
v_mul_f32_e32 v50, v238, v50
v_mul_f32_e32 v51, v238, v51
v_mul_f32_e32 v52, v238, v52
v_mul_f32_e32 v53, v238, v53
v_mul_f32_e32 v54, v238, v54
v_mul_f32_e32 v55, v238, v55
s_nop 1
)"
R"(
s_branch .Lk_bbb9
.Lk_bra10:
v_max_f32_e32 v236, v236, v135
v_mov_b32_e32 v246, v236
s_nop 1
v_permlane16_swap_b32_e32 v236, v246
v_max_f32_e32 v236, v236, v246
v_mov_b32_e32 v246, v236
s_nop 1
v_permlane32_swap_b32_e32 v236, v246
v_max_f32_e32 v236, v236, v246
v_max_f32_e32 v236, 0, v236
v_exp_f32_e64 v238, -v236
v_sub_f32_e32 v192, v192, v236
v_sub_f32_e32 v193, v193, v236
v_sub_f32_e32 v194, v194, v236
v_sub_f32_e32 v195, v195, v236
v_sub_f32_e32 v160, v160, v236
v_sub_f32_e32 v161, v161, v236
v_sub_f32_e32 v162, v162, v236
v_sub_f32_e32 v163, v163, v236
v_sub_f32_e32 v164, v164, v236
v_sub_f32_e32 v165, v165, v236
v_sub_f32_e32 v166, v166, v236
v_sub_f32_e32 v167, v167, v236
v_sub_f32_e32 v128, v128, v236
v_sub_f32_e32 v129, v129, v236
v_sub_f32_e32 v130, v130, v236
v_sub_f32_e32 v131, v131, v236
v_sub_f32_e32 v132, v132, v236
v_sub_f32_e32 v133, v133, v236
v_sub_f32_e32 v134, v134, v236
v_sub_f32_e32 v135, v135, v236
v_mul_f32_e32 v244, v238, v244
s_nop 1
s_branch .Lk_bba10
.Lk_brb10:
s_nop 7
v_mul_f32_e32 v0, v238, v0
v_mul_f32_e32 v1, v238, v1
v_mul_f32_e32 v2, v238, v2
v_mul_f32_e32 v3, v238, v3
v_mul_f32_e32 v4, v238, v4
v_mul_f32_e32 v5, v238, v5
v_mul_f32_e32 v6, v238, v6
v_mul_f32_e32 v7, v238, v7
v_mul_f32_e32 v16, v238, v16
v_mul_f32_e32 v17, v238, v17
v_mul_f32_e32 v18, v238, v18
v_mul_f32_e32 v19, v238, v19
v_mul_f32_e32 v20, v238, v20
v_mul_f32_e32 v21, v238, v21
v_mul_f32_e32 v22, v238, v22
v_mul_f32_e32 v23, v238, v23
v_mul_f32_e32 v32, v238, v32
v_mul_f32_e32 v33, v238, v33
v_mul_f32_e32 v34, v238, v34
v_mul_f32_e32 v35, v238, v35
v_mul_f32_e32 v36, v238, v36
v_mul_f32_e32 v37, v238, v37
v_mul_f32_e32 v38, v238, v38
v_mul_f32_e32 v39, v238, v39
v_mul_f32_e32 v48, v238, v48
v_mul_f32_e32 v49, v238, v49
v_mul_f32_e32 v50, v238, v50
v_mul_f32_e32 v51, v238, v51
v_mul_f32_e32 v52, v238, v52
v_mul_f32_e32 v53, v238, v53
v_mul_f32_e32 v54, v238, v54
v_mul_f32_e32 v55, v238, v55
s_nop 1
s_branch .Lk_bbb10
.Lk_bra11:
v_max_f32_e32 v236, v236, v167
v_mov_b32_e32 v246, v236
s_nop 1
v_permlane16_swap_b32_e32 v236, v246
v_max_f32_e32 v236, v236, v246
v_mov_b32_e32 v246, v236
s_nop 1
v_permlane32_swap_b32_e32 v236, v246
v_max_f32_e32 v236, v236, v246
v_max_f32_e32 v236, 0, v236
v_exp_f32_e64 v238, -v236
v_sub_f32_e32 v192, v192, v236
v_sub_f32_e32 v193, v193, v236
v_sub_f32_e32 v194, v194, v236
v_sub_f32_e32 v195, v195, v236
v_sub_f32_e32 v144, v144, v236
v_sub_f32_e32 v145, v145, v236
v_sub_f32_e32 v146, v146, v236
v_sub_f32_e32 v147, v147, v236
v_sub_f32_e32 v148, v148, v236
v_sub_f32_e32 v149, v149, v236
v_sub_f32_e32 v150, v150, v236
v_sub_f32_e32 v151, v151, v236
v_sub_f32_e32 v160, v160, v236
v_sub_f32_e32 v161, v161, v236
v_sub_f32_e32 v162, v162, v236
v_sub_f32_e32 v163, v163, v236
v_sub_f32_e32 v164, v164, v236
v_sub_f32_e32 v165, v165, v236
v_sub_f32_e32 v166, v166, v236
v_sub_f32_e32 v167, v167, v236
v_mul_f32_e32 v244, v238, v244
s_nop 1
s_branch .Lk_bba11
.Lk_brb11:
s_nop 7
v_mul_f32_e32 v0, v238, v0
v_mul_f32_e32 v1, v238, v1
v_mul_f32_e32 v2, v238, v2
v_mul_f32_e32 v3, v238, v3
v_mul_f32_e32 v4, v238, v4
v_mul_f32_e32 v5, v238, v5
v_mul_f32_e32 v6, v238, v6
v_mul_f32_e32 v7, v238, v7
v_mul_f32_e32 v16, v238, v16
v_mul_f32_e32 v17, v238, v17
v_mul_f32_e32 v18, v238, v18
v_mul_f32_e32 v19, v238, v19
v_mul_f32_e32 v20, v238, v20
v_mul_f32_e32 v21, v238, v21
v_mul_f32_e32 v22, v238, v22
v_mul_f32_e32 v23, v238, v23
v_mul_f32_e32 v32, v238, v32
v_mul_f32_e32 v33, v238, v33
v_mul_f32_e32 v34, v238, v34
v_mul_f32_e32 v35, v238, v35
v_mul_f32_e32 v36, v238, v36
v_mul_f32_e32 v37, v238, v37
v_mul_f32_e32 v38, v238, v38
v_mul_f32_e32 v39, v238, v39
v_mul_f32_e32 v48, v238, v48
v_mul_f32_e32 v49, v238, v49
v_mul_f32_e32 v50, v238, v50
v_mul_f32_e32 v51, v238, v51
v_mul_f32_e32 v52, v238, v52
v_mul_f32_e32 v53, v238, v53
v_mul_f32_e32 v54, v238, v54
v_mul_f32_e32 v55, v238, v55
s_nop 1
s_branch .Lk_bbb11
.Lk_bra12:
v_max_f32_e32 v236, v236, v151
v_mov_b32_e32 v246, v236
s_nop 1
v_permlane16_swap_b32_e32 v236, v246
v_max_f32_e32 v236, v236, v246
v_mov_b32_e32 v246, v236
s_nop 1
v_permlane32_swap_b32_e32 v236, v246
v_max_f32_e32 v236, v236, v246
v_max_f32_e32 v236, 0, v236
v_exp_f32_e64 v238, -v236
v_sub_f32_e32 v192, v192, v236
v_sub_f32_e32 v193, v193, v236
v_sub_f32_e32 v194, v194, v236
v_sub_f32_e32 v195, v195, v236
v_sub_f32_e32 v128, v128, v236
v_sub_f32_e32 v129, v129, v236
v_sub_f32_e32 v130, v130, v236
v_sub_f32_e32 v131, v131, v236
v_sub_f32_e32 v132, v132, v236
v_sub_f32_e32 v133, v133, v236
v_sub_f32_e32 v134, v134, v236
v_sub_f32_e32 v135, v135, v236
v_sub_f32_e32 v144, v144, v236
v_sub_f32_e32 v145, v145, v236
v_sub_f32_e32 v146, v146, v236
v_sub_f32_e32 v147, v147, v236
v_sub_f32_e32 v148, v148, v236
v_sub_f32_e32 v149, v149, v236
v_sub_f32_e32 v150, v150, v236
v_sub_f32_e32 v151, v151, v236
v_mul_f32_e32 v244, v238, v244
s_nop 1
s_branch .Lk_bba12
.Lk_brb12:
s_nop 7
v_mul_f32_e32 v0, v238, v0
v_mul_f32_e32 v1, v238, v1
v_mul_f32_e32 v2, v238, v2
v_mul_f32_e32 v3, v238, v3
v_mul_f32_e32 v4, v238, v4
v_mul_f32_e32 v5, v238, v5
v_mul_f32_e32 v6, v238, v6
v_mul_f32_e32 v7, v238, v7
v_mul_f32_e32 v16, v238, v16
v_mul_f32_e32 v17, v238, v17
v_mul_f32_e32 v18, v238, v18
v_mul_f32_e32 v19, v238, v19
v_mul_f32_e32 v20, v238, v20
v_mul_f32_e32 v21, v238, v21
v_mul_f32_e32 v22, v238, v22
v_mul_f32_e32 v23, v238, v23
v_mul_f32_e32 v32, v238, v32
v_mul_f32_e32 v33, v238, v33
v_mul_f32_e32 v34, v238, v34
v_mul_f32_e32 v35, v238, v35
)"
R"(
v_mul_f32_e32 v36, v238, v36
v_mul_f32_e32 v37, v238, v37
v_mul_f32_e32 v38, v238, v38
v_mul_f32_e32 v39, v238, v39
v_mul_f32_e32 v48, v238, v48
v_mul_f32_e32 v49, v238, v49
v_mul_f32_e32 v50, v238, v50
v_mul_f32_e32 v51, v238, v51
v_mul_f32_e32 v52, v238, v52
v_mul_f32_e32 v53, v238, v53
v_mul_f32_e32 v54, v238, v54
v_mul_f32_e32 v55, v238, v55
s_nop 1
s_branch .Lk_bbb12
.Lk_bra13:
v_max_f32_e32 v236, v236, v135
v_mov_b32_e32 v246, v236
s_nop 1
v_permlane16_swap_b32_e32 v236, v246
v_max_f32_e32 v236, v236, v246
v_mov_b32_e32 v246, v236
s_nop 1
v_permlane32_swap_b32_e32 v236, v246
v_max_f32_e32 v236, v236, v246
v_max_f32_e32 v236, 0, v236
v_exp_f32_e64 v238, -v236
v_sub_f32_e32 v192, v192, v236
v_sub_f32_e32 v193, v193, v236
v_sub_f32_e32 v194, v194, v236
v_sub_f32_e32 v195, v195, v236
v_sub_f32_e32 v160, v160, v236
v_sub_f32_e32 v161, v161, v236
v_sub_f32_e32 v162, v162, v236
v_sub_f32_e32 v163, v163, v236
v_sub_f32_e32 v164, v164, v236
v_sub_f32_e32 v165, v165, v236
v_sub_f32_e32 v166, v166, v236
v_sub_f32_e32 v167, v167, v236
v_sub_f32_e32 v128, v128, v236
v_sub_f32_e32 v129, v129, v236
v_sub_f32_e32 v130, v130, v236
v_sub_f32_e32 v131, v131, v236
v_sub_f32_e32 v132, v132, v236
v_sub_f32_e32 v133, v133, v236
v_sub_f32_e32 v134, v134, v236
v_sub_f32_e32 v135, v135, v236
v_mul_f32_e32 v244, v238, v244
s_nop 1
s_branch .Lk_bba13
.Lk_brb13:
s_nop 7
v_mul_f32_e32 v0, v238, v0
v_mul_f32_e32 v1, v238, v1
v_mul_f32_e32 v2, v238, v2
v_mul_f32_e32 v3, v238, v3
v_mul_f32_e32 v4, v238, v4
v_mul_f32_e32 v5, v238, v5
v_mul_f32_e32 v6, v238, v6
v_mul_f32_e32 v7, v238, v7
v_mul_f32_e32 v16, v238, v16
v_mul_f32_e32 v17, v238, v17
v_mul_f32_e32 v18, v238, v18
v_mul_f32_e32 v19, v238, v19
v_mul_f32_e32 v20, v238, v20
v_mul_f32_e32 v21, v238, v21
v_mul_f32_e32 v22, v238, v22
v_mul_f32_e32 v23, v238, v23
v_mul_f32_e32 v32, v238, v32
v_mul_f32_e32 v33, v238, v33
v_mul_f32_e32 v34, v238, v34
v_mul_f32_e32 v35, v238, v35
v_mul_f32_e32 v36, v238, v36
v_mul_f32_e32 v37, v238, v37
v_mul_f32_e32 v38, v238, v38
v_mul_f32_e32 v39, v238, v39
v_mul_f32_e32 v48, v238, v48
v_mul_f32_e32 v49, v238, v49
v_mul_f32_e32 v50, v238, v50
v_mul_f32_e32 v51, v238, v51
v_mul_f32_e32 v52, v238, v52
v_mul_f32_e32 v53, v238, v53
v_mul_f32_e32 v54, v238, v54
v_mul_f32_e32 v55, v238, v55
s_nop 1
s_branch .Lk_bbb13
.Lk_bra14:
v_max_f32_e32 v236, v236, v167
v_mov_b32_e32 v246, v236
s_nop 1
v_permlane16_swap_b32_e32 v236, v246
v_max_f32_e32 v236, v236, v246
v_mov_b32_e32 v246, v236
s_nop 1
v_permlane32_swap_b32_e32 v236, v246
v_max_f32_e32 v236, v236, v246
v_max_f32_e32 v236, 0, v236
v_exp_f32_e64 v238, -v236
v_sub_f32_e32 v192, v192, v236
v_sub_f32_e32 v193, v193, v236
v_sub_f32_e32 v194, v194, v236
v_sub_f32_e32 v195, v195, v236
v_sub_f32_e32 v144, v144, v236
v_sub_f32_e32 v145, v145, v236
v_sub_f32_e32 v146, v146, v236
v_sub_f32_e32 v147, v147, v236
v_sub_f32_e32 v148, v148, v236
v_sub_f32_e32 v149, v149, v236
v_sub_f32_e32 v150, v150, v236
v_sub_f32_e32 v151, v151, v236
v_sub_f32_e32 v160, v160, v236
v_sub_f32_e32 v161, v161, v236
v_sub_f32_e32 v162, v162, v236
v_sub_f32_e32 v163, v163, v236
v_sub_f32_e32 v164, v164, v236
v_sub_f32_e32 v165, v165, v236
v_sub_f32_e32 v166, v166, v236
v_sub_f32_e32 v167, v167, v236
v_mul_f32_e32 v244, v238, v244
s_nop 1
s_branch .Lk_bba14
.Lk_brb14:
s_nop 7
v_mul_f32_e32 v0, v238, v0
v_mul_f32_e32 v1, v238, v1
v_mul_f32_e32 v2, v238, v2
v_mul_f32_e32 v3, v238, v3
v_mul_f32_e32 v4, v238, v4
v_mul_f32_e32 v5, v238, v5
v_mul_f32_e32 v6, v238, v6
v_mul_f32_e32 v7, v238, v7
v_mul_f32_e32 v16, v238, v16
v_mul_f32_e32 v17, v238, v17
v_mul_f32_e32 v18, v238, v18
v_mul_f32_e32 v19, v238, v19
v_mul_f32_e32 v20, v238, v20
v_mul_f32_e32 v21, v238, v21
v_mul_f32_e32 v22, v238, v22
v_mul_f32_e32 v23, v238, v23
v_mul_f32_e32 v32, v238, v32
v_mul_f32_e32 v33, v238, v33
v_mul_f32_e32 v34, v238, v34
v_mul_f32_e32 v35, v238, v35
v_mul_f32_e32 v36, v238, v36
v_mul_f32_e32 v37, v238, v37
v_mul_f32_e32 v38, v238, v38
v_mul_f32_e32 v39, v238, v39
v_mul_f32_e32 v48, v238, v48
v_mul_f32_e32 v49, v238, v49
v_mul_f32_e32 v50, v238, v50
v_mul_f32_e32 v51, v238, v51
v_mul_f32_e32 v52, v238, v52
v_mul_f32_e32 v53, v238, v53
v_mul_f32_e32 v54, v238, v54
v_mul_f32_e32 v55, v238, v55
s_nop 1
s_branch .Lk_bbb14
.Lk_bra15:
v_max_f32_e32 v236, v236, v151
v_mov_b32_e32 v246, v236
s_nop 1
v_permlane16_swap_b32_e32 v236, v246
v_max_f32_e32 v236, v236, v246
v_mov_b32_e32 v246, v236
s_nop 1
v_permlane32_swap_b32_e32 v236, v246
v_max_f32_e32 v236, v236, v246
v_max_f32_e32 v236, 0, v236
v_exp_f32_e64 v238, -v236
v_sub_f32_e32 v192, v192, v236
v_sub_f32_e32 v193, v193, v236
v_sub_f32_e32 v194, v194, v236
v_sub_f32_e32 v195, v195, v236
v_sub_f32_e32 v128, v128, v236
v_sub_f32_e32 v129, v129, v236
v_sub_f32_e32 v130, v130, v236
v_sub_f32_e32 v131, v131, v236
v_sub_f32_e32 v132, v132, v236
v_sub_f32_e32 v133, v133, v236
v_sub_f32_e32 v134, v134, v236
v_sub_f32_e32 v135, v135, v236
v_sub_f32_e32 v144, v144, v236
v_sub_f32_e32 v145, v145, v236
v_sub_f32_e32 v146, v146, v236
v_sub_f32_e32 v147, v147, v236
v_sub_f32_e32 v148, v148, v236
v_sub_f32_e32 v149, v149, v236
v_sub_f32_e32 v150, v150, v236
v_sub_f32_e32 v151, v151, v236
v_mul_f32_e32 v244, v238, v244
s_nop 1
s_branch .Lk_bba15
.Lk_brb15:
s_nop 7
v_mul_f32_e32 v0, v238, v0
v_mul_f32_e32 v1, v238, v1
v_mul_f32_e32 v2, v238, v2
v_mul_f32_e32 v3, v238, v3
v_mul_f32_e32 v4, v238, v4
v_mul_f32_e32 v5, v238, v5
v_mul_f32_e32 v6, v238, v6
)"
R"(
v_mul_f32_e32 v7, v238, v7
v_mul_f32_e32 v16, v238, v16
v_mul_f32_e32 v17, v238, v17
v_mul_f32_e32 v18, v238, v18
v_mul_f32_e32 v19, v238, v19
v_mul_f32_e32 v20, v238, v20
v_mul_f32_e32 v21, v238, v21
v_mul_f32_e32 v22, v238, v22
v_mul_f32_e32 v23, v238, v23
v_mul_f32_e32 v32, v238, v32
v_mul_f32_e32 v33, v238, v33
v_mul_f32_e32 v34, v238, v34
v_mul_f32_e32 v35, v238, v35
v_mul_f32_e32 v36, v238, v36
v_mul_f32_e32 v37, v238, v37
v_mul_f32_e32 v38, v238, v38
v_mul_f32_e32 v39, v238, v39
v_mul_f32_e32 v48, v238, v48
v_mul_f32_e32 v49, v238, v49
v_mul_f32_e32 v50, v238, v50
v_mul_f32_e32 v51, v238, v51
v_mul_f32_e32 v52, v238, v52
v_mul_f32_e32 v53, v238, v53
v_mul_f32_e32 v54, v238, v54
v_mul_f32_e32 v55, v238, v55
s_nop 1
s_branch .Lk_bbb15
)"
    :: "s"(__builtin_amdgcn_kernarg_segment_ptr()), "s"(wid), "s"(blockIdx.x), "s"((unsigned)(size_t)lds)
    : "v0", "v1", "v2", "v3", "v4", "v5", "v6", "v7", "v8", "v9", "v10", "v11", "v12", "v13", "v14", "v15", "v16", "v17", "v18", "v19", "v20", "v21", "v22", "v23", "v24", "v25", "v26", "v27", "v28", "v29", "v30", "v31", "v32", "v33", "v34", "v35", "v36", "v37", "v38", "v39", "v40", "v41", "v42", "v43", "v44", "v45", "v46", "v47", "v48", "v49", "v50", "v51", "v52", "v53", "v54", "v55", "v56", "v57", "v58", "v59", "v60", "v61", "v62", "v63", "v64", "v65", "v66", "v67", "v68", "v69", "v70", "v71", "v72", "v73", "v74", "v75", "v76", "v77", "v78", "v79", "v80", "v81", "v82", "v83", "v84", "v85", "v86", "v87", "v88", "v89", "v90", "v91", "v92", "v93", "v94", "v95", "v96", "v97", "v98", "v99", "v100", "v101", "v102", "v103", "v104", "v105", "v106", "v107", "v108", "v109", "v110", "v111", "v112", "v113", "v114", "v115", "v116", "v117", "v118", "v119", "v120", "v121", "v122", "v123", "v124", "v125", "v126", "v127", "v128", "v129", "v130", "v131", "v132", "v133", "v134", "v135", "v136", "v137", "v138", "v139", "v140", "v141", "v142", "v143", "v144", "v145", "v146", "v147", "v148", "v149", "v150", "v151", "v152", "v153", "v154", "v155", "v156", "v157", "v158", "v159", "v160", "v161", "v162", "v163", "v164", "v165", "v166", "v167", "v168", "v169", "v170", "v171", "v172", "v173", "v174", "v175", "v176", "v177", "v178", "v179", "v180", "v181", "v182", "v183", "v184", "v185", "v186", "v187", "v188", "v189", "v190", "v191", "v192", "v193", "v194", "v195", "v196", "v197", "v198", "v199", "v200", "v201", "v202", "v203", "v204", "v205", "v206", "v207", "v208", "v209", "v210", "v211", "v212", "v213", "v214", "v215", "v216", "v217", "v218", "v219", "v220", "v221", "v222", "v223", "v224", "v225", "v226", "v227", "v228", "v229", "v230", "v231", "v232", "v233", "v234", "v235", "v236", "v237", "v238", "v239", "v240", "v241", "v242", "v243", "v244", "v245", "v246", "v247", "v248", "v249", "v250", "v251", "v252", "v253", "v254", "v255", "a0", "a1", "a2", "a3", "a4", "a5", "a6", "a7", "a8", "a9", "a10", "a11", "a12", "a13", "a14", "a15", "a16", "a17", "a18", "a19", "a20", "a21", "a22", "a23", "a24", "a25", "a26", "a27", "a28", "a29", "a30", "a31", "a32", "a33", "a34", "a35", "a36", "a37", "a38", "a39", "a40", "a41", "a42", "a43", "a44", "a45", "a46", "a47", "a48", "a49", "a50", "a51", "a52", "a53", "a54", "a55", "a56", "a57", "a58", "a59", "a60", "a61", "a62", "a63", "a64", "a65", "a66", "a67", "a68", "a69", "a70", "a71", "a72", "a73", "a74", "a75", "a76", "a77", "a78", "a79", "a80", "a81", "a82", "a83", "a84", "a85", "a86", "a87", "a88", "a89", "a90", "a91", "a92", "a93", "a94", "a95", "a96", "a97", "a98", "a99", "a100", "a101", "a102", "a103", "a104", "a105", "a106", "a107", "a108", "a109", "a110", "a111", "a112", "a113", "a114", "a115", "a116", "a117", "a118", "a119", "a120", "a121", "a122", "a123", "a124", "a125", "a126", "a127", "a128", "a129", "a130", "a131", "a132", "a133", "a134", "a135", "a136", "a137", "a138", "a139", "a140", "a141", "a142", "a143", "a144", "a145", "a146", "a147", "a148", "a149", "a150", "a151", "a152", "a153", "a154", "a155", "a156", "a157", "a158", "a159", "a160", "a161", "a162", "a163", "a164", "a165", "a166", "a167", "a168", "a169", "a170", "a171", "a172", "a173", "a174", "a175", "a176", "a177", "a178", "a179", "a180", "a181", "a182", "a183", "a184", "a185", "a186", "a187", "a188", "a189", "a190", "a191", "a192", "a193", "a194", "a195", "a196", "a197", "a198", "a199", "a200", "a201", "a202", "a203", "a204", "a205", "a206", "a207", "a208", "a209", "a210", "a211", "a212", "a213", "a214", "a215", "a216", "a217", "a218", "a219", "a220", "a221", "a222", "a223", "a224", "a225", "a226", "a227", "a228", "a229", "a230", "a231", "a232", "a233", "a234", "a235", "a236", "a237", "a238", "a239", "a240", "a241", "a242", "a243", "a244", "a245", "a246", "a247", "a248", "a249", "a250", "a251", "a252", "a253", "a254", "a255", "s8", "s9", "s10", "s11", "s12", "s13", "s14", "s15", "s16", "s17", "s18", "s19", "s20", "s21", "s22", "s23", "s24", "s25", "s26", "s27", "s28", "s29", "s30", "s31", "s34", "s35", "s36", "s37", "s38", "s39", "s40", "s41", "s42", "s43", "s44", "s45", "s46", "s47", "s48", "s49", "s50", "s51", "s52", "s53", "s54", "s55", "s56", "s57", "s58", "s59", "s60", "s61", "s62", "s63", "s64", "s65", "s66", "s67", "s68", "s69", "s70", "s71", "s72", "s73", "s74", "s75", "s76", "s77", "s78", "s79", "s80", "s81", "s82", "s83", "s84", "s85", "s86", "s87", "s88", "s89", "s90", "s91", "s92", "s93", "s94", "s95", "s96", "s97", "s98", "vcc", "scc", "memory");
}

extern "C" void kernel_launch(void* const* d_in, const int* in_sizes, int n_in, void* d_out, int out_size, void* d_ws, size_t ws_size,
                              hipStream_t stream) {
  KArgs a;
  for (int br = 0; br < 2; ++br) {
    for (int i = 0; i < 5; ++i) a.p[8 * br + i] = d_in[5 * br + i];
    a.p[8 * br + 5] = d_out; a.p[8 * br + 6] = d_ws; a.p[8 * br + 7] = d_ws;
  }
  mha2_fused_kernel<<<256, 256, 0, stream>>>(a);
}
```
